```python
import torch

def _softmax(x, axis=-1):
    return torch.softmax(x, dim=axis)
def _gelu(x):
    return torch.nn.functional.gelu(x, approximate="tanh")
def _silu(x):
    return torch.nn.functional.silu(x)
def _sigmoid(x):
    return torch.sigmoid(x)
def _layernorm(x, axis=-1, eps=1e-5):
    m = x.mean(dim=axis, keepdim=True)
    v = x.var(dim=axis, keepdim=True, unbiased=False)
    return (x - m) / torch.sqrt(v + eps)
def _scatter_add(tgt, idx, src, axis=0):
    return tgt.clone().index_add_(axis, idx, src)
def _segment_sum(src, seg, n):
    out = torch.zeros((n,) + src.shape[1:], dtype=src.dtype)
    return out.index_add_(0, seg, src)
def _pad(x, pw):
    flat = tuple(v for pair in reversed(pw) for v in pair)
    return torch.nn.functional.pad(x, flat)

kernel_name = 'gated_mix__compressed_kv_cla__sliding_local_dilated__prompt'

def setup_inputs():

    _w = (lambda a: (lambda t: t.to(torch.bfloat16) if t.is_floating_point() else t)(torch.as_tensor(a)))
    def _A_setup():
        B, H, Sq, Sk, D, M = 1, 16, 2048, 2048, 256, 448
        Dc = 4 * D
        return {
            "x": _w((lambda *s: torch.randn(*s, dtype=torch.bfloat16) * 0.05)(B, Sq, M)),
            "c_kv": _w((lambda *s: torch.randn(*s, dtype=torch.bfloat16) * 1.6)(B, Sk, Dc)),
            "w_q": _w((lambda *s: torch.randn(*s, dtype=torch.bfloat16) * 3.2)(M, H, D) * (M ** -0.5)),
            "w_uk": _w((lambda *s: torch.randn(*s, dtype=torch.bfloat16) * 3.2)(Dc, H, D) * (Dc ** -0.5)),
            "w_uv": _w((lambda *s: torch.randn(*s, dtype=torch.bfloat16) * 1.6)(Dc, H, D) * (Dc ** -0.5)),
            "w_o": _w((lambda *s: torch.randn(*s, dtype=torch.bfloat16) * 1.6)(H, D, M) * ((H * D) ** -0.5)),
        }
    _A_in = _A_setup()

    def _B_setup():
        B, S, D = 1, 2048, 448
        return {
            "x": _w((lambda *s: torch.randn(*s, dtype=torch.bfloat16) * 0.05)(B, S, D)),
            "wqkv": _w((lambda *s: torch.randn(*s, dtype=torch.bfloat16) * 1.6)(D, 3 * D)),
            "wo": _w((lambda *s: torch.randn(*s, dtype=torch.bfloat16) * 0.05)(D, D)),
        }
    _B_in = _B_setup()

    return {
        "A_x": _A_in["x"],
        "A_c_kv": _A_in["c_kv"],
        "A_w_q": _A_in["w_q"],
        "A_w_uk": _A_in["w_uk"],
        "A_w_uv": _A_in["w_uv"],
        "A_w_o": _A_in["w_o"],
        "B_x": _B_in["x"],
        "B_wqkv": _B_in["wqkv"],
        "B_wo": _B_in["wo"],
        "gate_w": _w((lambda *s: torch.randn(*s, dtype=torch.bfloat16) * 0.05)(448) * 16.0),
    }

def reference(A_x, A_c_kv, A_w_q, A_w_uk, A_w_uv, A_w_o, B_x, B_wqkv, B_wo, gate_w):
    def _A_ref(x, c_kv, w_q, w_uk, w_uv, w_o):
        B, Sq, M = x.shape[0], x.shape[1], x.shape[2]
        Sk = c_kv.shape[1]
        H, D = w_q.shape[1], w_q.shape[2]
        q = torch.einsum("bqm,mhd->bhqd", x, w_q)
        k = torch.einsum("bkc,chd->bhkd", c_kv, w_uk)
        v = torch.einsum("bkc,chd->bhkd", c_kv, w_uv)
        scores = torch.einsum("bhqd,bhkd->bhqk", q, k) * (D ** -0.5)
        off = Sk - Sq
        qi = torch.arange(Sq)[:, None]
        kj = torch.arange(Sk)[None, :]
        cmask = kj <= (qi + off)
        scores = torch.where(cmask[None, None], scores, -1e9)
        p = _softmax(scores, axis=-1)
        ctx = torch.einsum("bhqk,bhkd->bhqd", p, v)
        return torch.einsum("bhqd,hdm->bqm", ctx, w_o)

    def _B_ref(x, wqkv, wo):
        B, S, D = x.shape
        H = 16; Dh = D // H
        W, dil = 8, 2
        qkv = (lambda x, *d: x.permute(*d))((x @ wqkv).reshape(B, S, 3, H, Dh), 2, 0, 3, 1, 4)
        q, k, v = qkv[0], qkv[1], qkv[2]
        scale = Dh ** -0.5
        pos = torch.arange(S)
        off = torch.arange(W) * dil
        idx = pos[:, None] - off[None, :]
        mask = idx >= 0
        idx_c = torch.where(mask, idx, 0).reshape(-1)
        kg = k[:, :, idx_c, :].reshape(B, H, S, W, Dh)
        vg = v[:, :, idx_c, :].reshape(B, H, S, W, Dh)
        logits = torch.einsum("bhsd,bhswd->bhsw", q, kg) * scale
        logits = torch.where(mask[None, None, :, :], logits, -1e9)
        attn = _softmax(logits, axis=-1)
        o = torch.einsum("bhsw,bhswd->bhsd", attn, vg)
        return (lambda x, *d: x.permute(*d))(o, 0, 2, 1, 3).reshape(B, S, D) @ wo

    x = A_x
    g = _sigmoid((x * gate_w).sum(-1, keepdims=True))
    a = _A_ref(x, A_c_kv, A_w_q, A_w_uk, A_w_uv, A_w_o)
    b = _B_ref(x, B_wqkv, B_wo)
    return g * a + (1 - g) * b
```

```cpp
#include <hip/hip_runtime.h>
#include <hip/hip_bf16.h>
#include <stdint.h>
typedef unsigned short u16;
typedef unsigned int u32;
typedef float f32x4 __attribute__((ext_vector_type(4)));
typedef u32 u32x2 __attribute__((ext_vector_type(2)));
typedef u32 u32x4 __attribute__((ext_vector_type(4)));
__device__ __forceinline__ float bflo(u32 w) { return __uint_as_float(w << 16); }
__device__ __forceinline__ float bfhi(u32 w) { return __uint_as_float(w & 0xffff0000u); }
__device__ __forceinline__ u32 pack2(float a, float b) {
  typedef float f2 __attribute__((ext_vector_type(2))); typedef __bf16 b2 __attribute__((ext_vector_type(2)));
  f2 v = {a, b}; b2 r = __builtin_convertvector(v, b2); return __builtin_bit_cast(u32, r);
}
typedef __attribute__((address_space(1))) unsigned long long* g64_t;
typedef __attribute__((address_space(1))) u32* g32_t;
__device__ __forceinline__ void st_dev64(void* p, u32x2 v) { __hip_atomic_store((g64_t)p, ((unsigned long long)v[1] << 32) | v[0], __ATOMIC_RELAXED, __HIP_MEMORY_SCOPE_AGENT); }
__device__ __forceinline__ void st_dev32(void* p, u32 v) { __hip_atomic_store((g32_t)p, v, __ATOMIC_RELAXED, __HIP_MEMORY_SCOPE_AGENT); }
__device__ __forceinline__ u32 ld_dev32(const void* p) { return __hip_atomic_load((g32_t)p, __ATOMIC_RELAXED, __HIP_MEMORY_SCOPE_AGENT); }
typedef __bf16 bf16x2_t __attribute__((ext_vector_type(2)));
__device__ __forceinline__ float dot2(u32 a, u32 b, float c) {
  return __builtin_amdgcn_fdot2_f32_bf16(__builtin_bit_cast(bf16x2_t, a), __builtin_bit_cast(bf16x2_t, b), c, false);
}

#define WS_Q 0ull
#define WS_K 16777216ull
#define WS_V 33554432ull
#define WS_CTX 58720256ull
#define WS_PART 79691776ull
#define WS_PSTAT 97517568ull
#define WS_SLAB 100663296ull
#define WS_GATE 125829120ull
#define WS_FLAGS 126877696ull
#define WS_DBG 130023424ull
#define F_EPOCH 126877696ull
#define F_ARR 126878720ull
#define F_BL 126881792ull
#define CTX_KSTEPS 142
#define TL_END1(ws)

struct GateRegs { u32 wv[4]; u32 xv[4][4]; };
__device__ __forceinline__ void gate_load(GateRegs& r, const u16* x, const u16* gw, int tok0, int lane) {
#pragma unroll
  for (int k = 0; k < 4; ++k) { int e = lane + 64 * k; r.wv[k] = e < 224 ? ((const u32*)gw)[e] : 0u; }
#pragma unroll
  for (int q = 0; q < 4; ++q) {
    const u32* xr = (const u32*)(x + (size_t)(tok0 + q) * 448);
#pragma unroll
    for (int k = 0; k < 4; ++k) { int e = lane + 64 * k; r.xv[q][k] = xr[e < 224 ? e : 223]; }
  }
}
__device__ __forceinline__ float row_sum16(float s) {
  s += __int_as_float(__builtin_amdgcn_update_dpp(0, __float_as_int(s), 0x111, 0xf, 0xf, true));
  s += __int_as_float(__builtin_amdgcn_update_dpp(0, __float_as_int(s), 0x112, 0xf, 0xf, true));
  s += __int_as_float(__builtin_amdgcn_update_dpp(0, __float_as_int(s), 0x114, 0xf, 0xf, true));
  s += __int_as_float(__builtin_amdgcn_update_dpp(0, __float_as_int(s), 0x118, 0xf, 0xf, true));
  return s;
}
__device__ __forceinline__ void gate_finish(const GateRegs& r, char* ws, int tok0, int lane) {
  float* g = (float*)(ws + WS_GATE);
  float tot[4];
#pragma unroll
  for (int q = 0; q < 4; ++q) {
    float s = 0.f;
#pragma unroll
    for (int k = 0; k < 4; ++k) s = dot2(r.xv[q][k], r.wv[k], s);
    const int si = __float_as_int(row_sum16(s));
    tot[q] = (__int_as_float(__builtin_amdgcn_readlane(si, 15)) + __int_as_float(__builtin_amdgcn_readlane(si, 31))) +
             (__int_as_float(__builtin_amdgcn_readlane(si, 47)) + __int_as_float(__builtin_amdgcn_readlane(si, 63)));
  }
  const float v = lane == 0 ? tot[0] : lane == 1 ? tot[1] : lane == 2 ? tot[2] : tot[3];
  if (lane < 4) st_dev32(g + tok0 + lane, __float_as_uint(1.f / (1.f + __expf(-v))));
}


__device__ __forceinline__ void local_attn(char* ws, const char* lds, int rt, int hp, int tid) {
  const int hh = tid >> 7, t = 16 + (tid & 127);
  const int tok = 128 * rt - 16 + t;
  const char* qp = lds + t * 400 + hh * 64;
  u32x4 q[4], k[8][4], v[8][4];
#pragma unroll
  for (int i = 0; i < 4; ++i) q[i] = *(const u32x4*)(qp + 16 * i);
#pragma unroll
  for (int w8 = 0; w8 < 8; ++w8)
#pragma unroll
    for (int i = 0; i < 4; ++i) k[w8][i] = *(const u32x4*)(qp - w8 * 800 + 128 + 16 * i);
#pragma unroll
  for (int w8 = 0; w8 < 8; ++w8)
#pragma unroll
    for (int i = 0; i < 4; ++i) v[w8][i] = *(const u32x4*)(qp - w8 * 800 + 256 + 16 * i);
  __builtin_amdgcn_sched_barrier(0);
  float lg[8];
#pragma unroll
  for (int w8 = 0; w8 < 8; ++w8) lg[w8] = 0.f;
#pragma unroll
  for (int d = 0; d < 14; ++d)
#pragma unroll
    for (int w8 = 0; w8 < 8; ++w8) lg[w8] = dot2(q[d >> 2][d & 3], k[w8][d >> 2][d & 3], lg[w8]);
  float mx = -3.0e38f;
#pragma unroll
  for (int w8 = 0; w8 < 8; ++w8) { lg[w8] = (tok - 2 * w8 >= 0) ? lg[w8] * 0.18898223650461361f : -1e9f; mx = fmaxf(mx, lg[w8]); }
  float sum = 0.f;
#pragma unroll
  for (int w8 = 0; w8 < 8; ++w8) { lg[w8] = __expf(lg[w8] - mx); sum += lg[w8]; }
  const float sc = 1.f / sum;
  u32 pp[4];
#pragma unroll
  for (int j = 0; j < 4; ++j) pp[j] = pack2(lg[2*j] * sc, lg[2*j+1] * sc);
  float o[28];
#pragma unroll
  for (int i = 0; i < 28; ++i) o[i] = 0.f;
#pragma unroll
  for (int j = 0; j < 4; ++j)
#pragma unroll
    for (int d = 0; d < 14; ++d) {
      const u32 a = v[2*j][d >> 2][d & 3], b = v[2*j+1][d >> 2][d & 3];
      o[2*d]   = dot2(__builtin_amdgcn_perm(b, a, 0x05040100u), pp[j], o[2*d]);
      o[2*d+1] = dot2(__builtin_amdgcn_perm(b, a, 0x07060302u), pp[j], o[2*d+1]);
    }
#pragma unroll
  for (int i = 0; i < 7; ++i) {
    int kk = (2 * hp + hh) * 28 + 4 * i;
    size_t off = ((size_t)(tok >> 4) * CTX_KSTEPS + 128 + (kk >> 5)) * 1024 + (size_t)((((kk >> 2) & 3) * 16 + (tok & 15)) * 16 + ((kk >> 4) & 1) * 8);
    u32x2 w = {pack2(o[4*i], o[4*i+1]), pack2(o[4*i+2], o[4*i+3])};
    st_dev64(ws + WS_CTX + off, w);
  }
}


extern "C" __global__ __launch_bounds__(256, 1) void gm_fused(const void* ckv, const void* wuk, const void* wuv, const void* x, const void* wq, const void* wqkv, void* ws, const void* gw, const void* wo, const void* bwo, void* out) {
  extern __shared__ __attribute__((aligned(16))) char lds[];
  typedef const __attribute__((address_space(4))) unsigned long long* karg_t;
  karg_t ka = (karg_t)__builtin_amdgcn_kernarg_segment_ptr();
#define ARG(i) ((char*)(__attribute__((address_space(1))) char*)ka[i])
  int tag;
  int wid = __builtin_amdgcn_readfirstlane((int)(threadIdx.x >> 6));
  asm volatile(
"s_getpc_b64 s[76:77]\n"
"L_cp_k1_%=:\n"
"v_mbcnt_lo_u32_b32 v2, -1, 0\n"
"v_mbcnt_hi_u32_b32 v2, -1, v2\n"
"s_lshl_b32 s78, %9, 6\n"
"v_add_u32 v2, s78, v2\n"
"v_lshlrev_b32 v2, 7, v2\n"
"v_add_u32 v3, 0, v2\n"
"v_min_u32 v3, GM_CODE_END-L_cp_k1_%=-4, v3\n"
"v_and_b32 v3, 0xfffffffc, v3\n"
"global_load_dword v51, v3, s[76:77]\n"
"v_add_u32 v3, 32768, v2\n"
"v_min_u32 v3, GM_CODE_END-L_cp_k1_%=-4, v3\n"
"v_and_b32 v3, 0xfffffffc, v3\n"
"global_load_dword v51, v3, s[76:77]\n"
"v_add_u32 v3, 65536, v2\n"
"v_min_u32 v3, GM_CODE_END-L_cp_k1_%=-4, v3\n"
"v_and_b32 v3, 0xfffffffc, v3\n"
"global_load_dword v51, v3, s[76:77]\n"
"v_add_u32 v3, 98304, v2\n"
"v_min_u32 v3, GM_CODE_END-L_cp_k1_%=-4, v3\n"
"v_and_b32 v3, 0xfffffffc, v3\n"
"global_load_dword v51, v3, s[76:77]\n"
"v_add_u32 v3, 131072, v2\n"
"v_min_u32 v3, GM_CODE_END-L_cp_k1_%=-4, v3\n"
"v_and_b32 v3, 0xfffffffc, v3\n"
"global_load_dword v51, v3, s[76:77]\n"
"v_mbcnt_lo_u32_b32 v0, -1, 0\n"
"v_mbcnt_hi_u32_b32 v0, -1, v0\n"
"s_and_b32 s71, %8, 7\n"
"s_lshr_b32 s72, %8, 3\n"
"s_mov_b32 s73, %9\n"
"s_lshr_b32 s74, s73, 1\n"
"s_and_b32 s75, s73, 1\n"
"s_mov_b64 s[44:45], %7\n"
"s_and_b32 s45, s45, 0xffff\n"
"s_mov_b32 s46, 268435456\n"
"s_mov_b32 s47, 0x20000\n"
"v_mov_b32 v61, 0\n"
"s_mov_b32 s76, 126877696\n"
"buffer_load_dword v61, v61, s[44:47], s76 offen sc1\n"
"s_mov_b64 s[36:37], %1\n"
"s_and_b32 s37, s37, 0xffff\n"
"s_mov_b32 s38, 4194304\n"
"s_mov_b32 s39, 0x20000\n"
"s_lshr_b32 s82, s72, 1\n"
"s_and_b32 s82, s82, 1\n"
"s_cmp_eq_u32 s82, 0\n"
"s_cselect_b64 s[40:41], %2, %3\n"
"s_and_b32 s41, s41, 0xffff\n"
"s_mov_b32 s42, 8388608\n"
"s_mov_b32 s43, 0x20000\n"
"s_and_b32 s77, s72, 1\n"
"s_lshl_b32 s78, s71, 1\n"
"s_add_u32 s77, s77, s78\n"
"s_lshr_b32 s78, s72, 2\n"
"s_lshl_b32 s48, s78, 19\n"
"s_lshl_b32 s49, s77, 9\n"
"s_mov_b32 s50, 524288\n"
"s_mov_b32 s51, 16\n"
"s_mov_b32 s70, 0x3f800000\n"
"s_mov_b32 s54, 0\n"
"s_lshl_b32 s53, s77, 20\n"
"s_lshl_b32 s79, s78, 17\n"
"s_add_u32 s53, s53, s79\n"
"s_lshl_b32 s79, s74, 16\n"
"s_add_u32 s53, s53, s79\n"
"s_add_u32 s79, s82, 12\n"
"s_lshl_b32 s79, s75, s79\n"
"s_add_u32 s53, s53, s79\n"
"s_lshl_b32 s79, s82, 24\n"
"s_add_u32 s79, s79, 16777216\n"
"s_add_u32 s53, s53, s79\n"
"s_mov_b32 s80, 2048\n"
"s_mov_b32 s81, 8192\n"
"v_lshrrev_b32 v1, 3, v0\n"
"v_and_b32 v2, 7, v0\n"
"v_lshrrev_b32 v3, 4, v0\n"
"v_xor_b32 v2, v2, v3\n"
"v_lshlrev_b32 v2, 4, v2\n"
"v_mul_u32_u24 v1, s80, v1\n"
"v_add_u32 v9, v1, v2\n"
"v_xor_b32 v10, 64, v9\n"
"s_lshl_b32 s76, s73, 6\n"
"s_add_u32 s76, s76, 0\n"
"s_mul_i32 s76, s76, s80\n"
"v_add_u32 v16, s76, v9\n"
"s_lshl_b32 s76, s73, 6\n"
"s_add_u32 s76, s76, 8\n"
"s_mul_i32 s76, s76, s80\n"
"v_add_u32 v17, s76, v10\n"
"s_lshl_b32 s76, s73, 6\n"
"s_add_u32 s76, s76, 16\n"
"s_mul_i32 s76, s76, s80\n"
"v_add_u32 v18, s76, v9\n"
"s_lshl_b32 s76, s73, 6\n"
"s_add_u32 s76, s76, 24\n"
"s_mul_i32 s76, s76, s80\n"
"v_add_u32 v19, s76, v10\n"
"s_lshl_b32 s76, s73, 6\n"
"s_add_u32 s76, s76, 32\n"
"s_mul_i32 s76, s76, s80\n"
"v_add_u32 v20, s76, v9\n"
"s_lshl_b32 s76, s73, 6\n"
"s_add_u32 s76, s76, 40\n"
"s_mul_i32 s76, s76, s80\n"
"v_add_u32 v21, s76, v10\n"
"s_lshl_b32 s76, s73, 6\n"
"s_add_u32 s76, s76, 48\n"
"s_mul_i32 s76, s76, s80\n"
"v_add_u32 v22, s76, v9\n"
"s_lshl_b32 s76, s73, 6\n"
"s_add_u32 s76, s76, 56\n"
"s_mul_i32 s76, s76, s80\n"
"v_add_u32 v23, s76, v10\n"
"v_lshrrev_b32 v1, 2, v0\n"
"v_and_b32 v1, 3, v1\n"
"v_mul_u32_u24 v1, s81, v1\n"
"v_lshrrev_b32 v2, 4, v0\n"
"v_lshlrev_b32 v2, 6, v2\n"
"v_and_b32 v3, 3, v0\n"
"v_lshlrev_b32 v3, 4, v3\n"
"v_add3_u32 v9, v1, v2, v3\n"
"v_xor_b32 v10, 32, v9\n"
"s_lshl_b32 s76, s73, 4\n"
"s_add_u32 s76, s76, 0\n"
"s_mul_i32 s76, s76, s81\n"
"v_add_u32 v24, s76, v9\n"
"s_lshl_b32 s76, s73, 4\n"
"s_add_u32 s76, s76, 0\n"
"s_mul_i32 s76, s76, s81\n"
"s_add_u32 s76, s76, 256\n"
"v_add_u32 v25, s76, v9\n"
"s_lshl_b32 s76, s73, 4\n"
"s_add_u32 s76, s76, 4\n"
"s_mul_i32 s76, s76, s81\n"
"v_add_u32 v26, s76, v9\n"
"s_lshl_b32 s76, s73, 4\n"
"s_add_u32 s76, s76, 4\n"
"s_mul_i32 s76, s76, s81\n"
"s_add_u32 s76, s76, 256\n"
"v_add_u32 v27, s76, v9\n"
"s_lshl_b32 s76, s73, 4\n"
"s_add_u32 s76, s76, 8\n"
"s_mul_i32 s76, s76, s81\n"
"v_add_u32 v28, s76, v10\n"
"s_lshl_b32 s76, s73, 4\n"
"s_add_u32 s76, s76, 8\n"
"s_mul_i32 s76, s76, s81\n"
"s_add_u32 s76, s76, 256\n"
"v_add_u32 v29, s76, v10\n"
"s_lshl_b32 s76, s73, 4\n"
"s_add_u32 s76, s76, 12\n"
"s_mul_i32 s76, s76, s81\n"
"v_add_u32 v30, s76, v10\n"
"s_lshl_b32 s76, s73, 4\n"
"s_add_u32 s76, s76, 12\n"
"s_mul_i32 s76, s76, s81\n"
"s_add_u32 s76, s76, 256\n"
"v_add_u32 v31, s76, v10\n"
"s_lshl_b32 s52, s73, 13\n"
"s_mov_b32 m0, s52\n"
"s_nop 0\n"
"buffer_load_dwordx4 v16, s[36:39], s48 offen  lds\n"
"s_add_u32 m0, m0, 0x400\n"
"s_nop 0\n"
"buffer_load_dwordx4 v17, s[36:39], s48 offen  lds\n"
"s_add_u32 m0, m0, 0x400\n"
"s_nop 0\n"
"buffer_load_dwordx4 v18, s[36:39], s48 offen  lds\n"
"s_add_u32 m0, m0, 0x400\n"
"s_nop 0\n"
"buffer_load_dwordx4 v19, s[36:39], s48 offen  lds\n"
"s_add_u32 m0, m0, 0x400\n"
"s_nop 0\n"
"buffer_load_dwordx4 v20, s[36:39], s48 offen  lds\n"
"s_add_u32 m0, m0, 0x400\n"
"s_nop 0\n"
"buffer_load_dwordx4 v21, s[36:39], s48 offen  lds\n"
"s_add_u32 m0, m0, 0x400\n"
"s_nop 0\n"
"buffer_load_dwordx4 v22, s[36:39], s48 offen  lds\n"
"s_add_u32 m0, m0, 0x400\n"
"s_nop 0\n"
"buffer_load_dwordx4 v23, s[36:39], s48 offen  lds\n"
"s_add_u32 m0, m0, 25600\n"
"s_nop 0\n"
"buffer_load_dwordx4 v24, s[40:43], s49 offen  lds\n"
"s_add_u32 m0, m0, 0x400\n"
"s_nop 0\n"
"buffer_load_dwordx4 v25, s[40:43], s49 offen  lds\n"
"s_add_u32 m0, m0, 0x400\n"
"s_nop 0\n"
"buffer_load_dwordx4 v26, s[40:43], s49 offen  lds\n"
"s_add_u32 m0, m0, 0x400\n"
"s_nop 0\n"
"buffer_load_dwordx4 v27, s[40:43], s49 offen  lds\n"
"s_add_u32 m0, m0, 0x400\n"
"s_nop 0\n"
"buffer_load_dwordx4 v28, s[40:43], s49 offen  lds\n"
"s_add_u32 m0, m0, 0x400\n"
"s_nop 0\n"
"buffer_load_dwordx4 v29, s[40:43], s49 offen  lds\n"
"s_add_u32 m0, m0, 0x400\n"
"s_nop 0\n"
"buffer_load_dwordx4 v30, s[40:43], s49 offen  lds\n"
"s_add_u32 m0, m0, 0x400\n"
"s_nop 0\n"
"buffer_load_dwordx4 v31, s[40:43], s49 offen  lds\n"
"s_add_u32 s48, s48, 128\n"
"s_add_u32 s49, s49, s50\n"
"s_xor_b32 s52, s52, 0x10000\n"
"s_mov_b32 m0, s52\n"
"s_nop 0\n"
"buffer_load_dwordx4 v16, s[36:39], s48 offen  lds\n"
"s_add_u32 m0, m0, 0x400\n"
"s_nop 0\n"
"buffer_load_dwordx4 v17, s[36:39], s48 offen  lds\n"
"s_add_u32 m0, m0, 0x400\n"
"s_nop 0\n"
"buffer_load_dwordx4 v18, s[36:39], s48 offen  lds\n"
"s_add_u32 m0, m0, 0x400\n"
"s_nop 0\n"
"buffer_load_dwordx4 v19, s[36:39], s48 offen  lds\n"
"s_add_u32 m0, m0, 0x400\n"
"s_nop 0\n"
"buffer_load_dwordx4 v20, s[36:39], s48 offen  lds\n"
"s_add_u32 m0, m0, 0x400\n"
"s_nop 0\n"
"buffer_load_dwordx4 v21, s[36:39], s48 offen  lds\n"
"s_add_u32 m0, m0, 0x400\n"
"s_nop 0\n"
"buffer_load_dwordx4 v22, s[36:39], s48 offen  lds\n"
"s_add_u32 m0, m0, 0x400\n"
"s_nop 0\n"
"buffer_load_dwordx4 v23, s[36:39], s48 offen  lds\n"
"s_add_u32 m0, m0, 25600\n"
"s_nop 0\n"
"buffer_load_dwordx4 v24, s[40:43], s49 offen  lds\n"
"s_add_u32 m0, m0, 0x400\n"
"s_nop 0\n"
"buffer_load_dwordx4 v25, s[40:43], s49 offen  lds\n"
"s_add_u32 m0, m0, 0x400\n"
"s_nop 0\n"
"buffer_load_dwordx4 v26, s[40:43], s49 offen  lds\n"
"s_add_u32 m0, m0, 0x400\n"
"s_nop 0\n"
"buffer_load_dwordx4 v27, s[40:43], s49 offen  lds\n"
"s_add_u32 m0, m0, 0x400\n"
"s_nop 0\n"
"buffer_load_dwordx4 v28, s[40:43], s49 offen  lds\n"
"s_add_u32 m0, m0, 0x400\n"
"s_nop 0\n"
"buffer_load_dwordx4 v29, s[40:43], s49 offen  lds\n"
"s_add_u32 m0, m0, 0x400\n"
"s_nop 0\n"
"buffer_load_dwordx4 v30, s[40:43], s49 offen  lds\n"
"s_add_u32 m0, m0, 0x400\n"
"s_nop 0\n"
"buffer_load_dwordx4 v31, s[40:43], s49 offen  lds\n"
"s_add_u32 s48, s48, 128\n"
"s_add_u32 s49, s49, s50\n"
"s_xor_b32 s52, s52, 0x10000\n"
"s_lshl_b32 s76, s72, 2\n"
"s_add_u32 s76, s76, s73\n"
"s_lshl_b32 s77, s76, 4\n"
"v_add_u32 v1, s77, v0\n"
"v_lshlrev_b32 v1, 11, v1\n"
"v_add_u32 v1, 256, v1\n"
"s_lshr_b32 s77, s76, 1\n"
"s_add_u32 s77, s77, 128\n"
"s_lshl_b32 s77, s77, 13\n"
"s_lshl_b32 s78, s71, 10\n"
"s_add_u32 s77, s77, s78\n"
"v_subrev_u32 v2, 16, v0\n"
"v_lshlrev_b32 v2, 7, v2\n"
"v_add_u32 v2, s77, v2\n"
"s_and_b32 s76, s76, 1\n"
"s_cmp_eq_u32 s76, 0\n"
"s_cselect_b64 s[76:77], %2, %3\n"
"s_mov_b64 s[78:79], %1\n"
"v_mov_b32 v12, s78\n"
"v_mov_b32 v13, s79\n"
"v_mov_b32 v14, s76\n"
"v_mov_b32 v15, s77\n"
"v_cmp_gt_u32 vcc, 16, v0\n"
"v_cmp_gt_u32 s[76:77], 24, v0\n"
"s_nop 1\n"
"s_andn2_b64 s[76:77], s[76:77], vcc\n"
"v_cndmask_b32 v1, 0, v1, vcc\n"
"v_cndmask_b32 v1, v1, v2, s[76:77]\n"
"v_cndmask_b32 v12, v12, v14, s[76:77]\n"
"v_cndmask_b32 v13, v13, v15, s[76:77]\n"
"v_mov_b32 v2, 128\n"
"v_cndmask_b32 v50, 0, v2, vcc\n"
"v_mov_b32 v2, 524288\n"
"v_cndmask_b32 v50, v50, v2, s[76:77]\n"
"v_add_co_u32 v48, vcc, v12, v1\n"
"s_nop 1\n"
"v_addc_co_u32 v49, vcc, 0, v13, vcc\n"
"global_load_dword v51, v[48:49], off\n"
"v_add_co_u32 v48, vcc, v48, v50\n"
"s_nop 1\n"
"v_addc_co_u32 v49, vcc, 0, v49, vcc\n"
"global_load_dword v51, v[48:49], off\n"
"v_add_co_u32 v48, vcc, v48, v50\n"
"s_nop 1\n"
"v_addc_co_u32 v49, vcc, 0, v49, vcc\n"
"global_load_dword v51, v[48:49], off\n"
"v_add_co_u32 v48, vcc, v48, v50\n"
"s_nop 1\n"
"v_addc_co_u32 v49, vcc, 0, v49, vcc\n"
"global_load_dword v51, v[48:49], off\n"
"v_add_co_u32 v48, vcc, v48, v50\n"
"s_nop 1\n"
"v_addc_co_u32 v49, vcc, 0, v49, vcc\n"
"s_mov_b32 s34, 10\n"
"s_mov_b32 s35, 0\n"
"s_mov_b32 s80, 896\n"
"s_cmp_lt_u32 s72, 16\n"
"s_cbranch_scc1 L_t2q_%=\n"
"s_branch L_t2b_%=\n"
"L_t2q_%=:\n"
"s_mov_b64 s[56:57], %4\n"
"s_and_b32 s57, s57, 0xffff\n"
"s_mov_b32 s58, 1835008\n"
"s_mov_b32 s59, 0x20000\n"
"s_mov_b64 s[60:61], %5\n"
"s_and_b32 s61, s61, 0xffff\n"
"s_mov_b32 s62, 3670016\n"
"s_mov_b32 s63, 0x20000\n"
"s_and_b32 s77, s72, 1\n"
"s_lshl_b32 s78, s71, 1\n"
"s_add_u32 s77, s77, s78\n"
"s_lshr_b32 s78, s72, 1\n"
"s_mov_b32 s81, 8192\n"
"s_mov_b32 s69, 0x3db8aa3b\n"
"s_mul_i32 s64, s78, 229376\n"
"s_lshl_b32 s65, s77, 9\n"
"s_mov_b32 s66, 524288\n"
"s_mov_b32 s67, 7\n"
"s_lshl_b32 s68, s77, 20\n"
"s_lshl_b32 s79, s78, 17\n"
"s_add_u32 s68, s68, s79\n"
"s_lshl_b32 s79, s74, 16\n"
"s_add_u32 s68, s68, s79\n"
"s_lshl_b32 s79, s75, 12\n"
"s_add_u32 s68, s68, s79\n"
"v_lshrrev_b32 v1, 2, v0\n"
"v_and_b32 v1, 3, v1\n"
"v_mul_u32_u24 v1, s81, v1\n"
"v_lshrrev_b32 v2, 4, v0\n"
"v_lshlrev_b32 v2, 6, v2\n"
"v_and_b32 v3, 3, v0\n"
"v_lshlrev_b32 v3, 4, v3\n"
"v_add3_u32 v9, v1, v2, v3\n"
"v_xor_b32 v10, 32, v9\n"
"s_lshl_b32 s76, s73, 4\n"
"s_add_u32 s76, s76, 0\n"
"s_mul_i32 s76, s76, s81\n"
"v_add_u32 v40, s76, v9\n"
"s_lshl_b32 s76, s73, 4\n"
"s_add_u32 s76, s76, 0\n"
"s_mul_i32 s76, s76, s81\n"
"s_add_u32 s76, s76, 256\n"
"v_add_u32 v41, s76, v9\n"
"s_lshl_b32 s76, s73, 4\n"
"s_add_u32 s76, s76, 4\n"
"s_mul_i32 s76, s76, s81\n"
"v_add_u32 v42, s76, v9\n"
"s_lshl_b32 s76, s73, 4\n"
"s_add_u32 s76, s76, 4\n"
"s_mul_i32 s76, s76, s81\n"
"s_add_u32 s76, s76, 256\n"
"v_add_u32 v43, s76, v9\n"
"s_lshl_b32 s76, s73, 4\n"
"s_add_u32 s76, s76, 8\n"
"s_mul_i32 s76, s76, s81\n"
"v_add_u32 v44, s76, v10\n"
"s_lshl_b32 s76, s73, 4\n"
"s_add_u32 s76, s76, 8\n"
"s_mul_i32 s76, s76, s81\n"
"s_add_u32 s76, s76, 256\n"
"v_add_u32 v45, s76, v10\n"
"s_lshl_b32 s76, s73, 4\n"
"s_add_u32 s76, s76, 12\n"
"s_mul_i32 s76, s76, s81\n"
"v_add_u32 v46, s76, v10\n"
"s_lshl_b32 s76, s73, 4\n"
"s_add_u32 s76, s76, 12\n"
"s_mul_i32 s76, s76, s81\n"
"s_add_u32 s76, s76, 256\n"
"v_add_u32 v47, s76, v10\n"
"s_branch L_t2x_%=\n"
"L_t2b_%=:\n"
"s_mov_b64 s[56:57], %4\n"
"s_and_b32 s57, s57, 0xffff\n"
"s_mov_b32 s58, 1835008\n"
"s_mov_b32 s59, 0x20000\n"
"s_mov_b64 s[60:61], %6\n"
"s_and_b32 s61, s61, 0xffff\n"
"s_mov_b32 s62, 1204224\n"
"s_mov_b32 s63, 0x20000\n"
"s_sub_u32 s78, s72, 16\n"
"s_mul_i32 s79, s78, 128\n"
"s_sub_u32 s79, s79, 16\n"
"s_mul_i32 s64, s79, 896\n"
"s_cmp_eq_u32 s78, 0\n"
"s_cselect_b32 s64, 0, s64\n"
"s_cselect_b32 s68, -16, 0\n"
"s_mov_b32 s81, 2688\n"
"s_mov_b32 s69, 0x3f800000\n"
"s_mov_b32 s65, 0\n"
"s_mov_b32 s66, 172032\n"
"s_mov_b32 s67, 7\n"
"s_branch L_t2d_%=\n"
"L_t2x_%=:\n"
"v_lshrrev_b32 v1, 3, v0\n"
"v_and_b32 v2, 7, v0\n"
"v_lshrrev_b32 v3, 4, v0\n"
"v_xor_b32 v2, v2, v3\n"
"v_lshlrev_b32 v2, 4, v2\n"
"v_mul_u32_u24 v1, s80, v1\n"
"v_add_u32 v9, v1, v2\n"
"v_xor_b32 v10, 64, v9\n"
"s_lshl_b32 s76, s73, 6\n"
"s_add_u32 s76, s76, 0\n"
"s_mul_i32 s76, s76, s80\n"
"v_add_u32 v32, s76, v9\n"
"s_lshl_b32 s76, s73, 6\n"
"s_add_u32 s76, s76, 8\n"
"s_mul_i32 s76, s76, s80\n"
"v_add_u32 v33, s76, v10\n"
"s_lshl_b32 s76, s73, 6\n"
"s_add_u32 s76, s76, 16\n"
"s_mul_i32 s76, s76, s80\n"
"v_add_u32 v34, s76, v9\n"
"s_lshl_b32 s76, s73, 6\n"
"s_add_u32 s76, s76, 24\n"
"s_mul_i32 s76, s76, s80\n"
"v_add_u32 v35, s76, v10\n"
"s_lshl_b32 s76, s73, 6\n"
"s_add_u32 s76, s76, 32\n"
"s_mul_i32 s76, s76, s80\n"
"v_add_u32 v36, s76, v9\n"
"s_lshl_b32 s76, s73, 6\n"
"s_add_u32 s76, s76, 40\n"
"s_mul_i32 s76, s76, s80\n"
"v_add_u32 v37, s76, v10\n"
"s_lshl_b32 s76, s73, 6\n"
"s_add_u32 s76, s76, 48\n"
"s_mul_i32 s76, s76, s80\n"
"v_add_u32 v38, s76, v9\n"
"s_lshl_b32 s76, s73, 6\n"
"s_add_u32 s76, s76, 56\n"
"s_mul_i32 s76, s76, s80\n"
"v_add_u32 v39, s76, v10\n"
"L_t2d_%=:\n"
"v_and_b32 v1, 15, v0\n"
"v_lshrrev_b32 v2, 4, v0\n"
"v_lshrrev_b32 v3, 1, v1\n"
"v_xor_b32 v3, v3, v2\n"
"v_lshlrev_b32 v3, 4, v3\n"
"v_lshl_add_u32 v3, v1, 7, v3\n"
"s_lshl_b32 s76, s74, 14\n"
"v_add_u32 v4, s76, v3\n"
"v_xor_b32 v5, 64, v4\n"
"v_lshlrev_b32 v3, 12, v2\n"
"v_and_b32 v1, 1, v2\n"
"v_lshl_add_u32 v3, v1, 5, v3\n"
"v_lshrrev_b32 v1, 2, v0\n"
"v_and_b32 v1, 3, v1\n"
"v_lshl_add_u32 v3, v1, 6, v3\n"
"v_and_b32 v1, 3, v0\n"
"v_lshl_add_u32 v3, v1, 3, v3\n"
"s_lshl_b32 s76, s75, 10\n"
"s_add_u32 s76, s76, 32768\n"
"v_add_u32 v6, s76, v3\n"
"v_xor_b32 v7, 32, v6\n"
"v_lshlrev_b32 v8, 4, v0\n"
"s_waitcnt vmcnt(20)\n"
"s_barrier\n"
"ds_read_b128 v[128:131], v4 offset:0\n"
"ds_read_b64_tr_b16 v[160:161], v6 offset:0\n"
"ds_read_b64_tr_b16 v[162:163], v6 offset:2048\n"
"ds_read_b128 v[132:135], v4 offset:2048\n"
"ds_read_b64_tr_b16 v[164:165], v7 offset:0\n"
"ds_read_b64_tr_b16 v[166:167], v7 offset:2048\n"
"ds_read_b128 v[136:139], v4 offset:4096\n"
"ds_read_b64_tr_b16 v[168:169], v6 offset:256\n"
"ds_read_b64_tr_b16 v[170:171], v6 offset:2304\n"
"ds_read_b128 v[140:143], v4 offset:6144\n"
"ds_read_b64_tr_b16 v[172:173], v7 offset:256\n"
"ds_read_b64_tr_b16 v[174:175], v7 offset:2304\n"
"ds_read_b128 v[144:147], v4 offset:8192\n"
"ds_read_b64_tr_b16 v[176:177], v6 offset:512\n"
"ds_read_b64_tr_b16 v[178:179], v6 offset:2560\n"
"ds_read_b128 v[148:151], v4 offset:10240\n"
"ds_read_b64_tr_b16 v[180:181], v7 offset:512\n"
"ds_read_b64_tr_b16 v[182:183], v7 offset:2560\n"
"ds_read_b128 v[152:155], v4 offset:12288\n"
"ds_read_b64_tr_b16 v[184:185], v6 offset:768\n"
"ds_read_b64_tr_b16 v[186:187], v6 offset:2816\n"
"ds_read_b128 v[156:159], v4 offset:14336\n"
"ds_read_b64_tr_b16 v[188:189], v7 offset:768\n"
"ds_read_b64_tr_b16 v[190:191], v7 offset:2816\n"
"v_readfirstlane_b32 s55, v61\n"
"s_nop 3\n"
"s_mul_i32 s55, s55, 0x9e3779b1\n"
"s_add_u32 s55, s55, 0x7f4a7c15\n"
"s_cmp_lg_u32 s73, 0\n"
"s_cbranch_scc1 L_fl_arr_%=\n"
"s_lshl_b32 s76, %8, 2\n"
"s_add_u32 s76, s76, 126878720\n"
"v_mov_b32 v1, s55\n"
"v_mov_b32 v2, 0\n"
"s_mov_b64 exec, 1\n"
"buffer_store_dword v1, v2, s[44:47], s76 offen sc1\n"
"s_mov_b64 exec, -1\n"
"L_fl_arr_%=:\n"
"s_waitcnt lgkmcnt(0)\n"
"s_cmp_eq_u32 s82, 0\n"
"s_cbranch_scc1 L_K_tile_%=\n"
"s_branch L_V_tile_%=\n"
"L_V_tile_%=:\n"
"v_mfma_f32_16x16x32_bf16 a[0:3], v[128:131], v[160:163], 0\n"
"v_mfma_f32_16x16x32_bf16 a[4:7], v[128:131], v[164:167], 0\n"
"ds_read_b128 v[192:195], v5 offset:0\n"
"v_mfma_f32_16x16x32_bf16 a[8:11], v[128:131], v[168:171], 0\n"
"v_mfma_f32_16x16x32_bf16 a[12:15], v[128:131], v[172:175], 0\n"
"ds_read_b64_tr_b16 v[224:225], v6 offset:16384\n"
"v_mfma_f32_16x16x32_bf16 a[16:19], v[128:131], v[176:179], 0\n"
"v_mfma_f32_16x16x32_bf16 a[20:23], v[128:131], v[180:183], 0\n"
"ds_read_b64_tr_b16 v[226:227], v6 offset:18432\n"
"v_mfma_f32_16x16x32_bf16 a[24:27], v[128:131], v[184:187], 0\n"
"v_mfma_f32_16x16x32_bf16 a[28:31], v[128:131], v[188:191], 0\n"
"ds_read_b128 v[196:199], v5 offset:2048\n"
"v_mfma_f32_16x16x32_bf16 a[32:35], v[132:135], v[160:163], 0\n"
"v_mfma_f32_16x16x32_bf16 a[36:39], v[132:135], v[164:167], 0\n"
"ds_read_b64_tr_b16 v[228:229], v7 offset:16384\n"
"v_mfma_f32_16x16x32_bf16 a[40:43], v[132:135], v[168:171], 0\n"
"v_mfma_f32_16x16x32_bf16 a[44:47], v[132:135], v[172:175], 0\n"
"ds_read_b64_tr_b16 v[230:231], v7 offset:18432\n"
"v_mfma_f32_16x16x32_bf16 a[48:51], v[132:135], v[176:179], 0\n"
"v_mfma_f32_16x16x32_bf16 a[52:55], v[132:135], v[180:183], 0\n"
"ds_read_b128 v[200:203], v5 offset:4096\n"
"v_mfma_f32_16x16x32_bf16 a[56:59], v[132:135], v[184:187], 0\n"
"v_mfma_f32_16x16x32_bf16 a[60:63], v[132:135], v[188:191], 0\n"
"ds_read_b64_tr_b16 v[232:233], v6 offset:16640\n"
"v_mfma_f32_16x16x32_bf16 a[64:67], v[136:139], v[160:163], 0\n"
"v_mfma_f32_16x16x32_bf16 a[68:71], v[136:139], v[164:167], 0\n"
"ds_read_b64_tr_b16 v[234:235], v6 offset:18688\n"
"v_mfma_f32_16x16x32_bf16 a[72:75], v[136:139], v[168:171], 0\n"
"v_mfma_f32_16x16x32_bf16 a[76:79], v[136:139], v[172:175], 0\n"
"ds_read_b128 v[204:207], v5 offset:6144\n"
"v_mfma_f32_16x16x32_bf16 a[80:83], v[136:139], v[176:179], 0\n"
"v_mfma_f32_16x16x32_bf16 a[84:87], v[136:139], v[180:183], 0\n"
"ds_read_b64_tr_b16 v[236:237], v7 offset:16640\n"
"v_mfma_f32_16x16x32_bf16 a[88:91], v[136:139], v[184:187], 0\n"
"v_mfma_f32_16x16x32_bf16 a[92:95], v[136:139], v[188:191], 0\n"
"ds_read_b64_tr_b16 v[238:239], v7 offset:18688\n"
"v_mfma_f32_16x16x32_bf16 a[96:99], v[140:143], v[160:163], 0\n"
"v_mfma_f32_16x16x32_bf16 a[100:103], v[140:143], v[164:167], 0\n"
"ds_read_b128 v[208:211], v5 offset:8192\n"
"v_mfma_f32_16x16x32_bf16 a[104:107], v[140:143], v[168:171], 0\n"
"v_mfma_f32_16x16x32_bf16 a[108:111], v[140:143], v[172:175], 0\n"
"ds_read_b64_tr_b16 v[240:241], v6 offset:16896\n"
"v_mfma_f32_16x16x32_bf16 a[112:115], v[140:143], v[176:179], 0\n"
"v_mfma_f32_16x16x32_bf16 a[116:119], v[140:143], v[180:183], 0\n"
"ds_read_b64_tr_b16 v[242:243], v6 offset:18944\n"
"v_mfma_f32_16x16x32_bf16 a[120:123], v[140:143], v[184:187], 0\n"
"v_mfma_f32_16x16x32_bf16 a[124:127], v[140:143], v[188:191], 0\n"
"ds_read_b128 v[212:215], v5 offset:10240\n"
"v_mfma_f32_16x16x32_bf16 a[128:131], v[144:147], v[160:163], 0\n"
"v_mfma_f32_16x16x32_bf16 a[132:135], v[144:147], v[164:167], 0\n"
"ds_read_b64_tr_b16 v[244:245], v7 offset:16896\n"
"v_mfma_f32_16x16x32_bf16 a[136:139], v[144:147], v[168:171], 0\n"
"v_mfma_f32_16x16x32_bf16 a[140:143], v[144:147], v[172:175], 0\n"
"ds_read_b64_tr_b16 v[246:247], v7 offset:18944\n"
"v_mfma_f32_16x16x32_bf16 a[144:147], v[144:147], v[176:179], 0\n"
"v_mfma_f32_16x16x32_bf16 a[148:151], v[144:147], v[180:183], 0\n"
"ds_read_b128 v[216:219], v5 offset:12288\n"
"v_mfma_f32_16x16x32_bf16 a[152:155], v[144:147], v[184:187], 0\n"
"v_mfma_f32_16x16x32_bf16 a[156:159], v[144:147], v[188:191], 0\n"
"ds_read_b64_tr_b16 v[248:249], v6 offset:17152\n"
"v_mfma_f32_16x16x32_bf16 a[160:163], v[148:151], v[160:163], 0\n"
"v_mfma_f32_16x16x32_bf16 a[164:167], v[148:151], v[164:167], 0\n"
"ds_read_b64_tr_b16 v[250:251], v6 offset:19200\n"
"v_mfma_f32_16x16x32_bf16 a[168:171], v[148:151], v[168:171], 0\n"
"v_mfma_f32_16x16x32_bf16 a[172:175], v[148:151], v[172:175], 0\n"
"ds_read_b128 v[220:223], v5 offset:14336\n"
"v_mfma_f32_16x16x32_bf16 a[176:179], v[148:151], v[176:179], 0\n"
"v_mfma_f32_16x16x32_bf16 a[180:183], v[148:151], v[180:183], 0\n"
"ds_read_b64_tr_b16 v[252:253], v7 offset:17152\n"
"v_mfma_f32_16x16x32_bf16 a[184:187], v[148:151], v[184:187], 0\n"
"v_mfma_f32_16x16x32_bf16 a[188:191], v[148:151], v[188:191], 0\n"
"ds_read_b64_tr_b16 v[254:255], v7 offset:19200\n"
"v_mfma_f32_16x16x32_bf16 a[192:195], v[152:155], v[160:163], 0\n"
"v_mfma_f32_16x16x32_bf16 a[196:199], v[152:155], v[164:167], 0\n"
"v_mfma_f32_16x16x32_bf16 a[200:203], v[152:155], v[168:171], 0\n"
"v_mfma_f32_16x16x32_bf16 a[204:207], v[152:155], v[172:175], 0\n"
"v_mfma_f32_16x16x32_bf16 a[208:211], v[152:155], v[176:179], 0\n"
"v_mfma_f32_16x16x32_bf16 a[212:215], v[152:155], v[180:183], 0\n"
"v_mfma_f32_16x16x32_bf16 a[216:219], v[152:155], v[184:187], 0\n"
"v_mfma_f32_16x16x32_bf16 a[220:223], v[152:155], v[188:191], 0\n"
"v_mfma_f32_16x16x32_bf16 a[224:227], v[156:159], v[160:163], 0\n"
"v_mfma_f32_16x16x32_bf16 a[228:231], v[156:159], v[164:167], 0\n"
"v_mfma_f32_16x16x32_bf16 a[232:235], v[156:159], v[168:171], 0\n"
"v_mfma_f32_16x16x32_bf16 a[236:239], v[156:159], v[172:175], 0\n"
"v_mfma_f32_16x16x32_bf16 a[240:243], v[156:159], v[176:179], 0\n"
"v_mfma_f32_16x16x32_bf16 a[244:247], v[156:159], v[180:183], 0\n"
"v_mfma_f32_16x16x32_bf16 a[248:251], v[156:159], v[184:187], 0\n"
"v_mfma_f32_16x16x32_bf16 a[252:255], v[156:159], v[188:191], 0\n"
"s_waitcnt vmcnt(4) lgkmcnt(0)\n"
"s_barrier\n"
"v_xor_b32 v4, 0x10000, v4\n"
"v_xor_b32 v5, 0x10000, v5\n"
"v_xor_b32 v6, 0x10000, v6\n"
"v_xor_b32 v7, 0x10000, v7\n"
"v_mfma_f32_16x16x32_bf16 a[0:3], v[192:195], v[224:227], a[0:3]\n"
"s_mov_b32 m0, s52\n"
"v_mfma_f32_16x16x32_bf16 a[4:7], v[192:195], v[228:231], a[4:7]\n"
"ds_read_b128 v[128:131], v4 offset:0\n"
"buffer_load_dwordx4 v16, s[36:39], s48 offen  lds\n"
"v_mfma_f32_16x16x32_bf16 a[8:11], v[192:195], v[232:235], a[8:11]\n"
"v_mfma_f32_16x16x32_bf16 a[12:15], v[192:195], v[236:239], a[12:15]\n"
"ds_read_b64_tr_b16 v[160:161], v6 offset:0\n"
"v_mfma_f32_16x16x32_bf16 a[16:19], v[192:195], v[240:243], a[16:19]\n"
"s_add_u32 m0, m0, 0x400\n"
"v_mfma_f32_16x16x32_bf16 a[20:23], v[192:195], v[244:247], a[20:23]\n"
"ds_read_b64_tr_b16 v[162:163], v6 offset:2048\n"
"buffer_load_dwordx4 v17, s[36:39], s48 offen  lds\n"
"v_mfma_f32_16x16x32_bf16 a[24:27], v[192:195], v[248:251], a[24:27]\n"
"v_mfma_f32_16x16x32_bf16 a[28:31], v[192:195], v[252:255], a[28:31]\n"
"ds_read_b128 v[132:135], v4 offset:2048\n"
"v_mfma_f32_16x16x32_bf16 a[32:35], v[196:199], v[224:227], a[32:35]\n"
"s_add_u32 m0, m0, 0x400\n"
"v_mfma_f32_16x16x32_bf16 a[36:39], v[196:199], v[228:231], a[36:39]\n"
"ds_read_b64_tr_b16 v[164:165], v7 offset:0\n"
"buffer_load_dwordx4 v18, s[36:39], s48 offen  lds\n"
"v_mfma_f32_16x16x32_bf16 a[40:43], v[196:199], v[232:235], a[40:43]\n"
"v_mfma_f32_16x16x32_bf16 a[44:47], v[196:199], v[236:239], a[44:47]\n"
"ds_read_b64_tr_b16 v[166:167], v7 offset:2048\n"
"v_mfma_f32_16x16x32_bf16 a[48:51], v[196:199], v[240:243], a[48:51]\n"
"s_add_u32 m0, m0, 0x400\n"
"v_mfma_f32_16x16x32_bf16 a[52:55], v[196:199], v[244:247], a[52:55]\n"
"ds_read_b128 v[136:139], v4 offset:4096\n"
"buffer_load_dwordx4 v19, s[36:39], s48 offen  lds\n"
"v_mfma_f32_16x16x32_bf16 a[56:59], v[196:199], v[248:251], a[56:59]\n"
"v_mfma_f32_16x16x32_bf16 a[60:63], v[196:199], v[252:255], a[60:63]\n"
"ds_read_b64_tr_b16 v[168:169], v6 offset:256\n"
"v_mfma_f32_16x16x32_bf16 a[64:67], v[200:203], v[224:227], a[64:67]\n"
"s_add_u32 m0, m0, 0x400\n"
"v_mfma_f32_16x16x32_bf16 a[68:71], v[200:203], v[228:231], a[68:71]\n"
"ds_read_b64_tr_b16 v[170:171], v6 offset:2304\n"
"buffer_load_dwordx4 v20, s[36:39], s48 offen  lds\n"
"v_mfma_f32_16x16x32_bf16 a[72:75], v[200:203], v[232:235], a[72:75]\n"
"v_mfma_f32_16x16x32_bf16 a[76:79], v[200:203], v[236:239], a[76:79]\n"
"ds_read_b128 v[140:143], v4 offset:6144\n"
"v_mfma_f32_16x16x32_bf16 a[80:83], v[200:203], v[240:243], a[80:83]\n"
"s_add_u32 m0, m0, 0x400\n"
"v_mfma_f32_16x16x32_bf16 a[84:87], v[200:203], v[244:247], a[84:87]\n"
"ds_read_b64_tr_b16 v[172:173], v7 offset:256\n"
"buffer_load_dwordx4 v21, s[36:39], s48 offen  lds\n"
"v_mfma_f32_16x16x32_bf16 a[88:91], v[200:203], v[248:251], a[88:91]\n"
"v_mfma_f32_16x16x32_bf16 a[92:95], v[200:203], v[252:255], a[92:95]\n"
"ds_read_b64_tr_b16 v[174:175], v7 offset:2304\n"
"v_mfma_f32_16x16x32_bf16 a[96:99], v[204:207], v[224:227], a[96:99]\n"
"s_add_u32 m0, m0, 0x400\n"
"v_mfma_f32_16x16x32_bf16 a[100:103], v[204:207], v[228:231], a[100:103]\n"
"ds_read_b128 v[144:147], v4 offset:8192\n"
"buffer_load_dwordx4 v22, s[36:39], s48 offen  lds\n"
"v_mfma_f32_16x16x32_bf16 a[104:107], v[204:207], v[232:235], a[104:107]\n"
"v_mfma_f32_16x16x32_bf16 a[108:111], v[204:207], v[236:239], a[108:111]\n"
"ds_read_b64_tr_b16 v[176:177], v6 offset:512\n"
"v_mfma_f32_16x16x32_bf16 a[112:115], v[204:207], v[240:243], a[112:115]\n"
"s_add_u32 m0, m0, 0x400\n"
"v_mfma_f32_16x16x32_bf16 a[116:119], v[204:207], v[244:247], a[116:119]\n"
"ds_read_b64_tr_b16 v[178:179], v6 offset:2560\n"
"buffer_load_dwordx4 v23, s[36:39], s48 offen  lds\n"
"v_mfma_f32_16x16x32_bf16 a[120:123], v[204:207], v[248:251], a[120:123]\n"
"v_mfma_f32_16x16x32_bf16 a[124:127], v[204:207], v[252:255], a[124:127]\n"
"ds_read_b128 v[148:151], v4 offset:10240\n"
"v_mfma_f32_16x16x32_bf16 a[128:131], v[208:211], v[224:227], a[128:131]\n"
"s_add_u32 m0, m0, 25600\n"
"v_mfma_f32_16x16x32_bf16 a[132:135], v[208:211], v[228:231], a[132:135]\n"
"ds_read_b64_tr_b16 v[180:181], v7 offset:512\n"
"buffer_load_dwordx4 v24, s[40:43], s49 offen  lds\n"
"v_mfma_f32_16x16x32_bf16 a[136:139], v[208:211], v[232:235], a[136:139]\n"
"v_mfma_f32_16x16x32_bf16 a[140:143], v[208:211], v[236:239], a[140:143]\n"
"ds_read_b64_tr_b16 v[182:183], v7 offset:2560\n"
"v_mfma_f32_16x16x32_bf16 a[144:147], v[208:211], v[240:243], a[144:147]\n"
"s_add_u32 m0, m0, 0x400\n"
"v_mfma_f32_16x16x32_bf16 a[148:151], v[208:211], v[244:247], a[148:151]\n"
"ds_read_b128 v[152:155], v4 offset:12288\n"
"buffer_load_dwordx4 v25, s[40:43], s49 offen  lds\n"
"v_mfma_f32_16x16x32_bf16 a[152:155], v[208:211], v[248:251], a[152:155]\n"
"v_mfma_f32_16x16x32_bf16 a[156:159], v[208:211], v[252:255], a[156:159]\n"
"ds_read_b64_tr_b16 v[184:185], v6 offset:768\n"
"v_mfma_f32_16x16x32_bf16 a[160:163], v[212:215], v[224:227], a[160:163]\n"
"s_add_u32 m0, m0, 0x400\n"
"v_mfma_f32_16x16x32_bf16 a[164:167], v[212:215], v[228:231], a[164:167]\n"
"ds_read_b64_tr_b16 v[186:187], v6 offset:2816\n"
"buffer_load_dwordx4 v26, s[40:43], s49 offen  lds\n"
"v_mfma_f32_16x16x32_bf16 a[168:171], v[212:215], v[232:235], a[168:171]\n"
"v_mfma_f32_16x16x32_bf16 a[172:175], v[212:215], v[236:239], a[172:175]\n"
"ds_read_b128 v[156:159], v4 offset:14336\n"
"v_mfma_f32_16x16x32_bf16 a[176:179], v[212:215], v[240:243], a[176:179]\n"
"s_add_u32 m0, m0, 0x400\n"
"v_mfma_f32_16x16x32_bf16 a[180:183], v[212:215], v[244:247], a[180:183]\n"
"ds_read_b64_tr_b16 v[188:189], v7 offset:768\n"
"buffer_load_dwordx4 v27, s[40:43], s49 offen  lds\n"
"v_mfma_f32_16x16x32_bf16 a[184:187], v[212:215], v[248:251], a[184:187]\n"
"v_mfma_f32_16x16x32_bf16 a[188:191], v[212:215], v[252:255], a[188:191]\n"
"ds_read_b64_tr_b16 v[190:191], v7 offset:2816\n"
"v_mfma_f32_16x16x32_bf16 a[192:195], v[216:219], v[224:227], a[192:195]\n"
"s_add_u32 m0, m0, 0x400\n"
"v_mfma_f32_16x16x32_bf16 a[196:199], v[216:219], v[228:231], a[196:199]\n"
"buffer_load_dwordx4 v28, s[40:43], s49 offen  lds\n"
"v_mfma_f32_16x16x32_bf16 a[200:203], v[216:219], v[232:235], a[200:203]\n"
"v_mfma_f32_16x16x32_bf16 a[204:207], v[216:219], v[236:239], a[204:207]\n"
"v_mfma_f32_16x16x32_bf16 a[208:211], v[216:219], v[240:243], a[208:211]\n"
"s_add_u32 m0, m0, 0x400\n"
"v_mfma_f32_16x16x32_bf16 a[212:215], v[216:219], v[244:247], a[212:215]\n"
"buffer_load_dwordx4 v29, s[40:43], s49 offen  lds\n"
"v_mfma_f32_16x16x32_bf16 a[216:219], v[216:219], v[248:251], a[216:219]\n"
"v_mfma_f32_16x16x32_bf16 a[220:223], v[216:219], v[252:255], a[220:223]\n"
"v_mfma_f32_16x16x32_bf16 a[224:227], v[220:223], v[224:227], a[224:227]\n"
"s_add_u32 m0, m0, 0x400\n"
"v_mfma_f32_16x16x32_bf16 a[228:231], v[220:223], v[228:231], a[228:231]\n"
"buffer_load_dwordx4 v30, s[40:43], s49 offen  lds\n"
"v_mfma_f32_16x16x32_bf16 a[232:235], v[220:223], v[232:235], a[232:235]\n"
"v_mfma_f32_16x16x32_bf16 a[236:239], v[220:223], v[236:239], a[236:239]\n"
"v_mfma_f32_16x16x32_bf16 a[240:243], v[220:223], v[240:243], a[240:243]\n"
"s_add_u32 m0, m0, 0x400\n"
"v_mfma_f32_16x16x32_bf16 a[244:247], v[220:223], v[244:247], a[244:247]\n"
"buffer_load_dwordx4 v31, s[40:43], s49 offen  lds\n"
"v_mfma_f32_16x16x32_bf16 a[248:251], v[220:223], v[248:251], a[248:251]\n"
"s_add_u32 s48, s48, 128\n"
"s_add_u32 s49, s49, s50\n"
"s_xor_b32 s52, s52, 0x10000\n"
"v_mfma_f32_16x16x32_bf16 a[252:255], v[220:223], v[252:255], a[252:255]\n"
"s_sub_u32 s34, s34, 1\n"
"s_cbranch_scc1 L_pfs_Vf_%=\n"
"global_load_dword v51, v[48:49], off\n"
"v_add_co_u32 v48, vcc, v48, v50\n"
"s_nop 1\n"
"v_addc_co_u32 v49, vcc, 0, v49, vcc\n"
"L_pfr_Vf_%=:\n"
"s_waitcnt lgkmcnt(0)\n"
"s_sub_u32 s51, s51, 1\n"
"L_V_loop_%=:\n"
"s_cmp_lg_u32 s51, 2\n"
"s_cbranch_scc1 L_V_nosw_%=\n"
"s_mov_b32 s36, s56\n"
"s_mov_b32 s40, s60\n"
"s_mov_b32 s37, s57\n"
"s_mov_b32 s41, s61\n"
"s_mov_b32 s38, s58\n"
"s_mov_b32 s42, s62\n"
"s_mov_b32 s39, s59\n"
"s_mov_b32 s43, s63\n"
"s_mov_b32 s48, s64\n"
"s_mov_b32 s49, s65\n"
"s_mov_b32 s50, s66\n"
"v_mov_b32 v16, v32\n"
"v_mov_b32 v24, v40\n"
"v_mov_b32 v17, v33\n"
"v_mov_b32 v25, v41\n"
"v_mov_b32 v18, v34\n"
"v_mov_b32 v26, v42\n"
"v_mov_b32 v19, v35\n"
"v_mov_b32 v27, v43\n"
"v_mov_b32 v20, v36\n"
"v_mov_b32 v28, v44\n"
"v_mov_b32 v21, v37\n"
"v_mov_b32 v29, v45\n"
"v_mov_b32 v22, v38\n"
"v_mov_b32 v30, v46\n"
"v_mov_b32 v23, v39\n"
"v_mov_b32 v31, v47\n"
"s_cmp_eq_u32 s69, 0x3f800000\n"
"s_cselect_b32 s38, 0, s38\n"
"s_cselect_b32 s42, 0, s42\n"
"L_V_nosw_%=:\n"
"v_mfma_f32_16x16x32_bf16 a[0:3], v[128:131], v[160:163], a[0:3]\n"
"v_mfma_f32_16x16x32_bf16 a[4:7], v[128:131], v[164:167], a[4:7]\n"
"ds_read_b128 v[192:195], v5 offset:0\n"
"v_mfma_f32_16x16x32_bf16 a[8:11], v[128:131], v[168:171], a[8:11]\n"
"v_mfma_f32_16x16x32_bf16 a[12:15], v[128:131], v[172:175], a[12:15]\n"
"ds_read_b64_tr_b16 v[224:225], v6 offset:16384\n"
"v_mfma_f32_16x16x32_bf16 a[16:19], v[128:131], v[176:179], a[16:19]\n"
"v_mfma_f32_16x16x32_bf16 a[20:23], v[128:131], v[180:183], a[20:23]\n"
"ds_read_b64_tr_b16 v[226:227], v6 offset:18432\n"
"v_mfma_f32_16x16x32_bf16 a[24:27], v[128:131], v[184:187], a[24:27]\n"
"v_mfma_f32_16x16x32_bf16 a[28:31], v[128:131], v[188:191], a[28:31]\n"
"ds_read_b128 v[196:199], v5 offset:2048\n"
"v_mfma_f32_16x16x32_bf16 a[32:35], v[132:135], v[160:163], a[32:35]\n"
"v_mfma_f32_16x16x32_bf16 a[36:39], v[132:135], v[164:167], a[36:39]\n"
"ds_read_b64_tr_b16 v[228:229], v7 offset:16384\n"
"v_mfma_f32_16x16x32_bf16 a[40:43], v[132:135], v[168:171], a[40:43]\n"
"v_mfma_f32_16x16x32_bf16 a[44:47], v[132:135], v[172:175], a[44:47]\n"
"ds_read_b64_tr_b16 v[230:231], v7 offset:18432\n"
"v_mfma_f32_16x16x32_bf16 a[48:51], v[132:135], v[176:179], a[48:51]\n"
"v_mfma_f32_16x16x32_bf16 a[52:55], v[132:135], v[180:183], a[52:55]\n"
"ds_read_b128 v[200:203], v5 offset:4096\n"
"v_mfma_f32_16x16x32_bf16 a[56:59], v[132:135], v[184:187], a[56:59]\n"
"v_mfma_f32_16x16x32_bf16 a[60:63], v[132:135], v[188:191], a[60:63]\n"
"ds_read_b64_tr_b16 v[232:233], v6 offset:16640\n"
"v_mfma_f32_16x16x32_bf16 a[64:67], v[136:139], v[160:163], a[64:67]\n"
"v_mfma_f32_16x16x32_bf16 a[68:71], v[136:139], v[164:167], a[68:71]\n"
"ds_read_b64_tr_b16 v[234:235], v6 offset:18688\n"
"v_mfma_f32_16x16x32_bf16 a[72:75], v[136:139], v[168:171], a[72:75]\n"
"v_mfma_f32_16x16x32_bf16 a[76:79], v[136:139], v[172:175], a[76:79]\n"
"ds_read_b128 v[204:207], v5 offset:6144\n"
"v_mfma_f32_16x16x32_bf16 a[80:83], v[136:139], v[176:179], a[80:83]\n"
"v_mfma_f32_16x16x32_bf16 a[84:87], v[136:139], v[180:183], a[84:87]\n"
"ds_read_b64_tr_b16 v[236:237], v7 offset:16640\n"
"v_mfma_f32_16x16x32_bf16 a[88:91], v[136:139], v[184:187], a[88:91]\n"
"v_mfma_f32_16x16x32_bf16 a[92:95], v[136:139], v[188:191], a[92:95]\n"
"ds_read_b64_tr_b16 v[238:239], v7 offset:18688\n"
"v_mfma_f32_16x16x32_bf16 a[96:99], v[140:143], v[160:163], a[96:99]\n"
"v_mfma_f32_16x16x32_bf16 a[100:103], v[140:143], v[164:167], a[100:103]\n"
"ds_read_b128 v[208:211], v5 offset:8192\n"
"v_mfma_f32_16x16x32_bf16 a[104:107], v[140:143], v[168:171], a[104:107]\n"
"v_mfma_f32_16x16x32_bf16 a[108:111], v[140:143], v[172:175], a[108:111]\n"
"ds_read_b64_tr_b16 v[240:241], v6 offset:16896\n"
"v_mfma_f32_16x16x32_bf16 a[112:115], v[140:143], v[176:179], a[112:115]\n"
"v_mfma_f32_16x16x32_bf16 a[116:119], v[140:143], v[180:183], a[116:119]\n"
"ds_read_b64_tr_b16 v[242:243], v6 offset:18944\n"
"v_mfma_f32_16x16x32_bf16 a[120:123], v[140:143], v[184:187], a[120:123]\n"
"v_mfma_f32_16x16x32_bf16 a[124:127], v[140:143], v[188:191], a[124:127]\n"
"ds_read_b128 v[212:215], v5 offset:10240\n"
"v_mfma_f32_16x16x32_bf16 a[128:131], v[144:147], v[160:163], a[128:131]\n"
"v_mfma_f32_16x16x32_bf16 a[132:135], v[144:147], v[164:167], a[132:135]\n"
"ds_read_b64_tr_b16 v[244:245], v7 offset:16896\n"
"v_mfma_f32_16x16x32_bf16 a[136:139], v[144:147], v[168:171], a[136:139]\n"
"v_mfma_f32_16x16x32_bf16 a[140:143], v[144:147], v[172:175], a[140:143]\n"
"ds_read_b64_tr_b16 v[246:247], v7 offset:18944\n"
"v_mfma_f32_16x16x32_bf16 a[144:147], v[144:147], v[176:179], a[144:147]\n"
"v_mfma_f32_16x16x32_bf16 a[148:151], v[144:147], v[180:183], a[148:151]\n"
"ds_read_b128 v[216:219], v5 offset:12288\n"
"v_mfma_f32_16x16x32_bf16 a[152:155], v[144:147], v[184:187], a[152:155]\n"
"v_mfma_f32_16x16x32_bf16 a[156:159], v[144:147], v[188:191], a[156:159]\n"
"ds_read_b64_tr_b16 v[248:249], v6 offset:17152\n"
"v_mfma_f32_16x16x32_bf16 a[160:163], v[148:151], v[160:163], a[160:163]\n"
"v_mfma_f32_16x16x32_bf16 a[164:167], v[148:151], v[164:167], a[164:167]\n"
"ds_read_b64_tr_b16 v[250:251], v6 offset:19200\n"
"v_mfma_f32_16x16x32_bf16 a[168:171], v[148:151], v[168:171], a[168:171]\n"
"v_mfma_f32_16x16x32_bf16 a[172:175], v[148:151], v[172:175], a[172:175]\n"
"ds_read_b128 v[220:223], v5 offset:14336\n"
"v_mfma_f32_16x16x32_bf16 a[176:179], v[148:151], v[176:179], a[176:179]\n"
"v_mfma_f32_16x16x32_bf16 a[180:183], v[148:151], v[180:183], a[180:183]\n"
"ds_read_b64_tr_b16 v[252:253], v7 offset:17152\n"
"v_mfma_f32_16x16x32_bf16 a[184:187], v[148:151], v[184:187], a[184:187]\n"
"v_mfma_f32_16x16x32_bf16 a[188:191], v[148:151], v[188:191], a[188:191]\n"
"ds_read_b64_tr_b16 v[254:255], v7 offset:19200\n"
"v_mfma_f32_16x16x32_bf16 a[192:195], v[152:155], v[160:163], a[192:195]\n"
"v_mfma_f32_16x16x32_bf16 a[196:199], v[152:155], v[164:167], a[196:199]\n"
"v_mfma_f32_16x16x32_bf16 a[200:203], v[152:155], v[168:171], a[200:203]\n"
"v_mfma_f32_16x16x32_bf16 a[204:207], v[152:155], v[172:175], a[204:207]\n"
"v_mfma_f32_16x16x32_bf16 a[208:211], v[152:155], v[176:179], a[208:211]\n"
"v_mfma_f32_16x16x32_bf16 a[212:215], v[152:155], v[180:183], a[212:215]\n"
"v_mfma_f32_16x16x32_bf16 a[216:219], v[152:155], v[184:187], a[216:219]\n"
"v_mfma_f32_16x16x32_bf16 a[220:223], v[152:155], v[188:191], a[220:223]\n"
"v_mfma_f32_16x16x32_bf16 a[224:227], v[156:159], v[160:163], a[224:227]\n"
"v_mfma_f32_16x16x32_bf16 a[228:231], v[156:159], v[164:167], a[228:231]\n"
"v_mfma_f32_16x16x32_bf16 a[232:235], v[156:159], v[168:171], a[232:235]\n"
"v_mfma_f32_16x16x32_bf16 a[236:239], v[156:159], v[172:175], a[236:239]\n"
"v_mfma_f32_16x16x32_bf16 a[240:243], v[156:159], v[176:179], a[240:243]\n"
"v_mfma_f32_16x16x32_bf16 a[244:247], v[156:159], v[180:183], a[244:247]\n"
"v_mfma_f32_16x16x32_bf16 a[248:251], v[156:159], v[184:187], a[248:251]\n"
"v_mfma_f32_16x16x32_bf16 a[252:255], v[156:159], v[188:191], a[252:255]\n"
"s_waitcnt vmcnt(1) lgkmcnt(0)\n"
"s_barrier\n"
"v_xor_b32 v4, 0x10000, v4\n"
"v_xor_b32 v5, 0x10000, v5\n"
"v_xor_b32 v6, 0x10000, v6\n"
"v_xor_b32 v7, 0x10000, v7\n"
"v_mfma_f32_16x16x32_bf16 a[0:3], v[192:195], v[224:227], a[0:3]\n"
"s_mov_b32 m0, s52\n"
"v_mfma_f32_16x16x32_bf16 a[4:7], v[192:195], v[228:231], a[4:7]\n"
"ds_read_b128 v[128:131], v4 offset:0\n"
"buffer_load_dwordx4 v16, s[36:39], s48 offen  lds\n"
"v_mfma_f32_16x16x32_bf16 a[8:11], v[192:195], v[232:235], a[8:11]\n"
"v_mfma_f32_16x16x32_bf16 a[12:15], v[192:195], v[236:239], a[12:15]\n"
"ds_read_b64_tr_b16 v[160:161], v6 offset:0\n"
"v_mfma_f32_16x16x32_bf16 a[16:19], v[192:195], v[240:243], a[16:19]\n"
"s_add_u32 m0, m0, 0x400\n"
"v_mfma_f32_16x16x32_bf16 a[20:23], v[192:195], v[244:247], a[20:23]\n"
"ds_read_b64_tr_b16 v[162:163], v6 offset:2048\n"
"buffer_load_dwordx4 v17, s[36:39], s48 offen  lds\n"
"v_mfma_f32_16x16x32_bf16 a[24:27], v[192:195], v[248:251], a[24:27]\n"
"v_mfma_f32_16x16x32_bf16 a[28:31], v[192:195], v[252:255], a[28:31]\n"
"ds_read_b128 v[132:135], v4 offset:2048\n"
"v_mfma_f32_16x16x32_bf16 a[32:35], v[196:199], v[224:227], a[32:35]\n"
"s_add_u32 m0, m0, 0x400\n"
"v_mfma_f32_16x16x32_bf16 a[36:39], v[196:199], v[228:231], a[36:39]\n"
"ds_read_b64_tr_b16 v[164:165], v7 offset:0\n"
"buffer_load_dwordx4 v18, s[36:39], s48 offen  lds\n"
"v_mfma_f32_16x16x32_bf16 a[40:43], v[196:199], v[232:235], a[40:43]\n"
"v_mfma_f32_16x16x32_bf16 a[44:47], v[196:199], v[236:239], a[44:47]\n"
"ds_read_b64_tr_b16 v[166:167], v7 offset:2048\n"
"v_mfma_f32_16x16x32_bf16 a[48:51], v[196:199], v[240:243], a[48:51]\n"
"s_add_u32 m0, m0, 0x400\n"
"v_mfma_f32_16x16x32_bf16 a[52:55], v[196:199], v[244:247], a[52:55]\n"
"ds_read_b128 v[136:139], v4 offset:4096\n"
"buffer_load_dwordx4 v19, s[36:39], s48 offen  lds\n"
"v_mfma_f32_16x16x32_bf16 a[56:59], v[196:199], v[248:251], a[56:59]\n"
"v_mfma_f32_16x16x32_bf16 a[60:63], v[196:199], v[252:255], a[60:63]\n"
"ds_read_b64_tr_b16 v[168:169], v6 offset:256\n"
"v_mfma_f32_16x16x32_bf16 a[64:67], v[200:203], v[224:227], a[64:67]\n"
"s_add_u32 m0, m0, 0x400\n"
"v_mfma_f32_16x16x32_bf16 a[68:71], v[200:203], v[228:231], a[68:71]\n"
"ds_read_b64_tr_b16 v[170:171], v6 offset:2304\n"
"buffer_load_dwordx4 v20, s[36:39], s48 offen  lds\n"
"v_mfma_f32_16x16x32_bf16 a[72:75], v[200:203], v[232:235], a[72:75]\n"
"v_mfma_f32_16x16x32_bf16 a[76:79], v[200:203], v[236:239], a[76:79]\n"
"ds_read_b128 v[140:143], v4 offset:6144\n"
"v_mfma_f32_16x16x32_bf16 a[80:83], v[200:203], v[240:243], a[80:83]\n"
"s_add_u32 m0, m0, 0x400\n"
"v_mfma_f32_16x16x32_bf16 a[84:87], v[200:203], v[244:247], a[84:87]\n"
"ds_read_b64_tr_b16 v[172:173], v7 offset:256\n"
"buffer_load_dwordx4 v21, s[36:39], s48 offen  lds\n"
"v_mfma_f32_16x16x32_bf16 a[88:91], v[200:203], v[248:251], a[88:91]\n"
"v_mfma_f32_16x16x32_bf16 a[92:95], v[200:203], v[252:255], a[92:95]\n"
"ds_read_b64_tr_b16 v[174:175], v7 offset:2304\n"
"v_mfma_f32_16x16x32_bf16 a[96:99], v[204:207], v[224:227], a[96:99]\n"
"s_add_u32 m0, m0, 0x400\n"
"v_mfma_f32_16x16x32_bf16 a[100:103], v[204:207], v[228:231], a[100:103]\n"
"ds_read_b128 v[144:147], v4 offset:8192\n"
"buffer_load_dwordx4 v22, s[36:39], s48 offen  lds\n"
"v_mfma_f32_16x16x32_bf16 a[104:107], v[204:207], v[232:235], a[104:107]\n"
"v_mfma_f32_16x16x32_bf16 a[108:111], v[204:207], v[236:239], a[108:111]\n"
"ds_read_b64_tr_b16 v[176:177], v6 offset:512\n"
"v_mfma_f32_16x16x32_bf16 a[112:115], v[204:207], v[240:243], a[112:115]\n"
"s_add_u32 m0, m0, 0x400\n"
"v_mfma_f32_16x16x32_bf16 a[116:119], v[204:207], v[244:247], a[116:119]\n"
"ds_read_b64_tr_b16 v[178:179], v6 offset:2560\n"
"buffer_load_dwordx4 v23, s[36:39], s48 offen  lds\n"
"v_mfma_f32_16x16x32_bf16 a[120:123], v[204:207], v[248:251], a[120:123]\n"
"v_mfma_f32_16x16x32_bf16 a[124:127], v[204:207], v[252:255], a[124:127]\n"
"ds_read_b128 v[148:151], v4 offset:10240\n"
"v_mfma_f32_16x16x32_bf16 a[128:131], v[208:211], v[224:227], a[128:131]\n"
"s_add_u32 m0, m0, 25600\n"
"v_mfma_f32_16x16x32_bf16 a[132:135], v[208:211], v[228:231], a[132:135]\n"
"ds_read_b64_tr_b16 v[180:181], v7 offset:512\n"
"buffer_load_dwordx4 v24, s[40:43], s49 offen  lds\n"
"v_mfma_f32_16x16x32_bf16 a[136:139], v[208:211], v[232:235], a[136:139]\n"
"v_mfma_f32_16x16x32_bf16 a[140:143], v[208:211], v[236:239], a[140:143]\n"
"ds_read_b64_tr_b16 v[182:183], v7 offset:2560\n"
"v_mfma_f32_16x16x32_bf16 a[144:147], v[208:211], v[240:243], a[144:147]\n"
"s_add_u32 m0, m0, 0x400\n"
"v_mfma_f32_16x16x32_bf16 a[148:151], v[208:211], v[244:247], a[148:151]\n"
"ds_read_b128 v[152:155], v4 offset:12288\n"
"buffer_load_dwordx4 v25, s[40:43], s49 offen  lds\n"
"v_mfma_f32_16x16x32_bf16 a[152:155], v[208:211], v[248:251], a[152:155]\n"
"v_mfma_f32_16x16x32_bf16 a[156:159], v[208:211], v[252:255], a[156:159]\n"
"ds_read_b64_tr_b16 v[184:185], v6 offset:768\n"
"v_mfma_f32_16x16x32_bf16 a[160:163], v[212:215], v[224:227], a[160:163]\n"
"s_add_u32 m0, m0, 0x400\n"
"v_mfma_f32_16x16x32_bf16 a[164:167], v[212:215], v[228:231], a[164:167]\n"
"ds_read_b64_tr_b16 v[186:187], v6 offset:2816\n"
"buffer_load_dwordx4 v26, s[40:43], s49 offen  lds\n"
"v_mfma_f32_16x16x32_bf16 a[168:171], v[212:215], v[232:235], a[168:171]\n"
"v_mfma_f32_16x16x32_bf16 a[172:175], v[212:215], v[236:239], a[172:175]\n"
"ds_read_b128 v[156:159], v4 offset:14336\n"
"v_mfma_f32_16x16x32_bf16 a[176:179], v[212:215], v[240:243], a[176:179]\n"
"s_add_u32 m0, m0, 0x400\n"
"v_mfma_f32_16x16x32_bf16 a[180:183], v[212:215], v[244:247], a[180:183]\n"
"ds_read_b64_tr_b16 v[188:189], v7 offset:768\n"
"buffer_load_dwordx4 v27, s[40:43], s49 offen  lds\n"
"v_mfma_f32_16x16x32_bf16 a[184:187], v[212:215], v[248:251], a[184:187]\n"
"v_mfma_f32_16x16x32_bf16 a[188:191], v[212:215], v[252:255], a[188:191]\n"
"ds_read_b64_tr_b16 v[190:191], v7 offset:2816\n"
"v_mfma_f32_16x16x32_bf16 a[192:195], v[216:219], v[224:227], a[192:195]\n"
"s_add_u32 m0, m0, 0x400\n"
"v_mfma_f32_16x16x32_bf16 a[196:199], v[216:219], v[228:231], a[196:199]\n"
"buffer_load_dwordx4 v28, s[40:43], s49 offen  lds\n"
"v_mfma_f32_16x16x32_bf16 a[200:203], v[216:219], v[232:235], a[200:203]\n"
"v_mfma_f32_16x16x32_bf16 a[204:207], v[216:219], v[236:239], a[204:207]\n"
"v_mfma_f32_16x16x32_bf16 a[208:211], v[216:219], v[240:243], a[208:211]\n"
"s_add_u32 m0, m0, 0x400\n"
"v_mfma_f32_16x16x32_bf16 a[212:215], v[216:219], v[244:247], a[212:215]\n"
"buffer_load_dwordx4 v29, s[40:43], s49 offen  lds\n"
"v_mfma_f32_16x16x32_bf16 a[216:219], v[216:219], v[248:251], a[216:219]\n"
"v_mfma_f32_16x16x32_bf16 a[220:223], v[216:219], v[252:255], a[220:223]\n"
"v_mfma_f32_16x16x32_bf16 a[224:227], v[220:223], v[224:227], a[224:227]\n"
"s_add_u32 m0, m0, 0x400\n"
"v_mfma_f32_16x16x32_bf16 a[228:231], v[220:223], v[228:231], a[228:231]\n"
"buffer_load_dwordx4 v30, s[40:43], s49 offen  lds\n"
"v_mfma_f32_16x16x32_bf16 a[232:235], v[220:223], v[232:235], a[232:235]\n"
"v_mfma_f32_16x16x32_bf16 a[236:239], v[220:223], v[236:239], a[236:239]\n"
"v_mfma_f32_16x16x32_bf16 a[240:243], v[220:223], v[240:243], a[240:243]\n"
"s_add_u32 m0, m0, 0x400\n"
"v_mfma_f32_16x16x32_bf16 a[244:247], v[220:223], v[244:247], a[244:247]\n"
"buffer_load_dwordx4 v31, s[40:43], s49 offen  lds\n"
"v_mfma_f32_16x16x32_bf16 a[248:251], v[220:223], v[248:251], a[248:251]\n"
"s_add_u32 s48, s48, 128\n"
"s_add_u32 s49, s49, s50\n"
"s_xor_b32 s52, s52, 0x10000\n"
"v_mfma_f32_16x16x32_bf16 a[252:255], v[220:223], v[252:255], a[252:255]\n"
"s_sub_u32 s34, s34, 1\n"
"s_cbranch_scc1 L_pfs_Vl_%=\n"
"global_load_dword v51, v[48:49], off\n"
"v_add_co_u32 v48, vcc, v48, v50\n"
"s_nop 1\n"
"v_addc_co_u32 v49, vcc, 0, v49, vcc\n"
"L_pfr_Vl_%=:\n"
"s_waitcnt lgkmcnt(0)\n"
"s_sub_u32 s51, s51, 1\n"
"s_cmp_lg_u32 s51, 0\n"
"s_cbranch_scc1 L_V_loop_%=\n"
"s_cmp_eq_u32 s69, 0x3f800000\n"
"s_cbranch_scc1 L_V_tobl_%=\n"
"s_nop 15\n"
"s_mov_b32 s83, s53\n"
"v_accvgpr_read_b32 v192, a0\n"
"v_accvgpr_read_b32 v193, a1\n"
"v_accvgpr_read_b32 v194, a2\n"
"v_accvgpr_read_b32 v195, a3\n"
"v_accvgpr_read_b32 v196, a32\n"
"v_accvgpr_read_b32 v197, a33\n"
"v_accvgpr_read_b32 v198, a34\n"
"v_accvgpr_read_b32 v199, a35\n"
"v_cvt_pk_bf16_f32 v200, v192, v193\n"
"v_cvt_pk_bf16_f32 v201, v194, v195\n"
"v_cvt_pk_bf16_f32 v202, v196, v197\n"
"v_cvt_pk_bf16_f32 v203, v198, v199\n"
"buffer_store_dwordx4 v[200:203], v8, s[44:47], s83 offen offset:0 sc1\n"
"v_accvgpr_read_b32 v208, a4\n"
"v_accvgpr_read_b32 v209, a5\n"
"v_accvgpr_read_b32 v210, a6\n"
"v_accvgpr_read_b32 v211, a7\n"
"v_accvgpr_read_b32 v212, a36\n"
"v_accvgpr_read_b32 v213, a37\n"
"v_accvgpr_read_b32 v214, a38\n"
"v_accvgpr_read_b32 v215, a39\n"
"v_cvt_pk_bf16_f32 v216, v208, v209\n"
"v_cvt_pk_bf16_f32 v217, v210, v211\n"
"v_cvt_pk_bf16_f32 v218, v212, v213\n"
"v_cvt_pk_bf16_f32 v219, v214, v215\n"
"buffer_store_dwordx4 v[216:219], v8, s[44:47], s83 offen offset:1024 sc1\n"
"v_accvgpr_read_b32 v224, a8\n"
"v_accvgpr_read_b32 v225, a9\n"
"v_accvgpr_read_b32 v226, a10\n"
"v_accvgpr_read_b32 v227, a11\n"
"v_accvgpr_read_b32 v228, a40\n"
"v_accvgpr_read_b32 v229, a41\n"
"v_accvgpr_read_b32 v230, a42\n"
"v_accvgpr_read_b32 v231, a43\n"
"v_cvt_pk_bf16_f32 v232, v224, v225\n"
"v_cvt_pk_bf16_f32 v233, v226, v227\n"
"v_cvt_pk_bf16_f32 v234, v228, v229\n"
"v_cvt_pk_bf16_f32 v235, v230, v231\n"
"buffer_store_dwordx4 v[232:235], v8, s[44:47], s83 offen offset:2048 sc1\n"
"v_accvgpr_read_b32 v240, a12\n"
"v_accvgpr_read_b32 v241, a13\n"
"v_accvgpr_read_b32 v242, a14\n"
"v_accvgpr_read_b32 v243, a15\n"
"v_accvgpr_read_b32 v244, a44\n"
"v_accvgpr_read_b32 v245, a45\n"
"v_accvgpr_read_b32 v246, a46\n"
"v_accvgpr_read_b32 v247, a47\n"
"v_cvt_pk_bf16_f32 v248, v240, v241\n"
"v_cvt_pk_bf16_f32 v249, v242, v243\n"
"v_cvt_pk_bf16_f32 v250, v244, v245\n"
"v_cvt_pk_bf16_f32 v251, v246, v247\n"
"buffer_store_dwordx4 v[248:251], v8, s[44:47], s83 offen offset:3072 sc1\n"
"s_add_u32 s83, s83, 4096\n"
"v_accvgpr_read_b32 v192, a16\n"
"v_accvgpr_read_b32 v193, a17\n"
"v_accvgpr_read_b32 v194, a18\n"
"v_accvgpr_read_b32 v195, a19\n"
"v_accvgpr_read_b32 v196, a48\n"
"v_accvgpr_read_b32 v197, a49\n"
"v_accvgpr_read_b32 v198, a50\n"
"v_accvgpr_read_b32 v199, a51\n"
"v_cvt_pk_bf16_f32 v200, v192, v193\n"
"v_cvt_pk_bf16_f32 v201, v194, v195\n"
"v_cvt_pk_bf16_f32 v202, v196, v197\n"
"v_cvt_pk_bf16_f32 v203, v198, v199\n"
"buffer_store_dwordx4 v[200:203], v8, s[44:47], s83 offen offset:0 sc1\n"
"v_accvgpr_read_b32 v208, a20\n"
"v_accvgpr_read_b32 v209, a21\n"
"v_accvgpr_read_b32 v210, a22\n"
"v_accvgpr_read_b32 v211, a23\n"
"v_accvgpr_read_b32 v212, a52\n"
"v_accvgpr_read_b32 v213, a53\n"
"v_accvgpr_read_b32 v214, a54\n"
"v_accvgpr_read_b32 v215, a55\n"
"v_cvt_pk_bf16_f32 v216, v208, v209\n"
"v_cvt_pk_bf16_f32 v217, v210, v211\n"
"v_cvt_pk_bf16_f32 v218, v212, v213\n"
"v_cvt_pk_bf16_f32 v219, v214, v215\n"
"buffer_store_dwordx4 v[216:219], v8, s[44:47], s83 offen offset:1024 sc1\n"
"v_accvgpr_read_b32 v224, a24\n"
"v_accvgpr_read_b32 v225, a25\n"
"v_accvgpr_read_b32 v226, a26\n"
"v_accvgpr_read_b32 v227, a27\n"
"v_accvgpr_read_b32 v228, a56\n"
"v_accvgpr_read_b32 v229, a57\n"
"v_accvgpr_read_b32 v230, a58\n"
"v_accvgpr_read_b32 v231, a59\n"
"v_cvt_pk_bf16_f32 v232, v224, v225\n"
"v_cvt_pk_bf16_f32 v233, v226, v227\n"
"v_cvt_pk_bf16_f32 v234, v228, v229\n"
"v_cvt_pk_bf16_f32 v235, v230, v231\n"
"buffer_store_dwordx4 v[232:235], v8, s[44:47], s83 offen offset:2048 sc1\n"
"v_accvgpr_read_b32 v240, a28\n"
"v_accvgpr_read_b32 v241, a29\n"
"v_accvgpr_read_b32 v242, a30\n"
"v_accvgpr_read_b32 v243, a31\n"
"v_accvgpr_read_b32 v244, a60\n"
"v_accvgpr_read_b32 v245, a61\n"
"v_accvgpr_read_b32 v246, a62\n"
"v_accvgpr_read_b32 v247, a63\n"
"v_cvt_pk_bf16_f32 v248, v240, v241\n"
"v_cvt_pk_bf16_f32 v249, v242, v243\n"
"v_cvt_pk_bf16_f32 v250, v244, v245\n"
"v_cvt_pk_bf16_f32 v251, v246, v247\n"
"buffer_store_dwordx4 v[248:251], v8, s[44:47], s83 offen offset:3072 sc1\n"
"s_add_u32 s83, s83, 12288\n"
"v_accvgpr_read_b32 v192, a64\n"
"v_accvgpr_read_b32 v193, a65\n"
"v_accvgpr_read_b32 v194, a66\n"
"v_accvgpr_read_b32 v195, a67\n"
"v_accvgpr_read_b32 v196, a96\n"
"v_accvgpr_read_b32 v197, a97\n"
"v_accvgpr_read_b32 v198, a98\n"
"v_accvgpr_read_b32 v199, a99\n"
"v_cvt_pk_bf16_f32 v200, v192, v193\n"
"v_cvt_pk_bf16_f32 v201, v194, v195\n"
"v_cvt_pk_bf16_f32 v202, v196, v197\n"
"v_cvt_pk_bf16_f32 v203, v198, v199\n"
"buffer_store_dwordx4 v[200:203], v8, s[44:47], s83 offen offset:0 sc1\n"
"v_accvgpr_read_b32 v208, a68\n"
"v_accvgpr_read_b32 v209, a69\n"
"v_accvgpr_read_b32 v210, a70\n"
"v_accvgpr_read_b32 v211, a71\n"
"v_accvgpr_read_b32 v212, a100\n"
"v_accvgpr_read_b32 v213, a101\n"
"v_accvgpr_read_b32 v214, a102\n"
"v_accvgpr_read_b32 v215, a103\n"
"v_cvt_pk_bf16_f32 v216, v208, v209\n"
"v_cvt_pk_bf16_f32 v217, v210, v211\n"
"v_cvt_pk_bf16_f32 v218, v212, v213\n"
"v_cvt_pk_bf16_f32 v219, v214, v215\n"
"buffer_store_dwordx4 v[216:219], v8, s[44:47], s83 offen offset:1024 sc1\n"
"v_accvgpr_read_b32 v224, a72\n"
"v_accvgpr_read_b32 v225, a73\n"
"v_accvgpr_read_b32 v226, a74\n"
"v_accvgpr_read_b32 v227, a75\n"
"v_accvgpr_read_b32 v228, a104\n"
"v_accvgpr_read_b32 v229, a105\n"
"v_accvgpr_read_b32 v230, a106\n"
"v_accvgpr_read_b32 v231, a107\n"
"v_cvt_pk_bf16_f32 v232, v224, v225\n"
"v_cvt_pk_bf16_f32 v233, v226, v227\n"
"v_cvt_pk_bf16_f32 v234, v228, v229\n"
"v_cvt_pk_bf16_f32 v235, v230, v231\n"
"buffer_store_dwordx4 v[232:235], v8, s[44:47], s83 offen offset:2048 sc1\n"
"v_accvgpr_read_b32 v240, a76\n"
"v_accvgpr_read_b32 v241, a77\n"
"v_accvgpr_read_b32 v242, a78\n"
"v_accvgpr_read_b32 v243, a79\n"
"v_accvgpr_read_b32 v244, a108\n"
"v_accvgpr_read_b32 v245, a109\n"
"v_accvgpr_read_b32 v246, a110\n"
"v_accvgpr_read_b32 v247, a111\n"
"v_cvt_pk_bf16_f32 v248, v240, v241\n"
"v_cvt_pk_bf16_f32 v249, v242, v243\n"
"v_cvt_pk_bf16_f32 v250, v244, v245\n"
"v_cvt_pk_bf16_f32 v251, v246, v247\n"
"buffer_store_dwordx4 v[248:251], v8, s[44:47], s83 offen offset:3072 sc1\n"
"s_add_u32 s83, s83, 4096\n"
"v_accvgpr_read_b32 v192, a80\n"
"v_accvgpr_read_b32 v193, a81\n"
"v_accvgpr_read_b32 v194, a82\n"
"v_accvgpr_read_b32 v195, a83\n"
"v_accvgpr_read_b32 v196, a112\n"
"v_accvgpr_read_b32 v197, a113\n"
"v_accvgpr_read_b32 v198, a114\n"
"v_accvgpr_read_b32 v199, a115\n"
"v_cvt_pk_bf16_f32 v200, v192, v193\n"
"v_cvt_pk_bf16_f32 v201, v194, v195\n"
"v_cvt_pk_bf16_f32 v202, v196, v197\n"
"v_cvt_pk_bf16_f32 v203, v198, v199\n"
"buffer_store_dwordx4 v[200:203], v8, s[44:47], s83 offen offset:0 sc1\n"
"v_accvgpr_read_b32 v208, a84\n"
"v_accvgpr_read_b32 v209, a85\n"
"v_accvgpr_read_b32 v210, a86\n"
"v_accvgpr_read_b32 v211, a87\n"
"v_accvgpr_read_b32 v212, a116\n"
"v_accvgpr_read_b32 v213, a117\n"
"v_accvgpr_read_b32 v214, a118\n"
"v_accvgpr_read_b32 v215, a119\n"
"v_cvt_pk_bf16_f32 v216, v208, v209\n"
"v_cvt_pk_bf16_f32 v217, v210, v211\n"
"v_cvt_pk_bf16_f32 v218, v212, v213\n"
"v_cvt_pk_bf16_f32 v219, v214, v215\n"
"buffer_store_dwordx4 v[216:219], v8, s[44:47], s83 offen offset:1024 sc1\n"
"v_accvgpr_read_b32 v224, a88\n"
"v_accvgpr_read_b32 v225, a89\n"
"v_accvgpr_read_b32 v226, a90\n"
"v_accvgpr_read_b32 v227, a91\n"
"v_accvgpr_read_b32 v228, a120\n"
"v_accvgpr_read_b32 v229, a121\n"
"v_accvgpr_read_b32 v230, a122\n"
"v_accvgpr_read_b32 v231, a123\n"
"v_cvt_pk_bf16_f32 v232, v224, v225\n"
"v_cvt_pk_bf16_f32 v233, v226, v227\n"
"v_cvt_pk_bf16_f32 v234, v228, v229\n"
"v_cvt_pk_bf16_f32 v235, v230, v231\n"
"buffer_store_dwordx4 v[232:235], v8, s[44:47], s83 offen offset:2048 sc1\n"
"v_accvgpr_read_b32 v240, a92\n"
"v_accvgpr_read_b32 v241, a93\n"
"v_accvgpr_read_b32 v242, a94\n"
"v_accvgpr_read_b32 v243, a95\n"
"v_accvgpr_read_b32 v244, a124\n"
"v_accvgpr_read_b32 v245, a125\n"
"v_accvgpr_read_b32 v246, a126\n"
"v_accvgpr_read_b32 v247, a127\n"
"v_cvt_pk_bf16_f32 v248, v240, v241\n"
"v_cvt_pk_bf16_f32 v249, v242, v243\n"
"v_cvt_pk_bf16_f32 v250, v244, v245\n"
"v_cvt_pk_bf16_f32 v251, v246, v247\n"
"buffer_store_dwordx4 v[248:251], v8, s[44:47], s83 offen offset:3072 sc1\n"
"s_add_u32 s83, s83, 12288\n"
"v_accvgpr_read_b32 v192, a128\n"
"v_accvgpr_read_b32 v193, a129\n"
"v_accvgpr_read_b32 v194, a130\n"
"v_accvgpr_read_b32 v195, a131\n"
"v_accvgpr_read_b32 v196, a160\n"
"v_accvgpr_read_b32 v197, a161\n"
"v_accvgpr_read_b32 v198, a162\n"
"v_accvgpr_read_b32 v199, a163\n"
"v_cvt_pk_bf16_f32 v64, v192, v193\n"
"v_cvt_pk_bf16_f32 v65, v194, v195\n"
"v_cvt_pk_bf16_f32 v66, v196, v197\n"
"v_cvt_pk_bf16_f32 v67, v198, v199\n"
"v_accvgpr_read_b32 v208, a132\n"
"v_accvgpr_read_b32 v209, a133\n"
"v_accvgpr_read_b32 v210, a134\n"
"v_accvgpr_read_b32 v211, a135\n"
"v_accvgpr_read_b32 v212, a164\n"
"v_accvgpr_read_b32 v213, a165\n"
"v_accvgpr_read_b32 v214, a166\n"
"v_accvgpr_read_b32 v215, a167\n"
"v_cvt_pk_bf16_f32 v68, v208, v209\n"
"v_cvt_pk_bf16_f32 v69, v210, v211\n"
"v_cvt_pk_bf16_f32 v70, v212, v213\n"
"v_cvt_pk_bf16_f32 v71, v214, v215\n"
"v_accvgpr_read_b32 v224, a136\n"
"v_accvgpr_read_b32 v225, a137\n"
"v_accvgpr_read_b32 v226, a138\n"
"v_accvgpr_read_b32 v227, a139\n"
"v_accvgpr_read_b32 v228, a168\n"
"v_accvgpr_read_b32 v229, a169\n"
"v_accvgpr_read_b32 v230, a170\n"
"v_accvgpr_read_b32 v231, a171\n"
"v_cvt_pk_bf16_f32 v72, v224, v225\n"
"v_cvt_pk_bf16_f32 v73, v226, v227\n"
"v_cvt_pk_bf16_f32 v74, v228, v229\n"
"v_cvt_pk_bf16_f32 v75, v230, v231\n"
"v_accvgpr_read_b32 v240, a140\n"
"v_accvgpr_read_b32 v241, a141\n"
"v_accvgpr_read_b32 v242, a142\n"
"v_accvgpr_read_b32 v243, a143\n"
"v_accvgpr_read_b32 v244, a172\n"
"v_accvgpr_read_b32 v245, a173\n"
"v_accvgpr_read_b32 v246, a174\n"
"v_accvgpr_read_b32 v247, a175\n"
"v_cvt_pk_bf16_f32 v76, v240, v241\n"
"v_cvt_pk_bf16_f32 v77, v242, v243\n"
"v_cvt_pk_bf16_f32 v78, v244, v245\n"
"v_cvt_pk_bf16_f32 v79, v246, v247\n"
"v_accvgpr_read_b32 v192, a144\n"
"v_accvgpr_read_b32 v193, a145\n"
"v_accvgpr_read_b32 v194, a146\n"
"v_accvgpr_read_b32 v195, a147\n"
"v_accvgpr_read_b32 v196, a176\n"
"v_accvgpr_read_b32 v197, a177\n"
"v_accvgpr_read_b32 v198, a178\n"
"v_accvgpr_read_b32 v199, a179\n"
"v_cvt_pk_bf16_f32 v80, v192, v193\n"
"v_cvt_pk_bf16_f32 v81, v194, v195\n"
"v_cvt_pk_bf16_f32 v82, v196, v197\n"
"v_cvt_pk_bf16_f32 v83, v198, v199\n"
"v_accvgpr_read_b32 v208, a148\n"
"v_accvgpr_read_b32 v209, a149\n"
"v_accvgpr_read_b32 v210, a150\n"
"v_accvgpr_read_b32 v211, a151\n"
"v_accvgpr_read_b32 v212, a180\n"
"v_accvgpr_read_b32 v213, a181\n"
"v_accvgpr_read_b32 v214, a182\n"
"v_accvgpr_read_b32 v215, a183\n"
"v_cvt_pk_bf16_f32 v84, v208, v209\n"
"v_cvt_pk_bf16_f32 v85, v210, v211\n"
"v_cvt_pk_bf16_f32 v86, v212, v213\n"
"v_cvt_pk_bf16_f32 v87, v214, v215\n"
"v_accvgpr_read_b32 v224, a152\n"
"v_accvgpr_read_b32 v225, a153\n"
"v_accvgpr_read_b32 v226, a154\n"
"v_accvgpr_read_b32 v227, a155\n"
"v_accvgpr_read_b32 v228, a184\n"
"v_accvgpr_read_b32 v229, a185\n"
"v_accvgpr_read_b32 v230, a186\n"
"v_accvgpr_read_b32 v231, a187\n"
"v_cvt_pk_bf16_f32 v88, v224, v225\n"
"v_cvt_pk_bf16_f32 v89, v226, v227\n"
"v_cvt_pk_bf16_f32 v90, v228, v229\n"
"v_cvt_pk_bf16_f32 v91, v230, v231\n"
"v_accvgpr_read_b32 v240, a156\n"
"v_accvgpr_read_b32 v241, a157\n"
"v_accvgpr_read_b32 v242, a158\n"
"v_accvgpr_read_b32 v243, a159\n"
"v_accvgpr_read_b32 v244, a188\n"
"v_accvgpr_read_b32 v245, a189\n"
"v_accvgpr_read_b32 v246, a190\n"
"v_accvgpr_read_b32 v247, a191\n"
"v_cvt_pk_bf16_f32 v92, v240, v241\n"
"v_cvt_pk_bf16_f32 v93, v242, v243\n"
"v_cvt_pk_bf16_f32 v94, v244, v245\n"
"v_cvt_pk_bf16_f32 v95, v246, v247\n"
"v_accvgpr_read_b32 v192, a192\n"
"v_accvgpr_read_b32 v193, a193\n"
"v_accvgpr_read_b32 v194, a194\n"
"v_accvgpr_read_b32 v195, a195\n"
"v_accvgpr_read_b32 v196, a224\n"
"v_accvgpr_read_b32 v197, a225\n"
"v_accvgpr_read_b32 v198, a226\n"
"v_accvgpr_read_b32 v199, a227\n"
"v_cvt_pk_bf16_f32 v96, v192, v193\n"
"v_cvt_pk_bf16_f32 v97, v194, v195\n"
"v_cvt_pk_bf16_f32 v98, v196, v197\n"
"v_cvt_pk_bf16_f32 v99, v198, v199\n"
"v_accvgpr_read_b32 v208, a196\n"
"v_accvgpr_read_b32 v209, a197\n"
"v_accvgpr_read_b32 v210, a198\n"
"v_accvgpr_read_b32 v211, a199\n"
"v_accvgpr_read_b32 v212, a228\n"
"v_accvgpr_read_b32 v213, a229\n"
"v_accvgpr_read_b32 v214, a230\n"
"v_accvgpr_read_b32 v215, a231\n"
"v_cvt_pk_bf16_f32 v100, v208, v209\n"
"v_cvt_pk_bf16_f32 v101, v210, v211\n"
"v_cvt_pk_bf16_f32 v102, v212, v213\n"
"v_cvt_pk_bf16_f32 v103, v214, v215\n"
"v_accvgpr_read_b32 v224, a200\n"
"v_accvgpr_read_b32 v225, a201\n"
"v_accvgpr_read_b32 v226, a202\n"
"v_accvgpr_read_b32 v227, a203\n"
"v_accvgpr_read_b32 v228, a232\n"
"v_accvgpr_read_b32 v229, a233\n"
"v_accvgpr_read_b32 v230, a234\n"
"v_accvgpr_read_b32 v231, a235\n"
"v_cvt_pk_bf16_f32 v104, v224, v225\n"
"v_cvt_pk_bf16_f32 v105, v226, v227\n"
"v_cvt_pk_bf16_f32 v106, v228, v229\n"
"v_cvt_pk_bf16_f32 v107, v230, v231\n"
"v_accvgpr_read_b32 v240, a204\n"
"v_accvgpr_read_b32 v241, a205\n"
"v_accvgpr_read_b32 v242, a206\n"
"v_accvgpr_read_b32 v243, a207\n"
"v_accvgpr_read_b32 v244, a236\n"
"v_accvgpr_read_b32 v245, a237\n"
"v_accvgpr_read_b32 v246, a238\n"
"v_accvgpr_read_b32 v247, a239\n"
"v_cvt_pk_bf16_f32 v108, v240, v241\n"
"v_cvt_pk_bf16_f32 v109, v242, v243\n"
"v_cvt_pk_bf16_f32 v110, v244, v245\n"
"v_cvt_pk_bf16_f32 v111, v246, v247\n"
"v_accvgpr_read_b32 v192, a208\n"
"v_accvgpr_read_b32 v193, a209\n"
"v_accvgpr_read_b32 v194, a210\n"
"v_accvgpr_read_b32 v195, a211\n"
"v_accvgpr_read_b32 v196, a240\n"
"v_accvgpr_read_b32 v197, a241\n"
"v_accvgpr_read_b32 v198, a242\n"
"v_accvgpr_read_b32 v199, a243\n"
"v_cvt_pk_bf16_f32 v112, v192, v193\n"
"v_cvt_pk_bf16_f32 v113, v194, v195\n"
"v_cvt_pk_bf16_f32 v114, v196, v197\n"
"v_cvt_pk_bf16_f32 v115, v198, v199\n"
"v_accvgpr_read_b32 v208, a212\n"
"v_accvgpr_read_b32 v209, a213\n"
"v_accvgpr_read_b32 v210, a214\n"
"v_accvgpr_read_b32 v211, a215\n"
"v_accvgpr_read_b32 v212, a244\n"
"v_accvgpr_read_b32 v213, a245\n"
"v_accvgpr_read_b32 v214, a246\n"
"v_accvgpr_read_b32 v215, a247\n"
"v_cvt_pk_bf16_f32 v116, v208, v209\n"
"v_cvt_pk_bf16_f32 v117, v210, v211\n"
"v_cvt_pk_bf16_f32 v118, v212, v213\n"
"v_cvt_pk_bf16_f32 v119, v214, v215\n"
"v_accvgpr_read_b32 v224, a216\n"
"v_accvgpr_read_b32 v225, a217\n"
"v_accvgpr_read_b32 v226, a218\n"
"v_accvgpr_read_b32 v227, a219\n"
"v_accvgpr_read_b32 v228, a248\n"
"v_accvgpr_read_b32 v229, a249\n"
"v_accvgpr_read_b32 v230, a250\n"
"v_accvgpr_read_b32 v231, a251\n"
"v_cvt_pk_bf16_f32 v120, v224, v225\n"
"v_cvt_pk_bf16_f32 v121, v226, v227\n"
"v_cvt_pk_bf16_f32 v122, v228, v229\n"
"v_cvt_pk_bf16_f32 v123, v230, v231\n"
"v_accvgpr_read_b32 v240, a220\n"
"v_accvgpr_read_b32 v241, a221\n"
"v_accvgpr_read_b32 v242, a222\n"
"v_accvgpr_read_b32 v243, a223\n"
"v_accvgpr_read_b32 v244, a252\n"
"v_accvgpr_read_b32 v245, a253\n"
"v_accvgpr_read_b32 v246, a254\n"
"v_accvgpr_read_b32 v247, a255\n"
"v_cvt_pk_bf16_f32 v124, v240, v241\n"
"v_cvt_pk_bf16_f32 v125, v242, v243\n"
"v_cvt_pk_bf16_f32 v126, v244, v245\n"
"v_cvt_pk_bf16_f32 v127, v246, v247\n"
"s_mov_b32 s98, 4096\n"
"s_mov_b32 s99, 12288\n"
"s_mov_b32 s54, 1\n"
"s_mov_b32 s51, s67\n"
"s_mov_b32 s53, s68\n"
"s_mov_b32 s70, s69\n"
"s_mov_b32 s58, 0\n"
"s_mov_b32 s62, 0\n"
"s_branch L_K2_tile_%=\n"
"L_V_tobl_%=:\n"
"s_waitcnt vmcnt(0)\n"
"s_barrier\n"
"s_lshl_b32 s52, s73, 10\n"
"s_mov_b32 s48, s64\n"
"s_mov_b32 s49, 0\n"
"v_lshrrev_b32 v1, 3, v0\n"
"v_and_b32 v2, 7, v0\n"
"v_lshrrev_b32 v3, 4, v0\n"
"v_xor_b32 v2, v2, v3\n"
"v_lshlrev_b32 v2, 4, v2\n"
"v_mul_u32_u24 v1, 896, v1\n"
"v_add_u32 v1, v1, v2\n"
"s_add_u32 s76, s73, 0\n"
"s_lshl_b32 s77, s76, 3\n"
"s_cmp_ge_u32 s77, 144\n"
"s_cselect_b32 s78, 0x40000000, 0\n"
"s_add_u32 s77, s77, s68\n"
"s_mul_i32 s77, s77, 896\n"
"s_add_u32 s77, s77, s78\n"
"s_and_b32 s76, s76, 1\n"
"s_lshl_b32 s76, s76, 6\n"
"v_xor_b32 v32, s76, v1\n"
"v_add_u32 v32, s77, v32\n"
"s_add_u32 s76, s73, 4\n"
"s_lshl_b32 s77, s76, 3\n"
"s_cmp_ge_u32 s77, 144\n"
"s_cselect_b32 s78, 0x40000000, 0\n"
"s_add_u32 s77, s77, s68\n"
"s_mul_i32 s77, s77, 896\n"
"s_add_u32 s77, s77, s78\n"
"s_and_b32 s76, s76, 1\n"
"s_lshl_b32 s76, s76, 6\n"
"v_xor_b32 v33, s76, v1\n"
"v_add_u32 v33, s77, v33\n"
"s_add_u32 s76, s73, 8\n"
"s_lshl_b32 s77, s76, 3\n"
"s_cmp_ge_u32 s77, 144\n"
"s_cselect_b32 s78, 0x40000000, 0\n"
"s_add_u32 s77, s77, s68\n"
"s_mul_i32 s77, s77, 896\n"
"s_add_u32 s77, s77, s78\n"
"s_and_b32 s76, s76, 1\n"
"s_lshl_b32 s76, s76, 6\n"
"v_xor_b32 v34, s76, v1\n"
"v_add_u32 v34, s77, v34\n"
"s_add_u32 s76, s73, 12\n"
"s_lshl_b32 s77, s76, 3\n"
"s_cmp_ge_u32 s77, 144\n"
"s_cselect_b32 s78, 0x40000000, 0\n"
"s_add_u32 s77, s77, s68\n"
"s_mul_i32 s77, s77, 896\n"
"s_add_u32 s77, s77, s78\n"
"s_and_b32 s76, s76, 1\n"
"s_lshl_b32 s76, s76, 6\n"
"v_xor_b32 v35, s76, v1\n"
"v_add_u32 v35, s77, v35\n"
"s_add_u32 s76, s73, 16\n"
"s_lshl_b32 s77, s76, 3\n"
"s_cmp_ge_u32 s77, 144\n"
"s_cselect_b32 s78, 0x40000000, 0\n"
"s_add_u32 s77, s77, s68\n"
"s_mul_i32 s77, s77, 896\n"
"s_add_u32 s77, s77, s78\n"
"s_and_b32 s76, s76, 1\n"
"s_lshl_b32 s76, s76, 6\n"
"v_xor_b32 v36, s76, v1\n"
"v_add_u32 v36, s77, v36\n"
"s_add_u32 s76, s73, 0\n"
"s_lshl_b32 s76, s76, 2\n"
"v_lshrrev_b32 v1, 4, v0\n"
"v_add_u32 v1, s76, v1\n"
"v_mul_u32_u24 v2, 43, v1\n"
"v_lshrrev_b32 v2, 8, v2\n"
"v_mul_u32_u24 v3, 6, v2\n"
"v_sub_u32 v1, v1, v3\n"
"v_lshrrev_b32 v3, 1, v2\n"
"v_and_b32 v3, 1, v3\n"
"v_lshlrev_b32 v3, 1, v3\n"
"v_and_b32 v11, 3, v0\n"
"v_xor_b32 v3, v3, v11\n"
"v_lshl_add_u32 v1, v1, 2, v3\n"
"v_lshrrev_b32 v3, 2, v0\n"
"v_and_b32 v3, 3, v3\n"
"v_lshl_add_u32 v2, v2, 2, v3\n"
"v_mul_u32_u24 v2, 2688, v2\n"
"v_lshl_add_u32 v2, v1, 4, v2\n"
"s_mul_i32 s76, s71, 112\n"
"v_add_u32 v2, s76, v2\n"
"v_mov_b32 v3, 784\n"
"v_cmp_le_u32 vcc, 7, v1\n"
"s_nop 1\n"
"v_cndmask_b32 v11, 0, v3, vcc\n"
"v_add_u32 v2, v2, v11\n"
"v_cmp_le_u32 vcc, 14, v1\n"
"s_nop 1\n"
"v_cndmask_b32 v11, 0, v3, vcc\n"
"v_add_u32 v2, v2, v11\n"
"v_mov_b32 v3, 0x40000000\n"
"v_cmp_le_u32 vcc, 21, v1\n"
"s_nop 1\n"
"v_cndmask_b32 v11, 0, v3, vcc\n"
"v_add_u32 v40, v2, v11\n"
"s_add_u32 s76, s73, 4\n"
"s_lshl_b32 s76, s76, 2\n"
"v_lshrrev_b32 v1, 4, v0\n"
"v_add_u32 v1, s76, v1\n"
"v_mul_u32_u24 v2, 43, v1\n"
"v_lshrrev_b32 v2, 8, v2\n"
"v_mul_u32_u24 v3, 6, v2\n"
"v_sub_u32 v1, v1, v3\n"
"v_lshrrev_b32 v3, 1, v2\n"
"v_and_b32 v3, 1, v3\n"
"v_lshlrev_b32 v3, 1, v3\n"
"v_and_b32 v11, 3, v0\n"
"v_xor_b32 v3, v3, v11\n"
"v_lshl_add_u32 v1, v1, 2, v3\n"
"v_lshrrev_b32 v3, 2, v0\n"
"v_and_b32 v3, 3, v3\n"
"v_lshl_add_u32 v2, v2, 2, v3\n"
"v_mul_u32_u24 v2, 2688, v2\n"
"v_lshl_add_u32 v2, v1, 4, v2\n"
"s_mul_i32 s76, s71, 112\n"
"v_add_u32 v2, s76, v2\n"
"v_mov_b32 v3, 784\n"
"v_cmp_le_u32 vcc, 7, v1\n"
"s_nop 1\n"
"v_cndmask_b32 v11, 0, v3, vcc\n"
"v_add_u32 v2, v2, v11\n"
"v_cmp_le_u32 vcc, 14, v1\n"
"s_nop 1\n"
"v_cndmask_b32 v11, 0, v3, vcc\n"
"v_add_u32 v2, v2, v11\n"
"v_mov_b32 v3, 0x40000000\n"
"v_cmp_le_u32 vcc, 21, v1\n"
"s_nop 1\n"
"v_cndmask_b32 v11, 0, v3, vcc\n"
"v_add_u32 v41, v2, v11\n"
"s_add_u32 s76, s73, 8\n"
"s_lshl_b32 s76, s76, 2\n"
"v_lshrrev_b32 v1, 4, v0\n"
"v_add_u32 v1, s76, v1\n"
"v_mul_u32_u24 v2, 43, v1\n"
"v_lshrrev_b32 v2, 8, v2\n"
"v_mul_u32_u24 v3, 6, v2\n"
"v_sub_u32 v1, v1, v3\n"
"v_lshrrev_b32 v3, 1, v2\n"
"v_and_b32 v3, 1, v3\n"
"v_lshlrev_b32 v3, 1, v3\n"
"v_and_b32 v11, 3, v0\n"
"v_xor_b32 v3, v3, v11\n"
"v_lshl_add_u32 v1, v1, 2, v3\n"
"v_lshrrev_b32 v3, 2, v0\n"
"v_and_b32 v3, 3, v3\n"
"v_lshl_add_u32 v2, v2, 2, v3\n"
"v_mul_u32_u24 v2, 2688, v2\n"
"v_lshl_add_u32 v2, v1, 4, v2\n"
"s_mul_i32 s76, s71, 112\n"
"v_add_u32 v2, s76, v2\n"
"v_mov_b32 v3, 784\n"
"v_cmp_le_u32 vcc, 7, v1\n"
"s_nop 1\n"
"v_cndmask_b32 v11, 0, v3, vcc\n"
"v_add_u32 v2, v2, v11\n"
"v_cmp_le_u32 vcc, 14, v1\n"
"s_nop 1\n"
"v_cndmask_b32 v11, 0, v3, vcc\n"
"v_add_u32 v2, v2, v11\n"
"v_mov_b32 v3, 0x40000000\n"
"v_cmp_le_u32 vcc, 21, v1\n"
"s_nop 1\n"
"v_cndmask_b32 v11, 0, v3, vcc\n"
"v_add_u32 v42, v2, v11\n"
"s_add_u32 s76, s73, 12\n"
"s_lshl_b32 s76, s76, 2\n"
"v_lshrrev_b32 v1, 4, v0\n"
"v_add_u32 v1, s76, v1\n"
"v_mul_u32_u24 v2, 43, v1\n"
"v_lshrrev_b32 v2, 8, v2\n"
"v_mul_u32_u24 v3, 6, v2\n"
"v_sub_u32 v1, v1, v3\n"
"v_lshrrev_b32 v3, 1, v2\n"
"v_and_b32 v3, 1, v3\n"
"v_lshlrev_b32 v3, 1, v3\n"
"v_and_b32 v11, 3, v0\n"
"v_xor_b32 v3, v3, v11\n"
"v_lshl_add_u32 v1, v1, 2, v3\n"
"v_lshrrev_b32 v3, 2, v0\n"
"v_and_b32 v3, 3, v3\n"
"v_lshl_add_u32 v2, v2, 2, v3\n"
"v_mul_u32_u24 v2, 2688, v2\n"
"v_lshl_add_u32 v2, v1, 4, v2\n"
"s_mul_i32 s76, s71, 112\n"
"v_add_u32 v2, s76, v2\n"
"v_mov_b32 v3, 784\n"
"v_cmp_le_u32 vcc, 7, v1\n"
"s_nop 1\n"
"v_cndmask_b32 v11, 0, v3, vcc\n"
"v_add_u32 v2, v2, v11\n"
"v_cmp_le_u32 vcc, 14, v1\n"
"s_nop 1\n"
"v_cndmask_b32 v11, 0, v3, vcc\n"
"v_add_u32 v2, v2, v11\n"
"v_mov_b32 v3, 0x40000000\n"
"v_cmp_le_u32 vcc, 21, v1\n"
"s_nop 1\n"
"v_cndmask_b32 v11, 0, v3, vcc\n"
"v_add_u32 v43, v2, v11\n"
"s_add_u32 s76, s73, 16\n"
"s_lshl_b32 s76, s76, 2\n"
"v_lshrrev_b32 v1, 4, v0\n"
"v_add_u32 v1, s76, v1\n"
"v_mul_u32_u24 v2, 43, v1\n"
"v_lshrrev_b32 v2, 8, v2\n"
"v_mul_u32_u24 v3, 6, v2\n"
"v_sub_u32 v1, v1, v3\n"
"v_lshrrev_b32 v3, 1, v2\n"
"v_and_b32 v3, 1, v3\n"
"v_lshlrev_b32 v3, 1, v3\n"
"v_and_b32 v11, 3, v0\n"
"v_xor_b32 v3, v3, v11\n"
"v_lshl_add_u32 v1, v1, 2, v3\n"
"v_lshrrev_b32 v3, 2, v0\n"
"v_and_b32 v3, 3, v3\n"
"v_lshl_add_u32 v2, v2, 2, v3\n"
"v_mul_u32_u24 v2, 2688, v2\n"
"v_lshl_add_u32 v2, v1, 4, v2\n"
"s_mul_i32 s76, s71, 112\n"
"v_add_u32 v2, s76, v2\n"
"v_mov_b32 v3, 784\n"
"v_cmp_le_u32 vcc, 7, v1\n"
"s_nop 1\n"
"v_cndmask_b32 v11, 0, v3, vcc\n"
"v_add_u32 v2, v2, v11\n"
"v_cmp_le_u32 vcc, 14, v1\n"
"s_nop 1\n"
"v_cndmask_b32 v11, 0, v3, vcc\n"
"v_add_u32 v2, v2, v11\n"
"v_mov_b32 v3, 0x40000000\n"
"v_cmp_le_u32 vcc, 21, v1\n"
"s_nop 1\n"
"v_cndmask_b32 v11, 0, v3, vcc\n"
"v_add_u32 v44, v2, v11\n"
"s_add_u32 s76, s73, 20\n"
"s_lshl_b32 s76, s76, 2\n"
"v_lshrrev_b32 v1, 4, v0\n"
"v_add_u32 v1, s76, v1\n"
"v_mul_u32_u24 v2, 43, v1\n"
"v_lshrrev_b32 v2, 8, v2\n"
"v_mul_u32_u24 v3, 6, v2\n"
"v_sub_u32 v1, v1, v3\n"
"v_lshrrev_b32 v3, 1, v2\n"
"v_and_b32 v3, 1, v3\n"
"v_lshlrev_b32 v3, 1, v3\n"
"v_and_b32 v11, 3, v0\n"
"v_xor_b32 v3, v3, v11\n"
"v_lshl_add_u32 v1, v1, 2, v3\n"
"v_lshrrev_b32 v3, 2, v0\n"
"v_and_b32 v3, 3, v3\n"
"v_lshl_add_u32 v2, v2, 2, v3\n"
"v_mul_u32_u24 v2, 2688, v2\n"
"v_lshl_add_u32 v2, v1, 4, v2\n"
"s_mul_i32 s76, s71, 112\n"
"v_add_u32 v2, s76, v2\n"
"v_mov_b32 v3, 784\n"
"v_cmp_le_u32 vcc, 7, v1\n"
"s_nop 1\n"
"v_cndmask_b32 v11, 0, v3, vcc\n"
"v_add_u32 v2, v2, v11\n"
"v_cmp_le_u32 vcc, 14, v1\n"
"s_nop 1\n"
"v_cndmask_b32 v11, 0, v3, vcc\n"
"v_add_u32 v2, v2, v11\n"
"v_mov_b32 v3, 0x40000000\n"
"v_cmp_le_u32 vcc, 21, v1\n"
"s_nop 1\n"
"v_cndmask_b32 v11, 0, v3, vcc\n"
"v_add_u32 v45, v2, v11\n"
"v_and_b32 v1, 15, v0\n"
"v_lshrrev_b32 v2, 4, v0\n"
"v_lshrrev_b32 v3, 1, v1\n"
"v_xor_b32 v3, v3, v2\n"
"v_lshlrev_b32 v3, 4, v3\n"
"v_lshl_add_u32 v4, v1, 7, v3\n"
"v_mul_u32_u24 v3, 3072, v2\n"
"v_and_b32 v1, 1, v2\n"
"v_lshl_add_u32 v3, v1, 5, v3\n"
"v_lshrrev_b32 v1, 2, v0\n"
"v_and_b32 v1, 3, v1\n"
"v_lshl_add_u32 v3, v1, 6, v3\n"
"v_and_b32 v1, 3, v0\n"
"v_lshl_add_u32 v3, v1, 3, v3\n"
"v_add_u32 v3, 20480, v3\n"
"s_mul_i32 s76, s73, 3\n"
"s_add_u32 s76, s76, 0\n"
"s_lshr_b32 s77, s76, 1\n"
"s_lshl_b32 s77, s77, 8\n"
"s_and_b32 s76, s76, 1\n"
"s_lshl_b32 s76, s76, 5\n"
"v_xor_b32 v5, s76, v3\n"
"v_add_u32 v5, s77, v5\n"
"s_mul_i32 s76, s73, 3\n"
"s_add_u32 s76, s76, 1\n"
"s_lshr_b32 s77, s76, 1\n"
"s_lshl_b32 s77, s77, 8\n"
"s_and_b32 s76, s76, 1\n"
"s_lshl_b32 s76, s76, 5\n"
"v_xor_b32 v6, s76, v3\n"
"v_add_u32 v6, s77, v6\n"
"s_mul_i32 s76, s73, 3\n"
"s_add_u32 s76, s76, 2\n"
"s_lshr_b32 s77, s76, 1\n"
"s_lshl_b32 s77, s77, 8\n"
"s_and_b32 s76, s76, 1\n"
"s_lshl_b32 s76, s76, 5\n"
"v_xor_b32 v7, s76, v3\n"
"v_add_u32 v7, s77, v7\n"
"s_nop 15\n"
"s_mov_b32 s83, s53\n"
"v_accvgpr_read_b32 v192, a192\n"
"v_accvgpr_read_b32 v193, a193\n"
"v_accvgpr_read_b32 v194, a194\n"
"v_accvgpr_read_b32 v195, a195\n"
"v_accvgpr_read_b32 v196, a224\n"
"v_accvgpr_read_b32 v197, a225\n"
"v_accvgpr_read_b32 v198, a226\n"
"v_accvgpr_read_b32 v199, a227\n"
"v_cvt_pk_bf16_f32 v128, v192, v193\n"
"v_cvt_pk_bf16_f32 v129, v194, v195\n"
"v_cvt_pk_bf16_f32 v130, v196, v197\n"
"v_cvt_pk_bf16_f32 v131, v198, v199\n"
"s_add_u32 m0, s52, 0\n"
"s_nop 0\n"
"buffer_load_dwordx4 v32, s[56:59], s48 offen lds\n"
"v_accvgpr_read_b32 v208, a196\n"
"v_accvgpr_read_b32 v209, a197\n"
"v_accvgpr_read_b32 v210, a198\n"
"v_accvgpr_read_b32 v211, a199\n"
"v_accvgpr_read_b32 v212, a228\n"
"v_accvgpr_read_b32 v213, a229\n"
"v_accvgpr_read_b32 v214, a230\n"
"v_accvgpr_read_b32 v215, a231\n"
"v_cvt_pk_bf16_f32 v132, v208, v209\n"
"v_cvt_pk_bf16_f32 v133, v210, v211\n"
"v_cvt_pk_bf16_f32 v134, v212, v213\n"
"v_cvt_pk_bf16_f32 v135, v214, v215\n"
"s_add_u32 m0, s52, 4096\n"
"s_nop 0\n"
"buffer_load_dwordx4 v33, s[56:59], s48 offen lds\n"
"v_accvgpr_read_b32 v224, a200\n"
"v_accvgpr_read_b32 v225, a201\n"
"v_accvgpr_read_b32 v226, a202\n"
"v_accvgpr_read_b32 v227, a203\n"
"v_accvgpr_read_b32 v228, a232\n"
"v_accvgpr_read_b32 v229, a233\n"
"v_accvgpr_read_b32 v230, a234\n"
"v_accvgpr_read_b32 v231, a235\n"
"v_cvt_pk_bf16_f32 v136, v224, v225\n"
"v_cvt_pk_bf16_f32 v137, v226, v227\n"
"v_cvt_pk_bf16_f32 v138, v228, v229\n"
"v_cvt_pk_bf16_f32 v139, v230, v231\n"
"s_add_u32 m0, s52, 8192\n"
"s_nop 0\n"
"buffer_load_dwordx4 v34, s[56:59], s48 offen lds\n"
"v_accvgpr_read_b32 v240, a204\n"
"v_accvgpr_read_b32 v241, a205\n"
"v_accvgpr_read_b32 v242, a206\n"
"v_accvgpr_read_b32 v243, a207\n"
"v_accvgpr_read_b32 v244, a236\n"
"v_accvgpr_read_b32 v245, a237\n"
"v_accvgpr_read_b32 v246, a238\n"
"v_accvgpr_read_b32 v247, a239\n"
"v_cvt_pk_bf16_f32 v140, v240, v241\n"
"v_cvt_pk_bf16_f32 v141, v242, v243\n"
"v_cvt_pk_bf16_f32 v142, v244, v245\n"
"v_cvt_pk_bf16_f32 v143, v246, v247\n"
"s_add_u32 m0, s52, 12288\n"
"s_nop 0\n"
"buffer_load_dwordx4 v35, s[56:59], s48 offen lds\n"
"v_accvgpr_read_b32 v192, a208\n"
"v_accvgpr_read_b32 v193, a209\n"
"v_accvgpr_read_b32 v194, a210\n"
"v_accvgpr_read_b32 v195, a211\n"
"v_accvgpr_read_b32 v196, a240\n"
"v_accvgpr_read_b32 v197, a241\n"
"v_accvgpr_read_b32 v198, a242\n"
"v_accvgpr_read_b32 v199, a243\n"
"v_cvt_pk_bf16_f32 v144, v192, v193\n"
"v_cvt_pk_bf16_f32 v145, v194, v195\n"
"v_cvt_pk_bf16_f32 v146, v196, v197\n"
"v_cvt_pk_bf16_f32 v147, v198, v199\n"
"s_add_u32 m0, s52, 16384\n"
"s_nop 0\n"
"buffer_load_dwordx4 v36, s[56:59], s48 offen lds\n"
"v_accvgpr_read_b32 v208, a212\n"
"v_accvgpr_read_b32 v209, a213\n"
"v_accvgpr_read_b32 v210, a214\n"
"v_accvgpr_read_b32 v211, a215\n"
"v_accvgpr_read_b32 v212, a244\n"
"v_accvgpr_read_b32 v213, a245\n"
"v_accvgpr_read_b32 v214, a246\n"
"v_accvgpr_read_b32 v215, a247\n"
"v_cvt_pk_bf16_f32 v148, v208, v209\n"
"v_cvt_pk_bf16_f32 v149, v210, v211\n"
"v_cvt_pk_bf16_f32 v150, v212, v213\n"
"v_cvt_pk_bf16_f32 v151, v214, v215\n"
"s_add_u32 m0, s52, 20480\n"
"s_nop 0\n"
"buffer_load_dwordx4 v40, s[60:63], s49 offen lds\n"
"v_accvgpr_read_b32 v224, a216\n"
"v_accvgpr_read_b32 v225, a217\n"
"v_accvgpr_read_b32 v226, a218\n"
"v_accvgpr_read_b32 v227, a219\n"
"v_accvgpr_read_b32 v228, a248\n"
"v_accvgpr_read_b32 v229, a249\n"
"v_accvgpr_read_b32 v230, a250\n"
"v_accvgpr_read_b32 v231, a251\n"
"v_cvt_pk_bf16_f32 v152, v224, v225\n"
"v_cvt_pk_bf16_f32 v153, v226, v227\n"
"v_cvt_pk_bf16_f32 v154, v228, v229\n"
"v_cvt_pk_bf16_f32 v155, v230, v231\n"
"s_add_u32 m0, s52, 24576\n"
"s_nop 0\n"
"buffer_load_dwordx4 v41, s[60:63], s49 offen lds\n"
"v_accvgpr_read_b32 v240, a220\n"
"v_accvgpr_read_b32 v241, a221\n"
"v_accvgpr_read_b32 v242, a222\n"
"v_accvgpr_read_b32 v243, a223\n"
"v_accvgpr_read_b32 v244, a252\n"
"v_accvgpr_read_b32 v245, a253\n"
"v_accvgpr_read_b32 v246, a254\n"
"v_accvgpr_read_b32 v247, a255\n"
"v_cvt_pk_bf16_f32 v156, v240, v241\n"
"v_cvt_pk_bf16_f32 v157, v242, v243\n"
"v_cvt_pk_bf16_f32 v158, v244, v245\n"
"v_cvt_pk_bf16_f32 v159, v246, v247\n"
"s_add_u32 m0, s52, 28672\n"
"s_nop 0\n"
"buffer_load_dwordx4 v42, s[60:63], s49 offen lds\n"
"v_accvgpr_write_b32 a224, v128\n"
"v_accvgpr_write_b32 a225, v129\n"
"v_accvgpr_write_b32 a226, v130\n"
"v_accvgpr_write_b32 a227, v131\n"
"v_accvgpr_write_b32 a228, v132\n"
"v_accvgpr_write_b32 a229, v133\n"
"v_accvgpr_write_b32 a230, v134\n"
"v_accvgpr_write_b32 a231, v135\n"
"v_accvgpr_write_b32 a232, v136\n"
"v_accvgpr_write_b32 a233, v137\n"
"v_accvgpr_write_b32 a234, v138\n"
"v_accvgpr_write_b32 a235, v139\n"
"v_accvgpr_write_b32 a236, v140\n"
"v_accvgpr_write_b32 a237, v141\n"
"v_accvgpr_write_b32 a238, v142\n"
"v_accvgpr_write_b32 a239, v143\n"
"v_accvgpr_write_b32 a240, v144\n"
"v_accvgpr_write_b32 a241, v145\n"
"v_accvgpr_write_b32 a242, v146\n"
"v_accvgpr_write_b32 a243, v147\n"
"v_accvgpr_write_b32 a244, v148\n"
"v_accvgpr_write_b32 a245, v149\n"
"v_accvgpr_write_b32 a246, v150\n"
"v_accvgpr_write_b32 a247, v151\n"
"v_accvgpr_write_b32 a248, v152\n"
"v_accvgpr_write_b32 a249, v153\n"
"v_accvgpr_write_b32 a250, v154\n"
"v_accvgpr_write_b32 a251, v155\n"
"v_accvgpr_write_b32 a252, v156\n"
"v_accvgpr_write_b32 a253, v157\n"
"v_accvgpr_write_b32 a254, v158\n"
"v_accvgpr_write_b32 a255, v159\n"
"v_accvgpr_read_b32 v192, a0\n"
"v_accvgpr_read_b32 v193, a1\n"
"v_accvgpr_read_b32 v194, a2\n"
"v_accvgpr_read_b32 v195, a3\n"
"v_accvgpr_read_b32 v196, a32\n"
"v_accvgpr_read_b32 v197, a33\n"
"v_accvgpr_read_b32 v198, a34\n"
"v_accvgpr_read_b32 v199, a35\n"
"v_cvt_pk_bf16_f32 v64, v192, v193\n"
"v_cvt_pk_bf16_f32 v65, v194, v195\n"
"v_cvt_pk_bf16_f32 v66, v196, v197\n"
"v_cvt_pk_bf16_f32 v67, v198, v199\n"
"s_add_u32 m0, s52, 32768\n"
"s_nop 0\n"
"buffer_load_dwordx4 v43, s[60:63], s49 offen lds\n"
"v_accvgpr_read_b32 v208, a4\n"
"v_accvgpr_read_b32 v209, a5\n"
"v_accvgpr_read_b32 v210, a6\n"
"v_accvgpr_read_b32 v211, a7\n"
"v_accvgpr_read_b32 v212, a36\n"
"v_accvgpr_read_b32 v213, a37\n"
"v_accvgpr_read_b32 v214, a38\n"
"v_accvgpr_read_b32 v215, a39\n"
"v_cvt_pk_bf16_f32 v68, v208, v209\n"
"v_cvt_pk_bf16_f32 v69, v210, v211\n"
"v_cvt_pk_bf16_f32 v70, v212, v213\n"
"v_cvt_pk_bf16_f32 v71, v214, v215\n"
"s_add_u32 m0, s52, 36864\n"
"s_nop 0\n"
"buffer_load_dwordx4 v44, s[60:63], s49 offen lds\n"
"v_accvgpr_read_b32 v224, a8\n"
"v_accvgpr_read_b32 v225, a9\n"
"v_accvgpr_read_b32 v226, a10\n"
"v_accvgpr_read_b32 v227, a11\n"
"v_accvgpr_read_b32 v228, a40\n"
"v_accvgpr_read_b32 v229, a41\n"
"v_accvgpr_read_b32 v230, a42\n"
"v_accvgpr_read_b32 v231, a43\n"
"v_cvt_pk_bf16_f32 v72, v224, v225\n"
"v_cvt_pk_bf16_f32 v73, v226, v227\n"
"v_cvt_pk_bf16_f32 v74, v228, v229\n"
"v_cvt_pk_bf16_f32 v75, v230, v231\n"
"s_add_u32 m0, s52, 40960\n"
"s_nop 0\n"
"buffer_load_dwordx4 v45, s[60:63], s49 offen lds\n"
"s_add_u32 s48, s48, 128\n"
"s_add_u32 s49, s49, 172032\n"
"v_accvgpr_read_b32 v240, a12\n"
"v_accvgpr_read_b32 v241, a13\n"
"v_accvgpr_read_b32 v242, a14\n"
"v_accvgpr_read_b32 v243, a15\n"
"v_accvgpr_read_b32 v244, a44\n"
"v_accvgpr_read_b32 v245, a45\n"
"v_accvgpr_read_b32 v246, a46\n"
"v_accvgpr_read_b32 v247, a47\n"
"v_cvt_pk_bf16_f32 v76, v240, v241\n"
"v_cvt_pk_bf16_f32 v77, v242, v243\n"
"v_cvt_pk_bf16_f32 v78, v244, v245\n"
"v_cvt_pk_bf16_f32 v79, v246, v247\n"
"s_add_u32 m0, s52, 45056\n"
"s_nop 0\n"
"buffer_load_dwordx4 v32, s[56:59], s48 offen lds\n"
"v_accvgpr_read_b32 v192, a16\n"
"v_accvgpr_read_b32 v193, a17\n"
"v_accvgpr_read_b32 v194, a18\n"
"v_accvgpr_read_b32 v195, a19\n"
"v_accvgpr_read_b32 v196, a48\n"
"v_accvgpr_read_b32 v197, a49\n"
"v_accvgpr_read_b32 v198, a50\n"
"v_accvgpr_read_b32 v199, a51\n"
"v_cvt_pk_bf16_f32 v80, v192, v193\n"
"v_cvt_pk_bf16_f32 v81, v194, v195\n"
"v_cvt_pk_bf16_f32 v82, v196, v197\n"
"v_cvt_pk_bf16_f32 v83, v198, v199\n"
"s_add_u32 m0, s52, 49152\n"
"s_nop 0\n"
"buffer_load_dwordx4 v33, s[56:59], s48 offen lds\n"
"v_accvgpr_read_b32 v208, a20\n"
"v_accvgpr_read_b32 v209, a21\n"
"v_accvgpr_read_b32 v210, a22\n"
"v_accvgpr_read_b32 v211, a23\n"
"v_accvgpr_read_b32 v212, a52\n"
"v_accvgpr_read_b32 v213, a53\n"
"v_accvgpr_read_b32 v214, a54\n"
"v_accvgpr_read_b32 v215, a55\n"
"v_cvt_pk_bf16_f32 v84, v208, v209\n"
"v_cvt_pk_bf16_f32 v85, v210, v211\n"
"v_cvt_pk_bf16_f32 v86, v212, v213\n"
"v_cvt_pk_bf16_f32 v87, v214, v215\n"
"s_add_u32 m0, s52, 53248\n"
"s_nop 0\n"
"buffer_load_dwordx4 v34, s[56:59], s48 offen lds\n"
"v_accvgpr_read_b32 v224, a24\n"
"v_accvgpr_read_b32 v225, a25\n"
"v_accvgpr_read_b32 v226, a26\n"
"v_accvgpr_read_b32 v227, a27\n"
"v_accvgpr_read_b32 v228, a56\n"
"v_accvgpr_read_b32 v229, a57\n"
"v_accvgpr_read_b32 v230, a58\n"
"v_accvgpr_read_b32 v231, a59\n"
"v_cvt_pk_bf16_f32 v88, v224, v225\n"
"v_cvt_pk_bf16_f32 v89, v226, v227\n"
"v_cvt_pk_bf16_f32 v90, v228, v229\n"
"v_cvt_pk_bf16_f32 v91, v230, v231\n"
"s_add_u32 m0, s52, 57344\n"
"s_nop 0\n"
"buffer_load_dwordx4 v35, s[56:59], s48 offen lds\n"
"v_accvgpr_read_b32 v240, a28\n"
"v_accvgpr_read_b32 v241, a29\n"
"v_accvgpr_read_b32 v242, a30\n"
"v_accvgpr_read_b32 v243, a31\n"
"v_accvgpr_read_b32 v244, a60\n"
"v_accvgpr_read_b32 v245, a61\n"
"v_accvgpr_read_b32 v246, a62\n"
"v_accvgpr_read_b32 v247, a63\n"
"v_cvt_pk_bf16_f32 v92, v240, v241\n"
"v_cvt_pk_bf16_f32 v93, v242, v243\n"
"v_cvt_pk_bf16_f32 v94, v244, v245\n"
"v_cvt_pk_bf16_f32 v95, v246, v247\n"
"s_add_u32 m0, s52, 61440\n"
"s_nop 0\n"
"buffer_load_dwordx4 v36, s[56:59], s48 offen lds\n"
"v_accvgpr_read_b32 v192, a64\n"
"v_accvgpr_read_b32 v193, a65\n"
"v_accvgpr_read_b32 v194, a66\n"
"v_accvgpr_read_b32 v195, a67\n"
"v_accvgpr_read_b32 v196, a96\n"
"v_accvgpr_read_b32 v197, a97\n"
"v_accvgpr_read_b32 v198, a98\n"
"v_accvgpr_read_b32 v199, a99\n"
"v_cvt_pk_bf16_f32 v96, v192, v193\n"
"v_cvt_pk_bf16_f32 v97, v194, v195\n"
"v_cvt_pk_bf16_f32 v98, v196, v197\n"
"v_cvt_pk_bf16_f32 v99, v198, v199\n"
"s_add_u32 m0, s52, 65536\n"
"s_nop 0\n"
"buffer_load_dwordx4 v40, s[60:63], s49 offen lds\n"
"v_accvgpr_read_b32 v208, a68\n"
"v_accvgpr_read_b32 v209, a69\n"
"v_accvgpr_read_b32 v210, a70\n"
"v_accvgpr_read_b32 v211, a71\n"
"v_accvgpr_read_b32 v212, a100\n"
"v_accvgpr_read_b32 v213, a101\n"
"v_accvgpr_read_b32 v214, a102\n"
"v_accvgpr_read_b32 v215, a103\n"
"v_cvt_pk_bf16_f32 v100, v208, v209\n"
"v_cvt_pk_bf16_f32 v101, v210, v211\n"
"v_cvt_pk_bf16_f32 v102, v212, v213\n"
"v_cvt_pk_bf16_f32 v103, v214, v215\n"
"s_add_u32 m0, s52, 69632\n"
"s_nop 0\n"
"buffer_load_dwordx4 v41, s[60:63], s49 offen lds\n"
"v_accvgpr_read_b32 v224, a72\n"
"v_accvgpr_read_b32 v225, a73\n"
"v_accvgpr_read_b32 v226, a74\n"
"v_accvgpr_read_b32 v227, a75\n"
"v_accvgpr_read_b32 v228, a104\n"
"v_accvgpr_read_b32 v229, a105\n"
"v_accvgpr_read_b32 v230, a106\n"
"v_accvgpr_read_b32 v231, a107\n"
"v_cvt_pk_bf16_f32 v104, v224, v225\n"
"v_cvt_pk_bf16_f32 v105, v226, v227\n"
"v_cvt_pk_bf16_f32 v106, v228, v229\n"
"v_cvt_pk_bf16_f32 v107, v230, v231\n"
"s_add_u32 m0, s52, 73728\n"
"s_nop 0\n"
"buffer_load_dwordx4 v42, s[60:63], s49 offen lds\n"
"v_accvgpr_read_b32 v240, a76\n"
"v_accvgpr_read_b32 v241, a77\n"
"v_accvgpr_read_b32 v242, a78\n"
"v_accvgpr_read_b32 v243, a79\n"
"v_accvgpr_read_b32 v244, a108\n"
"v_accvgpr_read_b32 v245, a109\n"
"v_accvgpr_read_b32 v246, a110\n"
"v_accvgpr_read_b32 v247, a111\n"
"v_cvt_pk_bf16_f32 v108, v240, v241\n"
"v_cvt_pk_bf16_f32 v109, v242, v243\n"
"v_cvt_pk_bf16_f32 v110, v244, v245\n"
"v_cvt_pk_bf16_f32 v111, v246, v247\n"
"s_add_u32 m0, s52, 77824\n"
"s_nop 0\n"
"buffer_load_dwordx4 v43, s[60:63], s49 offen lds\n"
"v_accvgpr_read_b32 v192, a80\n"
"v_accvgpr_read_b32 v193, a81\n"
"v_accvgpr_read_b32 v194, a82\n"
"v_accvgpr_read_b32 v195, a83\n"
"v_accvgpr_read_b32 v196, a112\n"
"v_accvgpr_read_b32 v197, a113\n"
"v_accvgpr_read_b32 v198, a114\n"
"v_accvgpr_read_b32 v199, a115\n"
"v_cvt_pk_bf16_f32 v112, v192, v193\n"
"v_cvt_pk_bf16_f32 v113, v194, v195\n"
"v_cvt_pk_bf16_f32 v114, v196, v197\n"
"v_cvt_pk_bf16_f32 v115, v198, v199\n"
"s_add_u32 m0, s52, 81920\n"
"s_nop 0\n"
"buffer_load_dwordx4 v44, s[60:63], s49 offen lds\n"
"v_accvgpr_read_b32 v208, a84\n"
"v_accvgpr_read_b32 v209, a85\n"
"v_accvgpr_read_b32 v210, a86\n"
"v_accvgpr_read_b32 v211, a87\n"
"v_accvgpr_read_b32 v212, a116\n"
"v_accvgpr_read_b32 v213, a117\n"
"v_accvgpr_read_b32 v214, a118\n"
"v_accvgpr_read_b32 v215, a119\n"
"v_cvt_pk_bf16_f32 v116, v208, v209\n"
"v_cvt_pk_bf16_f32 v117, v210, v211\n"
"v_cvt_pk_bf16_f32 v118, v212, v213\n"
"v_cvt_pk_bf16_f32 v119, v214, v215\n"
"s_add_u32 m0, s52, 86016\n"
"s_nop 0\n"
"buffer_load_dwordx4 v45, s[60:63], s49 offen lds\n"
"s_add_u32 s48, s48, 128\n"
"s_add_u32 s49, s49, 172032\n"
"v_accvgpr_read_b32 v224, a88\n"
"v_accvgpr_read_b32 v225, a89\n"
"v_accvgpr_read_b32 v226, a90\n"
"v_accvgpr_read_b32 v227, a91\n"
"v_accvgpr_read_b32 v228, a120\n"
"v_accvgpr_read_b32 v229, a121\n"
"v_accvgpr_read_b32 v230, a122\n"
"v_accvgpr_read_b32 v231, a123\n"
"v_cvt_pk_bf16_f32 v120, v224, v225\n"
"v_cvt_pk_bf16_f32 v121, v226, v227\n"
"v_cvt_pk_bf16_f32 v122, v228, v229\n"
"v_cvt_pk_bf16_f32 v123, v230, v231\n"
"s_add_u32 m0, s52, 90112\n"
"s_nop 0\n"
"buffer_load_dwordx4 v32, s[56:59], s48 offen lds\n"
"v_accvgpr_read_b32 v240, a92\n"
"v_accvgpr_read_b32 v241, a93\n"
"v_accvgpr_read_b32 v242, a94\n"
"v_accvgpr_read_b32 v243, a95\n"
"v_accvgpr_read_b32 v244, a124\n"
"v_accvgpr_read_b32 v245, a125\n"
"v_accvgpr_read_b32 v246, a126\n"
"v_accvgpr_read_b32 v247, a127\n"
"v_cvt_pk_bf16_f32 v124, v240, v241\n"
"v_cvt_pk_bf16_f32 v125, v242, v243\n"
"v_cvt_pk_bf16_f32 v126, v244, v245\n"
"v_cvt_pk_bf16_f32 v127, v246, v247\n"
"s_add_u32 m0, s52, 94208\n"
"s_nop 0\n"
"buffer_load_dwordx4 v33, s[56:59], s48 offen lds\n"
"v_accvgpr_read_b32 v192, a128\n"
"v_accvgpr_read_b32 v193, a129\n"
"v_accvgpr_read_b32 v194, a130\n"
"v_accvgpr_read_b32 v195, a131\n"
"v_accvgpr_read_b32 v196, a160\n"
"v_accvgpr_read_b32 v197, a161\n"
"v_accvgpr_read_b32 v198, a162\n"
"v_accvgpr_read_b32 v199, a163\n"
"v_cvt_pk_bf16_f32 v200, v192, v193\n"
"v_cvt_pk_bf16_f32 v201, v194, v195\n"
"v_cvt_pk_bf16_f32 v202, v196, v197\n"
"v_cvt_pk_bf16_f32 v203, v198, v199\n"
"s_add_u32 m0, s52, 98304\n"
"s_nop 0\n"
"buffer_load_dwordx4 v34, s[56:59], s48 offen lds\n"
"v_accvgpr_write_b32 a192, v200\n"
"v_accvgpr_write_b32 a193, v201\n"
"v_accvgpr_write_b32 a194, v202\n"
"v_accvgpr_write_b32 a195, v203\n"
"v_accvgpr_read_b32 v208, a132\n"
"v_accvgpr_read_b32 v209, a133\n"
"v_accvgpr_read_b32 v210, a134\n"
"v_accvgpr_read_b32 v211, a135\n"
"v_accvgpr_read_b32 v212, a164\n"
"v_accvgpr_read_b32 v213, a165\n"
"v_accvgpr_read_b32 v214, a166\n"
"v_accvgpr_read_b32 v215, a167\n"
"v_cvt_pk_bf16_f32 v216, v208, v209\n"
"v_cvt_pk_bf16_f32 v217, v210, v211\n"
"v_cvt_pk_bf16_f32 v218, v212, v213\n"
"v_cvt_pk_bf16_f32 v219, v214, v215\n"
"s_add_u32 m0, s52, 102400\n"
"s_nop 0\n"
"buffer_load_dwordx4 v35, s[56:59], s48 offen lds\n"
"v_accvgpr_write_b32 a196, v216\n"
"v_accvgpr_write_b32 a197, v217\n"
"v_accvgpr_write_b32 a198, v218\n"
"v_accvgpr_write_b32 a199, v219\n"
"v_accvgpr_read_b32 v224, a136\n"
"v_accvgpr_read_b32 v225, a137\n"
"v_accvgpr_read_b32 v226, a138\n"
"v_accvgpr_read_b32 v227, a139\n"
"v_accvgpr_read_b32 v228, a168\n"
"v_accvgpr_read_b32 v229, a169\n"
"v_accvgpr_read_b32 v230, a170\n"
"v_accvgpr_read_b32 v231, a171\n"
"v_cvt_pk_bf16_f32 v232, v224, v225\n"
"v_cvt_pk_bf16_f32 v233, v226, v227\n"
"v_cvt_pk_bf16_f32 v234, v228, v229\n"
"v_cvt_pk_bf16_f32 v235, v230, v231\n"
"s_add_u32 m0, s52, 106496\n"
"s_nop 0\n"
"buffer_load_dwordx4 v36, s[56:59], s48 offen lds\n"
"v_accvgpr_write_b32 a200, v232\n"
"v_accvgpr_write_b32 a201, v233\n"
"v_accvgpr_write_b32 a202, v234\n"
"v_accvgpr_write_b32 a203, v235\n"
"v_accvgpr_read_b32 v240, a140\n"
"v_accvgpr_read_b32 v241, a141\n"
"v_accvgpr_read_b32 v242, a142\n"
"v_accvgpr_read_b32 v243, a143\n"
"v_accvgpr_read_b32 v244, a172\n"
"v_accvgpr_read_b32 v245, a173\n"
"v_accvgpr_read_b32 v246, a174\n"
"v_accvgpr_read_b32 v247, a175\n"
"v_cvt_pk_bf16_f32 v248, v240, v241\n"
"v_cvt_pk_bf16_f32 v249, v242, v243\n"
"v_cvt_pk_bf16_f32 v250, v244, v245\n"
"v_cvt_pk_bf16_f32 v251, v246, v247\n"
"s_add_u32 m0, s52, 110592\n"
"s_nop 0\n"
"buffer_load_dwordx4 v40, s[60:63], s49 offen lds\n"
"v_accvgpr_write_b32 a204, v248\n"
"v_accvgpr_write_b32 a205, v249\n"
"v_accvgpr_write_b32 a206, v250\n"
"v_accvgpr_write_b32 a207, v251\n"
"v_accvgpr_read_b32 v192, a144\n"
"v_accvgpr_read_b32 v193, a145\n"
"v_accvgpr_read_b32 v194, a146\n"
"v_accvgpr_read_b32 v195, a147\n"
"v_accvgpr_read_b32 v196, a176\n"
"v_accvgpr_read_b32 v197, a177\n"
"v_accvgpr_read_b32 v198, a178\n"
"v_accvgpr_read_b32 v199, a179\n"
"v_cvt_pk_bf16_f32 v200, v192, v193\n"
"v_cvt_pk_bf16_f32 v201, v194, v195\n"
"v_cvt_pk_bf16_f32 v202, v196, v197\n"
"v_cvt_pk_bf16_f32 v203, v198, v199\n"
"s_add_u32 m0, s52, 114688\n"
"s_nop 0\n"
"buffer_load_dwordx4 v41, s[60:63], s49 offen lds\n"
"v_accvgpr_write_b32 a208, v200\n"
"v_accvgpr_write_b32 a209, v201\n"
"v_accvgpr_write_b32 a210, v202\n"
"v_accvgpr_write_b32 a211, v203\n"
"v_accvgpr_read_b32 v208, a148\n"
"v_accvgpr_read_b32 v209, a149\n"
"v_accvgpr_read_b32 v210, a150\n"
"v_accvgpr_read_b32 v211, a151\n"
"v_accvgpr_read_b32 v212, a180\n"
"v_accvgpr_read_b32 v213, a181\n"
"v_accvgpr_read_b32 v214, a182\n"
"v_accvgpr_read_b32 v215, a183\n"
"v_cvt_pk_bf16_f32 v216, v208, v209\n"
"v_cvt_pk_bf16_f32 v217, v210, v211\n"
"v_cvt_pk_bf16_f32 v218, v212, v213\n"
"v_cvt_pk_bf16_f32 v219, v214, v215\n"
"s_add_u32 m0, s52, 118784\n"
"s_nop 0\n"
"buffer_load_dwordx4 v42, s[60:63], s49 offen lds\n"
"v_accvgpr_write_b32 a212, v216\n"
"v_accvgpr_write_b32 a213, v217\n"
"v_accvgpr_write_b32 a214, v218\n"
"v_accvgpr_write_b32 a215, v219\n"
"v_accvgpr_read_b32 v224, a152\n"
"v_accvgpr_read_b32 v225, a153\n"
"v_accvgpr_read_b32 v226, a154\n"
"v_accvgpr_read_b32 v227, a155\n"
"v_accvgpr_read_b32 v228, a184\n"
"v_accvgpr_read_b32 v229, a185\n"
"v_accvgpr_read_b32 v230, a186\n"
"v_accvgpr_read_b32 v231, a187\n"
"v_cvt_pk_bf16_f32 v232, v224, v225\n"
"v_cvt_pk_bf16_f32 v233, v226, v227\n"
"v_cvt_pk_bf16_f32 v234, v228, v229\n"
"v_cvt_pk_bf16_f32 v235, v230, v231\n"
"s_add_u32 m0, s52, 122880\n"
"s_nop 0\n"
"buffer_load_dwordx4 v43, s[60:63], s49 offen lds\n"
"v_accvgpr_write_b32 a216, v232\n"
"v_accvgpr_write_b32 a217, v233\n"
"v_accvgpr_write_b32 a218, v234\n"
"v_accvgpr_write_b32 a219, v235\n"
"v_accvgpr_read_b32 v240, a156\n"
"v_accvgpr_read_b32 v241, a157\n"
"v_accvgpr_read_b32 v242, a158\n"
"v_accvgpr_read_b32 v243, a159\n"
"v_accvgpr_read_b32 v244, a188\n"
"v_accvgpr_read_b32 v245, a189\n"
"v_accvgpr_read_b32 v246, a190\n"
"v_accvgpr_read_b32 v247, a191\n"
"v_cvt_pk_bf16_f32 v248, v240, v241\n"
"v_cvt_pk_bf16_f32 v249, v242, v243\n"
"v_cvt_pk_bf16_f32 v250, v244, v245\n"
"v_cvt_pk_bf16_f32 v251, v246, v247\n"
"s_add_u32 m0, s52, 126976\n"
"s_nop 0\n"
"buffer_load_dwordx4 v44, s[60:63], s49 offen lds\n"
"v_accvgpr_write_b32 a220, v248\n"
"v_accvgpr_write_b32 a221, v249\n"
"v_accvgpr_write_b32 a222, v250\n"
"v_accvgpr_write_b32 a223, v251\n"
"s_add_u32 m0, s52, 131072\n"
"s_nop 0\n"
"buffer_load_dwordx4 v45, s[60:63], s49 offen lds\n"
"s_add_u32 s48, s48, 128\n"
"s_add_u32 s49, s49, 172032\n"
"s_mov_b32 s98, 4096\n"
"s_mov_b32 s99, 12288\n"
"s_branch L_B_tile_%=\n"
"L_K_tile_%=:\n"
"v_mfma_f32_16x16x32_bf16 a[0:3], v[160:163], v[128:131], 0\n"
"v_mfma_f32_16x16x32_bf16 a[4:7], v[160:163], v[132:135], 0\n"
"ds_read_b128 v[192:195], v5 offset:0\n"
"v_mfma_f32_16x16x32_bf16 a[8:11], v[160:163], v[136:139], 0\n"
"v_mfma_f32_16x16x32_bf16 a[12:15], v[160:163], v[140:143], 0\n"
"ds_read_b64_tr_b16 v[224:225], v6 offset:16384\n"
"v_mfma_f32_16x16x32_bf16 a[16:19], v[160:163], v[144:147], 0\n"
"v_mfma_f32_16x16x32_bf16 a[20:23], v[160:163], v[148:151], 0\n"
"ds_read_b64_tr_b16 v[226:227], v6 offset:18432\n"
"v_mfma_f32_16x16x32_bf16 a[24:27], v[160:163], v[152:155], 0\n"
"v_mfma_f32_16x16x32_bf16 a[28:31], v[160:163], v[156:159], 0\n"
"ds_read_b128 v[196:199], v5 offset:2048\n"
"v_mfma_f32_16x16x32_bf16 a[32:35], v[164:167], v[128:131], 0\n"
"v_mfma_f32_16x16x32_bf16 a[36:39], v[164:167], v[132:135], 0\n"
"ds_read_b64_tr_b16 v[228:229], v7 offset:16384\n"
"v_mfma_f32_16x16x32_bf16 a[40:43], v[164:167], v[136:139], 0\n"
"v_mfma_f32_16x16x32_bf16 a[44:47], v[164:167], v[140:143], 0\n"
"ds_read_b64_tr_b16 v[230:231], v7 offset:18432\n"
"v_mfma_f32_16x16x32_bf16 a[48:51], v[164:167], v[144:147], 0\n"
"v_mfma_f32_16x16x32_bf16 a[52:55], v[164:167], v[148:151], 0\n"
"ds_read_b128 v[200:203], v5 offset:4096\n"
"v_mfma_f32_16x16x32_bf16 a[56:59], v[164:167], v[152:155], 0\n"
"v_mfma_f32_16x16x32_bf16 a[60:63], v[164:167], v[156:159], 0\n"
"ds_read_b64_tr_b16 v[232:233], v6 offset:16640\n"
"v_mfma_f32_16x16x32_bf16 a[64:67], v[168:171], v[128:131], 0\n"
"v_mfma_f32_16x16x32_bf16 a[68:71], v[168:171], v[132:135], 0\n"
"ds_read_b64_tr_b16 v[234:235], v6 offset:18688\n"
"v_mfma_f32_16x16x32_bf16 a[72:75], v[168:171], v[136:139], 0\n"
"v_mfma_f32_16x16x32_bf16 a[76:79], v[168:171], v[140:143], 0\n"
"ds_read_b128 v[204:207], v5 offset:6144\n"
"v_mfma_f32_16x16x32_bf16 a[80:83], v[168:171], v[144:147], 0\n"
"v_mfma_f32_16x16x32_bf16 a[84:87], v[168:171], v[148:151], 0\n"
"ds_read_b64_tr_b16 v[236:237], v7 offset:16640\n"
"v_mfma_f32_16x16x32_bf16 a[88:91], v[168:171], v[152:155], 0\n"
"v_mfma_f32_16x16x32_bf16 a[92:95], v[168:171], v[156:159], 0\n"
"ds_read_b64_tr_b16 v[238:239], v7 offset:18688\n"
"v_mfma_f32_16x16x32_bf16 a[96:99], v[172:175], v[128:131], 0\n"
"v_mfma_f32_16x16x32_bf16 a[100:103], v[172:175], v[132:135], 0\n"
"ds_read_b128 v[208:211], v5 offset:8192\n"
"v_mfma_f32_16x16x32_bf16 a[104:107], v[172:175], v[136:139], 0\n"
"v_mfma_f32_16x16x32_bf16 a[108:111], v[172:175], v[140:143], 0\n"
"ds_read_b64_tr_b16 v[240:241], v6 offset:16896\n"
"v_mfma_f32_16x16x32_bf16 a[112:115], v[172:175], v[144:147], 0\n"
"v_mfma_f32_16x16x32_bf16 a[116:119], v[172:175], v[148:151], 0\n"
"ds_read_b64_tr_b16 v[242:243], v6 offset:18944\n"
"v_mfma_f32_16x16x32_bf16 a[120:123], v[172:175], v[152:155], 0\n"
"v_mfma_f32_16x16x32_bf16 a[124:127], v[172:175], v[156:159], 0\n"
"ds_read_b128 v[212:215], v5 offset:10240\n"
"v_mfma_f32_16x16x32_bf16 a[128:131], v[176:179], v[128:131], 0\n"
"v_mfma_f32_16x16x32_bf16 a[132:135], v[176:179], v[132:135], 0\n"
"ds_read_b64_tr_b16 v[244:245], v7 offset:16896\n"
"v_mfma_f32_16x16x32_bf16 a[136:139], v[176:179], v[136:139], 0\n"
"v_mfma_f32_16x16x32_bf16 a[140:143], v[176:179], v[140:143], 0\n"
"ds_read_b64_tr_b16 v[246:247], v7 offset:18944\n"
"v_mfma_f32_16x16x32_bf16 a[144:147], v[176:179], v[144:147], 0\n"
"v_mfma_f32_16x16x32_bf16 a[148:151], v[176:179], v[148:151], 0\n"
"ds_read_b128 v[216:219], v5 offset:12288\n"
"v_mfma_f32_16x16x32_bf16 a[152:155], v[176:179], v[152:155], 0\n"
"v_mfma_f32_16x16x32_bf16 a[156:159], v[176:179], v[156:159], 0\n"
"ds_read_b64_tr_b16 v[248:249], v6 offset:17152\n"
"v_mfma_f32_16x16x32_bf16 a[160:163], v[180:183], v[128:131], 0\n"
"v_mfma_f32_16x16x32_bf16 a[164:167], v[180:183], v[132:135], 0\n"
"ds_read_b64_tr_b16 v[250:251], v6 offset:19200\n"
"v_mfma_f32_16x16x32_bf16 a[168:171], v[180:183], v[136:139], 0\n"
"v_mfma_f32_16x16x32_bf16 a[172:175], v[180:183], v[140:143], 0\n"
"ds_read_b128 v[220:223], v5 offset:14336\n"
"v_mfma_f32_16x16x32_bf16 a[176:179], v[180:183], v[144:147], 0\n"
"v_mfma_f32_16x16x32_bf16 a[180:183], v[180:183], v[148:151], 0\n"
"ds_read_b64_tr_b16 v[252:253], v7 offset:17152\n"
"v_mfma_f32_16x16x32_bf16 a[184:187], v[180:183], v[152:155], 0\n"
"v_mfma_f32_16x16x32_bf16 a[188:191], v[180:183], v[156:159], 0\n"
"ds_read_b64_tr_b16 v[254:255], v7 offset:19200\n"
"v_mfma_f32_16x16x32_bf16 a[192:195], v[184:187], v[128:131], 0\n"
"v_mfma_f32_16x16x32_bf16 a[196:199], v[184:187], v[132:135], 0\n"
"v_mfma_f32_16x16x32_bf16 a[200:203], v[184:187], v[136:139], 0\n"
"v_mfma_f32_16x16x32_bf16 a[204:207], v[184:187], v[140:143], 0\n"
"v_mfma_f32_16x16x32_bf16 a[208:211], v[184:187], v[144:147], 0\n"
"v_mfma_f32_16x16x32_bf16 a[212:215], v[184:187], v[148:151], 0\n"
"v_mfma_f32_16x16x32_bf16 a[216:219], v[184:187], v[152:155], 0\n"
"v_mfma_f32_16x16x32_bf16 a[220:223], v[184:187], v[156:159], 0\n"
"v_mfma_f32_16x16x32_bf16 a[224:227], v[188:191], v[128:131], 0\n"
"v_mfma_f32_16x16x32_bf16 a[228:231], v[188:191], v[132:135], 0\n"
"v_mfma_f32_16x16x32_bf16 a[232:235], v[188:191], v[136:139], 0\n"
"v_mfma_f32_16x16x32_bf16 a[236:239], v[188:191], v[140:143], 0\n"
"v_mfma_f32_16x16x32_bf16 a[240:243], v[188:191], v[144:147], 0\n"
"v_mfma_f32_16x16x32_bf16 a[244:247], v[188:191], v[148:151], 0\n"
"v_mfma_f32_16x16x32_bf16 a[248:251], v[188:191], v[152:155], 0\n"
"v_mfma_f32_16x16x32_bf16 a[252:255], v[188:191], v[156:159], 0\n"
"s_waitcnt vmcnt(4) lgkmcnt(0)\n"
"s_barrier\n"
"v_xor_b32 v4, 0x10000, v4\n"
"v_xor_b32 v5, 0x10000, v5\n"
"v_xor_b32 v6, 0x10000, v6\n"
"v_xor_b32 v7, 0x10000, v7\n"
"v_mfma_f32_16x16x32_bf16 a[0:3], v[224:227], v[192:195], a[0:3]\n"
"s_mov_b32 m0, s52\n"
"v_mfma_f32_16x16x32_bf16 a[4:7], v[224:227], v[196:199], a[4:7]\n"
"ds_read_b128 v[128:131], v4 offset:0\n"
"buffer_load_dwordx4 v16, s[36:39], s48 offen  lds\n"
"v_mfma_f32_16x16x32_bf16 a[8:11], v[224:227], v[200:203], a[8:11]\n"
"v_mfma_f32_16x16x32_bf16 a[12:15], v[224:227], v[204:207], a[12:15]\n"
"ds_read_b64_tr_b16 v[160:161], v6 offset:0\n"
"v_mfma_f32_16x16x32_bf16 a[16:19], v[224:227], v[208:211], a[16:19]\n"
"s_add_u32 m0, m0, 0x400\n"
"v_mfma_f32_16x16x32_bf16 a[20:23], v[224:227], v[212:215], a[20:23]\n"
"ds_read_b64_tr_b16 v[162:163], v6 offset:2048\n"
"buffer_load_dwordx4 v17, s[36:39], s48 offen  lds\n"
"v_mfma_f32_16x16x32_bf16 a[24:27], v[224:227], v[216:219], a[24:27]\n"
"v_mfma_f32_16x16x32_bf16 a[28:31], v[224:227], v[220:223], a[28:31]\n"
"ds_read_b128 v[132:135], v4 offset:2048\n"
"v_mfma_f32_16x16x32_bf16 a[32:35], v[228:231], v[192:195], a[32:35]\n"
"s_add_u32 m0, m0, 0x400\n"
"v_mfma_f32_16x16x32_bf16 a[36:39], v[228:231], v[196:199], a[36:39]\n"
"ds_read_b64_tr_b16 v[164:165], v7 offset:0\n"
"buffer_load_dwordx4 v18, s[36:39], s48 offen  lds\n"
"v_mfma_f32_16x16x32_bf16 a[40:43], v[228:231], v[200:203], a[40:43]\n"
"v_mfma_f32_16x16x32_bf16 a[44:47], v[228:231], v[204:207], a[44:47]\n"
"ds_read_b64_tr_b16 v[166:167], v7 offset:2048\n"
"v_mfma_f32_16x16x32_bf16 a[48:51], v[228:231], v[208:211], a[48:51]\n"
"s_add_u32 m0, m0, 0x400\n"
"v_mfma_f32_16x16x32_bf16 a[52:55], v[228:231], v[212:215], a[52:55]\n"
"ds_read_b128 v[136:139], v4 offset:4096\n"
"buffer_load_dwordx4 v19, s[36:39], s48 offen  lds\n"
"v_mfma_f32_16x16x32_bf16 a[56:59], v[228:231], v[216:219], a[56:59]\n"
"v_mfma_f32_16x16x32_bf16 a[60:63], v[228:231], v[220:223], a[60:63]\n"
"ds_read_b64_tr_b16 v[168:169], v6 offset:256\n"
"v_mfma_f32_16x16x32_bf16 a[64:67], v[232:235], v[192:195], a[64:67]\n"
"s_add_u32 m0, m0, 0x400\n"
"v_mfma_f32_16x16x32_bf16 a[68:71], v[232:235], v[196:199], a[68:71]\n"
"ds_read_b64_tr_b16 v[170:171], v6 offset:2304\n"
"buffer_load_dwordx4 v20, s[36:39], s48 offen  lds\n"
"v_mfma_f32_16x16x32_bf16 a[72:75], v[232:235], v[200:203], a[72:75]\n"
"v_mfma_f32_16x16x32_bf16 a[76:79], v[232:235], v[204:207], a[76:79]\n"
"ds_read_b128 v[140:143], v4 offset:6144\n"
"v_mfma_f32_16x16x32_bf16 a[80:83], v[232:235], v[208:211], a[80:83]\n"
"s_add_u32 m0, m0, 0x400\n"
"v_mfma_f32_16x16x32_bf16 a[84:87], v[232:235], v[212:215], a[84:87]\n"
"ds_read_b64_tr_b16 v[172:173], v7 offset:256\n"
"buffer_load_dwordx4 v21, s[36:39], s48 offen  lds\n"
"v_mfma_f32_16x16x32_bf16 a[88:91], v[232:235], v[216:219], a[88:91]\n"
"v_mfma_f32_16x16x32_bf16 a[92:95], v[232:235], v[220:223], a[92:95]\n"
"ds_read_b64_tr_b16 v[174:175], v7 offset:2304\n"
"v_mfma_f32_16x16x32_bf16 a[96:99], v[236:239], v[192:195], a[96:99]\n"
"s_add_u32 m0, m0, 0x400\n"
"v_mfma_f32_16x16x32_bf16 a[100:103], v[236:239], v[196:199], a[100:103]\n"
"ds_read_b128 v[144:147], v4 offset:8192\n"
"buffer_load_dwordx4 v22, s[36:39], s48 offen  lds\n"
"v_mfma_f32_16x16x32_bf16 a[104:107], v[236:239], v[200:203], a[104:107]\n"
"v_mfma_f32_16x16x32_bf16 a[108:111], v[236:239], v[204:207], a[108:111]\n"
"ds_read_b64_tr_b16 v[176:177], v6 offset:512\n"
"v_mfma_f32_16x16x32_bf16 a[112:115], v[236:239], v[208:211], a[112:115]\n"
"s_add_u32 m0, m0, 0x400\n"
"v_mfma_f32_16x16x32_bf16 a[116:119], v[236:239], v[212:215], a[116:119]\n"
"ds_read_b64_tr_b16 v[178:179], v6 offset:2560\n"
"buffer_load_dwordx4 v23, s[36:39], s48 offen  lds\n"
"v_mfma_f32_16x16x32_bf16 a[120:123], v[236:239], v[216:219], a[120:123]\n"
"v_mfma_f32_16x16x32_bf16 a[124:127], v[236:239], v[220:223], a[124:127]\n"
"ds_read_b128 v[148:151], v4 offset:10240\n"
"v_mfma_f32_16x16x32_bf16 a[128:131], v[240:243], v[192:195], a[128:131]\n"
"s_add_u32 m0, m0, 25600\n"
"v_mfma_f32_16x16x32_bf16 a[132:135], v[240:243], v[196:199], a[132:135]\n"
"ds_read_b64_tr_b16 v[180:181], v7 offset:512\n"
"buffer_load_dwordx4 v24, s[40:43], s49 offen  lds\n"
"v_mfma_f32_16x16x32_bf16 a[136:139], v[240:243], v[200:203], a[136:139]\n"
"v_mfma_f32_16x16x32_bf16 a[140:143], v[240:243], v[204:207], a[140:143]\n"
"ds_read_b64_tr_b16 v[182:183], v7 offset:2560\n"
"v_mfma_f32_16x16x32_bf16 a[144:147], v[240:243], v[208:211], a[144:147]\n"
"s_add_u32 m0, m0, 0x400\n"
"v_mfma_f32_16x16x32_bf16 a[148:151], v[240:243], v[212:215], a[148:151]\n"
"ds_read_b128 v[152:155], v4 offset:12288\n"
"buffer_load_dwordx4 v25, s[40:43], s49 offen  lds\n"
"v_mfma_f32_16x16x32_bf16 a[152:155], v[240:243], v[216:219], a[152:155]\n"
"v_mfma_f32_16x16x32_bf16 a[156:159], v[240:243], v[220:223], a[156:159]\n"
"ds_read_b64_tr_b16 v[184:185], v6 offset:768\n"
"v_mfma_f32_16x16x32_bf16 a[160:163], v[244:247], v[192:195], a[160:163]\n"
"s_add_u32 m0, m0, 0x400\n"
"v_mfma_f32_16x16x32_bf16 a[164:167], v[244:247], v[196:199], a[164:167]\n"
"ds_read_b64_tr_b16 v[186:187], v6 offset:2816\n"
"buffer_load_dwordx4 v26, s[40:43], s49 offen  lds\n"
"v_mfma_f32_16x16x32_bf16 a[168:171], v[244:247], v[200:203], a[168:171]\n"
"v_mfma_f32_16x16x32_bf16 a[172:175], v[244:247], v[204:207], a[172:175]\n"
"ds_read_b128 v[156:159], v4 offset:14336\n"
"v_mfma_f32_16x16x32_bf16 a[176:179], v[244:247], v[208:211], a[176:179]\n"
"s_add_u32 m0, m0, 0x400\n"
"v_mfma_f32_16x16x32_bf16 a[180:183], v[244:247], v[212:215], a[180:183]\n"
"ds_read_b64_tr_b16 v[188:189], v7 offset:768\n"
"buffer_load_dwordx4 v27, s[40:43], s49 offen  lds\n"
"v_mfma_f32_16x16x32_bf16 a[184:187], v[244:247], v[216:219], a[184:187]\n"
"v_mfma_f32_16x16x32_bf16 a[188:191], v[244:247], v[220:223], a[188:191]\n"
"ds_read_b64_tr_b16 v[190:191], v7 offset:2816\n"
"v_mfma_f32_16x16x32_bf16 a[192:195], v[248:251], v[192:195], a[192:195]\n"
"s_add_u32 m0, m0, 0x400\n"
"v_mfma_f32_16x16x32_bf16 a[196:199], v[248:251], v[196:199], a[196:199]\n"
"buffer_load_dwordx4 v28, s[40:43], s49 offen  lds\n"
"v_mfma_f32_16x16x32_bf16 a[200:203], v[248:251], v[200:203], a[200:203]\n"
"v_mfma_f32_16x16x32_bf16 a[204:207], v[248:251], v[204:207], a[204:207]\n"
"v_mfma_f32_16x16x32_bf16 a[208:211], v[248:251], v[208:211], a[208:211]\n"
"s_add_u32 m0, m0, 0x400\n"
"v_mfma_f32_16x16x32_bf16 a[212:215], v[248:251], v[212:215], a[212:215]\n"
"buffer_load_dwordx4 v29, s[40:43], s49 offen  lds\n"
"v_mfma_f32_16x16x32_bf16 a[216:219], v[248:251], v[216:219], a[216:219]\n"
"v_mfma_f32_16x16x32_bf16 a[220:223], v[248:251], v[220:223], a[220:223]\n"
"v_mfma_f32_16x16x32_bf16 a[224:227], v[252:255], v[192:195], a[224:227]\n"
"s_add_u32 m0, m0, 0x400\n"
"v_mfma_f32_16x16x32_bf16 a[228:231], v[252:255], v[196:199], a[228:231]\n"
"buffer_load_dwordx4 v30, s[40:43], s49 offen  lds\n"
"v_mfma_f32_16x16x32_bf16 a[232:235], v[252:255], v[200:203], a[232:235]\n"
"v_mfma_f32_16x16x32_bf16 a[236:239], v[252:255], v[204:207], a[236:239]\n"
"v_mfma_f32_16x16x32_bf16 a[240:243], v[252:255], v[208:211], a[240:243]\n"
"s_add_u32 m0, m0, 0x400\n"
"v_mfma_f32_16x16x32_bf16 a[244:247], v[252:255], v[212:215], a[244:247]\n"
"buffer_load_dwordx4 v31, s[40:43], s49 offen  lds\n"
"v_mfma_f32_16x16x32_bf16 a[248:251], v[252:255], v[216:219], a[248:251]\n"
"s_add_u32 s48, s48, 128\n"
"s_add_u32 s49, s49, s50\n"
"s_xor_b32 s52, s52, 0x10000\n"
"v_mfma_f32_16x16x32_bf16 a[252:255], v[252:255], v[220:223], a[252:255]\n"
"s_sub_u32 s34, s34, 1\n"
"s_cbranch_scc1 L_pfs_Kf_%=\n"
"global_load_dword v51, v[48:49], off\n"
"v_add_co_u32 v48, vcc, v48, v50\n"
"s_nop 1\n"
"v_addc_co_u32 v49, vcc, 0, v49, vcc\n"
"L_pfr_Kf_%=:\n"
"s_waitcnt lgkmcnt(0)\n"
"s_sub_u32 s51, s51, 1\n"
"L_K_loop_%=:\n"
"s_cmp_lg_u32 s51, 2\n"
"s_cbranch_scc1 L_K_nosw_%=\n"
"s_mov_b32 s36, s56\n"
"s_mov_b32 s40, s60\n"
"s_mov_b32 s37, s57\n"
"s_mov_b32 s41, s61\n"
"s_mov_b32 s38, s58\n"
"s_mov_b32 s42, s62\n"
"s_mov_b32 s39, s59\n"
"s_mov_b32 s43, s63\n"
"s_mov_b32 s48, s64\n"
"s_mov_b32 s49, s65\n"
"s_mov_b32 s50, s66\n"
"v_mov_b32 v16, v32\n"
"v_mov_b32 v24, v40\n"
"v_mov_b32 v17, v33\n"
"v_mov_b32 v25, v41\n"
"v_mov_b32 v18, v34\n"
"v_mov_b32 v26, v42\n"
"v_mov_b32 v19, v35\n"
"v_mov_b32 v27, v43\n"
"v_mov_b32 v20, v36\n"
"v_mov_b32 v28, v44\n"
"v_mov_b32 v21, v37\n"
"v_mov_b32 v29, v45\n"
"v_mov_b32 v22, v38\n"
"v_mov_b32 v30, v46\n"
"v_mov_b32 v23, v39\n"
"v_mov_b32 v31, v47\n"
"s_cmp_eq_u32 s69, 0x3f800000\n"
"s_cselect_b32 s38, 0, s38\n"
"s_cselect_b32 s42, 0, s42\n"
"L_K_nosw_%=:\n"
"v_mfma_f32_16x16x32_bf16 a[0:3], v[160:163], v[128:131], a[0:3]\n"
"v_mfma_f32_16x16x32_bf16 a[4:7], v[160:163], v[132:135], a[4:7]\n"
"ds_read_b128 v[192:195], v5 offset:0\n"
"v_mfma_f32_16x16x32_bf16 a[8:11], v[160:163], v[136:139], a[8:11]\n"
"v_mfma_f32_16x16x32_bf16 a[12:15], v[160:163], v[140:143], a[12:15]\n"
"ds_read_b64_tr_b16 v[224:225], v6 offset:16384\n"
"v_mfma_f32_16x16x32_bf16 a[16:19], v[160:163], v[144:147], a[16:19]\n"
"v_mfma_f32_16x16x32_bf16 a[20:23], v[160:163], v[148:151], a[20:23]\n"
"ds_read_b64_tr_b16 v[226:227], v6 offset:18432\n"
"v_mfma_f32_16x16x32_bf16 a[24:27], v[160:163], v[152:155], a[24:27]\n"
"v_mfma_f32_16x16x32_bf16 a[28:31], v[160:163], v[156:159], a[28:31]\n"
"ds_read_b128 v[196:199], v5 offset:2048\n"
"v_mfma_f32_16x16x32_bf16 a[32:35], v[164:167], v[128:131], a[32:35]\n"
"v_mfma_f32_16x16x32_bf16 a[36:39], v[164:167], v[132:135], a[36:39]\n"
"ds_read_b64_tr_b16 v[228:229], v7 offset:16384\n"
"v_mfma_f32_16x16x32_bf16 a[40:43], v[164:167], v[136:139], a[40:43]\n"
"v_mfma_f32_16x16x32_bf16 a[44:47], v[164:167], v[140:143], a[44:47]\n"
"ds_read_b64_tr_b16 v[230:231], v7 offset:18432\n"
"v_mfma_f32_16x16x32_bf16 a[48:51], v[164:167], v[144:147], a[48:51]\n"
"v_mfma_f32_16x16x32_bf16 a[52:55], v[164:167], v[148:151], a[52:55]\n"
"ds_read_b128 v[200:203], v5 offset:4096\n"
"v_mfma_f32_16x16x32_bf16 a[56:59], v[164:167], v[152:155], a[56:59]\n"
"v_mfma_f32_16x16x32_bf16 a[60:63], v[164:167], v[156:159], a[60:63]\n"
"ds_read_b64_tr_b16 v[232:233], v6 offset:16640\n"
"v_mfma_f32_16x16x32_bf16 a[64:67], v[168:171], v[128:131], a[64:67]\n"
"v_mfma_f32_16x16x32_bf16 a[68:71], v[168:171], v[132:135], a[68:71]\n"
"ds_read_b64_tr_b16 v[234:235], v6 offset:18688\n"
"v_mfma_f32_16x16x32_bf16 a[72:75], v[168:171], v[136:139], a[72:75]\n"
"v_mfma_f32_16x16x32_bf16 a[76:79], v[168:171], v[140:143], a[76:79]\n"
"ds_read_b128 v[204:207], v5 offset:6144\n"
"v_mfma_f32_16x16x32_bf16 a[80:83], v[168:171], v[144:147], a[80:83]\n"
"v_mfma_f32_16x16x32_bf16 a[84:87], v[168:171], v[148:151], a[84:87]\n"
"ds_read_b64_tr_b16 v[236:237], v7 offset:16640\n"
"v_mfma_f32_16x16x32_bf16 a[88:91], v[168:171], v[152:155], a[88:91]\n"
"v_mfma_f32_16x16x32_bf16 a[92:95], v[168:171], v[156:159], a[92:95]\n"
"ds_read_b64_tr_b16 v[238:239], v7 offset:18688\n"
"v_mfma_f32_16x16x32_bf16 a[96:99], v[172:175], v[128:131], a[96:99]\n"
"v_mfma_f32_16x16x32_bf16 a[100:103], v[172:175], v[132:135], a[100:103]\n"
"ds_read_b128 v[208:211], v5 offset:8192\n"
"v_mfma_f32_16x16x32_bf16 a[104:107], v[172:175], v[136:139], a[104:107]\n"
"v_mfma_f32_16x16x32_bf16 a[108:111], v[172:175], v[140:143], a[108:111]\n"
"ds_read_b64_tr_b16 v[240:241], v6 offset:16896\n"
"v_mfma_f32_16x16x32_bf16 a[112:115], v[172:175], v[144:147], a[112:115]\n"
"v_mfma_f32_16x16x32_bf16 a[116:119], v[172:175], v[148:151], a[116:119]\n"
"ds_read_b64_tr_b16 v[242:243], v6 offset:18944\n"
"v_mfma_f32_16x16x32_bf16 a[120:123], v[172:175], v[152:155], a[120:123]\n"
"v_mfma_f32_16x16x32_bf16 a[124:127], v[172:175], v[156:159], a[124:127]\n"
"ds_read_b128 v[212:215], v5 offset:10240\n"
"v_mfma_f32_16x16x32_bf16 a[128:131], v[176:179], v[128:131], a[128:131]\n"
"v_mfma_f32_16x16x32_bf16 a[132:135], v[176:179], v[132:135], a[132:135]\n"
"ds_read_b64_tr_b16 v[244:245], v7 offset:16896\n"
"v_mfma_f32_16x16x32_bf16 a[136:139], v[176:179], v[136:139], a[136:139]\n"
"v_mfma_f32_16x16x32_bf16 a[140:143], v[176:179], v[140:143], a[140:143]\n"
"ds_read_b64_tr_b16 v[246:247], v7 offset:18944\n"
"v_mfma_f32_16x16x32_bf16 a[144:147], v[176:179], v[144:147], a[144:147]\n"
"v_mfma_f32_16x16x32_bf16 a[148:151], v[176:179], v[148:151], a[148:151]\n"
"ds_read_b128 v[216:219], v5 offset:12288\n"
"v_mfma_f32_16x16x32_bf16 a[152:155], v[176:179], v[152:155], a[152:155]\n"
"v_mfma_f32_16x16x32_bf16 a[156:159], v[176:179], v[156:159], a[156:159]\n"
"ds_read_b64_tr_b16 v[248:249], v6 offset:17152\n"
"v_mfma_f32_16x16x32_bf16 a[160:163], v[180:183], v[128:131], a[160:163]\n"
"v_mfma_f32_16x16x32_bf16 a[164:167], v[180:183], v[132:135], a[164:167]\n"
"ds_read_b64_tr_b16 v[250:251], v6 offset:19200\n"
"v_mfma_f32_16x16x32_bf16 a[168:171], v[180:183], v[136:139], a[168:171]\n"
"v_mfma_f32_16x16x32_bf16 a[172:175], v[180:183], v[140:143], a[172:175]\n"
"ds_read_b128 v[220:223], v5 offset:14336\n"
"v_mfma_f32_16x16x32_bf16 a[176:179], v[180:183], v[144:147], a[176:179]\n"
"v_mfma_f32_16x16x32_bf16 a[180:183], v[180:183], v[148:151], a[180:183]\n"
"ds_read_b64_tr_b16 v[252:253], v7 offset:17152\n"
"v_mfma_f32_16x16x32_bf16 a[184:187], v[180:183], v[152:155], a[184:187]\n"
"v_mfma_f32_16x16x32_bf16 a[188:191], v[180:183], v[156:159], a[188:191]\n"
"ds_read_b64_tr_b16 v[254:255], v7 offset:19200\n"
"v_mfma_f32_16x16x32_bf16 a[192:195], v[184:187], v[128:131], a[192:195]\n"
"v_mfma_f32_16x16x32_bf16 a[196:199], v[184:187], v[132:135], a[196:199]\n"
"v_mfma_f32_16x16x32_bf16 a[200:203], v[184:187], v[136:139], a[200:203]\n"
"v_mfma_f32_16x16x32_bf16 a[204:207], v[184:187], v[140:143], a[204:207]\n"
"v_mfma_f32_16x16x32_bf16 a[208:211], v[184:187], v[144:147], a[208:211]\n"
"v_mfma_f32_16x16x32_bf16 a[212:215], v[184:187], v[148:151], a[212:215]\n"
"v_mfma_f32_16x16x32_bf16 a[216:219], v[184:187], v[152:155], a[216:219]\n"
"v_mfma_f32_16x16x32_bf16 a[220:223], v[184:187], v[156:159], a[220:223]\n"
"v_mfma_f32_16x16x32_bf16 a[224:227], v[188:191], v[128:131], a[224:227]\n"
"v_mfma_f32_16x16x32_bf16 a[228:231], v[188:191], v[132:135], a[228:231]\n"
"v_mfma_f32_16x16x32_bf16 a[232:235], v[188:191], v[136:139], a[232:235]\n"
"v_mfma_f32_16x16x32_bf16 a[236:239], v[188:191], v[140:143], a[236:239]\n"
"v_mfma_f32_16x16x32_bf16 a[240:243], v[188:191], v[144:147], a[240:243]\n"
"v_mfma_f32_16x16x32_bf16 a[244:247], v[188:191], v[148:151], a[244:247]\n"
"v_mfma_f32_16x16x32_bf16 a[248:251], v[188:191], v[152:155], a[248:251]\n"
"v_mfma_f32_16x16x32_bf16 a[252:255], v[188:191], v[156:159], a[252:255]\n"
"s_waitcnt vmcnt(1) lgkmcnt(0)\n"
"s_barrier\n"
"v_xor_b32 v4, 0x10000, v4\n"
"v_xor_b32 v5, 0x10000, v5\n"
"v_xor_b32 v6, 0x10000, v6\n"
"v_xor_b32 v7, 0x10000, v7\n"
"v_mfma_f32_16x16x32_bf16 a[0:3], v[224:227], v[192:195], a[0:3]\n"
"s_mov_b32 m0, s52\n"
"v_mfma_f32_16x16x32_bf16 a[4:7], v[224:227], v[196:199], a[4:7]\n"
"ds_read_b128 v[128:131], v4 offset:0\n"
"buffer_load_dwordx4 v16, s[36:39], s48 offen  lds\n"
"v_mfma_f32_16x16x32_bf16 a[8:11], v[224:227], v[200:203], a[8:11]\n"
"v_mfma_f32_16x16x32_bf16 a[12:15], v[224:227], v[204:207], a[12:15]\n"
"ds_read_b64_tr_b16 v[160:161], v6 offset:0\n"
"v_mfma_f32_16x16x32_bf16 a[16:19], v[224:227], v[208:211], a[16:19]\n"
"s_add_u32 m0, m0, 0x400\n"
"v_mfma_f32_16x16x32_bf16 a[20:23], v[224:227], v[212:215], a[20:23]\n"
"ds_read_b64_tr_b16 v[162:163], v6 offset:2048\n"
"buffer_load_dwordx4 v17, s[36:39], s48 offen  lds\n"
"v_mfma_f32_16x16x32_bf16 a[24:27], v[224:227], v[216:219], a[24:27]\n"
"v_mfma_f32_16x16x32_bf16 a[28:31], v[224:227], v[220:223], a[28:31]\n"
"ds_read_b128 v[132:135], v4 offset:2048\n"
"v_mfma_f32_16x16x32_bf16 a[32:35], v[228:231], v[192:195], a[32:35]\n"
"s_add_u32 m0, m0, 0x400\n"
"v_mfma_f32_16x16x32_bf16 a[36:39], v[228:231], v[196:199], a[36:39]\n"
"ds_read_b64_tr_b16 v[164:165], v7 offset:0\n"
"buffer_load_dwordx4 v18, s[36:39], s48 offen  lds\n"
"v_mfma_f32_16x16x32_bf16 a[40:43], v[228:231], v[200:203], a[40:43]\n"
"v_mfma_f32_16x16x32_bf16 a[44:47], v[228:231], v[204:207], a[44:47]\n"
"ds_read_b64_tr_b16 v[166:167], v7 offset:2048\n"
"v_mfma_f32_16x16x32_bf16 a[48:51], v[228:231], v[208:211], a[48:51]\n"
"s_add_u32 m0, m0, 0x400\n"
"v_mfma_f32_16x16x32_bf16 a[52:55], v[228:231], v[212:215], a[52:55]\n"
"ds_read_b128 v[136:139], v4 offset:4096\n"
"buffer_load_dwordx4 v19, s[36:39], s48 offen  lds\n"
"v_mfma_f32_16x16x32_bf16 a[56:59], v[228:231], v[216:219], a[56:59]\n"
"v_mfma_f32_16x16x32_bf16 a[60:63], v[228:231], v[220:223], a[60:63]\n"
"ds_read_b64_tr_b16 v[168:169], v6 offset:256\n"
"v_mfma_f32_16x16x32_bf16 a[64:67], v[232:235], v[192:195], a[64:67]\n"
"s_add_u32 m0, m0, 0x400\n"
"v_mfma_f32_16x16x32_bf16 a[68:71], v[232:235], v[196:199], a[68:71]\n"
"ds_read_b64_tr_b16 v[170:171], v6 offset:2304\n"
"buffer_load_dwordx4 v20, s[36:39], s48 offen  lds\n"
"v_mfma_f32_16x16x32_bf16 a[72:75], v[232:235], v[200:203], a[72:75]\n"
"v_mfma_f32_16x16x32_bf16 a[76:79], v[232:235], v[204:207], a[76:79]\n"
"ds_read_b128 v[140:143], v4 offset:6144\n"
"v_mfma_f32_16x16x32_bf16 a[80:83], v[232:235], v[208:211], a[80:83]\n"
"s_add_u32 m0, m0, 0x400\n"
"v_mfma_f32_16x16x32_bf16 a[84:87], v[232:235], v[212:215], a[84:87]\n"
"ds_read_b64_tr_b16 v[172:173], v7 offset:256\n"
"buffer_load_dwordx4 v21, s[36:39], s48 offen  lds\n"
"v_mfma_f32_16x16x32_bf16 a[88:91], v[232:235], v[216:219], a[88:91]\n"
"v_mfma_f32_16x16x32_bf16 a[92:95], v[232:235], v[220:223], a[92:95]\n"
"ds_read_b64_tr_b16 v[174:175], v7 offset:2304\n"
"v_mfma_f32_16x16x32_bf16 a[96:99], v[236:239], v[192:195], a[96:99]\n"
"s_add_u32 m0, m0, 0x400\n"
"v_mfma_f32_16x16x32_bf16 a[100:103], v[236:239], v[196:199], a[100:103]\n"
"ds_read_b128 v[144:147], v4 offset:8192\n"
"buffer_load_dwordx4 v22, s[36:39], s48 offen  lds\n"
"v_mfma_f32_16x16x32_bf16 a[104:107], v[236:239], v[200:203], a[104:107]\n"
"v_mfma_f32_16x16x32_bf16 a[108:111], v[236:239], v[204:207], a[108:111]\n"
"ds_read_b64_tr_b16 v[176:177], v6 offset:512\n"
"v_mfma_f32_16x16x32_bf16 a[112:115], v[236:239], v[208:211], a[112:115]\n"
"s_add_u32 m0, m0, 0x400\n"
"v_mfma_f32_16x16x32_bf16 a[116:119], v[236:239], v[212:215], a[116:119]\n"
"ds_read_b64_tr_b16 v[178:179], v6 offset:2560\n"
"buffer_load_dwordx4 v23, s[36:39], s48 offen  lds\n"
"v_mfma_f32_16x16x32_bf16 a[120:123], v[236:239], v[216:219], a[120:123]\n"
"v_mfma_f32_16x16x32_bf16 a[124:127], v[236:239], v[220:223], a[124:127]\n"
"ds_read_b128 v[148:151], v4 offset:10240\n"
"v_mfma_f32_16x16x32_bf16 a[128:131], v[240:243], v[192:195], a[128:131]\n"
"s_add_u32 m0, m0, 25600\n"
"v_mfma_f32_16x16x32_bf16 a[132:135], v[240:243], v[196:199], a[132:135]\n"
"ds_read_b64_tr_b16 v[180:181], v7 offset:512\n"
"buffer_load_dwordx4 v24, s[40:43], s49 offen  lds\n"
"v_mfma_f32_16x16x32_bf16 a[136:139], v[240:243], v[200:203], a[136:139]\n"
"v_mfma_f32_16x16x32_bf16 a[140:143], v[240:243], v[204:207], a[140:143]\n"
"ds_read_b64_tr_b16 v[182:183], v7 offset:2560\n"
"v_mfma_f32_16x16x32_bf16 a[144:147], v[240:243], v[208:211], a[144:147]\n"
"s_add_u32 m0, m0, 0x400\n"
"v_mfma_f32_16x16x32_bf16 a[148:151], v[240:243], v[212:215], a[148:151]\n"
"ds_read_b128 v[152:155], v4 offset:12288\n"
"buffer_load_dwordx4 v25, s[40:43], s49 offen  lds\n"
"v_mfma_f32_16x16x32_bf16 a[152:155], v[240:243], v[216:219], a[152:155]\n"
"v_mfma_f32_16x16x32_bf16 a[156:159], v[240:243], v[220:223], a[156:159]\n"
"ds_read_b64_tr_b16 v[184:185], v6 offset:768\n"
"v_mfma_f32_16x16x32_bf16 a[160:163], v[244:247], v[192:195], a[160:163]\n"
"s_add_u32 m0, m0, 0x400\n"
"v_mfma_f32_16x16x32_bf16 a[164:167], v[244:247], v[196:199], a[164:167]\n"
"ds_read_b64_tr_b16 v[186:187], v6 offset:2816\n"
"buffer_load_dwordx4 v26, s[40:43], s49 offen  lds\n"
"v_mfma_f32_16x16x32_bf16 a[168:171], v[244:247], v[200:203], a[168:171]\n"
"v_mfma_f32_16x16x32_bf16 a[172:175], v[244:247], v[204:207], a[172:175]\n"
"ds_read_b128 v[156:159], v4 offset:14336\n"
"v_mfma_f32_16x16x32_bf16 a[176:179], v[244:247], v[208:211], a[176:179]\n"
"s_add_u32 m0, m0, 0x400\n"
"v_mfma_f32_16x16x32_bf16 a[180:183], v[244:247], v[212:215], a[180:183]\n"
"ds_read_b64_tr_b16 v[188:189], v7 offset:768\n"
"buffer_load_dwordx4 v27, s[40:43], s49 offen  lds\n"
"v_mfma_f32_16x16x32_bf16 a[184:187], v[244:247], v[216:219], a[184:187]\n"
"v_mfma_f32_16x16x32_bf16 a[188:191], v[244:247], v[220:223], a[188:191]\n"
"ds_read_b64_tr_b16 v[190:191], v7 offset:2816\n"
"v_mfma_f32_16x16x32_bf16 a[192:195], v[248:251], v[192:195], a[192:195]\n"
"s_add_u32 m0, m0, 0x400\n"
"v_mfma_f32_16x16x32_bf16 a[196:199], v[248:251], v[196:199], a[196:199]\n"
"buffer_load_dwordx4 v28, s[40:43], s49 offen  lds\n"
"v_mfma_f32_16x16x32_bf16 a[200:203], v[248:251], v[200:203], a[200:203]\n"
"v_mfma_f32_16x16x32_bf16 a[204:207], v[248:251], v[204:207], a[204:207]\n"
"v_mfma_f32_16x16x32_bf16 a[208:211], v[248:251], v[208:211], a[208:211]\n"
"s_add_u32 m0, m0, 0x400\n"
"v_mfma_f32_16x16x32_bf16 a[212:215], v[248:251], v[212:215], a[212:215]\n"
"buffer_load_dwordx4 v29, s[40:43], s49 offen  lds\n"
"v_mfma_f32_16x16x32_bf16 a[216:219], v[248:251], v[216:219], a[216:219]\n"
"v_mfma_f32_16x16x32_bf16 a[220:223], v[248:251], v[220:223], a[220:223]\n"
"v_mfma_f32_16x16x32_bf16 a[224:227], v[252:255], v[192:195], a[224:227]\n"
"s_add_u32 m0, m0, 0x400\n"
"v_mfma_f32_16x16x32_bf16 a[228:231], v[252:255], v[196:199], a[228:231]\n"
"buffer_load_dwordx4 v30, s[40:43], s49 offen  lds\n"
"v_mfma_f32_16x16x32_bf16 a[232:235], v[252:255], v[200:203], a[232:235]\n"
"v_mfma_f32_16x16x32_bf16 a[236:239], v[252:255], v[204:207], a[236:239]\n"
"v_mfma_f32_16x16x32_bf16 a[240:243], v[252:255], v[208:211], a[240:243]\n"
"s_add_u32 m0, m0, 0x400\n"
"v_mfma_f32_16x16x32_bf16 a[244:247], v[252:255], v[212:215], a[244:247]\n"
"buffer_load_dwordx4 v31, s[40:43], s49 offen  lds\n"
"v_mfma_f32_16x16x32_bf16 a[248:251], v[252:255], v[216:219], a[248:251]\n"
"s_add_u32 s48, s48, 128\n"
"s_add_u32 s49, s49, s50\n"
"s_xor_b32 s52, s52, 0x10000\n"
"v_mfma_f32_16x16x32_bf16 a[252:255], v[252:255], v[220:223], a[252:255]\n"
"s_sub_u32 s34, s34, 1\n"
"s_cbranch_scc1 L_pfs_Kl_%=\n"
"global_load_dword v51, v[48:49], off\n"
"v_add_co_u32 v48, vcc, v48, v50\n"
"s_nop 1\n"
"v_addc_co_u32 v49, vcc, 0, v49, vcc\n"
"L_pfr_Kl_%=:\n"
"s_waitcnt lgkmcnt(0)\n"
"s_sub_u32 s51, s51, 1\n"
"s_cmp_lg_u32 s51, 0\n"
"s_cbranch_scc1 L_K_loop_%=\n"
"s_cmp_eq_u32 s69, 0x3f800000\n"
"s_cbranch_scc1 L_K_tobl_%=\n"
"s_nop 15\n"
"s_mov_b32 s83, s53\n"
"v_accvgpr_read_b32 v192, a0\n"
"v_accvgpr_read_b32 v193, a1\n"
"v_accvgpr_read_b32 v194, a2\n"
"v_accvgpr_read_b32 v195, a3\n"
"v_accvgpr_read_b32 v196, a32\n"
"v_accvgpr_read_b32 v197, a33\n"
"v_accvgpr_read_b32 v198, a34\n"
"v_accvgpr_read_b32 v199, a35\n"
"v_cvt_pk_bf16_f32 v200, v192, v193\n"
"v_cvt_pk_bf16_f32 v201, v194, v195\n"
"v_cvt_pk_bf16_f32 v202, v196, v197\n"
"v_cvt_pk_bf16_f32 v203, v198, v199\n"
"buffer_store_dwordx4 v[200:203], v8, s[44:47], s83 offen offset:0 sc1\n"
"v_accvgpr_read_b32 v208, a64\n"
"v_accvgpr_read_b32 v209, a65\n"
"v_accvgpr_read_b32 v210, a66\n"
"v_accvgpr_read_b32 v211, a67\n"
"v_accvgpr_read_b32 v212, a96\n"
"v_accvgpr_read_b32 v213, a97\n"
"v_accvgpr_read_b32 v214, a98\n"
"v_accvgpr_read_b32 v215, a99\n"
"v_cvt_pk_bf16_f32 v216, v208, v209\n"
"v_cvt_pk_bf16_f32 v217, v210, v211\n"
"v_cvt_pk_bf16_f32 v218, v212, v213\n"
"v_cvt_pk_bf16_f32 v219, v214, v215\n"
"buffer_store_dwordx4 v[216:219], v8, s[44:47], s83 offen offset:1024 sc1\n"
"v_accvgpr_read_b32 v224, a128\n"
"v_accvgpr_read_b32 v225, a129\n"
"v_accvgpr_read_b32 v226, a130\n"
"v_accvgpr_read_b32 v227, a131\n"
"v_accvgpr_read_b32 v228, a160\n"
"v_accvgpr_read_b32 v229, a161\n"
"v_accvgpr_read_b32 v230, a162\n"
"v_accvgpr_read_b32 v231, a163\n"
"v_cvt_pk_bf16_f32 v232, v224, v225\n"
"v_cvt_pk_bf16_f32 v233, v226, v227\n"
"v_cvt_pk_bf16_f32 v234, v228, v229\n"
"v_cvt_pk_bf16_f32 v235, v230, v231\n"
"buffer_store_dwordx4 v[232:235], v8, s[44:47], s83 offen offset:2048 sc1\n"
"v_accvgpr_read_b32 v240, a192\n"
"v_accvgpr_read_b32 v241, a193\n"
"v_accvgpr_read_b32 v242, a194\n"
"v_accvgpr_read_b32 v243, a195\n"
"v_accvgpr_read_b32 v244, a224\n"
"v_accvgpr_read_b32 v245, a225\n"
"v_accvgpr_read_b32 v246, a226\n"
"v_accvgpr_read_b32 v247, a227\n"
"v_cvt_pk_bf16_f32 v248, v240, v241\n"
"v_cvt_pk_bf16_f32 v249, v242, v243\n"
"v_cvt_pk_bf16_f32 v250, v244, v245\n"
"v_cvt_pk_bf16_f32 v251, v246, v247\n"
"buffer_store_dwordx4 v[248:251], v8, s[44:47], s83 offen offset:3072 sc1\n"
"s_add_u32 s83, s83, 8192\n"
"v_accvgpr_read_b32 v192, a4\n"
"v_accvgpr_read_b32 v193, a5\n"
"v_accvgpr_read_b32 v194, a6\n"
"v_accvgpr_read_b32 v195, a7\n"
"v_accvgpr_read_b32 v196, a36\n"
"v_accvgpr_read_b32 v197, a37\n"
"v_accvgpr_read_b32 v198, a38\n"
"v_accvgpr_read_b32 v199, a39\n"
"v_cvt_pk_bf16_f32 v200, v192, v193\n"
"v_cvt_pk_bf16_f32 v201, v194, v195\n"
"v_cvt_pk_bf16_f32 v202, v196, v197\n"
"v_cvt_pk_bf16_f32 v203, v198, v199\n"
"buffer_store_dwordx4 v[200:203], v8, s[44:47], s83 offen offset:0 sc1\n"
"v_accvgpr_read_b32 v208, a68\n"
"v_accvgpr_read_b32 v209, a69\n"
"v_accvgpr_read_b32 v210, a70\n"
"v_accvgpr_read_b32 v211, a71\n"
"v_accvgpr_read_b32 v212, a100\n"
"v_accvgpr_read_b32 v213, a101\n"
"v_accvgpr_read_b32 v214, a102\n"
"v_accvgpr_read_b32 v215, a103\n"
"v_cvt_pk_bf16_f32 v216, v208, v209\n"
"v_cvt_pk_bf16_f32 v217, v210, v211\n"
"v_cvt_pk_bf16_f32 v218, v212, v213\n"
"v_cvt_pk_bf16_f32 v219, v214, v215\n"
"buffer_store_dwordx4 v[216:219], v8, s[44:47], s83 offen offset:1024 sc1\n"
"v_accvgpr_read_b32 v224, a132\n"
"v_accvgpr_read_b32 v225, a133\n"
"v_accvgpr_read_b32 v226, a134\n"
"v_accvgpr_read_b32 v227, a135\n"
"v_accvgpr_read_b32 v228, a164\n"
"v_accvgpr_read_b32 v229, a165\n"
"v_accvgpr_read_b32 v230, a166\n"
"v_accvgpr_read_b32 v231, a167\n"
"v_cvt_pk_bf16_f32 v232, v224, v225\n"
"v_cvt_pk_bf16_f32 v233, v226, v227\n"
"v_cvt_pk_bf16_f32 v234, v228, v229\n"
"v_cvt_pk_bf16_f32 v235, v230, v231\n"
"buffer_store_dwordx4 v[232:235], v8, s[44:47], s83 offen offset:2048 sc1\n"
"v_accvgpr_read_b32 v240, a196\n"
"v_accvgpr_read_b32 v241, a197\n"
"v_accvgpr_read_b32 v242, a198\n"
"v_accvgpr_read_b32 v243, a199\n"
"v_accvgpr_read_b32 v244, a228\n"
"v_accvgpr_read_b32 v245, a229\n"
"v_accvgpr_read_b32 v246, a230\n"
"v_accvgpr_read_b32 v247, a231\n"
"v_cvt_pk_bf16_f32 v248, v240, v241\n"
"v_cvt_pk_bf16_f32 v249, v242, v243\n"
"v_cvt_pk_bf16_f32 v250, v244, v245\n"
"v_cvt_pk_bf16_f32 v251, v246, v247\n"
"buffer_store_dwordx4 v[248:251], v8, s[44:47], s83 offen offset:3072 sc1\n"
"s_add_u32 s83, s83, 8192\n"
"v_accvgpr_read_b32 v192, a8\n"
"v_accvgpr_read_b32 v193, a9\n"
"v_accvgpr_read_b32 v194, a10\n"
"v_accvgpr_read_b32 v195, a11\n"
"v_accvgpr_read_b32 v196, a40\n"
"v_accvgpr_read_b32 v197, a41\n"
"v_accvgpr_read_b32 v198, a42\n"
"v_accvgpr_read_b32 v199, a43\n"
"v_cvt_pk_bf16_f32 v200, v192, v193\n"
"v_cvt_pk_bf16_f32 v201, v194, v195\n"
"v_cvt_pk_bf16_f32 v202, v196, v197\n"
"v_cvt_pk_bf16_f32 v203, v198, v199\n"
"buffer_store_dwordx4 v[200:203], v8, s[44:47], s83 offen offset:0 sc1\n"
"v_accvgpr_read_b32 v208, a72\n"
"v_accvgpr_read_b32 v209, a73\n"
"v_accvgpr_read_b32 v210, a74\n"
"v_accvgpr_read_b32 v211, a75\n"
"v_accvgpr_read_b32 v212, a104\n"
"v_accvgpr_read_b32 v213, a105\n"
"v_accvgpr_read_b32 v214, a106\n"
"v_accvgpr_read_b32 v215, a107\n"
"v_cvt_pk_bf16_f32 v216, v208, v209\n"
"v_cvt_pk_bf16_f32 v217, v210, v211\n"
"v_cvt_pk_bf16_f32 v218, v212, v213\n"
"v_cvt_pk_bf16_f32 v219, v214, v215\n"
"buffer_store_dwordx4 v[216:219], v8, s[44:47], s83 offen offset:1024 sc1\n"
"v_accvgpr_read_b32 v224, a136\n"
"v_accvgpr_read_b32 v225, a137\n"
"v_accvgpr_read_b32 v226, a138\n"
"v_accvgpr_read_b32 v227, a139\n"
"v_accvgpr_read_b32 v228, a168\n"
"v_accvgpr_read_b32 v229, a169\n"
"v_accvgpr_read_b32 v230, a170\n"
"v_accvgpr_read_b32 v231, a171\n"
"v_cvt_pk_bf16_f32 v232, v224, v225\n"
"v_cvt_pk_bf16_f32 v233, v226, v227\n"
"v_cvt_pk_bf16_f32 v234, v228, v229\n"
"v_cvt_pk_bf16_f32 v235, v230, v231\n"
"buffer_store_dwordx4 v[232:235], v8, s[44:47], s83 offen offset:2048 sc1\n"
"v_accvgpr_read_b32 v240, a200\n"
"v_accvgpr_read_b32 v241, a201\n"
"v_accvgpr_read_b32 v242, a202\n"
"v_accvgpr_read_b32 v243, a203\n"
"v_accvgpr_read_b32 v244, a232\n"
"v_accvgpr_read_b32 v245, a233\n"
"v_accvgpr_read_b32 v246, a234\n"
"v_accvgpr_read_b32 v247, a235\n"
"v_cvt_pk_bf16_f32 v248, v240, v241\n"
"v_cvt_pk_bf16_f32 v249, v242, v243\n"
"v_cvt_pk_bf16_f32 v250, v244, v245\n"
"v_cvt_pk_bf16_f32 v251, v246, v247\n"
"buffer_store_dwordx4 v[248:251], v8, s[44:47], s83 offen offset:3072 sc1\n"
"s_add_u32 s83, s83, 8192\n"
"v_accvgpr_read_b32 v192, a12\n"
"v_accvgpr_read_b32 v193, a13\n"
"v_accvgpr_read_b32 v194, a14\n"
"v_accvgpr_read_b32 v195, a15\n"
"v_accvgpr_read_b32 v196, a44\n"
"v_accvgpr_read_b32 v197, a45\n"
"v_accvgpr_read_b32 v198, a46\n"
"v_accvgpr_read_b32 v199, a47\n"
"v_cvt_pk_bf16_f32 v200, v192, v193\n"
"v_cvt_pk_bf16_f32 v201, v194, v195\n"
"v_cvt_pk_bf16_f32 v202, v196, v197\n"
"v_cvt_pk_bf16_f32 v203, v198, v199\n"
"buffer_store_dwordx4 v[200:203], v8, s[44:47], s83 offen offset:0 sc1\n"
"v_accvgpr_read_b32 v208, a76\n"
"v_accvgpr_read_b32 v209, a77\n"
"v_accvgpr_read_b32 v210, a78\n"
"v_accvgpr_read_b32 v211, a79\n"
"v_accvgpr_read_b32 v212, a108\n"
"v_accvgpr_read_b32 v213, a109\n"
"v_accvgpr_read_b32 v214, a110\n"
"v_accvgpr_read_b32 v215, a111\n"
"v_cvt_pk_bf16_f32 v216, v208, v209\n"
"v_cvt_pk_bf16_f32 v217, v210, v211\n"
"v_cvt_pk_bf16_f32 v218, v212, v213\n"
"v_cvt_pk_bf16_f32 v219, v214, v215\n"
"buffer_store_dwordx4 v[216:219], v8, s[44:47], s83 offen offset:1024 sc1\n"
"v_accvgpr_read_b32 v224, a140\n"
"v_accvgpr_read_b32 v225, a141\n"
"v_accvgpr_read_b32 v226, a142\n"
"v_accvgpr_read_b32 v227, a143\n"
"v_accvgpr_read_b32 v228, a172\n"
"v_accvgpr_read_b32 v229, a173\n"
"v_accvgpr_read_b32 v230, a174\n"
"v_accvgpr_read_b32 v231, a175\n"
"v_cvt_pk_bf16_f32 v232, v224, v225\n"
"v_cvt_pk_bf16_f32 v233, v226, v227\n"
"v_cvt_pk_bf16_f32 v234, v228, v229\n"
"v_cvt_pk_bf16_f32 v235, v230, v231\n"
"buffer_store_dwordx4 v[232:235], v8, s[44:47], s83 offen offset:2048 sc1\n"
"v_accvgpr_read_b32 v240, a204\n"
"v_accvgpr_read_b32 v241, a205\n"
"v_accvgpr_read_b32 v242, a206\n"
"v_accvgpr_read_b32 v243, a207\n"
"v_accvgpr_read_b32 v244, a236\n"
"v_accvgpr_read_b32 v245, a237\n"
"v_accvgpr_read_b32 v246, a238\n"
"v_accvgpr_read_b32 v247, a239\n"
"v_cvt_pk_bf16_f32 v248, v240, v241\n"
"v_cvt_pk_bf16_f32 v249, v242, v243\n"
"v_cvt_pk_bf16_f32 v250, v244, v245\n"
"v_cvt_pk_bf16_f32 v251, v246, v247\n"
"buffer_store_dwordx4 v[248:251], v8, s[44:47], s83 offen offset:3072 sc1\n"
"s_add_u32 s83, s83, 8192\n"
"v_accvgpr_read_b32 v192, a16\n"
"v_accvgpr_read_b32 v193, a17\n"
"v_accvgpr_read_b32 v194, a18\n"
"v_accvgpr_read_b32 v195, a19\n"
"v_accvgpr_read_b32 v196, a48\n"
"v_accvgpr_read_b32 v197, a49\n"
"v_accvgpr_read_b32 v198, a50\n"
"v_accvgpr_read_b32 v199, a51\n"
"v_cvt_pk_bf16_f32 v64, v192, v193\n"
"v_cvt_pk_bf16_f32 v65, v194, v195\n"
"v_cvt_pk_bf16_f32 v66, v196, v197\n"
"v_cvt_pk_bf16_f32 v67, v198, v199\n"
"v_accvgpr_read_b32 v208, a80\n"
"v_accvgpr_read_b32 v209, a81\n"
"v_accvgpr_read_b32 v210, a82\n"
"v_accvgpr_read_b32 v211, a83\n"
"v_accvgpr_read_b32 v212, a112\n"
"v_accvgpr_read_b32 v213, a113\n"
"v_accvgpr_read_b32 v214, a114\n"
"v_accvgpr_read_b32 v215, a115\n"
"v_cvt_pk_bf16_f32 v68, v208, v209\n"
"v_cvt_pk_bf16_f32 v69, v210, v211\n"
"v_cvt_pk_bf16_f32 v70, v212, v213\n"
"v_cvt_pk_bf16_f32 v71, v214, v215\n"
"v_accvgpr_read_b32 v224, a144\n"
"v_accvgpr_read_b32 v225, a145\n"
"v_accvgpr_read_b32 v226, a146\n"
"v_accvgpr_read_b32 v227, a147\n"
"v_accvgpr_read_b32 v228, a176\n"
"v_accvgpr_read_b32 v229, a177\n"
"v_accvgpr_read_b32 v230, a178\n"
"v_accvgpr_read_b32 v231, a179\n"
"v_cvt_pk_bf16_f32 v72, v224, v225\n"
"v_cvt_pk_bf16_f32 v73, v226, v227\n"
"v_cvt_pk_bf16_f32 v74, v228, v229\n"
"v_cvt_pk_bf16_f32 v75, v230, v231\n"
"v_accvgpr_read_b32 v240, a208\n"
"v_accvgpr_read_b32 v241, a209\n"
"v_accvgpr_read_b32 v242, a210\n"
"v_accvgpr_read_b32 v243, a211\n"
"v_accvgpr_read_b32 v244, a240\n"
"v_accvgpr_read_b32 v245, a241\n"
"v_accvgpr_read_b32 v246, a242\n"
"v_accvgpr_read_b32 v247, a243\n"
"v_cvt_pk_bf16_f32 v76, v240, v241\n"
"v_cvt_pk_bf16_f32 v77, v242, v243\n"
"v_cvt_pk_bf16_f32 v78, v244, v245\n"
"v_cvt_pk_bf16_f32 v79, v246, v247\n"
"v_accvgpr_read_b32 v192, a20\n"
"v_accvgpr_read_b32 v193, a21\n"
"v_accvgpr_read_b32 v194, a22\n"
"v_accvgpr_read_b32 v195, a23\n"
"v_accvgpr_read_b32 v196, a52\n"
"v_accvgpr_read_b32 v197, a53\n"
"v_accvgpr_read_b32 v198, a54\n"
"v_accvgpr_read_b32 v199, a55\n"
"v_cvt_pk_bf16_f32 v80, v192, v193\n"
"v_cvt_pk_bf16_f32 v81, v194, v195\n"
"v_cvt_pk_bf16_f32 v82, v196, v197\n"
"v_cvt_pk_bf16_f32 v83, v198, v199\n"
"v_accvgpr_read_b32 v208, a84\n"
"v_accvgpr_read_b32 v209, a85\n"
"v_accvgpr_read_b32 v210, a86\n"
"v_accvgpr_read_b32 v211, a87\n"
"v_accvgpr_read_b32 v212, a116\n"
"v_accvgpr_read_b32 v213, a117\n"
"v_accvgpr_read_b32 v214, a118\n"
"v_accvgpr_read_b32 v215, a119\n"
"v_cvt_pk_bf16_f32 v84, v208, v209\n"
"v_cvt_pk_bf16_f32 v85, v210, v211\n"
"v_cvt_pk_bf16_f32 v86, v212, v213\n"
"v_cvt_pk_bf16_f32 v87, v214, v215\n"
"v_accvgpr_read_b32 v224, a148\n"
"v_accvgpr_read_b32 v225, a149\n"
"v_accvgpr_read_b32 v226, a150\n"
"v_accvgpr_read_b32 v227, a151\n"
"v_accvgpr_read_b32 v228, a180\n"
"v_accvgpr_read_b32 v229, a181\n"
"v_accvgpr_read_b32 v230, a182\n"
"v_accvgpr_read_b32 v231, a183\n"
"v_cvt_pk_bf16_f32 v88, v224, v225\n"
"v_cvt_pk_bf16_f32 v89, v226, v227\n"
"v_cvt_pk_bf16_f32 v90, v228, v229\n"
"v_cvt_pk_bf16_f32 v91, v230, v231\n"
"v_accvgpr_read_b32 v240, a212\n"
"v_accvgpr_read_b32 v241, a213\n"
"v_accvgpr_read_b32 v242, a214\n"
"v_accvgpr_read_b32 v243, a215\n"
"v_accvgpr_read_b32 v244, a244\n"
"v_accvgpr_read_b32 v245, a245\n"
"v_accvgpr_read_b32 v246, a246\n"
"v_accvgpr_read_b32 v247, a247\n"
"v_cvt_pk_bf16_f32 v92, v240, v241\n"
"v_cvt_pk_bf16_f32 v93, v242, v243\n"
"v_cvt_pk_bf16_f32 v94, v244, v245\n"
"v_cvt_pk_bf16_f32 v95, v246, v247\n"
"v_accvgpr_read_b32 v192, a24\n"
"v_accvgpr_read_b32 v193, a25\n"
"v_accvgpr_read_b32 v194, a26\n"
"v_accvgpr_read_b32 v195, a27\n"
"v_accvgpr_read_b32 v196, a56\n"
"v_accvgpr_read_b32 v197, a57\n"
"v_accvgpr_read_b32 v198, a58\n"
"v_accvgpr_read_b32 v199, a59\n"
"v_cvt_pk_bf16_f32 v96, v192, v193\n"
"v_cvt_pk_bf16_f32 v97, v194, v195\n"
"v_cvt_pk_bf16_f32 v98, v196, v197\n"
"v_cvt_pk_bf16_f32 v99, v198, v199\n"
"v_accvgpr_read_b32 v208, a88\n"
"v_accvgpr_read_b32 v209, a89\n"
"v_accvgpr_read_b32 v210, a90\n"
"v_accvgpr_read_b32 v211, a91\n"
"v_accvgpr_read_b32 v212, a120\n"
"v_accvgpr_read_b32 v213, a121\n"
"v_accvgpr_read_b32 v214, a122\n"
"v_accvgpr_read_b32 v215, a123\n"
"v_cvt_pk_bf16_f32 v100, v208, v209\n"
"v_cvt_pk_bf16_f32 v101, v210, v211\n"
"v_cvt_pk_bf16_f32 v102, v212, v213\n"
"v_cvt_pk_bf16_f32 v103, v214, v215\n"
"v_accvgpr_read_b32 v224, a152\n"
"v_accvgpr_read_b32 v225, a153\n"
"v_accvgpr_read_b32 v226, a154\n"
"v_accvgpr_read_b32 v227, a155\n"
"v_accvgpr_read_b32 v228, a184\n"
"v_accvgpr_read_b32 v229, a185\n"
"v_accvgpr_read_b32 v230, a186\n"
"v_accvgpr_read_b32 v231, a187\n"
"v_cvt_pk_bf16_f32 v104, v224, v225\n"
"v_cvt_pk_bf16_f32 v105, v226, v227\n"
"v_cvt_pk_bf16_f32 v106, v228, v229\n"
"v_cvt_pk_bf16_f32 v107, v230, v231\n"
"v_accvgpr_read_b32 v240, a216\n"
"v_accvgpr_read_b32 v241, a217\n"
"v_accvgpr_read_b32 v242, a218\n"
"v_accvgpr_read_b32 v243, a219\n"
"v_accvgpr_read_b32 v244, a248\n"
"v_accvgpr_read_b32 v245, a249\n"
"v_accvgpr_read_b32 v246, a250\n"
"v_accvgpr_read_b32 v247, a251\n"
"v_cvt_pk_bf16_f32 v108, v240, v241\n"
"v_cvt_pk_bf16_f32 v109, v242, v243\n"
"v_cvt_pk_bf16_f32 v110, v244, v245\n"
"v_cvt_pk_bf16_f32 v111, v246, v247\n"
"v_accvgpr_read_b32 v192, a28\n"
"v_accvgpr_read_b32 v193, a29\n"
"v_accvgpr_read_b32 v194, a30\n"
"v_accvgpr_read_b32 v195, a31\n"
"v_accvgpr_read_b32 v196, a60\n"
"v_accvgpr_read_b32 v197, a61\n"
"v_accvgpr_read_b32 v198, a62\n"
"v_accvgpr_read_b32 v199, a63\n"
"v_cvt_pk_bf16_f32 v112, v192, v193\n"
"v_cvt_pk_bf16_f32 v113, v194, v195\n"
"v_cvt_pk_bf16_f32 v114, v196, v197\n"
"v_cvt_pk_bf16_f32 v115, v198, v199\n"
"v_accvgpr_read_b32 v208, a92\n"
"v_accvgpr_read_b32 v209, a93\n"
"v_accvgpr_read_b32 v210, a94\n"
"v_accvgpr_read_b32 v211, a95\n"
"v_accvgpr_read_b32 v212, a124\n"
"v_accvgpr_read_b32 v213, a125\n"
"v_accvgpr_read_b32 v214, a126\n"
"v_accvgpr_read_b32 v215, a127\n"
"v_cvt_pk_bf16_f32 v116, v208, v209\n"
"v_cvt_pk_bf16_f32 v117, v210, v211\n"
"v_cvt_pk_bf16_f32 v118, v212, v213\n"
"v_cvt_pk_bf16_f32 v119, v214, v215\n"
"v_accvgpr_read_b32 v224, a156\n"
"v_accvgpr_read_b32 v225, a157\n"
"v_accvgpr_read_b32 v226, a158\n"
"v_accvgpr_read_b32 v227, a159\n"
"v_accvgpr_read_b32 v228, a188\n"
"v_accvgpr_read_b32 v229, a189\n"
"v_accvgpr_read_b32 v230, a190\n"
"v_accvgpr_read_b32 v231, a191\n"
"v_cvt_pk_bf16_f32 v120, v224, v225\n"
"v_cvt_pk_bf16_f32 v121, v226, v227\n"
"v_cvt_pk_bf16_f32 v122, v228, v229\n"
"v_cvt_pk_bf16_f32 v123, v230, v231\n"
"v_accvgpr_read_b32 v240, a220\n"
"v_accvgpr_read_b32 v241, a221\n"
"v_accvgpr_read_b32 v242, a222\n"
"v_accvgpr_read_b32 v243, a223\n"
"v_accvgpr_read_b32 v244, a252\n"
"v_accvgpr_read_b32 v245, a253\n"
"v_accvgpr_read_b32 v246, a254\n"
"v_accvgpr_read_b32 v247, a255\n"
"v_cvt_pk_bf16_f32 v124, v240, v241\n"
"v_cvt_pk_bf16_f32 v125, v242, v243\n"
"v_cvt_pk_bf16_f32 v126, v244, v245\n"
"v_cvt_pk_bf16_f32 v127, v246, v247\n"
"s_mov_b32 s98, 8192\n"
"s_mov_b32 s99, 8192\n"
"s_mov_b32 s54, 1\n"
"s_mov_b32 s51, s67\n"
"s_mov_b32 s53, s68\n"
"s_mov_b32 s70, s69\n"
"s_mov_b32 s58, 0\n"
"s_mov_b32 s62, 0\n"
"s_branch L_K2_tile_%=\n"
"L_K_tobl_%=:\n"
"s_waitcnt vmcnt(0)\n"
"s_barrier\n"
"s_lshl_b32 s52, s73, 10\n"
"s_mov_b32 s48, s64\n"
"s_mov_b32 s49, 0\n"
"v_lshrrev_b32 v1, 3, v0\n"
"v_and_b32 v2, 7, v0\n"
"v_lshrrev_b32 v3, 4, v0\n"
"v_xor_b32 v2, v2, v3\n"
"v_lshlrev_b32 v2, 4, v2\n"
"v_mul_u32_u24 v1, 896, v1\n"
"v_add_u32 v1, v1, v2\n"
"s_add_u32 s76, s73, 0\n"
"s_lshl_b32 s77, s76, 3\n"
"s_cmp_ge_u32 s77, 144\n"
"s_cselect_b32 s78, 0x40000000, 0\n"
"s_add_u32 s77, s77, s68\n"
"s_mul_i32 s77, s77, 896\n"
"s_add_u32 s77, s77, s78\n"
"s_and_b32 s76, s76, 1\n"
"s_lshl_b32 s76, s76, 6\n"
"v_xor_b32 v32, s76, v1\n"
"v_add_u32 v32, s77, v32\n"
"s_add_u32 s76, s73, 4\n"
"s_lshl_b32 s77, s76, 3\n"
"s_cmp_ge_u32 s77, 144\n"
"s_cselect_b32 s78, 0x40000000, 0\n"
"s_add_u32 s77, s77, s68\n"
"s_mul_i32 s77, s77, 896\n"
"s_add_u32 s77, s77, s78\n"
"s_and_b32 s76, s76, 1\n"
"s_lshl_b32 s76, s76, 6\n"
"v_xor_b32 v33, s76, v1\n"
"v_add_u32 v33, s77, v33\n"
"s_add_u32 s76, s73, 8\n"
"s_lshl_b32 s77, s76, 3\n"
"s_cmp_ge_u32 s77, 144\n"
"s_cselect_b32 s78, 0x40000000, 0\n"
"s_add_u32 s77, s77, s68\n"
"s_mul_i32 s77, s77, 896\n"
"s_add_u32 s77, s77, s78\n"
"s_and_b32 s76, s76, 1\n"
"s_lshl_b32 s76, s76, 6\n"
"v_xor_b32 v34, s76, v1\n"
"v_add_u32 v34, s77, v34\n"
"s_add_u32 s76, s73, 12\n"
"s_lshl_b32 s77, s76, 3\n"
"s_cmp_ge_u32 s77, 144\n"
"s_cselect_b32 s78, 0x40000000, 0\n"
"s_add_u32 s77, s77, s68\n"
"s_mul_i32 s77, s77, 896\n"
"s_add_u32 s77, s77, s78\n"
"s_and_b32 s76, s76, 1\n"
"s_lshl_b32 s76, s76, 6\n"
"v_xor_b32 v35, s76, v1\n"
"v_add_u32 v35, s77, v35\n"
"s_add_u32 s76, s73, 16\n"
"s_lshl_b32 s77, s76, 3\n"
"s_cmp_ge_u32 s77, 144\n"
"s_cselect_b32 s78, 0x40000000, 0\n"
"s_add_u32 s77, s77, s68\n"
"s_mul_i32 s77, s77, 896\n"
"s_add_u32 s77, s77, s78\n"
"s_and_b32 s76, s76, 1\n"
"s_lshl_b32 s76, s76, 6\n"
"v_xor_b32 v36, s76, v1\n"
"v_add_u32 v36, s77, v36\n"
"s_add_u32 s76, s73, 0\n"
"s_lshl_b32 s76, s76, 2\n"
"v_lshrrev_b32 v1, 4, v0\n"
"v_add_u32 v1, s76, v1\n"
"v_mul_u32_u24 v2, 43, v1\n"
"v_lshrrev_b32 v2, 8, v2\n"
"v_mul_u32_u24 v3, 6, v2\n"
"v_sub_u32 v1, v1, v3\n"
"v_lshrrev_b32 v3, 1, v2\n"
"v_and_b32 v3, 1, v3\n"
"v_lshlrev_b32 v3, 1, v3\n"
"v_and_b32 v11, 3, v0\n"
"v_xor_b32 v3, v3, v11\n"
"v_lshl_add_u32 v1, v1, 2, v3\n"
"v_lshrrev_b32 v3, 2, v0\n"
"v_and_b32 v3, 3, v3\n"
"v_lshl_add_u32 v2, v2, 2, v3\n"
"v_mul_u32_u24 v2, 2688, v2\n"
"v_lshl_add_u32 v2, v1, 4, v2\n"
"s_mul_i32 s76, s71, 112\n"
"v_add_u32 v2, s76, v2\n"
"v_mov_b32 v3, 784\n"
"v_cmp_le_u32 vcc, 7, v1\n"
"s_nop 1\n"
"v_cndmask_b32 v11, 0, v3, vcc\n"
"v_add_u32 v2, v2, v11\n"
"v_cmp_le_u32 vcc, 14, v1\n"
"s_nop 1\n"
"v_cndmask_b32 v11, 0, v3, vcc\n"
"v_add_u32 v2, v2, v11\n"
"v_mov_b32 v3, 0x40000000\n"
"v_cmp_le_u32 vcc, 21, v1\n"
"s_nop 1\n"
"v_cndmask_b32 v11, 0, v3, vcc\n"
"v_add_u32 v40, v2, v11\n"
"s_add_u32 s76, s73, 4\n"
"s_lshl_b32 s76, s76, 2\n"
"v_lshrrev_b32 v1, 4, v0\n"
"v_add_u32 v1, s76, v1\n"
"v_mul_u32_u24 v2, 43, v1\n"
"v_lshrrev_b32 v2, 8, v2\n"
"v_mul_u32_u24 v3, 6, v2\n"
"v_sub_u32 v1, v1, v3\n"
"v_lshrrev_b32 v3, 1, v2\n"
"v_and_b32 v3, 1, v3\n"
"v_lshlrev_b32 v3, 1, v3\n"
"v_and_b32 v11, 3, v0\n"
"v_xor_b32 v3, v3, v11\n"
"v_lshl_add_u32 v1, v1, 2, v3\n"
"v_lshrrev_b32 v3, 2, v0\n"
"v_and_b32 v3, 3, v3\n"
"v_lshl_add_u32 v2, v2, 2, v3\n"
"v_mul_u32_u24 v2, 2688, v2\n"
"v_lshl_add_u32 v2, v1, 4, v2\n"
"s_mul_i32 s76, s71, 112\n"
"v_add_u32 v2, s76, v2\n"
"v_mov_b32 v3, 784\n"
"v_cmp_le_u32 vcc, 7, v1\n"
"s_nop 1\n"
"v_cndmask_b32 v11, 0, v3, vcc\n"
"v_add_u32 v2, v2, v11\n"
"v_cmp_le_u32 vcc, 14, v1\n"
"s_nop 1\n"
"v_cndmask_b32 v11, 0, v3, vcc\n"
"v_add_u32 v2, v2, v11\n"
"v_mov_b32 v3, 0x40000000\n"
"v_cmp_le_u32 vcc, 21, v1\n"
"s_nop 1\n"
"v_cndmask_b32 v11, 0, v3, vcc\n"
"v_add_u32 v41, v2, v11\n"
"s_add_u32 s76, s73, 8\n"
"s_lshl_b32 s76, s76, 2\n"
"v_lshrrev_b32 v1, 4, v0\n"
"v_add_u32 v1, s76, v1\n"
"v_mul_u32_u24 v2, 43, v1\n"
"v_lshrrev_b32 v2, 8, v2\n"
"v_mul_u32_u24 v3, 6, v2\n"
"v_sub_u32 v1, v1, v3\n"
"v_lshrrev_b32 v3, 1, v2\n"
"v_and_b32 v3, 1, v3\n"
"v_lshlrev_b32 v3, 1, v3\n"
"v_and_b32 v11, 3, v0\n"
"v_xor_b32 v3, v3, v11\n"
"v_lshl_add_u32 v1, v1, 2, v3\n"
"v_lshrrev_b32 v3, 2, v0\n"
"v_and_b32 v3, 3, v3\n"
"v_lshl_add_u32 v2, v2, 2, v3\n"
"v_mul_u32_u24 v2, 2688, v2\n"
"v_lshl_add_u32 v2, v1, 4, v2\n"
"s_mul_i32 s76, s71, 112\n"
"v_add_u32 v2, s76, v2\n"
"v_mov_b32 v3, 784\n"
"v_cmp_le_u32 vcc, 7, v1\n"
"s_nop 1\n"
"v_cndmask_b32 v11, 0, v3, vcc\n"
"v_add_u32 v2, v2, v11\n"
"v_cmp_le_u32 vcc, 14, v1\n"
"s_nop 1\n"
"v_cndmask_b32 v11, 0, v3, vcc\n"
"v_add_u32 v2, v2, v11\n"
"v_mov_b32 v3, 0x40000000\n"
"v_cmp_le_u32 vcc, 21, v1\n"
"s_nop 1\n"
"v_cndmask_b32 v11, 0, v3, vcc\n"
"v_add_u32 v42, v2, v11\n"
"s_add_u32 s76, s73, 12\n"
"s_lshl_b32 s76, s76, 2\n"
"v_lshrrev_b32 v1, 4, v0\n"
"v_add_u32 v1, s76, v1\n"
"v_mul_u32_u24 v2, 43, v1\n"
"v_lshrrev_b32 v2, 8, v2\n"
"v_mul_u32_u24 v3, 6, v2\n"
"v_sub_u32 v1, v1, v3\n"
"v_lshrrev_b32 v3, 1, v2\n"
"v_and_b32 v3, 1, v3\n"
"v_lshlrev_b32 v3, 1, v3\n"
"v_and_b32 v11, 3, v0\n"
"v_xor_b32 v3, v3, v11\n"
"v_lshl_add_u32 v1, v1, 2, v3\n"
"v_lshrrev_b32 v3, 2, v0\n"
"v_and_b32 v3, 3, v3\n"
"v_lshl_add_u32 v2, v2, 2, v3\n"
"v_mul_u32_u24 v2, 2688, v2\n"
"v_lshl_add_u32 v2, v1, 4, v2\n"
"s_mul_i32 s76, s71, 112\n"
"v_add_u32 v2, s76, v2\n"
"v_mov_b32 v3, 784\n"
"v_cmp_le_u32 vcc, 7, v1\n"
"s_nop 1\n"
"v_cndmask_b32 v11, 0, v3, vcc\n"
"v_add_u32 v2, v2, v11\n"
"v_cmp_le_u32 vcc, 14, v1\n"
"s_nop 1\n"
"v_cndmask_b32 v11, 0, v3, vcc\n"
"v_add_u32 v2, v2, v11\n"
"v_mov_b32 v3, 0x40000000\n"
"v_cmp_le_u32 vcc, 21, v1\n"
"s_nop 1\n"
"v_cndmask_b32 v11, 0, v3, vcc\n"
"v_add_u32 v43, v2, v11\n"
"s_add_u32 s76, s73, 16\n"
"s_lshl_b32 s76, s76, 2\n"
"v_lshrrev_b32 v1, 4, v0\n"
"v_add_u32 v1, s76, v1\n"
"v_mul_u32_u24 v2, 43, v1\n"
"v_lshrrev_b32 v2, 8, v2\n"
"v_mul_u32_u24 v3, 6, v2\n"
"v_sub_u32 v1, v1, v3\n"
"v_lshrrev_b32 v3, 1, v2\n"
"v_and_b32 v3, 1, v3\n"
"v_lshlrev_b32 v3, 1, v3\n"
"v_and_b32 v11, 3, v0\n"
"v_xor_b32 v3, v3, v11\n"
"v_lshl_add_u32 v1, v1, 2, v3\n"
"v_lshrrev_b32 v3, 2, v0\n"
"v_and_b32 v3, 3, v3\n"
"v_lshl_add_u32 v2, v2, 2, v3\n"
"v_mul_u32_u24 v2, 2688, v2\n"
"v_lshl_add_u32 v2, v1, 4, v2\n"
"s_mul_i32 s76, s71, 112\n"
"v_add_u32 v2, s76, v2\n"
"v_mov_b32 v3, 784\n"
"v_cmp_le_u32 vcc, 7, v1\n"
"s_nop 1\n"
"v_cndmask_b32 v11, 0, v3, vcc\n"
"v_add_u32 v2, v2, v11\n"
"v_cmp_le_u32 vcc, 14, v1\n"
"s_nop 1\n"
"v_cndmask_b32 v11, 0, v3, vcc\n"
"v_add_u32 v2, v2, v11\n"
"v_mov_b32 v3, 0x40000000\n"
"v_cmp_le_u32 vcc, 21, v1\n"
"s_nop 1\n"
"v_cndmask_b32 v11, 0, v3, vcc\n"
"v_add_u32 v44, v2, v11\n"
"s_add_u32 s76, s73, 20\n"
"s_lshl_b32 s76, s76, 2\n"
"v_lshrrev_b32 v1, 4, v0\n"
"v_add_u32 v1, s76, v1\n"
"v_mul_u32_u24 v2, 43, v1\n"
"v_lshrrev_b32 v2, 8, v2\n"
"v_mul_u32_u24 v3, 6, v2\n"
"v_sub_u32 v1, v1, v3\n"
"v_lshrrev_b32 v3, 1, v2\n"
"v_and_b32 v3, 1, v3\n"
"v_lshlrev_b32 v3, 1, v3\n"
"v_and_b32 v11, 3, v0\n"
"v_xor_b32 v3, v3, v11\n"
"v_lshl_add_u32 v1, v1, 2, v3\n"
"v_lshrrev_b32 v3, 2, v0\n"
"v_and_b32 v3, 3, v3\n"
"v_lshl_add_u32 v2, v2, 2, v3\n"
"v_mul_u32_u24 v2, 2688, v2\n"
"v_lshl_add_u32 v2, v1, 4, v2\n"
"s_mul_i32 s76, s71, 112\n"
"v_add_u32 v2, s76, v2\n"
"v_mov_b32 v3, 784\n"
"v_cmp_le_u32 vcc, 7, v1\n"
"s_nop 1\n"
"v_cndmask_b32 v11, 0, v3, vcc\n"
"v_add_u32 v2, v2, v11\n"
"v_cmp_le_u32 vcc, 14, v1\n"
"s_nop 1\n"
"v_cndmask_b32 v11, 0, v3, vcc\n"
"v_add_u32 v2, v2, v11\n"
"v_mov_b32 v3, 0x40000000\n"
"v_cmp_le_u32 vcc, 21, v1\n"
"s_nop 1\n"
"v_cndmask_b32 v11, 0, v3, vcc\n"
"v_add_u32 v45, v2, v11\n"
"v_and_b32 v1, 15, v0\n"
"v_lshrrev_b32 v2, 4, v0\n"
"v_lshrrev_b32 v3, 1, v1\n"
"v_xor_b32 v3, v3, v2\n"
"v_lshlrev_b32 v3, 4, v3\n"
"v_lshl_add_u32 v4, v1, 7, v3\n"
"v_mul_u32_u24 v3, 3072, v2\n"
"v_and_b32 v1, 1, v2\n"
"v_lshl_add_u32 v3, v1, 5, v3\n"
"v_lshrrev_b32 v1, 2, v0\n"
"v_and_b32 v1, 3, v1\n"
"v_lshl_add_u32 v3, v1, 6, v3\n"
"v_and_b32 v1, 3, v0\n"
"v_lshl_add_u32 v3, v1, 3, v3\n"
"v_add_u32 v3, 20480, v3\n"
"s_mul_i32 s76, s73, 3\n"
"s_add_u32 s76, s76, 0\n"
"s_lshr_b32 s77, s76, 1\n"
"s_lshl_b32 s77, s77, 8\n"
"s_and_b32 s76, s76, 1\n"
"s_lshl_b32 s76, s76, 5\n"
"v_xor_b32 v5, s76, v3\n"
"v_add_u32 v5, s77, v5\n"
"s_mul_i32 s76, s73, 3\n"
"s_add_u32 s76, s76, 1\n"
"s_lshr_b32 s77, s76, 1\n"
"s_lshl_b32 s77, s77, 8\n"
"s_and_b32 s76, s76, 1\n"
"s_lshl_b32 s76, s76, 5\n"
"v_xor_b32 v6, s76, v3\n"
"v_add_u32 v6, s77, v6\n"
"s_mul_i32 s76, s73, 3\n"
"s_add_u32 s76, s76, 2\n"
"s_lshr_b32 s77, s76, 1\n"
"s_lshl_b32 s77, s77, 8\n"
"s_and_b32 s76, s76, 1\n"
"s_lshl_b32 s76, s76, 5\n"
"v_xor_b32 v7, s76, v3\n"
"v_add_u32 v7, s77, v7\n"
"s_nop 15\n"
"s_mov_b32 s83, s53\n"
"v_accvgpr_read_b32 v192, a192\n"
"v_accvgpr_read_b32 v193, a193\n"
"v_accvgpr_read_b32 v194, a194\n"
"v_accvgpr_read_b32 v195, a195\n"
"v_accvgpr_read_b32 v196, a224\n"
"v_accvgpr_read_b32 v197, a225\n"
"v_accvgpr_read_b32 v198, a226\n"
"v_accvgpr_read_b32 v199, a227\n"
"v_cvt_pk_bf16_f32 v76, v192, v193\n"
"v_cvt_pk_bf16_f32 v77, v194, v195\n"
"v_cvt_pk_bf16_f32 v78, v196, v197\n"
"v_cvt_pk_bf16_f32 v79, v198, v199\n"
"s_add_u32 m0, s52, 0\n"
"s_nop 0\n"
"buffer_load_dwordx4 v32, s[56:59], s48 offen lds\n"
"v_accvgpr_read_b32 v208, a196\n"
"v_accvgpr_read_b32 v209, a197\n"
"v_accvgpr_read_b32 v210, a198\n"
"v_accvgpr_read_b32 v211, a199\n"
"v_accvgpr_read_b32 v212, a228\n"
"v_accvgpr_read_b32 v213, a229\n"
"v_accvgpr_read_b32 v214, a230\n"
"v_accvgpr_read_b32 v215, a231\n"
"v_cvt_pk_bf16_f32 v92, v208, v209\n"
"v_cvt_pk_bf16_f32 v93, v210, v211\n"
"v_cvt_pk_bf16_f32 v94, v212, v213\n"
"v_cvt_pk_bf16_f32 v95, v214, v215\n"
"s_add_u32 m0, s52, 4096\n"
"s_nop 0\n"
"buffer_load_dwordx4 v33, s[56:59], s48 offen lds\n"
"v_accvgpr_read_b32 v224, a200\n"
"v_accvgpr_read_b32 v225, a201\n"
"v_accvgpr_read_b32 v226, a202\n"
"v_accvgpr_read_b32 v227, a203\n"
"v_accvgpr_read_b32 v228, a232\n"
"v_accvgpr_read_b32 v229, a233\n"
"v_accvgpr_read_b32 v230, a234\n"
"v_accvgpr_read_b32 v231, a235\n"
"v_cvt_pk_bf16_f32 v108, v224, v225\n"
"v_cvt_pk_bf16_f32 v109, v226, v227\n"
"v_cvt_pk_bf16_f32 v110, v228, v229\n"
"v_cvt_pk_bf16_f32 v111, v230, v231\n"
"s_add_u32 m0, s52, 8192\n"
"s_nop 0\n"
"buffer_load_dwordx4 v34, s[56:59], s48 offen lds\n"
"v_accvgpr_read_b32 v240, a204\n"
"v_accvgpr_read_b32 v241, a205\n"
"v_accvgpr_read_b32 v242, a206\n"
"v_accvgpr_read_b32 v243, a207\n"
"v_accvgpr_read_b32 v244, a236\n"
"v_accvgpr_read_b32 v245, a237\n"
"v_accvgpr_read_b32 v246, a238\n"
"v_accvgpr_read_b32 v247, a239\n"
"v_cvt_pk_bf16_f32 v124, v240, v241\n"
"v_cvt_pk_bf16_f32 v125, v242, v243\n"
"v_cvt_pk_bf16_f32 v126, v244, v245\n"
"v_cvt_pk_bf16_f32 v127, v246, v247\n"
"s_add_u32 m0, s52, 12288\n"
"s_nop 0\n"
"buffer_load_dwordx4 v35, s[56:59], s48 offen lds\n"
"v_accvgpr_read_b32 v192, a208\n"
"v_accvgpr_read_b32 v193, a209\n"
"v_accvgpr_read_b32 v194, a210\n"
"v_accvgpr_read_b32 v195, a211\n"
"v_accvgpr_read_b32 v196, a240\n"
"v_accvgpr_read_b32 v197, a241\n"
"v_accvgpr_read_b32 v198, a242\n"
"v_accvgpr_read_b32 v199, a243\n"
"v_cvt_pk_bf16_f32 v128, v192, v193\n"
"v_cvt_pk_bf16_f32 v129, v194, v195\n"
"v_cvt_pk_bf16_f32 v130, v196, v197\n"
"v_cvt_pk_bf16_f32 v131, v198, v199\n"
"s_add_u32 m0, s52, 16384\n"
"s_nop 0\n"
"buffer_load_dwordx4 v36, s[56:59], s48 offen lds\n"
"v_accvgpr_read_b32 v208, a212\n"
"v_accvgpr_read_b32 v209, a213\n"
"v_accvgpr_read_b32 v210, a214\n"
"v_accvgpr_read_b32 v211, a215\n"
"v_accvgpr_read_b32 v212, a244\n"
"v_accvgpr_read_b32 v213, a245\n"
"v_accvgpr_read_b32 v214, a246\n"
"v_accvgpr_read_b32 v215, a247\n"
"v_cvt_pk_bf16_f32 v132, v208, v209\n"
"v_cvt_pk_bf16_f32 v133, v210, v211\n"
"v_cvt_pk_bf16_f32 v134, v212, v213\n"
"v_cvt_pk_bf16_f32 v135, v214, v215\n"
"s_add_u32 m0, s52, 20480\n"
"s_nop 0\n"
"buffer_load_dwordx4 v40, s[60:63], s49 offen lds\n"
"v_accvgpr_read_b32 v224, a216\n"
"v_accvgpr_read_b32 v225, a217\n"
"v_accvgpr_read_b32 v226, a218\n"
"v_accvgpr_read_b32 v227, a219\n"
"v_accvgpr_read_b32 v228, a248\n"
"v_accvgpr_read_b32 v229, a249\n"
"v_accvgpr_read_b32 v230, a250\n"
"v_accvgpr_read_b32 v231, a251\n"
"v_cvt_pk_bf16_f32 v136, v224, v225\n"
"v_cvt_pk_bf16_f32 v137, v226, v227\n"
"v_cvt_pk_bf16_f32 v138, v228, v229\n"
"v_cvt_pk_bf16_f32 v139, v230, v231\n"
"s_add_u32 m0, s52, 24576\n"
"s_nop 0\n"
"buffer_load_dwordx4 v41, s[60:63], s49 offen lds\n"
"v_accvgpr_read_b32 v240, a220\n"
"v_accvgpr_read_b32 v241, a221\n"
"v_accvgpr_read_b32 v242, a222\n"
"v_accvgpr_read_b32 v243, a223\n"
"v_accvgpr_read_b32 v244, a252\n"
"v_accvgpr_read_b32 v245, a253\n"
"v_accvgpr_read_b32 v246, a254\n"
"v_accvgpr_read_b32 v247, a255\n"
"v_cvt_pk_bf16_f32 v140, v240, v241\n"
"v_cvt_pk_bf16_f32 v141, v242, v243\n"
"v_cvt_pk_bf16_f32 v142, v244, v245\n"
"v_cvt_pk_bf16_f32 v143, v246, v247\n"
"s_add_u32 m0, s52, 28672\n"
"s_nop 0\n"
"buffer_load_dwordx4 v42, s[60:63], s49 offen lds\n"
"v_accvgpr_write_b32 a204, v128\n"
"v_accvgpr_write_b32 a205, v129\n"
"v_accvgpr_write_b32 a206, v130\n"
"v_accvgpr_write_b32 a207, v131\n"
"v_accvgpr_write_b32 a220, v132\n"
"v_accvgpr_write_b32 a221, v133\n"
"v_accvgpr_write_b32 a222, v134\n"
"v_accvgpr_write_b32 a223, v135\n"
"v_accvgpr_write_b32 a236, v136\n"
"v_accvgpr_write_b32 a237, v137\n"
"v_accvgpr_write_b32 a238, v138\n"
"v_accvgpr_write_b32 a239, v139\n"
"v_accvgpr_write_b32 a252, v140\n"
"v_accvgpr_write_b32 a253, v141\n"
"v_accvgpr_write_b32 a254, v142\n"
"v_accvgpr_write_b32 a255, v143\n"
"v_accvgpr_read_b32 v192, a0\n"
"v_accvgpr_read_b32 v193, a1\n"
"v_accvgpr_read_b32 v194, a2\n"
"v_accvgpr_read_b32 v195, a3\n"
"v_accvgpr_read_b32 v196, a32\n"
"v_accvgpr_read_b32 v197, a33\n"
"v_accvgpr_read_b32 v198, a34\n"
"v_accvgpr_read_b32 v199, a35\n"
"v_cvt_pk_bf16_f32 v64, v192, v193\n"
"v_cvt_pk_bf16_f32 v65, v194, v195\n"
"v_cvt_pk_bf16_f32 v66, v196, v197\n"
"v_cvt_pk_bf16_f32 v67, v198, v199\n"
"s_add_u32 m0, s52, 32768\n"
"s_nop 0\n"
"buffer_load_dwordx4 v43, s[60:63], s49 offen lds\n"
"v_accvgpr_read_b32 v208, a64\n"
"v_accvgpr_read_b32 v209, a65\n"
"v_accvgpr_read_b32 v210, a66\n"
"v_accvgpr_read_b32 v211, a67\n"
"v_accvgpr_read_b32 v212, a96\n"
"v_accvgpr_read_b32 v213, a97\n"
"v_accvgpr_read_b32 v214, a98\n"
"v_accvgpr_read_b32 v215, a99\n"
"v_cvt_pk_bf16_f32 v68, v208, v209\n"
"v_cvt_pk_bf16_f32 v69, v210, v211\n"
"v_cvt_pk_bf16_f32 v70, v212, v213\n"
"v_cvt_pk_bf16_f32 v71, v214, v215\n"
"s_add_u32 m0, s52, 36864\n"
"s_nop 0\n"
"buffer_load_dwordx4 v44, s[60:63], s49 offen lds\n"
"v_accvgpr_read_b32 v224, a128\n"
"v_accvgpr_read_b32 v225, a129\n"
"v_accvgpr_read_b32 v226, a130\n"
"v_accvgpr_read_b32 v227, a131\n"
"v_accvgpr_read_b32 v228, a160\n"
"v_accvgpr_read_b32 v229, a161\n"
"v_accvgpr_read_b32 v230, a162\n"
"v_accvgpr_read_b32 v231, a163\n"
"v_cvt_pk_bf16_f32 v72, v224, v225\n"
"v_cvt_pk_bf16_f32 v73, v226, v227\n"
"v_cvt_pk_bf16_f32 v74, v228, v229\n"
"v_cvt_pk_bf16_f32 v75, v230, v231\n"
"s_add_u32 m0, s52, 40960\n"
"s_nop 0\n"
"buffer_load_dwordx4 v45, s[60:63], s49 offen lds\n"
"s_add_u32 s48, s48, 128\n"
"s_add_u32 s49, s49, 172032\n"
"v_accvgpr_read_b32 v240, a4\n"
"v_accvgpr_read_b32 v241, a5\n"
"v_accvgpr_read_b32 v242, a6\n"
"v_accvgpr_read_b32 v243, a7\n"
"v_accvgpr_read_b32 v244, a36\n"
"v_accvgpr_read_b32 v245, a37\n"
"v_accvgpr_read_b32 v246, a38\n"
"v_accvgpr_read_b32 v247, a39\n"
"v_cvt_pk_bf16_f32 v80, v240, v241\n"
"v_cvt_pk_bf16_f32 v81, v242, v243\n"
"v_cvt_pk_bf16_f32 v82, v244, v245\n"
"v_cvt_pk_bf16_f32 v83, v246, v247\n"
"s_add_u32 m0, s52, 45056\n"
"s_nop 0\n"
"buffer_load_dwordx4 v32, s[56:59], s48 offen lds\n"
"v_accvgpr_read_b32 v192, a68\n"
"v_accvgpr_read_b32 v193, a69\n"
"v_accvgpr_read_b32 v194, a70\n"
"v_accvgpr_read_b32 v195, a71\n"
"v_accvgpr_read_b32 v196, a100\n"
"v_accvgpr_read_b32 v197, a101\n"
"v_accvgpr_read_b32 v198, a102\n"
"v_accvgpr_read_b32 v199, a103\n"
"v_cvt_pk_bf16_f32 v84, v192, v193\n"
"v_cvt_pk_bf16_f32 v85, v194, v195\n"
"v_cvt_pk_bf16_f32 v86, v196, v197\n"
"v_cvt_pk_bf16_f32 v87, v198, v199\n"
"s_add_u32 m0, s52, 49152\n"
"s_nop 0\n"
"buffer_load_dwordx4 v33, s[56:59], s48 offen lds\n"
"v_accvgpr_read_b32 v208, a132\n"
"v_accvgpr_read_b32 v209, a133\n"
"v_accvgpr_read_b32 v210, a134\n"
"v_accvgpr_read_b32 v211, a135\n"
"v_accvgpr_read_b32 v212, a164\n"
"v_accvgpr_read_b32 v213, a165\n"
"v_accvgpr_read_b32 v214, a166\n"
"v_accvgpr_read_b32 v215, a167\n"
"v_cvt_pk_bf16_f32 v88, v208, v209\n"
"v_cvt_pk_bf16_f32 v89, v210, v211\n"
"v_cvt_pk_bf16_f32 v90, v212, v213\n"
"v_cvt_pk_bf16_f32 v91, v214, v215\n"
"s_add_u32 m0, s52, 53248\n"
"s_nop 0\n"
"buffer_load_dwordx4 v34, s[56:59], s48 offen lds\n"
"v_accvgpr_read_b32 v224, a8\n"
"v_accvgpr_read_b32 v225, a9\n"
"v_accvgpr_read_b32 v226, a10\n"
"v_accvgpr_read_b32 v227, a11\n"
"v_accvgpr_read_b32 v228, a40\n"
"v_accvgpr_read_b32 v229, a41\n"
"v_accvgpr_read_b32 v230, a42\n"
"v_accvgpr_read_b32 v231, a43\n"
"v_cvt_pk_bf16_f32 v96, v224, v225\n"
"v_cvt_pk_bf16_f32 v97, v226, v227\n"
"v_cvt_pk_bf16_f32 v98, v228, v229\n"
"v_cvt_pk_bf16_f32 v99, v230, v231\n"
"s_add_u32 m0, s52, 57344\n"
"s_nop 0\n"
"buffer_load_dwordx4 v35, s[56:59], s48 offen lds\n"
"v_accvgpr_read_b32 v240, a72\n"
"v_accvgpr_read_b32 v241, a73\n"
"v_accvgpr_read_b32 v242, a74\n"
"v_accvgpr_read_b32 v243, a75\n"
"v_accvgpr_read_b32 v244, a104\n"
"v_accvgpr_read_b32 v245, a105\n"
"v_accvgpr_read_b32 v246, a106\n"
"v_accvgpr_read_b32 v247, a107\n"
"v_cvt_pk_bf16_f32 v100, v240, v241\n"
"v_cvt_pk_bf16_f32 v101, v242, v243\n"
"v_cvt_pk_bf16_f32 v102, v244, v245\n"
"v_cvt_pk_bf16_f32 v103, v246, v247\n"
"s_add_u32 m0, s52, 61440\n"
"s_nop 0\n"
"buffer_load_dwordx4 v36, s[56:59], s48 offen lds\n"
"v_accvgpr_read_b32 v192, a136\n"
"v_accvgpr_read_b32 v193, a137\n"
"v_accvgpr_read_b32 v194, a138\n"
"v_accvgpr_read_b32 v195, a139\n"
"v_accvgpr_read_b32 v196, a168\n"
"v_accvgpr_read_b32 v197, a169\n"
"v_accvgpr_read_b32 v198, a170\n"
"v_accvgpr_read_b32 v199, a171\n"
"v_cvt_pk_bf16_f32 v104, v192, v193\n"
"v_cvt_pk_bf16_f32 v105, v194, v195\n"
"v_cvt_pk_bf16_f32 v106, v196, v197\n"
"v_cvt_pk_bf16_f32 v107, v198, v199\n"
"s_add_u32 m0, s52, 65536\n"
"s_nop 0\n"
"buffer_load_dwordx4 v40, s[60:63], s49 offen lds\n"
"v_accvgpr_read_b32 v208, a12\n"
"v_accvgpr_read_b32 v209, a13\n"
"v_accvgpr_read_b32 v210, a14\n"
"v_accvgpr_read_b32 v211, a15\n"
"v_accvgpr_read_b32 v212, a44\n"
"v_accvgpr_read_b32 v213, a45\n"
"v_accvgpr_read_b32 v214, a46\n"
"v_accvgpr_read_b32 v215, a47\n"
"v_cvt_pk_bf16_f32 v112, v208, v209\n"
"v_cvt_pk_bf16_f32 v113, v210, v211\n"
"v_cvt_pk_bf16_f32 v114, v212, v213\n"
"v_cvt_pk_bf16_f32 v115, v214, v215\n"
"s_add_u32 m0, s52, 69632\n"
"s_nop 0\n"
"buffer_load_dwordx4 v41, s[60:63], s49 offen lds\n"
"v_accvgpr_read_b32 v224, a76\n"
"v_accvgpr_read_b32 v225, a77\n"
"v_accvgpr_read_b32 v226, a78\n"
"v_accvgpr_read_b32 v227, a79\n"
"v_accvgpr_read_b32 v228, a108\n"
"v_accvgpr_read_b32 v229, a109\n"
"v_accvgpr_read_b32 v230, a110\n"
"v_accvgpr_read_b32 v231, a111\n"
"v_cvt_pk_bf16_f32 v116, v224, v225\n"
"v_cvt_pk_bf16_f32 v117, v226, v227\n"
"v_cvt_pk_bf16_f32 v118, v228, v229\n"
"v_cvt_pk_bf16_f32 v119, v230, v231\n"
"s_add_u32 m0, s52, 73728\n"
"s_nop 0\n"
"buffer_load_dwordx4 v42, s[60:63], s49 offen lds\n"
"v_accvgpr_read_b32 v240, a140\n"
"v_accvgpr_read_b32 v241, a141\n"
"v_accvgpr_read_b32 v242, a142\n"
"v_accvgpr_read_b32 v243, a143\n"
"v_accvgpr_read_b32 v244, a172\n"
"v_accvgpr_read_b32 v245, a173\n"
"v_accvgpr_read_b32 v246, a174\n"
"v_accvgpr_read_b32 v247, a175\n"
"v_cvt_pk_bf16_f32 v120, v240, v241\n"
"v_cvt_pk_bf16_f32 v121, v242, v243\n"
"v_cvt_pk_bf16_f32 v122, v244, v245\n"
"v_cvt_pk_bf16_f32 v123, v246, v247\n"
"s_add_u32 m0, s52, 77824\n"
"s_nop 0\n"
"buffer_load_dwordx4 v43, s[60:63], s49 offen lds\n"
"v_accvgpr_read_b32 v192, a16\n"
"v_accvgpr_read_b32 v193, a17\n"
"v_accvgpr_read_b32 v194, a18\n"
"v_accvgpr_read_b32 v195, a19\n"
"v_accvgpr_read_b32 v196, a48\n"
"v_accvgpr_read_b32 v197, a49\n"
"v_accvgpr_read_b32 v198, a50\n"
"v_accvgpr_read_b32 v199, a51\n"
"v_cvt_pk_bf16_f32 v200, v192, v193\n"
"v_cvt_pk_bf16_f32 v201, v194, v195\n"
"v_cvt_pk_bf16_f32 v202, v196, v197\n"
"v_cvt_pk_bf16_f32 v203, v198, v199\n"
"s_add_u32 m0, s52, 81920\n"
"s_nop 0\n"
"buffer_load_dwordx4 v44, s[60:63], s49 offen lds\n"
"v_accvgpr_write_b32 a192, v200\n"
"v_accvgpr_write_b32 a193, v201\n"
"v_accvgpr_write_b32 a194, v202\n"
"v_accvgpr_write_b32 a195, v203\n"
"v_accvgpr_read_b32 v208, a80\n"
"v_accvgpr_read_b32 v209, a81\n"
"v_accvgpr_read_b32 v210, a82\n"
"v_accvgpr_read_b32 v211, a83\n"
"v_accvgpr_read_b32 v212, a112\n"
"v_accvgpr_read_b32 v213, a113\n"
"v_accvgpr_read_b32 v214, a114\n"
"v_accvgpr_read_b32 v215, a115\n"
"v_cvt_pk_bf16_f32 v216, v208, v209\n"
"v_cvt_pk_bf16_f32 v217, v210, v211\n"
"v_cvt_pk_bf16_f32 v218, v212, v213\n"
"v_cvt_pk_bf16_f32 v219, v214, v215\n"
"s_add_u32 m0, s52, 86016\n"
"s_nop 0\n"
"buffer_load_dwordx4 v45, s[60:63], s49 offen lds\n"
"s_add_u32 s48, s48, 128\n"
"s_add_u32 s49, s49, 172032\n"
"v_accvgpr_write_b32 a196, v216\n"
"v_accvgpr_write_b32 a197, v217\n"
"v_accvgpr_write_b32 a198, v218\n"
"v_accvgpr_write_b32 a199, v219\n"
"v_accvgpr_read_b32 v224, a144\n"
"v_accvgpr_read_b32 v225, a145\n"
"v_accvgpr_read_b32 v226, a146\n"
"v_accvgpr_read_b32 v227, a147\n"
"v_accvgpr_read_b32 v228, a176\n"
"v_accvgpr_read_b32 v229, a177\n"
"v_accvgpr_read_b32 v230, a178\n"
"v_accvgpr_read_b32 v231, a179\n"
"v_cvt_pk_bf16_f32 v232, v224, v225\n"
"v_cvt_pk_bf16_f32 v233, v226, v227\n"
"v_cvt_pk_bf16_f32 v234, v228, v229\n"
"v_cvt_pk_bf16_f32 v235, v230, v231\n"
"s_add_u32 m0, s52, 90112\n"
"s_nop 0\n"
"buffer_load_dwordx4 v32, s[56:59], s48 offen lds\n"
"v_accvgpr_write_b32 a200, v232\n"
"v_accvgpr_write_b32 a201, v233\n"
"v_accvgpr_write_b32 a202, v234\n"
"v_accvgpr_write_b32 a203, v235\n"
"v_accvgpr_read_b32 v240, a20\n"
"v_accvgpr_read_b32 v241, a21\n"
"v_accvgpr_read_b32 v242, a22\n"
"v_accvgpr_read_b32 v243, a23\n"
"v_accvgpr_read_b32 v244, a52\n"
"v_accvgpr_read_b32 v245, a53\n"
"v_accvgpr_read_b32 v246, a54\n"
"v_accvgpr_read_b32 v247, a55\n"
"v_cvt_pk_bf16_f32 v248, v240, v241\n"
"v_cvt_pk_bf16_f32 v249, v242, v243\n"
"v_cvt_pk_bf16_f32 v250, v244, v245\n"
"v_cvt_pk_bf16_f32 v251, v246, v247\n"
"s_add_u32 m0, s52, 94208\n"
"s_nop 0\n"
"buffer_load_dwordx4 v33, s[56:59], s48 offen lds\n"
"v_accvgpr_write_b32 a208, v248\n"
"v_accvgpr_write_b32 a209, v249\n"
"v_accvgpr_write_b32 a210, v250\n"
"v_accvgpr_write_b32 a211, v251\n"
"v_accvgpr_read_b32 v192, a84\n"
"v_accvgpr_read_b32 v193, a85\n"
"v_accvgpr_read_b32 v194, a86\n"
"v_accvgpr_read_b32 v195, a87\n"
"v_accvgpr_read_b32 v196, a116\n"
"v_accvgpr_read_b32 v197, a117\n"
"v_accvgpr_read_b32 v198, a118\n"
"v_accvgpr_read_b32 v199, a119\n"
"v_cvt_pk_bf16_f32 v200, v192, v193\n"
"v_cvt_pk_bf16_f32 v201, v194, v195\n"
"v_cvt_pk_bf16_f32 v202, v196, v197\n"
"v_cvt_pk_bf16_f32 v203, v198, v199\n"
"s_add_u32 m0, s52, 98304\n"
"s_nop 0\n"
"buffer_load_dwordx4 v34, s[56:59], s48 offen lds\n"
"v_accvgpr_write_b32 a212, v200\n"
"v_accvgpr_write_b32 a213, v201\n"
"v_accvgpr_write_b32 a214, v202\n"
"v_accvgpr_write_b32 a215, v203\n"
"v_accvgpr_read_b32 v208, a148\n"
"v_accvgpr_read_b32 v209, a149\n"
"v_accvgpr_read_b32 v210, a150\n"
"v_accvgpr_read_b32 v211, a151\n"
"v_accvgpr_read_b32 v212, a180\n"
"v_accvgpr_read_b32 v213, a181\n"
"v_accvgpr_read_b32 v214, a182\n"
"v_accvgpr_read_b32 v215, a183\n"
"v_cvt_pk_bf16_f32 v216, v208, v209\n"
"v_cvt_pk_bf16_f32 v217, v210, v211\n"
"v_cvt_pk_bf16_f32 v218, v212, v213\n"
"v_cvt_pk_bf16_f32 v219, v214, v215\n"
"s_add_u32 m0, s52, 102400\n"
"s_nop 0\n"
"buffer_load_dwordx4 v35, s[56:59], s48 offen lds\n"
"v_accvgpr_write_b32 a216, v216\n"
"v_accvgpr_write_b32 a217, v217\n"
"v_accvgpr_write_b32 a218, v218\n"
"v_accvgpr_write_b32 a219, v219\n"
"v_accvgpr_read_b32 v224, a24\n"
"v_accvgpr_read_b32 v225, a25\n"
"v_accvgpr_read_b32 v226, a26\n"
"v_accvgpr_read_b32 v227, a27\n"
"v_accvgpr_read_b32 v228, a56\n"
"v_accvgpr_read_b32 v229, a57\n"
"v_accvgpr_read_b32 v230, a58\n"
"v_accvgpr_read_b32 v231, a59\n"
"v_cvt_pk_bf16_f32 v232, v224, v225\n"
"v_cvt_pk_bf16_f32 v233, v226, v227\n"
"v_cvt_pk_bf16_f32 v234, v228, v229\n"
"v_cvt_pk_bf16_f32 v235, v230, v231\n"
"s_add_u32 m0, s52, 106496\n"
"s_nop 0\n"
"buffer_load_dwordx4 v36, s[56:59], s48 offen lds\n"
"v_accvgpr_write_b32 a224, v232\n"
"v_accvgpr_write_b32 a225, v233\n"
"v_accvgpr_write_b32 a226, v234\n"
"v_accvgpr_write_b32 a227, v235\n"
"v_accvgpr_read_b32 v240, a88\n"
"v_accvgpr_read_b32 v241, a89\n"
"v_accvgpr_read_b32 v242, a90\n"
"v_accvgpr_read_b32 v243, a91\n"
"v_accvgpr_read_b32 v244, a120\n"
"v_accvgpr_read_b32 v245, a121\n"
"v_accvgpr_read_b32 v246, a122\n"
"v_accvgpr_read_b32 v247, a123\n"
"v_cvt_pk_bf16_f32 v248, v240, v241\n"
"v_cvt_pk_bf16_f32 v249, v242, v243\n"
"v_cvt_pk_bf16_f32 v250, v244, v245\n"
"v_cvt_pk_bf16_f32 v251, v246, v247\n"
"s_add_u32 m0, s52, 110592\n"
"s_nop 0\n"
"buffer_load_dwordx4 v40, s[60:63], s49 offen lds\n"
"v_accvgpr_write_b32 a228, v248\n"
"v_accvgpr_write_b32 a229, v249\n"
"v_accvgpr_write_b32 a230, v250\n"
"v_accvgpr_write_b32 a231, v251\n"
"v_accvgpr_read_b32 v192, a152\n"
"v_accvgpr_read_b32 v193, a153\n"
"v_accvgpr_read_b32 v194, a154\n"
"v_accvgpr_read_b32 v195, a155\n"
"v_accvgpr_read_b32 v196, a184\n"
"v_accvgpr_read_b32 v197, a185\n"
"v_accvgpr_read_b32 v198, a186\n"
"v_accvgpr_read_b32 v199, a187\n"
"v_cvt_pk_bf16_f32 v200, v192, v193\n"
"v_cvt_pk_bf16_f32 v201, v194, v195\n"
"v_cvt_pk_bf16_f32 v202, v196, v197\n"
"v_cvt_pk_bf16_f32 v203, v198, v199\n"
"s_add_u32 m0, s52, 114688\n"
"s_nop 0\n"
"buffer_load_dwordx4 v41, s[60:63], s49 offen lds\n"
"v_accvgpr_write_b32 a232, v200\n"
"v_accvgpr_write_b32 a233, v201\n"
"v_accvgpr_write_b32 a234, v202\n"
"v_accvgpr_write_b32 a235, v203\n"
"v_accvgpr_read_b32 v208, a28\n"
"v_accvgpr_read_b32 v209, a29\n"
"v_accvgpr_read_b32 v210, a30\n"
"v_accvgpr_read_b32 v211, a31\n"
"v_accvgpr_read_b32 v212, a60\n"
"v_accvgpr_read_b32 v213, a61\n"
"v_accvgpr_read_b32 v214, a62\n"
"v_accvgpr_read_b32 v215, a63\n"
"v_cvt_pk_bf16_f32 v216, v208, v209\n"
"v_cvt_pk_bf16_f32 v217, v210, v211\n"
"v_cvt_pk_bf16_f32 v218, v212, v213\n"
"v_cvt_pk_bf16_f32 v219, v214, v215\n"
"s_add_u32 m0, s52, 118784\n"
"s_nop 0\n"
"buffer_load_dwordx4 v42, s[60:63], s49 offen lds\n"
"v_accvgpr_write_b32 a240, v216\n"
"v_accvgpr_write_b32 a241, v217\n"
"v_accvgpr_write_b32 a242, v218\n"
"v_accvgpr_write_b32 a243, v219\n"
"v_accvgpr_read_b32 v224, a92\n"
"v_accvgpr_read_b32 v225, a93\n"
"v_accvgpr_read_b32 v226, a94\n"
"v_accvgpr_read_b32 v227, a95\n"
"v_accvgpr_read_b32 v228, a124\n"
"v_accvgpr_read_b32 v229, a125\n"
"v_accvgpr_read_b32 v230, a126\n"
"v_accvgpr_read_b32 v231, a127\n"
"v_cvt_pk_bf16_f32 v232, v224, v225\n"
"v_cvt_pk_bf16_f32 v233, v226, v227\n"
"v_cvt_pk_bf16_f32 v234, v228, v229\n"
"v_cvt_pk_bf16_f32 v235, v230, v231\n"
"s_add_u32 m0, s52, 122880\n"
"s_nop 0\n"
"buffer_load_dwordx4 v43, s[60:63], s49 offen lds\n"
"v_accvgpr_write_b32 a244, v232\n"
"v_accvgpr_write_b32 a245, v233\n"
"v_accvgpr_write_b32 a246, v234\n"
"v_accvgpr_write_b32 a247, v235\n"
"v_accvgpr_read_b32 v240, a156\n"
"v_accvgpr_read_b32 v241, a157\n"
"v_accvgpr_read_b32 v242, a158\n"
"v_accvgpr_read_b32 v243, a159\n"
"v_accvgpr_read_b32 v244, a188\n"
"v_accvgpr_read_b32 v245, a189\n"
"v_accvgpr_read_b32 v246, a190\n"
"v_accvgpr_read_b32 v247, a191\n"
"v_cvt_pk_bf16_f32 v248, v240, v241\n"
"v_cvt_pk_bf16_f32 v249, v242, v243\n"
"v_cvt_pk_bf16_f32 v250, v244, v245\n"
"v_cvt_pk_bf16_f32 v251, v246, v247\n"
"s_add_u32 m0, s52, 126976\n"
"s_nop 0\n"
"buffer_load_dwordx4 v44, s[60:63], s49 offen lds\n"
"v_accvgpr_write_b32 a248, v248\n"
"v_accvgpr_write_b32 a249, v249\n"
"v_accvgpr_write_b32 a250, v250\n"
"v_accvgpr_write_b32 a251, v251\n"
"s_add_u32 m0, s52, 131072\n"
"s_nop 0\n"
"buffer_load_dwordx4 v45, s[60:63], s49 offen lds\n"
"s_add_u32 s48, s48, 128\n"
"s_add_u32 s49, s49, 172032\n"
"s_mov_b32 s98, 8192\n"
"s_mov_b32 s99, 8192\n"
"s_branch L_B_tile_%=\n"
"L_K2_tile_%=:\n"
"v_mfma_f32_16x16x32_bf16 a[0:3], v[160:163], v[128:131], 0\n"
"v_mfma_f32_16x16x32_bf16 a[4:7], v[160:163], v[132:135], 0\n"
"ds_read_b128 v[192:195], v5 offset:0\n"
"v_mfma_f32_16x16x32_bf16 a[8:11], v[160:163], v[136:139], 0\n"
"v_mfma_f32_16x16x32_bf16 a[12:15], v[160:163], v[140:143], 0\n"
"ds_read_b64_tr_b16 v[224:225], v6 offset:16384\n"
"v_mfma_f32_16x16x32_bf16 a[16:19], v[160:163], v[144:147], 0\n"
"v_mfma_f32_16x16x32_bf16 a[20:23], v[160:163], v[148:151], 0\n"
"ds_read_b64_tr_b16 v[226:227], v6 offset:18432\n"
"v_mfma_f32_16x16x32_bf16 a[24:27], v[160:163], v[152:155], 0\n"
"buffer_store_dwordx4 v[64:67], v8, s[44:47], s83 offen offset:0 sc1\n"
"v_mfma_f32_16x16x32_bf16 a[28:31], v[160:163], v[156:159], 0\n"
"ds_read_b128 v[196:199], v5 offset:2048\n"
"v_mfma_f32_16x16x32_bf16 a[32:35], v[164:167], v[128:131], 0\n"
"v_mfma_f32_16x16x32_bf16 a[36:39], v[164:167], v[132:135], 0\n"
"ds_read_b64_tr_b16 v[228:229], v7 offset:16384\n"
"v_mfma_f32_16x16x32_bf16 a[40:43], v[164:167], v[136:139], 0\n"
"v_mfma_f32_16x16x32_bf16 a[44:47], v[164:167], v[140:143], 0\n"
"ds_read_b64_tr_b16 v[230:231], v7 offset:18432\n"
"v_mfma_f32_16x16x32_bf16 a[48:51], v[164:167], v[144:147], 0\n"
"v_mfma_f32_16x16x32_bf16 a[52:55], v[164:167], v[148:151], 0\n"
"ds_read_b128 v[200:203], v5 offset:4096\n"
"v_mfma_f32_16x16x32_bf16 a[56:59], v[164:167], v[152:155], 0\n"
"v_mfma_f32_16x16x32_bf16 a[60:63], v[164:167], v[156:159], 0\n"
"ds_read_b64_tr_b16 v[232:233], v6 offset:16640\n"
"v_mfma_f32_16x16x32_bf16 a[64:67], v[168:171], v[128:131], 0\n"
"v_mfma_f32_16x16x32_bf16 a[68:71], v[168:171], v[132:135], 0\n"
"ds_read_b64_tr_b16 v[234:235], v6 offset:18688\n"
"v_mfma_f32_16x16x32_bf16 a[72:75], v[168:171], v[136:139], 0\n"
"v_mfma_f32_16x16x32_bf16 a[76:79], v[168:171], v[140:143], 0\n"
"ds_read_b128 v[204:207], v5 offset:6144\n"
"v_mfma_f32_16x16x32_bf16 a[80:83], v[168:171], v[144:147], 0\n"
"v_mfma_f32_16x16x32_bf16 a[84:87], v[168:171], v[148:151], 0\n"
"ds_read_b64_tr_b16 v[236:237], v7 offset:16640\n"
"buffer_store_dwordx4 v[68:71], v8, s[44:47], s83 offen offset:1024 sc1\n"
"v_mfma_f32_16x16x32_bf16 a[88:91], v[168:171], v[152:155], 0\n"
"v_mfma_f32_16x16x32_bf16 a[92:95], v[168:171], v[156:159], 0\n"
"ds_read_b64_tr_b16 v[238:239], v7 offset:18688\n"
"v_mfma_f32_16x16x32_bf16 a[96:99], v[172:175], v[128:131], 0\n"
"v_mfma_f32_16x16x32_bf16 a[100:103], v[172:175], v[132:135], 0\n"
"ds_read_b128 v[208:211], v5 offset:8192\n"
"v_mfma_f32_16x16x32_bf16 a[104:107], v[172:175], v[136:139], 0\n"
"v_mfma_f32_16x16x32_bf16 a[108:111], v[172:175], v[140:143], 0\n"
"ds_read_b64_tr_b16 v[240:241], v6 offset:16896\n"
"v_mfma_f32_16x16x32_bf16 a[112:115], v[172:175], v[144:147], 0\n"
"v_mfma_f32_16x16x32_bf16 a[116:119], v[172:175], v[148:151], 0\n"
"ds_read_b64_tr_b16 v[242:243], v6 offset:18944\n"
"v_mfma_f32_16x16x32_bf16 a[120:123], v[172:175], v[152:155], 0\n"
"v_mfma_f32_16x16x32_bf16 a[124:127], v[172:175], v[156:159], 0\n"
"ds_read_b128 v[212:215], v5 offset:10240\n"
"v_mfma_f32_16x16x32_bf16 a[128:131], v[176:179], v[128:131], 0\n"
"v_mfma_f32_16x16x32_bf16 a[132:135], v[176:179], v[132:135], 0\n"
"ds_read_b64_tr_b16 v[244:245], v7 offset:16896\n"
"v_mfma_f32_16x16x32_bf16 a[136:139], v[176:179], v[136:139], 0\n"
"v_mfma_f32_16x16x32_bf16 a[140:143], v[176:179], v[140:143], 0\n"
"ds_read_b64_tr_b16 v[246:247], v7 offset:18944\n"
"v_mfma_f32_16x16x32_bf16 a[144:147], v[176:179], v[144:147], 0\n"
"buffer_store_dwordx4 v[72:75], v8, s[44:47], s83 offen offset:2048 sc1\n"
"v_mfma_f32_16x16x32_bf16 a[148:151], v[176:179], v[148:151], 0\n"
"ds_read_b128 v[216:219], v5 offset:12288\n"
"v_mfma_f32_16x16x32_bf16 a[152:155], v[176:179], v[152:155], 0\n"
"v_mfma_f32_16x16x32_bf16 a[156:159], v[176:179], v[156:159], 0\n"
"ds_read_b64_tr_b16 v[248:249], v6 offset:17152\n"
"v_mfma_f32_16x16x32_bf16 a[160:163], v[180:183], v[128:131], 0\n"
"v_mfma_f32_16x16x32_bf16 a[164:167], v[180:183], v[132:135], 0\n"
"ds_read_b64_tr_b16 v[250:251], v6 offset:19200\n"
"v_mfma_f32_16x16x32_bf16 a[168:171], v[180:183], v[136:139], 0\n"
"v_mfma_f32_16x16x32_bf16 a[172:175], v[180:183], v[140:143], 0\n"
"ds_read_b128 v[220:223], v5 offset:14336\n"
"v_mfma_f32_16x16x32_bf16 a[176:179], v[180:183], v[144:147], 0\n"
"v_mfma_f32_16x16x32_bf16 a[180:183], v[180:183], v[148:151], 0\n"
"ds_read_b64_tr_b16 v[252:253], v7 offset:17152\n"
"v_mfma_f32_16x16x32_bf16 a[184:187], v[180:183], v[152:155], 0\n"
"v_mfma_f32_16x16x32_bf16 a[188:191], v[180:183], v[156:159], 0\n"
"ds_read_b64_tr_b16 v[254:255], v7 offset:19200\n"
"v_mfma_f32_16x16x32_bf16 a[192:195], v[184:187], v[128:131], 0\n"
"v_mfma_f32_16x16x32_bf16 a[196:199], v[184:187], v[132:135], 0\n"
"v_mfma_f32_16x16x32_bf16 a[200:203], v[184:187], v[136:139], 0\n"
"v_mfma_f32_16x16x32_bf16 a[204:207], v[184:187], v[140:143], 0\n"
"buffer_store_dwordx4 v[76:79], v8, s[44:47], s83 offen offset:3072 sc1\n"
"s_add_u32 s83, s83, s98\n"
"v_mfma_f32_16x16x32_bf16 a[208:211], v[184:187], v[144:147], 0\n"
"v_mfma_f32_16x16x32_bf16 a[212:215], v[184:187], v[148:151], 0\n"
"v_mfma_f32_16x16x32_bf16 a[216:219], v[184:187], v[152:155], 0\n"
"v_mfma_f32_16x16x32_bf16 a[220:223], v[184:187], v[156:159], 0\n"
"v_mfma_f32_16x16x32_bf16 a[224:227], v[188:191], v[128:131], 0\n"
"v_mfma_f32_16x16x32_bf16 a[228:231], v[188:191], v[132:135], 0\n"
"v_mfma_f32_16x16x32_bf16 a[232:235], v[188:191], v[136:139], 0\n"
"v_mfma_f32_16x16x32_bf16 a[236:239], v[188:191], v[140:143], 0\n"
"v_mfma_f32_16x16x32_bf16 a[240:243], v[188:191], v[144:147], 0\n"
"v_mfma_f32_16x16x32_bf16 a[244:247], v[188:191], v[148:151], 0\n"
"v_mfma_f32_16x16x32_bf16 a[248:251], v[188:191], v[152:155], 0\n"
"v_mfma_f32_16x16x32_bf16 a[252:255], v[188:191], v[156:159], 0\n"
"s_waitcnt vmcnt(21) lgkmcnt(0)\n"
"s_barrier\n"
"v_xor_b32 v4, 0x10000, v4\n"
"v_xor_b32 v5, 0x10000, v5\n"
"v_xor_b32 v6, 0x10000, v6\n"
"v_xor_b32 v7, 0x10000, v7\n"
"v_mfma_f32_16x16x32_bf16 a[0:3], v[224:227], v[192:195], a[0:3]\n"
"s_mov_b32 m0, s52\n"
"v_mfma_f32_16x16x32_bf16 a[4:7], v[224:227], v[196:199], a[4:7]\n"
"ds_read_b128 v[128:131], v4 offset:0\n"
"buffer_load_dwordx4 v16, s[36:39], s48 offen  lds\n"
"v_mfma_f32_16x16x32_bf16 a[8:11], v[224:227], v[200:203], a[8:11]\n"
"v_mfma_f32_16x16x32_bf16 a[12:15], v[224:227], v[204:207], a[12:15]\n"
"ds_read_b64_tr_b16 v[160:161], v6 offset:0\n"
"v_mfma_f32_16x16x32_bf16 a[16:19], v[224:227], v[208:211], a[16:19]\n"
"s_add_u32 m0, m0, 0x400\n"
"v_mfma_f32_16x16x32_bf16 a[20:23], v[224:227], v[212:215], a[20:23]\n"
"ds_read_b64_tr_b16 v[162:163], v6 offset:2048\n"
"buffer_load_dwordx4 v17, s[36:39], s48 offen  lds\n"
"v_mfma_f32_16x16x32_bf16 a[24:27], v[224:227], v[216:219], a[24:27]\n"
"v_mfma_f32_16x16x32_bf16 a[28:31], v[224:227], v[220:223], a[28:31]\n"
"ds_read_b128 v[132:135], v4 offset:2048\n"
"v_mfma_f32_16x16x32_bf16 a[32:35], v[228:231], v[192:195], a[32:35]\n"
"s_add_u32 m0, m0, 0x400\n"
"v_mfma_f32_16x16x32_bf16 a[36:39], v[228:231], v[196:199], a[36:39]\n"
"ds_read_b64_tr_b16 v[164:165], v7 offset:0\n"
"buffer_load_dwordx4 v18, s[36:39], s48 offen  lds\n"
"v_mfma_f32_16x16x32_bf16 a[40:43], v[228:231], v[200:203], a[40:43]\n"
"v_mfma_f32_16x16x32_bf16 a[44:47], v[228:231], v[204:207], a[44:47]\n"
"ds_read_b64_tr_b16 v[166:167], v7 offset:2048\n"
"v_mfma_f32_16x16x32_bf16 a[48:51], v[228:231], v[208:211], a[48:51]\n"
"s_add_u32 m0, m0, 0x400\n"
"v_mfma_f32_16x16x32_bf16 a[52:55], v[228:231], v[212:215], a[52:55]\n"
"ds_read_b128 v[136:139], v4 offset:4096\n"
"buffer_load_dwordx4 v19, s[36:39], s48 offen  lds\n"
"v_mfma_f32_16x16x32_bf16 a[56:59], v[228:231], v[216:219], a[56:59]\n"
"v_mfma_f32_16x16x32_bf16 a[60:63], v[228:231], v[220:223], a[60:63]\n"
"ds_read_b64_tr_b16 v[168:169], v6 offset:256\n"
"v_mfma_f32_16x16x32_bf16 a[64:67], v[232:235], v[192:195], a[64:67]\n"
"s_add_u32 m0, m0, 0x400\n"
"v_mfma_f32_16x16x32_bf16 a[68:71], v[232:235], v[196:199], a[68:71]\n"
"ds_read_b64_tr_b16 v[170:171], v6 offset:2304\n"
"buffer_load_dwordx4 v20, s[36:39], s48 offen  lds\n"
"v_mfma_f32_16x16x32_bf16 a[72:75], v[232:235], v[200:203], a[72:75]\n"
"v_mfma_f32_16x16x32_bf16 a[76:79], v[232:235], v[204:207], a[76:79]\n"
"ds_read_b128 v[140:143], v4 offset:6144\n"
"v_mfma_f32_16x16x32_bf16 a[80:83], v[232:235], v[208:211], a[80:83]\n"
"s_add_u32 m0, m0, 0x400\n"
"v_mfma_f32_16x16x32_bf16 a[84:87], v[232:235], v[212:215], a[84:87]\n"
"ds_read_b64_tr_b16 v[172:173], v7 offset:256\n"
"buffer_load_dwordx4 v21, s[36:39], s48 offen  lds\n"
"v_mfma_f32_16x16x32_bf16 a[88:91], v[232:235], v[216:219], a[88:91]\n"
"v_mfma_f32_16x16x32_bf16 a[92:95], v[232:235], v[220:223], a[92:95]\n"
"ds_read_b64_tr_b16 v[174:175], v7 offset:2304\n"
"v_mfma_f32_16x16x32_bf16 a[96:99], v[236:239], v[192:195], a[96:99]\n"
"s_add_u32 m0, m0, 0x400\n"
"v_mfma_f32_16x16x32_bf16 a[100:103], v[236:239], v[196:199], a[100:103]\n"
"ds_read_b128 v[144:147], v4 offset:8192\n"
"buffer_load_dwordx4 v22, s[36:39], s48 offen  lds\n"
"v_mfma_f32_16x16x32_bf16 a[104:107], v[236:239], v[200:203], a[104:107]\n"
"v_mfma_f32_16x16x32_bf16 a[108:111], v[236:239], v[204:207], a[108:111]\n"
"ds_read_b64_tr_b16 v[176:177], v6 offset:512\n"
"v_mfma_f32_16x16x32_bf16 a[112:115], v[236:239], v[208:211], a[112:115]\n"
"s_add_u32 m0, m0, 0x400\n"
"v_mfma_f32_16x16x32_bf16 a[116:119], v[236:239], v[212:215], a[116:119]\n"
"ds_read_b64_tr_b16 v[178:179], v6 offset:2560\n"
"buffer_load_dwordx4 v23, s[36:39], s48 offen  lds\n"
"v_mfma_f32_16x16x32_bf16 a[120:123], v[236:239], v[216:219], a[120:123]\n"
"v_mfma_f32_16x16x32_bf16 a[124:127], v[236:239], v[220:223], a[124:127]\n"
"ds_read_b128 v[148:151], v4 offset:10240\n"
"v_mfma_f32_16x16x32_bf16 a[128:131], v[240:243], v[192:195], a[128:131]\n"
"s_add_u32 m0, m0, 25600\n"
"v_mfma_f32_16x16x32_bf16 a[132:135], v[240:243], v[196:199], a[132:135]\n"
"ds_read_b64_tr_b16 v[180:181], v7 offset:512\n"
"buffer_load_dwordx4 v24, s[40:43], s49 offen  lds\n"
"v_mfma_f32_16x16x32_bf16 a[136:139], v[240:243], v[200:203], a[136:139]\n"
"v_mfma_f32_16x16x32_bf16 a[140:143], v[240:243], v[204:207], a[140:143]\n"
"ds_read_b64_tr_b16 v[182:183], v7 offset:2560\n"
"v_mfma_f32_16x16x32_bf16 a[144:147], v[240:243], v[208:211], a[144:147]\n"
"s_add_u32 m0, m0, 0x400\n"
"v_mfma_f32_16x16x32_bf16 a[148:151], v[240:243], v[212:215], a[148:151]\n"
"ds_read_b128 v[152:155], v4 offset:12288\n"
"buffer_load_dwordx4 v25, s[40:43], s49 offen  lds\n"
"v_mfma_f32_16x16x32_bf16 a[152:155], v[240:243], v[216:219], a[152:155]\n"
"v_mfma_f32_16x16x32_bf16 a[156:159], v[240:243], v[220:223], a[156:159]\n"
"ds_read_b64_tr_b16 v[184:185], v6 offset:768\n"
"v_mfma_f32_16x16x32_bf16 a[160:163], v[244:247], v[192:195], a[160:163]\n"
"s_add_u32 m0, m0, 0x400\n"
"v_mfma_f32_16x16x32_bf16 a[164:167], v[244:247], v[196:199], a[164:167]\n"
"ds_read_b64_tr_b16 v[186:187], v6 offset:2816\n"
"buffer_load_dwordx4 v26, s[40:43], s49 offen  lds\n"
"v_mfma_f32_16x16x32_bf16 a[168:171], v[244:247], v[200:203], a[168:171]\n"
"v_mfma_f32_16x16x32_bf16 a[172:175], v[244:247], v[204:207], a[172:175]\n"
"ds_read_b128 v[156:159], v4 offset:14336\n"
"v_mfma_f32_16x16x32_bf16 a[176:179], v[244:247], v[208:211], a[176:179]\n"
"s_add_u32 m0, m0, 0x400\n"
"v_mfma_f32_16x16x32_bf16 a[180:183], v[244:247], v[212:215], a[180:183]\n"
"ds_read_b64_tr_b16 v[188:189], v7 offset:768\n"
"buffer_load_dwordx4 v27, s[40:43], s49 offen  lds\n"
"v_mfma_f32_16x16x32_bf16 a[184:187], v[244:247], v[216:219], a[184:187]\n"
"v_mfma_f32_16x16x32_bf16 a[188:191], v[244:247], v[220:223], a[188:191]\n"
"ds_read_b64_tr_b16 v[190:191], v7 offset:2816\n"
"v_mfma_f32_16x16x32_bf16 a[192:195], v[248:251], v[192:195], a[192:195]\n"
"s_add_u32 m0, m0, 0x400\n"
"v_mfma_f32_16x16x32_bf16 a[196:199], v[248:251], v[196:199], a[196:199]\n"
"buffer_load_dwordx4 v28, s[40:43], s49 offen  lds\n"
"v_mfma_f32_16x16x32_bf16 a[200:203], v[248:251], v[200:203], a[200:203]\n"
"v_mfma_f32_16x16x32_bf16 a[204:207], v[248:251], v[204:207], a[204:207]\n"
"v_mfma_f32_16x16x32_bf16 a[208:211], v[248:251], v[208:211], a[208:211]\n"
"s_add_u32 m0, m0, 0x400\n"
"v_mfma_f32_16x16x32_bf16 a[212:215], v[248:251], v[212:215], a[212:215]\n"
"buffer_load_dwordx4 v29, s[40:43], s49 offen  lds\n"
"v_mfma_f32_16x16x32_bf16 a[216:219], v[248:251], v[216:219], a[216:219]\n"
"v_mfma_f32_16x16x32_bf16 a[220:223], v[248:251], v[220:223], a[220:223]\n"
"v_mfma_f32_16x16x32_bf16 a[224:227], v[252:255], v[192:195], a[224:227]\n"
"s_add_u32 m0, m0, 0x400\n"
"v_mfma_f32_16x16x32_bf16 a[228:231], v[252:255], v[196:199], a[228:231]\n"
"buffer_load_dwordx4 v30, s[40:43], s49 offen  lds\n"
"v_mfma_f32_16x16x32_bf16 a[232:235], v[252:255], v[200:203], a[232:235]\n"
"v_mfma_f32_16x16x32_bf16 a[236:239], v[252:255], v[204:207], a[236:239]\n"
"v_mfma_f32_16x16x32_bf16 a[240:243], v[252:255], v[208:211], a[240:243]\n"
"s_add_u32 m0, m0, 0x400\n"
"v_mfma_f32_16x16x32_bf16 a[244:247], v[252:255], v[212:215], a[244:247]\n"
"buffer_load_dwordx4 v31, s[40:43], s49 offen  lds\n"
"v_mfma_f32_16x16x32_bf16 a[248:251], v[252:255], v[216:219], a[248:251]\n"
"v_mfma_f32_16x16x32_bf16 a[252:255], v[252:255], v[220:223], a[252:255]\n"
"s_add_u32 s48, s48, 128\n"
"s_add_u32 s49, s49, s50\n"
"s_xor_b32 s52, s52, 0x10000\n"
"s_waitcnt lgkmcnt(0)\n"
"v_mfma_f32_16x16x32_bf16 a[0:3], v[160:163], v[128:131], a[0:3]\n"
"v_mfma_f32_16x16x32_bf16 a[4:7], v[160:163], v[132:135], a[4:7]\n"
"ds_read_b128 v[192:195], v5 offset:0\n"
"v_mfma_f32_16x16x32_bf16 a[8:11], v[160:163], v[136:139], a[8:11]\n"
"v_mfma_f32_16x16x32_bf16 a[12:15], v[160:163], v[140:143], a[12:15]\n"
"ds_read_b64_tr_b16 v[224:225], v6 offset:16384\n"
"v_mfma_f32_16x16x32_bf16 a[16:19], v[160:163], v[144:147], a[16:19]\n"
"v_mfma_f32_16x16x32_bf16 a[20:23], v[160:163], v[148:151], a[20:23]\n"
"ds_read_b64_tr_b16 v[226:227], v6 offset:18432\n"
"v_mfma_f32_16x16x32_bf16 a[24:27], v[160:163], v[152:155], a[24:27]\n"
"buffer_store_dwordx4 v[80:83], v8, s[44:47], s83 offen offset:0 sc1\n"
"v_mfma_f32_16x16x32_bf16 a[28:31], v[160:163], v[156:159], a[28:31]\n"
"ds_read_b128 v[196:199], v5 offset:2048\n"
"v_mfma_f32_16x16x32_bf16 a[32:35], v[164:167], v[128:131], a[32:35]\n"
"v_mfma_f32_16x16x32_bf16 a[36:39], v[164:167], v[132:135], a[36:39]\n"
"ds_read_b64_tr_b16 v[228:229], v7 offset:16384\n"
"v_mfma_f32_16x16x32_bf16 a[40:43], v[164:167], v[136:139], a[40:43]\n"
"v_mfma_f32_16x16x32_bf16 a[44:47], v[164:167], v[140:143], a[44:47]\n"
"ds_read_b64_tr_b16 v[230:231], v7 offset:18432\n"
"v_mfma_f32_16x16x32_bf16 a[48:51], v[164:167], v[144:147], a[48:51]\n"
"v_mfma_f32_16x16x32_bf16 a[52:55], v[164:167], v[148:151], a[52:55]\n"
"ds_read_b128 v[200:203], v5 offset:4096\n"
"v_mfma_f32_16x16x32_bf16 a[56:59], v[164:167], v[152:155], a[56:59]\n"
"v_mfma_f32_16x16x32_bf16 a[60:63], v[164:167], v[156:159], a[60:63]\n"
"ds_read_b64_tr_b16 v[232:233], v6 offset:16640\n"
"v_mfma_f32_16x16x32_bf16 a[64:67], v[168:171], v[128:131], a[64:67]\n"
"v_mfma_f32_16x16x32_bf16 a[68:71], v[168:171], v[132:135], a[68:71]\n"
"ds_read_b64_tr_b16 v[234:235], v6 offset:18688\n"
"v_mfma_f32_16x16x32_bf16 a[72:75], v[168:171], v[136:139], a[72:75]\n"
"v_mfma_f32_16x16x32_bf16 a[76:79], v[168:171], v[140:143], a[76:79]\n"
"ds_read_b128 v[204:207], v5 offset:6144\n"
"v_mfma_f32_16x16x32_bf16 a[80:83], v[168:171], v[144:147], a[80:83]\n"
"v_mfma_f32_16x16x32_bf16 a[84:87], v[168:171], v[148:151], a[84:87]\n"
"ds_read_b64_tr_b16 v[236:237], v7 offset:16640\n"
"buffer_store_dwordx4 v[84:87], v8, s[44:47], s83 offen offset:1024 sc1\n"
"v_mfma_f32_16x16x32_bf16 a[88:91], v[168:171], v[152:155], a[88:91]\n"
"v_mfma_f32_16x16x32_bf16 a[92:95], v[168:171], v[156:159], a[92:95]\n"
"ds_read_b64_tr_b16 v[238:239], v7 offset:18688\n"
"v_mfma_f32_16x16x32_bf16 a[96:99], v[172:175], v[128:131], a[96:99]\n"
"v_mfma_f32_16x16x32_bf16 a[100:103], v[172:175], v[132:135], a[100:103]\n"
"ds_read_b128 v[208:211], v5 offset:8192\n"
"v_mfma_f32_16x16x32_bf16 a[104:107], v[172:175], v[136:139], a[104:107]\n"
"v_mfma_f32_16x16x32_bf16 a[108:111], v[172:175], v[140:143], a[108:111]\n"
"ds_read_b64_tr_b16 v[240:241], v6 offset:16896\n"
"v_mfma_f32_16x16x32_bf16 a[112:115], v[172:175], v[144:147], a[112:115]\n"
"v_mfma_f32_16x16x32_bf16 a[116:119], v[172:175], v[148:151], a[116:119]\n"
"ds_read_b64_tr_b16 v[242:243], v6 offset:18944\n"
"v_mfma_f32_16x16x32_bf16 a[120:123], v[172:175], v[152:155], a[120:123]\n"
"v_mfma_f32_16x16x32_bf16 a[124:127], v[172:175], v[156:159], a[124:127]\n"
"ds_read_b128 v[212:215], v5 offset:10240\n"
"v_mfma_f32_16x16x32_bf16 a[128:131], v[176:179], v[128:131], a[128:131]\n"
"v_mfma_f32_16x16x32_bf16 a[132:135], v[176:179], v[132:135], a[132:135]\n"
"ds_read_b64_tr_b16 v[244:245], v7 offset:16896\n"
"v_mfma_f32_16x16x32_bf16 a[136:139], v[176:179], v[136:139], a[136:139]\n"
"v_mfma_f32_16x16x32_bf16 a[140:143], v[176:179], v[140:143], a[140:143]\n"
"ds_read_b64_tr_b16 v[246:247], v7 offset:18944\n"
"v_mfma_f32_16x16x32_bf16 a[144:147], v[176:179], v[144:147], a[144:147]\n"
"buffer_store_dwordx4 v[88:91], v8, s[44:47], s83 offen offset:2048 sc1\n"
"v_mfma_f32_16x16x32_bf16 a[148:151], v[176:179], v[148:151], a[148:151]\n"
"ds_read_b128 v[216:219], v5 offset:12288\n"
"v_mfma_f32_16x16x32_bf16 a[152:155], v[176:179], v[152:155], a[152:155]\n"
"v_mfma_f32_16x16x32_bf16 a[156:159], v[176:179], v[156:159], a[156:159]\n"
"ds_read_b64_tr_b16 v[248:249], v6 offset:17152\n"
"v_mfma_f32_16x16x32_bf16 a[160:163], v[180:183], v[128:131], a[160:163]\n"
"v_mfma_f32_16x16x32_bf16 a[164:167], v[180:183], v[132:135], a[164:167]\n"
"ds_read_b64_tr_b16 v[250:251], v6 offset:19200\n"
"v_mfma_f32_16x16x32_bf16 a[168:171], v[180:183], v[136:139], a[168:171]\n"
"v_mfma_f32_16x16x32_bf16 a[172:175], v[180:183], v[140:143], a[172:175]\n"
"ds_read_b128 v[220:223], v5 offset:14336\n"
"v_mfma_f32_16x16x32_bf16 a[176:179], v[180:183], v[144:147], a[176:179]\n"
"v_mfma_f32_16x16x32_bf16 a[180:183], v[180:183], v[148:151], a[180:183]\n"
"ds_read_b64_tr_b16 v[252:253], v7 offset:17152\n"
"v_mfma_f32_16x16x32_bf16 a[184:187], v[180:183], v[152:155], a[184:187]\n"
"v_mfma_f32_16x16x32_bf16 a[188:191], v[180:183], v[156:159], a[188:191]\n"
"ds_read_b64_tr_b16 v[254:255], v7 offset:19200\n"
"v_mfma_f32_16x16x32_bf16 a[192:195], v[184:187], v[128:131], a[192:195]\n"
"v_mfma_f32_16x16x32_bf16 a[196:199], v[184:187], v[132:135], a[196:199]\n"
"v_mfma_f32_16x16x32_bf16 a[200:203], v[184:187], v[136:139], a[200:203]\n"
"v_mfma_f32_16x16x32_bf16 a[204:207], v[184:187], v[140:143], a[204:207]\n"
"buffer_store_dwordx4 v[92:95], v8, s[44:47], s83 offen offset:3072 sc1\n"
"s_add_u32 s83, s83, s99\n"
"v_mfma_f32_16x16x32_bf16 a[208:211], v[184:187], v[144:147], a[208:211]\n"
"v_mfma_f32_16x16x32_bf16 a[212:215], v[184:187], v[148:151], a[212:215]\n"
"v_mfma_f32_16x16x32_bf16 a[216:219], v[184:187], v[152:155], a[216:219]\n"
"v_mfma_f32_16x16x32_bf16 a[220:223], v[184:187], v[156:159], a[220:223]\n"
"v_mfma_f32_16x16x32_bf16 a[224:227], v[188:191], v[128:131], a[224:227]\n"
"v_mfma_f32_16x16x32_bf16 a[228:231], v[188:191], v[132:135], a[228:231]\n"
"v_mfma_f32_16x16x32_bf16 a[232:235], v[188:191], v[136:139], a[232:235]\n"
"v_mfma_f32_16x16x32_bf16 a[236:239], v[188:191], v[140:143], a[236:239]\n"
"v_mfma_f32_16x16x32_bf16 a[240:243], v[188:191], v[144:147], a[240:243]\n"
"v_mfma_f32_16x16x32_bf16 a[244:247], v[188:191], v[148:151], a[244:247]\n"
"v_mfma_f32_16x16x32_bf16 a[248:251], v[188:191], v[152:155], a[248:251]\n"
"v_mfma_f32_16x16x32_bf16 a[252:255], v[188:191], v[156:159], a[252:255]\n"
"s_waitcnt vmcnt(4) lgkmcnt(0)\n"
"s_barrier\n"
"v_xor_b32 v4, 0x10000, v4\n"
"v_xor_b32 v5, 0x10000, v5\n"
"v_xor_b32 v6, 0x10000, v6\n"
"v_xor_b32 v7, 0x10000, v7\n"
"v_mfma_f32_16x16x32_bf16 a[0:3], v[224:227], v[192:195], a[0:3]\n"
"s_mov_b32 m0, s52\n"
"v_mfma_f32_16x16x32_bf16 a[4:7], v[224:227], v[196:199], a[4:7]\n"
"ds_read_b128 v[128:131], v4 offset:0\n"
"buffer_load_dwordx4 v16, s[36:39], s48 offen  lds\n"
"v_mfma_f32_16x16x32_bf16 a[8:11], v[224:227], v[200:203], a[8:11]\n"
"v_mfma_f32_16x16x32_bf16 a[12:15], v[224:227], v[204:207], a[12:15]\n"
"ds_read_b64_tr_b16 v[160:161], v6 offset:0\n"
"v_mfma_f32_16x16x32_bf16 a[16:19], v[224:227], v[208:211], a[16:19]\n"
"s_add_u32 m0, m0, 0x400\n"
"v_mfma_f32_16x16x32_bf16 a[20:23], v[224:227], v[212:215], a[20:23]\n"
"ds_read_b64_tr_b16 v[162:163], v6 offset:2048\n"
"buffer_load_dwordx4 v17, s[36:39], s48 offen  lds\n"
"v_mfma_f32_16x16x32_bf16 a[24:27], v[224:227], v[216:219], a[24:27]\n"
"v_mfma_f32_16x16x32_bf16 a[28:31], v[224:227], v[220:223], a[28:31]\n"
"ds_read_b128 v[132:135], v4 offset:2048\n"
"v_mfma_f32_16x16x32_bf16 a[32:35], v[228:231], v[192:195], a[32:35]\n"
"s_add_u32 m0, m0, 0x400\n"
"v_mfma_f32_16x16x32_bf16 a[36:39], v[228:231], v[196:199], a[36:39]\n"
"ds_read_b64_tr_b16 v[164:165], v7 offset:0\n"
"buffer_load_dwordx4 v18, s[36:39], s48 offen  lds\n"
"v_mfma_f32_16x16x32_bf16 a[40:43], v[228:231], v[200:203], a[40:43]\n"
"v_mfma_f32_16x16x32_bf16 a[44:47], v[228:231], v[204:207], a[44:47]\n"
"ds_read_b64_tr_b16 v[166:167], v7 offset:2048\n"
"v_mfma_f32_16x16x32_bf16 a[48:51], v[228:231], v[208:211], a[48:51]\n"
"s_add_u32 m0, m0, 0x400\n"
"v_mfma_f32_16x16x32_bf16 a[52:55], v[228:231], v[212:215], a[52:55]\n"
"ds_read_b128 v[136:139], v4 offset:4096\n"
"buffer_load_dwordx4 v19, s[36:39], s48 offen  lds\n"
"v_mfma_f32_16x16x32_bf16 a[56:59], v[228:231], v[216:219], a[56:59]\n"
"v_mfma_f32_16x16x32_bf16 a[60:63], v[228:231], v[220:223], a[60:63]\n"
"ds_read_b64_tr_b16 v[168:169], v6 offset:256\n"
"v_mfma_f32_16x16x32_bf16 a[64:67], v[232:235], v[192:195], a[64:67]\n"
"s_add_u32 m0, m0, 0x400\n"
"v_mfma_f32_16x16x32_bf16 a[68:71], v[232:235], v[196:199], a[68:71]\n"
"ds_read_b64_tr_b16 v[170:171], v6 offset:2304\n"
"buffer_load_dwordx4 v20, s[36:39], s48 offen  lds\n"
"v_mfma_f32_16x16x32_bf16 a[72:75], v[232:235], v[200:203], a[72:75]\n"
"v_mfma_f32_16x16x32_bf16 a[76:79], v[232:235], v[204:207], a[76:79]\n"
"ds_read_b128 v[140:143], v4 offset:6144\n"
"v_mfma_f32_16x16x32_bf16 a[80:83], v[232:235], v[208:211], a[80:83]\n"
"s_add_u32 m0, m0, 0x400\n"
"v_mfma_f32_16x16x32_bf16 a[84:87], v[232:235], v[212:215], a[84:87]\n"
"ds_read_b64_tr_b16 v[172:173], v7 offset:256\n"
"buffer_load_dwordx4 v21, s[36:39], s48 offen  lds\n"
"v_mfma_f32_16x16x32_bf16 a[88:91], v[232:235], v[216:219], a[88:91]\n"
"v_mfma_f32_16x16x32_bf16 a[92:95], v[232:235], v[220:223], a[92:95]\n"
"ds_read_b64_tr_b16 v[174:175], v7 offset:2304\n"
"v_mfma_f32_16x16x32_bf16 a[96:99], v[236:239], v[192:195], a[96:99]\n"
"s_add_u32 m0, m0, 0x400\n"
"v_mfma_f32_16x16x32_bf16 a[100:103], v[236:239], v[196:199], a[100:103]\n"
"ds_read_b128 v[144:147], v4 offset:8192\n"
"buffer_load_dwordx4 v22, s[36:39], s48 offen  lds\n"
"v_mfma_f32_16x16x32_bf16 a[104:107], v[236:239], v[200:203], a[104:107]\n"
"v_mfma_f32_16x16x32_bf16 a[108:111], v[236:239], v[204:207], a[108:111]\n"
"ds_read_b64_tr_b16 v[176:177], v6 offset:512\n"
"v_mfma_f32_16x16x32_bf16 a[112:115], v[236:239], v[208:211], a[112:115]\n"
"s_add_u32 m0, m0, 0x400\n"
"v_mfma_f32_16x16x32_bf16 a[116:119], v[236:239], v[212:215], a[116:119]\n"
"ds_read_b64_tr_b16 v[178:179], v6 offset:2560\n"
"buffer_load_dwordx4 v23, s[36:39], s48 offen  lds\n"
"v_mfma_f32_16x16x32_bf16 a[120:123], v[236:239], v[216:219], a[120:123]\n"
"v_mfma_f32_16x16x32_bf16 a[124:127], v[236:239], v[220:223], a[124:127]\n"
"ds_read_b128 v[148:151], v4 offset:10240\n"
"v_mfma_f32_16x16x32_bf16 a[128:131], v[240:243], v[192:195], a[128:131]\n"
"s_add_u32 m0, m0, 25600\n"
"v_mfma_f32_16x16x32_bf16 a[132:135], v[240:243], v[196:199], a[132:135]\n"
"ds_read_b64_tr_b16 v[180:181], v7 offset:512\n"
"buffer_load_dwordx4 v24, s[40:43], s49 offen  lds\n"
"v_mfma_f32_16x16x32_bf16 a[136:139], v[240:243], v[200:203], a[136:139]\n"
"v_mfma_f32_16x16x32_bf16 a[140:143], v[240:243], v[204:207], a[140:143]\n"
"ds_read_b64_tr_b16 v[182:183], v7 offset:2560\n"
"v_mfma_f32_16x16x32_bf16 a[144:147], v[240:243], v[208:211], a[144:147]\n"
"s_add_u32 m0, m0, 0x400\n"
"v_mfma_f32_16x16x32_bf16 a[148:151], v[240:243], v[212:215], a[148:151]\n"
"ds_read_b128 v[152:155], v4 offset:12288\n"
"buffer_load_dwordx4 v25, s[40:43], s49 offen  lds\n"
"v_mfma_f32_16x16x32_bf16 a[152:155], v[240:243], v[216:219], a[152:155]\n"
"v_mfma_f32_16x16x32_bf16 a[156:159], v[240:243], v[220:223], a[156:159]\n"
"ds_read_b64_tr_b16 v[184:185], v6 offset:768\n"
"v_mfma_f32_16x16x32_bf16 a[160:163], v[244:247], v[192:195], a[160:163]\n"
"s_add_u32 m0, m0, 0x400\n"
"v_mfma_f32_16x16x32_bf16 a[164:167], v[244:247], v[196:199], a[164:167]\n"
"ds_read_b64_tr_b16 v[186:187], v6 offset:2816\n"
"buffer_load_dwordx4 v26, s[40:43], s49 offen  lds\n"
"v_mfma_f32_16x16x32_bf16 a[168:171], v[244:247], v[200:203], a[168:171]\n"
"v_mfma_f32_16x16x32_bf16 a[172:175], v[244:247], v[204:207], a[172:175]\n"
"ds_read_b128 v[156:159], v4 offset:14336\n"
"v_mfma_f32_16x16x32_bf16 a[176:179], v[244:247], v[208:211], a[176:179]\n"
"s_add_u32 m0, m0, 0x400\n"
"v_mfma_f32_16x16x32_bf16 a[180:183], v[244:247], v[212:215], a[180:183]\n"
"ds_read_b64_tr_b16 v[188:189], v7 offset:768\n"
"buffer_load_dwordx4 v27, s[40:43], s49 offen  lds\n"
"v_mfma_f32_16x16x32_bf16 a[184:187], v[244:247], v[216:219], a[184:187]\n"
"v_mfma_f32_16x16x32_bf16 a[188:191], v[244:247], v[220:223], a[188:191]\n"
"ds_read_b64_tr_b16 v[190:191], v7 offset:2816\n"
"v_mfma_f32_16x16x32_bf16 a[192:195], v[248:251], v[192:195], a[192:195]\n"
"s_add_u32 m0, m0, 0x400\n"
"v_mfma_f32_16x16x32_bf16 a[196:199], v[248:251], v[196:199], a[196:199]\n"
"buffer_load_dwordx4 v28, s[40:43], s49 offen  lds\n"
"v_mfma_f32_16x16x32_bf16 a[200:203], v[248:251], v[200:203], a[200:203]\n"
"v_mfma_f32_16x16x32_bf16 a[204:207], v[248:251], v[204:207], a[204:207]\n"
"v_mfma_f32_16x16x32_bf16 a[208:211], v[248:251], v[208:211], a[208:211]\n"
"s_add_u32 m0, m0, 0x400\n"
"v_mfma_f32_16x16x32_bf16 a[212:215], v[248:251], v[212:215], a[212:215]\n"
"buffer_load_dwordx4 v29, s[40:43], s49 offen  lds\n"
"v_mfma_f32_16x16x32_bf16 a[216:219], v[248:251], v[216:219], a[216:219]\n"
"v_mfma_f32_16x16x32_bf16 a[220:223], v[248:251], v[220:223], a[220:223]\n"
"v_mfma_f32_16x16x32_bf16 a[224:227], v[252:255], v[192:195], a[224:227]\n"
"s_add_u32 m0, m0, 0x400\n"
"v_mfma_f32_16x16x32_bf16 a[228:231], v[252:255], v[196:199], a[228:231]\n"
"buffer_load_dwordx4 v30, s[40:43], s49 offen  lds\n"
"v_mfma_f32_16x16x32_bf16 a[232:235], v[252:255], v[200:203], a[232:235]\n"
"v_mfma_f32_16x16x32_bf16 a[236:239], v[252:255], v[204:207], a[236:239]\n"
"v_mfma_f32_16x16x32_bf16 a[240:243], v[252:255], v[208:211], a[240:243]\n"
"s_add_u32 m0, m0, 0x400\n"
"v_mfma_f32_16x16x32_bf16 a[244:247], v[252:255], v[212:215], a[244:247]\n"
"buffer_load_dwordx4 v31, s[40:43], s49 offen  lds\n"
"v_mfma_f32_16x16x32_bf16 a[248:251], v[252:255], v[216:219], a[248:251]\n"
"v_mfma_f32_16x16x32_bf16 a[252:255], v[252:255], v[220:223], a[252:255]\n"
"s_add_u32 s48, s48, 128\n"
"s_add_u32 s49, s49, s50\n"
"s_xor_b32 s52, s52, 0x10000\n"
"s_waitcnt lgkmcnt(0)\n"
"v_mfma_f32_16x16x32_bf16 a[0:3], v[160:163], v[128:131], a[0:3]\n"
"v_mfma_f32_16x16x32_bf16 a[4:7], v[160:163], v[132:135], a[4:7]\n"
"ds_read_b128 v[192:195], v5 offset:0\n"
"v_mfma_f32_16x16x32_bf16 a[8:11], v[160:163], v[136:139], a[8:11]\n"
"v_mfma_f32_16x16x32_bf16 a[12:15], v[160:163], v[140:143], a[12:15]\n"
"ds_read_b64_tr_b16 v[224:225], v6 offset:16384\n"
"v_mfma_f32_16x16x32_bf16 a[16:19], v[160:163], v[144:147], a[16:19]\n"
"v_mfma_f32_16x16x32_bf16 a[20:23], v[160:163], v[148:151], a[20:23]\n"
"ds_read_b64_tr_b16 v[226:227], v6 offset:18432\n"
"v_mfma_f32_16x16x32_bf16 a[24:27], v[160:163], v[152:155], a[24:27]\n"
"buffer_store_dwordx4 v[96:99], v8, s[44:47], s83 offen offset:0 sc1\n"
"v_mfma_f32_16x16x32_bf16 a[28:31], v[160:163], v[156:159], a[28:31]\n"
"ds_read_b128 v[196:199], v5 offset:2048\n"
"v_mfma_f32_16x16x32_bf16 a[32:35], v[164:167], v[128:131], a[32:35]\n"
"v_mfma_f32_16x16x32_bf16 a[36:39], v[164:167], v[132:135], a[36:39]\n"
"ds_read_b64_tr_b16 v[228:229], v7 offset:16384\n"
"v_mfma_f32_16x16x32_bf16 a[40:43], v[164:167], v[136:139], a[40:43]\n"
"v_mfma_f32_16x16x32_bf16 a[44:47], v[164:167], v[140:143], a[44:47]\n"
"ds_read_b64_tr_b16 v[230:231], v7 offset:18432\n"
"v_mfma_f32_16x16x32_bf16 a[48:51], v[164:167], v[144:147], a[48:51]\n"
"v_mfma_f32_16x16x32_bf16 a[52:55], v[164:167], v[148:151], a[52:55]\n"
"ds_read_b128 v[200:203], v5 offset:4096\n"
"v_mfma_f32_16x16x32_bf16 a[56:59], v[164:167], v[152:155], a[56:59]\n"
"v_mfma_f32_16x16x32_bf16 a[60:63], v[164:167], v[156:159], a[60:63]\n"
"ds_read_b64_tr_b16 v[232:233], v6 offset:16640\n"
"v_mfma_f32_16x16x32_bf16 a[64:67], v[168:171], v[128:131], a[64:67]\n"
"v_mfma_f32_16x16x32_bf16 a[68:71], v[168:171], v[132:135], a[68:71]\n"
"ds_read_b64_tr_b16 v[234:235], v6 offset:18688\n"
"v_mfma_f32_16x16x32_bf16 a[72:75], v[168:171], v[136:139], a[72:75]\n"
"v_mfma_f32_16x16x32_bf16 a[76:79], v[168:171], v[140:143], a[76:79]\n"
"ds_read_b128 v[204:207], v5 offset:6144\n"
"v_mfma_f32_16x16x32_bf16 a[80:83], v[168:171], v[144:147], a[80:83]\n"
"v_mfma_f32_16x16x32_bf16 a[84:87], v[168:171], v[148:151], a[84:87]\n"
"ds_read_b64_tr_b16 v[236:237], v7 offset:16640\n"
"buffer_store_dwordx4 v[100:103], v8, s[44:47], s83 offen offset:1024 sc1\n"
"v_mfma_f32_16x16x32_bf16 a[88:91], v[168:171], v[152:155], a[88:91]\n"
"v_mfma_f32_16x16x32_bf16 a[92:95], v[168:171], v[156:159], a[92:95]\n"
"ds_read_b64_tr_b16 v[238:239], v7 offset:18688\n"
"v_mfma_f32_16x16x32_bf16 a[96:99], v[172:175], v[128:131], a[96:99]\n"
"v_mfma_f32_16x16x32_bf16 a[100:103], v[172:175], v[132:135], a[100:103]\n"
"ds_read_b128 v[208:211], v5 offset:8192\n"
"v_mfma_f32_16x16x32_bf16 a[104:107], v[172:175], v[136:139], a[104:107]\n"
"v_mfma_f32_16x16x32_bf16 a[108:111], v[172:175], v[140:143], a[108:111]\n"
"ds_read_b64_tr_b16 v[240:241], v6 offset:16896\n"
"v_mfma_f32_16x16x32_bf16 a[112:115], v[172:175], v[144:147], a[112:115]\n"
"v_mfma_f32_16x16x32_bf16 a[116:119], v[172:175], v[148:151], a[116:119]\n"
"ds_read_b64_tr_b16 v[242:243], v6 offset:18944\n"
"v_mfma_f32_16x16x32_bf16 a[120:123], v[172:175], v[152:155], a[120:123]\n"
"v_mfma_f32_16x16x32_bf16 a[124:127], v[172:175], v[156:159], a[124:127]\n"
"ds_read_b128 v[212:215], v5 offset:10240\n"
"v_mfma_f32_16x16x32_bf16 a[128:131], v[176:179], v[128:131], a[128:131]\n"
"v_mfma_f32_16x16x32_bf16 a[132:135], v[176:179], v[132:135], a[132:135]\n"
"ds_read_b64_tr_b16 v[244:245], v7 offset:16896\n"
"v_mfma_f32_16x16x32_bf16 a[136:139], v[176:179], v[136:139], a[136:139]\n"
"v_mfma_f32_16x16x32_bf16 a[140:143], v[176:179], v[140:143], a[140:143]\n"
"ds_read_b64_tr_b16 v[246:247], v7 offset:18944\n"
"v_mfma_f32_16x16x32_bf16 a[144:147], v[176:179], v[144:147], a[144:147]\n"
"buffer_store_dwordx4 v[104:107], v8, s[44:47], s83 offen offset:2048 sc1\n"
"v_mfma_f32_16x16x32_bf16 a[148:151], v[176:179], v[148:151], a[148:151]\n"
"ds_read_b128 v[216:219], v5 offset:12288\n"
"v_mfma_f32_16x16x32_bf16 a[152:155], v[176:179], v[152:155], a[152:155]\n"
"v_mfma_f32_16x16x32_bf16 a[156:159], v[176:179], v[156:159], a[156:159]\n"
"ds_read_b64_tr_b16 v[248:249], v6 offset:17152\n"
"v_mfma_f32_16x16x32_bf16 a[160:163], v[180:183], v[128:131], a[160:163]\n"
"v_mfma_f32_16x16x32_bf16 a[164:167], v[180:183], v[132:135], a[164:167]\n"
"ds_read_b64_tr_b16 v[250:251], v6 offset:19200\n"
"v_mfma_f32_16x16x32_bf16 a[168:171], v[180:183], v[136:139], a[168:171]\n"
"v_mfma_f32_16x16x32_bf16 a[172:175], v[180:183], v[140:143], a[172:175]\n"
"ds_read_b128 v[220:223], v5 offset:14336\n"
"v_mfma_f32_16x16x32_bf16 a[176:179], v[180:183], v[144:147], a[176:179]\n"
"v_mfma_f32_16x16x32_bf16 a[180:183], v[180:183], v[148:151], a[180:183]\n"
"ds_read_b64_tr_b16 v[252:253], v7 offset:17152\n"
"v_mfma_f32_16x16x32_bf16 a[184:187], v[180:183], v[152:155], a[184:187]\n"
"v_mfma_f32_16x16x32_bf16 a[188:191], v[180:183], v[156:159], a[188:191]\n"
"ds_read_b64_tr_b16 v[254:255], v7 offset:19200\n"
"v_mfma_f32_16x16x32_bf16 a[192:195], v[184:187], v[128:131], a[192:195]\n"
"v_mfma_f32_16x16x32_bf16 a[196:199], v[184:187], v[132:135], a[196:199]\n"
"v_mfma_f32_16x16x32_bf16 a[200:203], v[184:187], v[136:139], a[200:203]\n"
"v_mfma_f32_16x16x32_bf16 a[204:207], v[184:187], v[140:143], a[204:207]\n"
"buffer_store_dwordx4 v[108:111], v8, s[44:47], s83 offen offset:3072 sc1\n"
"s_add_u32 s83, s83, s98\n"
"v_mfma_f32_16x16x32_bf16 a[208:211], v[184:187], v[144:147], a[208:211]\n"
"v_mfma_f32_16x16x32_bf16 a[212:215], v[184:187], v[148:151], a[212:215]\n"
"v_mfma_f32_16x16x32_bf16 a[216:219], v[184:187], v[152:155], a[216:219]\n"
"v_mfma_f32_16x16x32_bf16 a[220:223], v[184:187], v[156:159], a[220:223]\n"
"v_mfma_f32_16x16x32_bf16 a[224:227], v[188:191], v[128:131], a[224:227]\n"
"v_mfma_f32_16x16x32_bf16 a[228:231], v[188:191], v[132:135], a[228:231]\n"
"v_mfma_f32_16x16x32_bf16 a[232:235], v[188:191], v[136:139], a[232:235]\n"
"v_mfma_f32_16x16x32_bf16 a[236:239], v[188:191], v[140:143], a[236:239]\n"
"v_mfma_f32_16x16x32_bf16 a[240:243], v[188:191], v[144:147], a[240:243]\n"
"v_mfma_f32_16x16x32_bf16 a[244:247], v[188:191], v[148:151], a[244:247]\n"
"v_mfma_f32_16x16x32_bf16 a[248:251], v[188:191], v[152:155], a[248:251]\n"
"v_mfma_f32_16x16x32_bf16 a[252:255], v[188:191], v[156:159], a[252:255]\n"
"s_waitcnt vmcnt(4) lgkmcnt(0)\n"
"s_barrier\n"
"v_xor_b32 v4, 0x10000, v4\n"
"v_xor_b32 v5, 0x10000, v5\n"
"v_xor_b32 v6, 0x10000, v6\n"
"v_xor_b32 v7, 0x10000, v7\n"
"v_mfma_f32_16x16x32_bf16 a[0:3], v[224:227], v[192:195], a[0:3]\n"
"s_mov_b32 m0, s52\n"
"v_mfma_f32_16x16x32_bf16 a[4:7], v[224:227], v[196:199], a[4:7]\n"
"ds_read_b128 v[128:131], v4 offset:0\n"
"buffer_load_dwordx4 v16, s[36:39], s48 offen  lds\n"
"v_mfma_f32_16x16x32_bf16 a[8:11], v[224:227], v[200:203], a[8:11]\n"
"v_mfma_f32_16x16x32_bf16 a[12:15], v[224:227], v[204:207], a[12:15]\n"
"ds_read_b64_tr_b16 v[160:161], v6 offset:0\n"
"v_mfma_f32_16x16x32_bf16 a[16:19], v[224:227], v[208:211], a[16:19]\n"
"s_add_u32 m0, m0, 0x400\n"
"v_mfma_f32_16x16x32_bf16 a[20:23], v[224:227], v[212:215], a[20:23]\n"
"ds_read_b64_tr_b16 v[162:163], v6 offset:2048\n"
"buffer_load_dwordx4 v17, s[36:39], s48 offen  lds\n"
"v_mfma_f32_16x16x32_bf16 a[24:27], v[224:227], v[216:219], a[24:27]\n"
"v_mfma_f32_16x16x32_bf16 a[28:31], v[224:227], v[220:223], a[28:31]\n"
"ds_read_b128 v[132:135], v4 offset:2048\n"
"v_mfma_f32_16x16x32_bf16 a[32:35], v[228:231], v[192:195], a[32:35]\n"
"s_add_u32 m0, m0, 0x400\n"
"v_mfma_f32_16x16x32_bf16 a[36:39], v[228:231], v[196:199], a[36:39]\n"
"ds_read_b64_tr_b16 v[164:165], v7 offset:0\n"
"buffer_load_dwordx4 v18, s[36:39], s48 offen  lds\n"
"v_mfma_f32_16x16x32_bf16 a[40:43], v[228:231], v[200:203], a[40:43]\n"
"v_mfma_f32_16x16x32_bf16 a[44:47], v[228:231], v[204:207], a[44:47]\n"
"ds_read_b64_tr_b16 v[166:167], v7 offset:2048\n"
"v_mfma_f32_16x16x32_bf16 a[48:51], v[228:231], v[208:211], a[48:51]\n"
"s_add_u32 m0, m0, 0x400\n"
"v_mfma_f32_16x16x32_bf16 a[52:55], v[228:231], v[212:215], a[52:55]\n"
"ds_read_b128 v[136:139], v4 offset:4096\n"
"buffer_load_dwordx4 v19, s[36:39], s48 offen  lds\n"
"v_mfma_f32_16x16x32_bf16 a[56:59], v[228:231], v[216:219], a[56:59]\n"
"v_mfma_f32_16x16x32_bf16 a[60:63], v[228:231], v[220:223], a[60:63]\n"
"ds_read_b64_tr_b16 v[168:169], v6 offset:256\n"
"v_mfma_f32_16x16x32_bf16 a[64:67], v[232:235], v[192:195], a[64:67]\n"
"s_add_u32 m0, m0, 0x400\n"
"v_mfma_f32_16x16x32_bf16 a[68:71], v[232:235], v[196:199], a[68:71]\n"
"ds_read_b64_tr_b16 v[170:171], v6 offset:2304\n"
"buffer_load_dwordx4 v20, s[36:39], s48 offen  lds\n"
"v_mfma_f32_16x16x32_bf16 a[72:75], v[232:235], v[200:203], a[72:75]\n"
"v_mfma_f32_16x16x32_bf16 a[76:79], v[232:235], v[204:207], a[76:79]\n"
"ds_read_b128 v[140:143], v4 offset:6144\n"
"v_mfma_f32_16x16x32_bf16 a[80:83], v[232:235], v[208:211], a[80:83]\n"
"s_add_u32 m0, m0, 0x400\n"
"v_mfma_f32_16x16x32_bf16 a[84:87], v[232:235], v[212:215], a[84:87]\n"
"ds_read_b64_tr_b16 v[172:173], v7 offset:256\n"
"buffer_load_dwordx4 v21, s[36:39], s48 offen  lds\n"
"v_mfma_f32_16x16x32_bf16 a[88:91], v[232:235], v[216:219], a[88:91]\n"
"v_mfma_f32_16x16x32_bf16 a[92:95], v[232:235], v[220:223], a[92:95]\n"
"ds_read_b64_tr_b16 v[174:175], v7 offset:2304\n"
"v_mfma_f32_16x16x32_bf16 a[96:99], v[236:239], v[192:195], a[96:99]\n"
"s_add_u32 m0, m0, 0x400\n"
"v_mfma_f32_16x16x32_bf16 a[100:103], v[236:239], v[196:199], a[100:103]\n"
"ds_read_b128 v[144:147], v4 offset:8192\n"
"buffer_load_dwordx4 v22, s[36:39], s48 offen  lds\n"
"v_mfma_f32_16x16x32_bf16 a[104:107], v[236:239], v[200:203], a[104:107]\n"
"v_mfma_f32_16x16x32_bf16 a[108:111], v[236:239], v[204:207], a[108:111]\n"
"ds_read_b64_tr_b16 v[176:177], v6 offset:512\n"
"v_mfma_f32_16x16x32_bf16 a[112:115], v[236:239], v[208:211], a[112:115]\n"
"s_add_u32 m0, m0, 0x400\n"
"v_mfma_f32_16x16x32_bf16 a[116:119], v[236:239], v[212:215], a[116:119]\n"
"ds_read_b64_tr_b16 v[178:179], v6 offset:2560\n"
"buffer_load_dwordx4 v23, s[36:39], s48 offen  lds\n"
"v_mfma_f32_16x16x32_bf16 a[120:123], v[236:239], v[216:219], a[120:123]\n"
"v_mfma_f32_16x16x32_bf16 a[124:127], v[236:239], v[220:223], a[124:127]\n"
"ds_read_b128 v[148:151], v4 offset:10240\n"
"v_mfma_f32_16x16x32_bf16 a[128:131], v[240:243], v[192:195], a[128:131]\n"
"s_add_u32 m0, m0, 25600\n"
"v_mfma_f32_16x16x32_bf16 a[132:135], v[240:243], v[196:199], a[132:135]\n"
"ds_read_b64_tr_b16 v[180:181], v7 offset:512\n"
"buffer_load_dwordx4 v24, s[40:43], s49 offen  lds\n"
"v_mfma_f32_16x16x32_bf16 a[136:139], v[240:243], v[200:203], a[136:139]\n"
"v_mfma_f32_16x16x32_bf16 a[140:143], v[240:243], v[204:207], a[140:143]\n"
"ds_read_b64_tr_b16 v[182:183], v7 offset:2560\n"
"v_mfma_f32_16x16x32_bf16 a[144:147], v[240:243], v[208:211], a[144:147]\n"
"s_add_u32 m0, m0, 0x400\n"
"v_mfma_f32_16x16x32_bf16 a[148:151], v[240:243], v[212:215], a[148:151]\n"
"ds_read_b128 v[152:155], v4 offset:12288\n"
"buffer_load_dwordx4 v25, s[40:43], s49 offen  lds\n"
"v_mfma_f32_16x16x32_bf16 a[152:155], v[240:243], v[216:219], a[152:155]\n"
"v_mfma_f32_16x16x32_bf16 a[156:159], v[240:243], v[220:223], a[156:159]\n"
"ds_read_b64_tr_b16 v[184:185], v6 offset:768\n"
"v_mfma_f32_16x16x32_bf16 a[160:163], v[244:247], v[192:195], a[160:163]\n"
"s_add_u32 m0, m0, 0x400\n"
"v_mfma_f32_16x16x32_bf16 a[164:167], v[244:247], v[196:199], a[164:167]\n"
"ds_read_b64_tr_b16 v[186:187], v6 offset:2816\n"
"buffer_load_dwordx4 v26, s[40:43], s49 offen  lds\n"
"v_mfma_f32_16x16x32_bf16 a[168:171], v[244:247], v[200:203], a[168:171]\n"
"v_mfma_f32_16x16x32_bf16 a[172:175], v[244:247], v[204:207], a[172:175]\n"
"ds_read_b128 v[156:159], v4 offset:14336\n"
"v_mfma_f32_16x16x32_bf16 a[176:179], v[244:247], v[208:211], a[176:179]\n"
"s_add_u32 m0, m0, 0x400\n"
"v_mfma_f32_16x16x32_bf16 a[180:183], v[244:247], v[212:215], a[180:183]\n"
"ds_read_b64_tr_b16 v[188:189], v7 offset:768\n"
"buffer_load_dwordx4 v27, s[40:43], s49 offen  lds\n"
"v_mfma_f32_16x16x32_bf16 a[184:187], v[244:247], v[216:219], a[184:187]\n"
"v_mfma_f32_16x16x32_bf16 a[188:191], v[244:247], v[220:223], a[188:191]\n"
"ds_read_b64_tr_b16 v[190:191], v7 offset:2816\n"
"v_mfma_f32_16x16x32_bf16 a[192:195], v[248:251], v[192:195], a[192:195]\n"
"s_add_u32 m0, m0, 0x400\n"
"v_mfma_f32_16x16x32_bf16 a[196:199], v[248:251], v[196:199], a[196:199]\n"
"buffer_load_dwordx4 v28, s[40:43], s49 offen  lds\n"
"v_mfma_f32_16x16x32_bf16 a[200:203], v[248:251], v[200:203], a[200:203]\n"
"v_mfma_f32_16x16x32_bf16 a[204:207], v[248:251], v[204:207], a[204:207]\n"
"v_mfma_f32_16x16x32_bf16 a[208:211], v[248:251], v[208:211], a[208:211]\n"
"s_add_u32 m0, m0, 0x400\n"
"v_mfma_f32_16x16x32_bf16 a[212:215], v[248:251], v[212:215], a[212:215]\n"
"buffer_load_dwordx4 v29, s[40:43], s49 offen  lds\n"
"v_mfma_f32_16x16x32_bf16 a[216:219], v[248:251], v[216:219], a[216:219]\n"
"v_mfma_f32_16x16x32_bf16 a[220:223], v[248:251], v[220:223], a[220:223]\n"
"v_mfma_f32_16x16x32_bf16 a[224:227], v[252:255], v[192:195], a[224:227]\n"
"s_add_u32 m0, m0, 0x400\n"
"v_mfma_f32_16x16x32_bf16 a[228:231], v[252:255], v[196:199], a[228:231]\n"
"buffer_load_dwordx4 v30, s[40:43], s49 offen  lds\n"
"v_mfma_f32_16x16x32_bf16 a[232:235], v[252:255], v[200:203], a[232:235]\n"
"v_mfma_f32_16x16x32_bf16 a[236:239], v[252:255], v[204:207], a[236:239]\n"
"v_mfma_f32_16x16x32_bf16 a[240:243], v[252:255], v[208:211], a[240:243]\n"
"s_add_u32 m0, m0, 0x400\n"
"v_mfma_f32_16x16x32_bf16 a[244:247], v[252:255], v[212:215], a[244:247]\n"
"buffer_load_dwordx4 v31, s[40:43], s49 offen  lds\n"
"v_mfma_f32_16x16x32_bf16 a[248:251], v[252:255], v[216:219], a[248:251]\n"
"v_mfma_f32_16x16x32_bf16 a[252:255], v[252:255], v[220:223], a[252:255]\n"
"s_add_u32 s48, s48, 128\n"
"s_add_u32 s49, s49, s50\n"
"s_xor_b32 s52, s52, 0x10000\n"
"s_waitcnt lgkmcnt(0)\n"
"v_mfma_f32_16x16x32_bf16 a[0:3], v[160:163], v[128:131], a[0:3]\n"
"v_mfma_f32_16x16x32_bf16 a[4:7], v[160:163], v[132:135], a[4:7]\n"
"ds_read_b128 v[192:195], v5 offset:0\n"
"v_mfma_f32_16x16x32_bf16 a[8:11], v[160:163], v[136:139], a[8:11]\n"
"v_mfma_f32_16x16x32_bf16 a[12:15], v[160:163], v[140:143], a[12:15]\n"
"ds_read_b64_tr_b16 v[224:225], v6 offset:16384\n"
"v_mfma_f32_16x16x32_bf16 a[16:19], v[160:163], v[144:147], a[16:19]\n"
"v_mfma_f32_16x16x32_bf16 a[20:23], v[160:163], v[148:151], a[20:23]\n"
"ds_read_b64_tr_b16 v[226:227], v6 offset:18432\n"
"v_mfma_f32_16x16x32_bf16 a[24:27], v[160:163], v[152:155], a[24:27]\n"
"buffer_store_dwordx4 v[112:115], v8, s[44:47], s83 offen offset:0 sc1\n"
"v_mfma_f32_16x16x32_bf16 a[28:31], v[160:163], v[156:159], a[28:31]\n"
"ds_read_b128 v[196:199], v5 offset:2048\n"
"v_mfma_f32_16x16x32_bf16 a[32:35], v[164:167], v[128:131], a[32:35]\n"
"v_mfma_f32_16x16x32_bf16 a[36:39], v[164:167], v[132:135], a[36:39]\n"
"ds_read_b64_tr_b16 v[228:229], v7 offset:16384\n"
"v_mfma_f32_16x16x32_bf16 a[40:43], v[164:167], v[136:139], a[40:43]\n"
"v_mfma_f32_16x16x32_bf16 a[44:47], v[164:167], v[140:143], a[44:47]\n"
"ds_read_b64_tr_b16 v[230:231], v7 offset:18432\n"
"v_mfma_f32_16x16x32_bf16 a[48:51], v[164:167], v[144:147], a[48:51]\n"
"v_mfma_f32_16x16x32_bf16 a[52:55], v[164:167], v[148:151], a[52:55]\n"
"ds_read_b128 v[200:203], v5 offset:4096\n"
"v_mfma_f32_16x16x32_bf16 a[56:59], v[164:167], v[152:155], a[56:59]\n"
"v_mfma_f32_16x16x32_bf16 a[60:63], v[164:167], v[156:159], a[60:63]\n"
"ds_read_b64_tr_b16 v[232:233], v6 offset:16640\n"
"v_mfma_f32_16x16x32_bf16 a[64:67], v[168:171], v[128:131], a[64:67]\n"
"v_mfma_f32_16x16x32_bf16 a[68:71], v[168:171], v[132:135], a[68:71]\n"
"ds_read_b64_tr_b16 v[234:235], v6 offset:18688\n"
"v_mfma_f32_16x16x32_bf16 a[72:75], v[168:171], v[136:139], a[72:75]\n"
"v_mfma_f32_16x16x32_bf16 a[76:79], v[168:171], v[140:143], a[76:79]\n"
"ds_read_b128 v[204:207], v5 offset:6144\n"
"v_mfma_f32_16x16x32_bf16 a[80:83], v[168:171], v[144:147], a[80:83]\n"
"v_mfma_f32_16x16x32_bf16 a[84:87], v[168:171], v[148:151], a[84:87]\n"
"ds_read_b64_tr_b16 v[236:237], v7 offset:16640\n"
"buffer_store_dwordx4 v[116:119], v8, s[44:47], s83 offen offset:1024 sc1\n"
"v_mfma_f32_16x16x32_bf16 a[88:91], v[168:171], v[152:155], a[88:91]\n"
"v_mfma_f32_16x16x32_bf16 a[92:95], v[168:171], v[156:159], a[92:95]\n"
"ds_read_b64_tr_b16 v[238:239], v7 offset:18688\n"
"v_mfma_f32_16x16x32_bf16 a[96:99], v[172:175], v[128:131], a[96:99]\n"
"v_mfma_f32_16x16x32_bf16 a[100:103], v[172:175], v[132:135], a[100:103]\n"
"ds_read_b128 v[208:211], v5 offset:8192\n"
"v_mfma_f32_16x16x32_bf16 a[104:107], v[172:175], v[136:139], a[104:107]\n"
"v_mfma_f32_16x16x32_bf16 a[108:111], v[172:175], v[140:143], a[108:111]\n"
"ds_read_b64_tr_b16 v[240:241], v6 offset:16896\n"
"v_mfma_f32_16x16x32_bf16 a[112:115], v[172:175], v[144:147], a[112:115]\n"
"v_mfma_f32_16x16x32_bf16 a[116:119], v[172:175], v[148:151], a[116:119]\n"
"ds_read_b64_tr_b16 v[242:243], v6 offset:18944\n"
"v_mfma_f32_16x16x32_bf16 a[120:123], v[172:175], v[152:155], a[120:123]\n"
"v_mfma_f32_16x16x32_bf16 a[124:127], v[172:175], v[156:159], a[124:127]\n"
"ds_read_b128 v[212:215], v5 offset:10240\n"
"v_mfma_f32_16x16x32_bf16 a[128:131], v[176:179], v[128:131], a[128:131]\n"
"v_mfma_f32_16x16x32_bf16 a[132:135], v[176:179], v[132:135], a[132:135]\n"
"ds_read_b64_tr_b16 v[244:245], v7 offset:16896\n"
"v_mfma_f32_16x16x32_bf16 a[136:139], v[176:179], v[136:139], a[136:139]\n"
"v_mfma_f32_16x16x32_bf16 a[140:143], v[176:179], v[140:143], a[140:143]\n"
"ds_read_b64_tr_b16 v[246:247], v7 offset:18944\n"
"v_mfma_f32_16x16x32_bf16 a[144:147], v[176:179], v[144:147], a[144:147]\n"
"buffer_store_dwordx4 v[120:123], v8, s[44:47], s83 offen offset:2048 sc1\n"
"v_mfma_f32_16x16x32_bf16 a[148:151], v[176:179], v[148:151], a[148:151]\n"
"ds_read_b128 v[216:219], v5 offset:12288\n"
"v_mfma_f32_16x16x32_bf16 a[152:155], v[176:179], v[152:155], a[152:155]\n"
"v_mfma_f32_16x16x32_bf16 a[156:159], v[176:179], v[156:159], a[156:159]\n"
"ds_read_b64_tr_b16 v[248:249], v6 offset:17152\n"
"v_mfma_f32_16x16x32_bf16 a[160:163], v[180:183], v[128:131], a[160:163]\n"
"v_mfma_f32_16x16x32_bf16 a[164:167], v[180:183], v[132:135], a[164:167]\n"
"ds_read_b64_tr_b16 v[250:251], v6 offset:19200\n"
"v_mfma_f32_16x16x32_bf16 a[168:171], v[180:183], v[136:139], a[168:171]\n"
"v_mfma_f32_16x16x32_bf16 a[172:175], v[180:183], v[140:143], a[172:175]\n"
"ds_read_b128 v[220:223], v5 offset:14336\n"
"v_mfma_f32_16x16x32_bf16 a[176:179], v[180:183], v[144:147], a[176:179]\n"
"v_mfma_f32_16x16x32_bf16 a[180:183], v[180:183], v[148:151], a[180:183]\n"
"ds_read_b64_tr_b16 v[252:253], v7 offset:17152\n"
"v_mfma_f32_16x16x32_bf16 a[184:187], v[180:183], v[152:155], a[184:187]\n"
"v_mfma_f32_16x16x32_bf16 a[188:191], v[180:183], v[156:159], a[188:191]\n"
"ds_read_b64_tr_b16 v[254:255], v7 offset:19200\n"
"v_mfma_f32_16x16x32_bf16 a[192:195], v[184:187], v[128:131], a[192:195]\n"
"v_mfma_f32_16x16x32_bf16 a[196:199], v[184:187], v[132:135], a[196:199]\n"
"v_mfma_f32_16x16x32_bf16 a[200:203], v[184:187], v[136:139], a[200:203]\n"
"v_mfma_f32_16x16x32_bf16 a[204:207], v[184:187], v[140:143], a[204:207]\n"
"buffer_store_dwordx4 v[124:127], v8, s[44:47], s83 offen offset:3072 sc1\n"
"v_mfma_f32_16x16x32_bf16 a[208:211], v[184:187], v[144:147], a[208:211]\n"
"v_mfma_f32_16x16x32_bf16 a[212:215], v[184:187], v[148:151], a[212:215]\n"
"v_mfma_f32_16x16x32_bf16 a[216:219], v[184:187], v[152:155], a[216:219]\n"
"v_mfma_f32_16x16x32_bf16 a[220:223], v[184:187], v[156:159], a[220:223]\n"
"v_mfma_f32_16x16x32_bf16 a[224:227], v[188:191], v[128:131], a[224:227]\n"
"v_mfma_f32_16x16x32_bf16 a[228:231], v[188:191], v[132:135], a[228:231]\n"
"v_mfma_f32_16x16x32_bf16 a[232:235], v[188:191], v[136:139], a[232:235]\n"
"v_mfma_f32_16x16x32_bf16 a[236:239], v[188:191], v[140:143], a[236:239]\n"
"v_mfma_f32_16x16x32_bf16 a[240:243], v[188:191], v[144:147], a[240:243]\n"
"v_mfma_f32_16x16x32_bf16 a[244:247], v[188:191], v[148:151], a[244:247]\n"
"v_mfma_f32_16x16x32_bf16 a[248:251], v[188:191], v[152:155], a[248:251]\n"
"v_mfma_f32_16x16x32_bf16 a[252:255], v[188:191], v[156:159], a[252:255]\n"
"s_waitcnt vmcnt(4) lgkmcnt(0)\n"
"s_barrier\n"
"v_xor_b32 v4, 0x10000, v4\n"
"v_xor_b32 v5, 0x10000, v5\n"
"v_xor_b32 v6, 0x10000, v6\n"
"v_xor_b32 v7, 0x10000, v7\n"
"v_mfma_f32_16x16x32_bf16 a[0:3], v[224:227], v[192:195], a[0:3]\n"
"s_mov_b32 m0, s52\n"
"v_mfma_f32_16x16x32_bf16 a[4:7], v[224:227], v[196:199], a[4:7]\n"
"ds_read_b128 v[128:131], v4 offset:0\n"
"buffer_load_dwordx4 v16, s[36:39], s48 offen  lds\n"
"v_mfma_f32_16x16x32_bf16 a[8:11], v[224:227], v[200:203], a[8:11]\n"
"v_mfma_f32_16x16x32_bf16 a[12:15], v[224:227], v[204:207], a[12:15]\n"
"ds_read_b64_tr_b16 v[160:161], v6 offset:0\n"
"v_mfma_f32_16x16x32_bf16 a[16:19], v[224:227], v[208:211], a[16:19]\n"
"s_add_u32 m0, m0, 0x400\n"
"v_mfma_f32_16x16x32_bf16 a[20:23], v[224:227], v[212:215], a[20:23]\n"
"ds_read_b64_tr_b16 v[162:163], v6 offset:2048\n"
"buffer_load_dwordx4 v17, s[36:39], s48 offen  lds\n"
"v_mfma_f32_16x16x32_bf16 a[24:27], v[224:227], v[216:219], a[24:27]\n"
"v_mfma_f32_16x16x32_bf16 a[28:31], v[224:227], v[220:223], a[28:31]\n"
"ds_read_b128 v[132:135], v4 offset:2048\n"
"v_mfma_f32_16x16x32_bf16 a[32:35], v[228:231], v[192:195], a[32:35]\n"
"s_add_u32 m0, m0, 0x400\n"
"v_mfma_f32_16x16x32_bf16 a[36:39], v[228:231], v[196:199], a[36:39]\n"
"ds_read_b64_tr_b16 v[164:165], v7 offset:0\n"
"buffer_load_dwordx4 v18, s[36:39], s48 offen  lds\n"
"v_mfma_f32_16x16x32_bf16 a[40:43], v[228:231], v[200:203], a[40:43]\n"
"v_mfma_f32_16x16x32_bf16 a[44:47], v[228:231], v[204:207], a[44:47]\n"
"ds_read_b64_tr_b16 v[166:167], v7 offset:2048\n"
"v_mfma_f32_16x16x32_bf16 a[48:51], v[228:231], v[208:211], a[48:51]\n"
"s_add_u32 m0, m0, 0x400\n"
"v_mfma_f32_16x16x32_bf16 a[52:55], v[228:231], v[212:215], a[52:55]\n"
"ds_read_b128 v[136:139], v4 offset:4096\n"
"buffer_load_dwordx4 v19, s[36:39], s48 offen  lds\n"
"v_mfma_f32_16x16x32_bf16 a[56:59], v[228:231], v[216:219], a[56:59]\n"
"v_mfma_f32_16x16x32_bf16 a[60:63], v[228:231], v[220:223], a[60:63]\n"
"ds_read_b64_tr_b16 v[168:169], v6 offset:256\n"
"v_mfma_f32_16x16x32_bf16 a[64:67], v[232:235], v[192:195], a[64:67]\n"
"s_add_u32 m0, m0, 0x400\n"
"v_mfma_f32_16x16x32_bf16 a[68:71], v[232:235], v[196:199], a[68:71]\n"
"ds_read_b64_tr_b16 v[170:171], v6 offset:2304\n"
"buffer_load_dwordx4 v20, s[36:39], s48 offen  lds\n"
"v_mfma_f32_16x16x32_bf16 a[72:75], v[232:235], v[200:203], a[72:75]\n"
"v_mfma_f32_16x16x32_bf16 a[76:79], v[232:235], v[204:207], a[76:79]\n"
"ds_read_b128 v[140:143], v4 offset:6144\n"
"v_mfma_f32_16x16x32_bf16 a[80:83], v[232:235], v[208:211], a[80:83]\n"
"s_add_u32 m0, m0, 0x400\n"
"v_mfma_f32_16x16x32_bf16 a[84:87], v[232:235], v[212:215], a[84:87]\n"
"ds_read_b64_tr_b16 v[172:173], v7 offset:256\n"
"buffer_load_dwordx4 v21, s[36:39], s48 offen  lds\n"
"v_mfma_f32_16x16x32_bf16 a[88:91], v[232:235], v[216:219], a[88:91]\n"
"v_mfma_f32_16x16x32_bf16 a[92:95], v[232:235], v[220:223], a[92:95]\n"
"ds_read_b64_tr_b16 v[174:175], v7 offset:2304\n"
"v_mfma_f32_16x16x32_bf16 a[96:99], v[236:239], v[192:195], a[96:99]\n"
"s_add_u32 m0, m0, 0x400\n"
"v_mfma_f32_16x16x32_bf16 a[100:103], v[236:239], v[196:199], a[100:103]\n"
"ds_read_b128 v[144:147], v4 offset:8192\n"
"buffer_load_dwordx4 v22, s[36:39], s48 offen  lds\n"
"v_mfma_f32_16x16x32_bf16 a[104:107], v[236:239], v[200:203], a[104:107]\n"
"v_mfma_f32_16x16x32_bf16 a[108:111], v[236:239], v[204:207], a[108:111]\n"
"ds_read_b64_tr_b16 v[176:177], v6 offset:512\n"
"v_mfma_f32_16x16x32_bf16 a[112:115], v[236:239], v[208:211], a[112:115]\n"
"s_add_u32 m0, m0, 0x400\n"
"v_mfma_f32_16x16x32_bf16 a[116:119], v[236:239], v[212:215], a[116:119]\n"
"ds_read_b64_tr_b16 v[178:179], v6 offset:2560\n"
"buffer_load_dwordx4 v23, s[36:39], s48 offen  lds\n"
"v_mfma_f32_16x16x32_bf16 a[120:123], v[236:239], v[216:219], a[120:123]\n"
"v_mfma_f32_16x16x32_bf16 a[124:127], v[236:239], v[220:223], a[124:127]\n"
"ds_read_b128 v[148:151], v4 offset:10240\n"
"v_mfma_f32_16x16x32_bf16 a[128:131], v[240:243], v[192:195], a[128:131]\n"
"s_add_u32 m0, m0, 25600\n"
"v_mfma_f32_16x16x32_bf16 a[132:135], v[240:243], v[196:199], a[132:135]\n"
"ds_read_b64_tr_b16 v[180:181], v7 offset:512\n"
"buffer_load_dwordx4 v24, s[40:43], s49 offen  lds\n"
"v_mfma_f32_16x16x32_bf16 a[136:139], v[240:243], v[200:203], a[136:139]\n"
"v_mfma_f32_16x16x32_bf16 a[140:143], v[240:243], v[204:207], a[140:143]\n"
"ds_read_b64_tr_b16 v[182:183], v7 offset:2560\n"
"v_mfma_f32_16x16x32_bf16 a[144:147], v[240:243], v[208:211], a[144:147]\n"
"s_add_u32 m0, m0, 0x400\n"
"v_mfma_f32_16x16x32_bf16 a[148:151], v[240:243], v[212:215], a[148:151]\n"
"ds_read_b128 v[152:155], v4 offset:12288\n"
"buffer_load_dwordx4 v25, s[40:43], s49 offen  lds\n"
"v_mfma_f32_16x16x32_bf16 a[152:155], v[240:243], v[216:219], a[152:155]\n"
"v_mfma_f32_16x16x32_bf16 a[156:159], v[240:243], v[220:223], a[156:159]\n"
"ds_read_b64_tr_b16 v[184:185], v6 offset:768\n"
"v_mfma_f32_16x16x32_bf16 a[160:163], v[244:247], v[192:195], a[160:163]\n"
"s_add_u32 m0, m0, 0x400\n"
"v_mfma_f32_16x16x32_bf16 a[164:167], v[244:247], v[196:199], a[164:167]\n"
"ds_read_b64_tr_b16 v[186:187], v6 offset:2816\n"
"buffer_load_dwordx4 v26, s[40:43], s49 offen  lds\n"
"v_mfma_f32_16x16x32_bf16 a[168:171], v[244:247], v[200:203], a[168:171]\n"
"v_mfma_f32_16x16x32_bf16 a[172:175], v[244:247], v[204:207], a[172:175]\n"
"ds_read_b128 v[156:159], v4 offset:14336\n"
"v_mfma_f32_16x16x32_bf16 a[176:179], v[244:247], v[208:211], a[176:179]\n"
"s_add_u32 m0, m0, 0x400\n"
"v_mfma_f32_16x16x32_bf16 a[180:183], v[244:247], v[212:215], a[180:183]\n"
"ds_read_b64_tr_b16 v[188:189], v7 offset:768\n"
"buffer_load_dwordx4 v27, s[40:43], s49 offen  lds\n"
"v_mfma_f32_16x16x32_bf16 a[184:187], v[244:247], v[216:219], a[184:187]\n"
"v_mfma_f32_16x16x32_bf16 a[188:191], v[244:247], v[220:223], a[188:191]\n"
"ds_read_b64_tr_b16 v[190:191], v7 offset:2816\n"
"v_mfma_f32_16x16x32_bf16 a[192:195], v[248:251], v[192:195], a[192:195]\n"
"s_add_u32 m0, m0, 0x400\n"
"v_mfma_f32_16x16x32_bf16 a[196:199], v[248:251], v[196:199], a[196:199]\n"
"buffer_load_dwordx4 v28, s[40:43], s49 offen  lds\n"
"v_mfma_f32_16x16x32_bf16 a[200:203], v[248:251], v[200:203], a[200:203]\n"
"v_mfma_f32_16x16x32_bf16 a[204:207], v[248:251], v[204:207], a[204:207]\n"
"v_mfma_f32_16x16x32_bf16 a[208:211], v[248:251], v[208:211], a[208:211]\n"
"s_add_u32 m0, m0, 0x400\n"
"v_mfma_f32_16x16x32_bf16 a[212:215], v[248:251], v[212:215], a[212:215]\n"
"buffer_load_dwordx4 v29, s[40:43], s49 offen  lds\n"
"v_mfma_f32_16x16x32_bf16 a[216:219], v[248:251], v[216:219], a[216:219]\n"
"v_mfma_f32_16x16x32_bf16 a[220:223], v[248:251], v[220:223], a[220:223]\n"
"v_mfma_f32_16x16x32_bf16 a[224:227], v[252:255], v[192:195], a[224:227]\n"
"s_add_u32 m0, m0, 0x400\n"
"v_mfma_f32_16x16x32_bf16 a[228:231], v[252:255], v[196:199], a[228:231]\n"
"buffer_load_dwordx4 v30, s[40:43], s49 offen  lds\n"
"v_mfma_f32_16x16x32_bf16 a[232:235], v[252:255], v[200:203], a[232:235]\n"
"v_mfma_f32_16x16x32_bf16 a[236:239], v[252:255], v[204:207], a[236:239]\n"
"v_mfma_f32_16x16x32_bf16 a[240:243], v[252:255], v[208:211], a[240:243]\n"
"s_add_u32 m0, m0, 0x400\n"
"v_mfma_f32_16x16x32_bf16 a[244:247], v[252:255], v[212:215], a[244:247]\n"
"buffer_load_dwordx4 v31, s[40:43], s49 offen  lds\n"
"v_mfma_f32_16x16x32_bf16 a[248:251], v[252:255], v[216:219], a[248:251]\n"
"v_mfma_f32_16x16x32_bf16 a[252:255], v[252:255], v[220:223], a[252:255]\n"
"s_add_u32 s48, s48, 128\n"
"s_add_u32 s49, s49, s50\n"
"s_xor_b32 s52, s52, 0x10000\n"
"s_waitcnt lgkmcnt(0)\n"
"v_mfma_f32_16x16x32_bf16 a[0:3], v[160:163], v[128:131], a[0:3]\n"
"v_mfma_f32_16x16x32_bf16 a[4:7], v[160:163], v[132:135], a[4:7]\n"
"ds_read_b128 v[192:195], v5 offset:0\n"
"v_mfma_f32_16x16x32_bf16 a[8:11], v[160:163], v[136:139], a[8:11]\n"
"v_mfma_f32_16x16x32_bf16 a[12:15], v[160:163], v[140:143], a[12:15]\n"
"ds_read_b64_tr_b16 v[224:225], v6 offset:16384\n"
"v_mfma_f32_16x16x32_bf16 a[16:19], v[160:163], v[144:147], a[16:19]\n"
"v_mfma_f32_16x16x32_bf16 a[20:23], v[160:163], v[148:151], a[20:23]\n"
"ds_read_b64_tr_b16 v[226:227], v6 offset:18432\n"
"v_mfma_f32_16x16x32_bf16 a[24:27], v[160:163], v[152:155], a[24:27]\n"
"v_mfma_f32_16x16x32_bf16 a[28:31], v[160:163], v[156:159], a[28:31]\n"
"ds_read_b128 v[196:199], v5 offset:2048\n"
"v_mfma_f32_16x16x32_bf16 a[32:35], v[164:167], v[128:131], a[32:35]\n"
"v_mfma_f32_16x16x32_bf16 a[36:39], v[164:167], v[132:135], a[36:39]\n"
"ds_read_b64_tr_b16 v[228:229], v7 offset:16384\n"
"v_mfma_f32_16x16x32_bf16 a[40:43], v[164:167], v[136:139], a[40:43]\n"
"v_mfma_f32_16x16x32_bf16 a[44:47], v[164:167], v[140:143], a[44:47]\n"
"ds_read_b64_tr_b16 v[230:231], v7 offset:18432\n"
"v_mfma_f32_16x16x32_bf16 a[48:51], v[164:167], v[144:147], a[48:51]\n"
"v_mfma_f32_16x16x32_bf16 a[52:55], v[164:167], v[148:151], a[52:55]\n"
"ds_read_b128 v[200:203], v5 offset:4096\n"
"v_mfma_f32_16x16x32_bf16 a[56:59], v[164:167], v[152:155], a[56:59]\n"
"v_mfma_f32_16x16x32_bf16 a[60:63], v[164:167], v[156:159], a[60:63]\n"
"ds_read_b64_tr_b16 v[232:233], v6 offset:16640\n"
"v_mfma_f32_16x16x32_bf16 a[64:67], v[168:171], v[128:131], a[64:67]\n"
"v_mfma_f32_16x16x32_bf16 a[68:71], v[168:171], v[132:135], a[68:71]\n"
"ds_read_b64_tr_b16 v[234:235], v6 offset:18688\n"
"v_mfma_f32_16x16x32_bf16 a[72:75], v[168:171], v[136:139], a[72:75]\n"
"v_mfma_f32_16x16x32_bf16 a[76:79], v[168:171], v[140:143], a[76:79]\n"
"ds_read_b128 v[204:207], v5 offset:6144\n"
"v_mfma_f32_16x16x32_bf16 a[80:83], v[168:171], v[144:147], a[80:83]\n"
"v_mfma_f32_16x16x32_bf16 a[84:87], v[168:171], v[148:151], a[84:87]\n"
"ds_read_b64_tr_b16 v[236:237], v7 offset:16640\n"
"v_mfma_f32_16x16x32_bf16 a[88:91], v[168:171], v[152:155], a[88:91]\n"
"v_mfma_f32_16x16x32_bf16 a[92:95], v[168:171], v[156:159], a[92:95]\n"
"ds_read_b64_tr_b16 v[238:239], v7 offset:18688\n"
"v_mfma_f32_16x16x32_bf16 a[96:99], v[172:175], v[128:131], a[96:99]\n"
"v_mfma_f32_16x16x32_bf16 a[100:103], v[172:175], v[132:135], a[100:103]\n"
"ds_read_b128 v[208:211], v5 offset:8192\n"
"v_mfma_f32_16x16x32_bf16 a[104:107], v[172:175], v[136:139], a[104:107]\n"
"v_mfma_f32_16x16x32_bf16 a[108:111], v[172:175], v[140:143], a[108:111]\n"
"ds_read_b64_tr_b16 v[240:241], v6 offset:16896\n"
"v_mfma_f32_16x16x32_bf16 a[112:115], v[172:175], v[144:147], a[112:115]\n"
"v_mfma_f32_16x16x32_bf16 a[116:119], v[172:175], v[148:151], a[116:119]\n"
"ds_read_b64_tr_b16 v[242:243], v6 offset:18944\n"
"v_mfma_f32_16x16x32_bf16 a[120:123], v[172:175], v[152:155], a[120:123]\n"
"v_mfma_f32_16x16x32_bf16 a[124:127], v[172:175], v[156:159], a[124:127]\n"
"ds_read_b128 v[212:215], v5 offset:10240\n"
"v_mfma_f32_16x16x32_bf16 a[128:131], v[176:179], v[128:131], a[128:131]\n"
"v_mfma_f32_16x16x32_bf16 a[132:135], v[176:179], v[132:135], a[132:135]\n"
"ds_read_b64_tr_b16 v[244:245], v7 offset:16896\n"
"v_mfma_f32_16x16x32_bf16 a[136:139], v[176:179], v[136:139], a[136:139]\n"
"v_mfma_f32_16x16x32_bf16 a[140:143], v[176:179], v[140:143], a[140:143]\n"
"ds_read_b64_tr_b16 v[246:247], v7 offset:18944\n"
"v_mfma_f32_16x16x32_bf16 a[144:147], v[176:179], v[144:147], a[144:147]\n"
"v_mfma_f32_16x16x32_bf16 a[148:151], v[176:179], v[148:151], a[148:151]\n"
"ds_read_b128 v[216:219], v5 offset:12288\n"
"v_mfma_f32_16x16x32_bf16 a[152:155], v[176:179], v[152:155], a[152:155]\n"
"v_mfma_f32_16x16x32_bf16 a[156:159], v[176:179], v[156:159], a[156:159]\n"
"ds_read_b64_tr_b16 v[248:249], v6 offset:17152\n"
"v_mfma_f32_16x16x32_bf16 a[160:163], v[180:183], v[128:131], a[160:163]\n"
"v_mfma_f32_16x16x32_bf16 a[164:167], v[180:183], v[132:135], a[164:167]\n"
"ds_read_b64_tr_b16 v[250:251], v6 offset:19200\n"
"v_mfma_f32_16x16x32_bf16 a[168:171], v[180:183], v[136:139], a[168:171]\n"
"v_mfma_f32_16x16x32_bf16 a[172:175], v[180:183], v[140:143], a[172:175]\n"
"ds_read_b128 v[220:223], v5 offset:14336\n"
"v_mfma_f32_16x16x32_bf16 a[176:179], v[180:183], v[144:147], a[176:179]\n"
"v_mfma_f32_16x16x32_bf16 a[180:183], v[180:183], v[148:151], a[180:183]\n"
"ds_read_b64_tr_b16 v[252:253], v7 offset:17152\n"
"v_mfma_f32_16x16x32_bf16 a[184:187], v[180:183], v[152:155], a[184:187]\n"
"v_mfma_f32_16x16x32_bf16 a[188:191], v[180:183], v[156:159], a[188:191]\n"
"ds_read_b64_tr_b16 v[254:255], v7 offset:19200\n"
"v_mfma_f32_16x16x32_bf16 a[192:195], v[184:187], v[128:131], a[192:195]\n"
"v_mfma_f32_16x16x32_bf16 a[196:199], v[184:187], v[132:135], a[196:199]\n"
"v_mfma_f32_16x16x32_bf16 a[200:203], v[184:187], v[136:139], a[200:203]\n"
"v_mfma_f32_16x16x32_bf16 a[204:207], v[184:187], v[140:143], a[204:207]\n"
"v_mfma_f32_16x16x32_bf16 a[208:211], v[184:187], v[144:147], a[208:211]\n"
"v_mfma_f32_16x16x32_bf16 a[212:215], v[184:187], v[148:151], a[212:215]\n"
"v_mfma_f32_16x16x32_bf16 a[216:219], v[184:187], v[152:155], a[216:219]\n"
"v_mfma_f32_16x16x32_bf16 a[220:223], v[184:187], v[156:159], a[220:223]\n"
"v_mfma_f32_16x16x32_bf16 a[224:227], v[188:191], v[128:131], a[224:227]\n"
"v_mfma_f32_16x16x32_bf16 a[228:231], v[188:191], v[132:135], a[228:231]\n"
"v_mfma_f32_16x16x32_bf16 a[232:235], v[188:191], v[136:139], a[232:235]\n"
"v_mfma_f32_16x16x32_bf16 a[236:239], v[188:191], v[140:143], a[236:239]\n"
"v_mfma_f32_16x16x32_bf16 a[240:243], v[188:191], v[144:147], a[240:243]\n"
"v_mfma_f32_16x16x32_bf16 a[244:247], v[188:191], v[148:151], a[244:247]\n"
"v_mfma_f32_16x16x32_bf16 a[248:251], v[188:191], v[152:155], a[248:251]\n"
"v_mfma_f32_16x16x32_bf16 a[252:255], v[188:191], v[156:159], a[252:255]\n"
"s_waitcnt vmcnt(0) lgkmcnt(0)\n"
"s_barrier\n"
"v_xor_b32 v4, 0x10000, v4\n"
"v_xor_b32 v5, 0x10000, v5\n"
"v_xor_b32 v6, 0x10000, v6\n"
"v_xor_b32 v7, 0x10000, v7\n"
"v_mfma_f32_16x16x32_bf16 a[0:3], v[224:227], v[192:195], a[0:3]\n"
"s_mov_b32 m0, s52\n"
"v_mfma_f32_16x16x32_bf16 a[4:7], v[224:227], v[196:199], a[4:7]\n"
"ds_read_b128 v[128:131], v4 offset:0\n"
"buffer_load_dwordx4 v16, s[36:39], s48 offen  lds\n"
"v_mfma_f32_16x16x32_bf16 a[8:11], v[224:227], v[200:203], a[8:11]\n"
"v_mfma_f32_16x16x32_bf16 a[12:15], v[224:227], v[204:207], a[12:15]\n"
"ds_read_b64_tr_b16 v[160:161], v6 offset:0\n"
"v_mfma_f32_16x16x32_bf16 a[16:19], v[224:227], v[208:211], a[16:19]\n"
"s_add_u32 m0, m0, 0x400\n"
"v_mfma_f32_16x16x32_bf16 a[20:23], v[224:227], v[212:215], a[20:23]\n"
"ds_read_b64_tr_b16 v[162:163], v6 offset:2048\n"
"buffer_load_dwordx4 v17, s[36:39], s48 offen  lds\n"
"v_mfma_f32_16x16x32_bf16 a[24:27], v[224:227], v[216:219], a[24:27]\n"
"v_mfma_f32_16x16x32_bf16 a[28:31], v[224:227], v[220:223], a[28:31]\n"
"ds_read_b128 v[132:135], v4 offset:2048\n"
"v_mfma_f32_16x16x32_bf16 a[32:35], v[228:231], v[192:195], a[32:35]\n"
"s_add_u32 m0, m0, 0x400\n"
"v_mfma_f32_16x16x32_bf16 a[36:39], v[228:231], v[196:199], a[36:39]\n"
"ds_read_b64_tr_b16 v[164:165], v7 offset:0\n"
"buffer_load_dwordx4 v18, s[36:39], s48 offen  lds\n"
"v_mfma_f32_16x16x32_bf16 a[40:43], v[228:231], v[200:203], a[40:43]\n"
"v_mfma_f32_16x16x32_bf16 a[44:47], v[228:231], v[204:207], a[44:47]\n"
"ds_read_b64_tr_b16 v[166:167], v7 offset:2048\n"
"v_mfma_f32_16x16x32_bf16 a[48:51], v[228:231], v[208:211], a[48:51]\n"
"s_add_u32 m0, m0, 0x400\n"
"v_mfma_f32_16x16x32_bf16 a[52:55], v[228:231], v[212:215], a[52:55]\n"
"ds_read_b128 v[136:139], v4 offset:4096\n"
"buffer_load_dwordx4 v19, s[36:39], s48 offen  lds\n"
"v_mfma_f32_16x16x32_bf16 a[56:59], v[228:231], v[216:219], a[56:59]\n"
"v_mfma_f32_16x16x32_bf16 a[60:63], v[228:231], v[220:223], a[60:63]\n"
"ds_read_b64_tr_b16 v[168:169], v6 offset:256\n"
"v_mfma_f32_16x16x32_bf16 a[64:67], v[232:235], v[192:195], a[64:67]\n"
"s_add_u32 m0, m0, 0x400\n"
"v_mfma_f32_16x16x32_bf16 a[68:71], v[232:235], v[196:199], a[68:71]\n"
"ds_read_b64_tr_b16 v[170:171], v6 offset:2304\n"
"buffer_load_dwordx4 v20, s[36:39], s48 offen  lds\n"
"v_mfma_f32_16x16x32_bf16 a[72:75], v[232:235], v[200:203], a[72:75]\n"
"v_mfma_f32_16x16x32_bf16 a[76:79], v[232:235], v[204:207], a[76:79]\n"
"ds_read_b128 v[140:143], v4 offset:6144\n"
"v_mfma_f32_16x16x32_bf16 a[80:83], v[232:235], v[208:211], a[80:83]\n"
"s_add_u32 m0, m0, 0x400\n"
"v_mfma_f32_16x16x32_bf16 a[84:87], v[232:235], v[212:215], a[84:87]\n"
"ds_read_b64_tr_b16 v[172:173], v7 offset:256\n"
"buffer_load_dwordx4 v21, s[36:39], s48 offen  lds\n"
"v_mfma_f32_16x16x32_bf16 a[88:91], v[232:235], v[216:219], a[88:91]\n"
"v_mfma_f32_16x16x32_bf16 a[92:95], v[232:235], v[220:223], a[92:95]\n"
"ds_read_b64_tr_b16 v[174:175], v7 offset:2304\n"
"v_mfma_f32_16x16x32_bf16 a[96:99], v[236:239], v[192:195], a[96:99]\n"
"s_add_u32 m0, m0, 0x400\n"
"v_mfma_f32_16x16x32_bf16 a[100:103], v[236:239], v[196:199], a[100:103]\n"
"ds_read_b128 v[144:147], v4 offset:8192\n"
"buffer_load_dwordx4 v22, s[36:39], s48 offen  lds\n"
"v_mfma_f32_16x16x32_bf16 a[104:107], v[236:239], v[200:203], a[104:107]\n"
"v_mfma_f32_16x16x32_bf16 a[108:111], v[236:239], v[204:207], a[108:111]\n"
"ds_read_b64_tr_b16 v[176:177], v6 offset:512\n"
"v_mfma_f32_16x16x32_bf16 a[112:115], v[236:239], v[208:211], a[112:115]\n"
"s_add_u32 m0, m0, 0x400\n"
"v_mfma_f32_16x16x32_bf16 a[116:119], v[236:239], v[212:215], a[116:119]\n"
"ds_read_b64_tr_b16 v[178:179], v6 offset:2560\n"
"buffer_load_dwordx4 v23, s[36:39], s48 offen  lds\n"
"v_mfma_f32_16x16x32_bf16 a[120:123], v[236:239], v[216:219], a[120:123]\n"
"v_mfma_f32_16x16x32_bf16 a[124:127], v[236:239], v[220:223], a[124:127]\n"
"ds_read_b128 v[148:151], v4 offset:10240\n"
"v_mfma_f32_16x16x32_bf16 a[128:131], v[240:243], v[192:195], a[128:131]\n"
"s_add_u32 m0, m0, 25600\n"
"v_mfma_f32_16x16x32_bf16 a[132:135], v[240:243], v[196:199], a[132:135]\n"
"ds_read_b64_tr_b16 v[180:181], v7 offset:512\n"
"buffer_load_dwordx4 v24, s[40:43], s49 offen  lds\n"
"v_mfma_f32_16x16x32_bf16 a[136:139], v[240:243], v[200:203], a[136:139]\n"
"v_mfma_f32_16x16x32_bf16 a[140:143], v[240:243], v[204:207], a[140:143]\n"
"ds_read_b64_tr_b16 v[182:183], v7 offset:2560\n"
"v_mfma_f32_16x16x32_bf16 a[144:147], v[240:243], v[208:211], a[144:147]\n"
"s_add_u32 m0, m0, 0x400\n"
"v_mfma_f32_16x16x32_bf16 a[148:151], v[240:243], v[212:215], a[148:151]\n"
"ds_read_b128 v[152:155], v4 offset:12288\n"
"buffer_load_dwordx4 v25, s[40:43], s49 offen  lds\n"
"v_mfma_f32_16x16x32_bf16 a[152:155], v[240:243], v[216:219], a[152:155]\n"
"v_mfma_f32_16x16x32_bf16 a[156:159], v[240:243], v[220:223], a[156:159]\n"
"ds_read_b64_tr_b16 v[184:185], v6 offset:768\n"
"v_mfma_f32_16x16x32_bf16 a[160:163], v[244:247], v[192:195], a[160:163]\n"
"s_add_u32 m0, m0, 0x400\n"
"v_mfma_f32_16x16x32_bf16 a[164:167], v[244:247], v[196:199], a[164:167]\n"
"ds_read_b64_tr_b16 v[186:187], v6 offset:2816\n"
"buffer_load_dwordx4 v26, s[40:43], s49 offen  lds\n"
"v_mfma_f32_16x16x32_bf16 a[168:171], v[244:247], v[200:203], a[168:171]\n"
"v_mfma_f32_16x16x32_bf16 a[172:175], v[244:247], v[204:207], a[172:175]\n"
"ds_read_b128 v[156:159], v4 offset:14336\n"
"v_mfma_f32_16x16x32_bf16 a[176:179], v[244:247], v[208:211], a[176:179]\n"
"s_add_u32 m0, m0, 0x400\n"
"v_mfma_f32_16x16x32_bf16 a[180:183], v[244:247], v[212:215], a[180:183]\n"
"ds_read_b64_tr_b16 v[188:189], v7 offset:768\n"
"buffer_load_dwordx4 v27, s[40:43], s49 offen  lds\n"
"v_mfma_f32_16x16x32_bf16 a[184:187], v[244:247], v[216:219], a[184:187]\n"
"v_mfma_f32_16x16x32_bf16 a[188:191], v[244:247], v[220:223], a[188:191]\n"
"ds_read_b64_tr_b16 v[190:191], v7 offset:2816\n"
"v_mfma_f32_16x16x32_bf16 a[192:195], v[248:251], v[192:195], a[192:195]\n"
"s_add_u32 m0, m0, 0x400\n"
"v_mfma_f32_16x16x32_bf16 a[196:199], v[248:251], v[196:199], a[196:199]\n"
"buffer_load_dwordx4 v28, s[40:43], s49 offen  lds\n"
"v_mfma_f32_16x16x32_bf16 a[200:203], v[248:251], v[200:203], a[200:203]\n"
"v_mfma_f32_16x16x32_bf16 a[204:207], v[248:251], v[204:207], a[204:207]\n"
"v_mfma_f32_16x16x32_bf16 a[208:211], v[248:251], v[208:211], a[208:211]\n"
"s_add_u32 m0, m0, 0x400\n"
"v_mfma_f32_16x16x32_bf16 a[212:215], v[248:251], v[212:215], a[212:215]\n"
"buffer_load_dwordx4 v29, s[40:43], s49 offen  lds\n"
"v_mfma_f32_16x16x32_bf16 a[216:219], v[248:251], v[216:219], a[216:219]\n"
"v_mfma_f32_16x16x32_bf16 a[220:223], v[248:251], v[220:223], a[220:223]\n"
"v_mfma_f32_16x16x32_bf16 a[224:227], v[252:255], v[192:195], a[224:227]\n"
"s_add_u32 m0, m0, 0x400\n"
"v_mfma_f32_16x16x32_bf16 a[228:231], v[252:255], v[196:199], a[228:231]\n"
"buffer_load_dwordx4 v30, s[40:43], s49 offen  lds\n"
"v_mfma_f32_16x16x32_bf16 a[232:235], v[252:255], v[200:203], a[232:235]\n"
"v_mfma_f32_16x16x32_bf16 a[236:239], v[252:255], v[204:207], a[236:239]\n"
"v_mfma_f32_16x16x32_bf16 a[240:243], v[252:255], v[208:211], a[240:243]\n"
"s_add_u32 m0, m0, 0x400\n"
"v_mfma_f32_16x16x32_bf16 a[244:247], v[252:255], v[212:215], a[244:247]\n"
"buffer_load_dwordx4 v31, s[40:43], s49 offen  lds\n"
"v_mfma_f32_16x16x32_bf16 a[248:251], v[252:255], v[216:219], a[248:251]\n"
"v_mfma_f32_16x16x32_bf16 a[252:255], v[252:255], v[220:223], a[252:255]\n"
"s_add_u32 s48, s48, 128\n"
"s_add_u32 s49, s49, s50\n"
"s_xor_b32 s52, s52, 0x10000\n"
"s_waitcnt lgkmcnt(0)\n"
"v_mfma_f32_16x16x32_bf16 a[0:3], v[160:163], v[128:131], a[0:3]\n"
"v_mfma_f32_16x16x32_bf16 a[4:7], v[160:163], v[132:135], a[4:7]\n"
"ds_read_b128 v[192:195], v5 offset:0\n"
"v_mfma_f32_16x16x32_bf16 a[8:11], v[160:163], v[136:139], a[8:11]\n"
"v_mfma_f32_16x16x32_bf16 a[12:15], v[160:163], v[140:143], a[12:15]\n"
"ds_read_b64_tr_b16 v[224:225], v6 offset:16384\n"
"v_mfma_f32_16x16x32_bf16 a[16:19], v[160:163], v[144:147], a[16:19]\n"
"v_mfma_f32_16x16x32_bf16 a[20:23], v[160:163], v[148:151], a[20:23]\n"
"ds_read_b64_tr_b16 v[226:227], v6 offset:18432\n"
"v_mfma_f32_16x16x32_bf16 a[24:27], v[160:163], v[152:155], a[24:27]\n"
"v_mfma_f32_16x16x32_bf16 a[28:31], v[160:163], v[156:159], a[28:31]\n"
"ds_read_b128 v[196:199], v5 offset:2048\n"
"v_mfma_f32_16x16x32_bf16 a[32:35], v[164:167], v[128:131], a[32:35]\n"
"v_mfma_f32_16x16x32_bf16 a[36:39], v[164:167], v[132:135], a[36:39]\n"
"ds_read_b64_tr_b16 v[228:229], v7 offset:16384\n"
"v_mfma_f32_16x16x32_bf16 a[40:43], v[164:167], v[136:139], a[40:43]\n"
"v_mfma_f32_16x16x32_bf16 a[44:47], v[164:167], v[140:143], a[44:47]\n"
"ds_read_b64_tr_b16 v[230:231], v7 offset:18432\n"
"v_mfma_f32_16x16x32_bf16 a[48:51], v[164:167], v[144:147], a[48:51]\n"
"v_mfma_f32_16x16x32_bf16 a[52:55], v[164:167], v[148:151], a[52:55]\n"
"ds_read_b128 v[200:203], v5 offset:4096\n"
"v_mfma_f32_16x16x32_bf16 a[56:59], v[164:167], v[152:155], a[56:59]\n"
"v_mfma_f32_16x16x32_bf16 a[60:63], v[164:167], v[156:159], a[60:63]\n"
"ds_read_b64_tr_b16 v[232:233], v6 offset:16640\n"
"v_mfma_f32_16x16x32_bf16 a[64:67], v[168:171], v[128:131], a[64:67]\n"
"v_mfma_f32_16x16x32_bf16 a[68:71], v[168:171], v[132:135], a[68:71]\n"
"ds_read_b64_tr_b16 v[234:235], v6 offset:18688\n"
"v_mfma_f32_16x16x32_bf16 a[72:75], v[168:171], v[136:139], a[72:75]\n"
"v_mfma_f32_16x16x32_bf16 a[76:79], v[168:171], v[140:143], a[76:79]\n"
"ds_read_b128 v[204:207], v5 offset:6144\n"
"v_mfma_f32_16x16x32_bf16 a[80:83], v[168:171], v[144:147], a[80:83]\n"
"v_mfma_f32_16x16x32_bf16 a[84:87], v[168:171], v[148:151], a[84:87]\n"
"ds_read_b64_tr_b16 v[236:237], v7 offset:16640\n"
"v_mfma_f32_16x16x32_bf16 a[88:91], v[168:171], v[152:155], a[88:91]\n"
"v_mfma_f32_16x16x32_bf16 a[92:95], v[168:171], v[156:159], a[92:95]\n"
"ds_read_b64_tr_b16 v[238:239], v7 offset:18688\n"
"v_mfma_f32_16x16x32_bf16 a[96:99], v[172:175], v[128:131], a[96:99]\n"
"v_mfma_f32_16x16x32_bf16 a[100:103], v[172:175], v[132:135], a[100:103]\n"
"ds_read_b128 v[208:211], v5 offset:8192\n"
"v_mfma_f32_16x16x32_bf16 a[104:107], v[172:175], v[136:139], a[104:107]\n"
"v_mfma_f32_16x16x32_bf16 a[108:111], v[172:175], v[140:143], a[108:111]\n"
"ds_read_b64_tr_b16 v[240:241], v6 offset:16896\n"
"v_mfma_f32_16x16x32_bf16 a[112:115], v[172:175], v[144:147], a[112:115]\n"
"v_mfma_f32_16x16x32_bf16 a[116:119], v[172:175], v[148:151], a[116:119]\n"
"ds_read_b64_tr_b16 v[242:243], v6 offset:18944\n"
"v_mfma_f32_16x16x32_bf16 a[120:123], v[172:175], v[152:155], a[120:123]\n"
"v_mfma_f32_16x16x32_bf16 a[124:127], v[172:175], v[156:159], a[124:127]\n"
"ds_read_b128 v[212:215], v5 offset:10240\n"
"v_mfma_f32_16x16x32_bf16 a[128:131], v[176:179], v[128:131], a[128:131]\n"
"v_mfma_f32_16x16x32_bf16 a[132:135], v[176:179], v[132:135], a[132:135]\n"
"ds_read_b64_tr_b16 v[244:245], v7 offset:16896\n"
"v_mfma_f32_16x16x32_bf16 a[136:139], v[176:179], v[136:139], a[136:139]\n"
"v_mfma_f32_16x16x32_bf16 a[140:143], v[176:179], v[140:143], a[140:143]\n"
"ds_read_b64_tr_b16 v[246:247], v7 offset:18944\n"
"v_mfma_f32_16x16x32_bf16 a[144:147], v[176:179], v[144:147], a[144:147]\n"
"v_mfma_f32_16x16x32_bf16 a[148:151], v[176:179], v[148:151], a[148:151]\n"
"ds_read_b128 v[216:219], v5 offset:12288\n"
"v_mfma_f32_16x16x32_bf16 a[152:155], v[176:179], v[152:155], a[152:155]\n"
"v_mfma_f32_16x16x32_bf16 a[156:159], v[176:179], v[156:159], a[156:159]\n"
"ds_read_b64_tr_b16 v[248:249], v6 offset:17152\n"
"v_mfma_f32_16x16x32_bf16 a[160:163], v[180:183], v[128:131], a[160:163]\n"
"v_mfma_f32_16x16x32_bf16 a[164:167], v[180:183], v[132:135], a[164:167]\n"
"ds_read_b64_tr_b16 v[250:251], v6 offset:19200\n"
"v_mfma_f32_16x16x32_bf16 a[168:171], v[180:183], v[136:139], a[168:171]\n"
"v_mfma_f32_16x16x32_bf16 a[172:175], v[180:183], v[140:143], a[172:175]\n"
"ds_read_b128 v[220:223], v5 offset:14336\n"
"v_mfma_f32_16x16x32_bf16 a[176:179], v[180:183], v[144:147], a[176:179]\n"
"v_mfma_f32_16x16x32_bf16 a[180:183], v[180:183], v[148:151], a[180:183]\n"
"ds_read_b64_tr_b16 v[252:253], v7 offset:17152\n"
"v_mfma_f32_16x16x32_bf16 a[184:187], v[180:183], v[152:155], a[184:187]\n"
"v_mfma_f32_16x16x32_bf16 a[188:191], v[180:183], v[156:159], a[188:191]\n"
"ds_read_b64_tr_b16 v[254:255], v7 offset:19200\n"
"v_mfma_f32_16x16x32_bf16 a[192:195], v[184:187], v[128:131], a[192:195]\n"
"v_mfma_f32_16x16x32_bf16 a[196:199], v[184:187], v[132:135], a[196:199]\n"
"v_mfma_f32_16x16x32_bf16 a[200:203], v[184:187], v[136:139], a[200:203]\n"
"v_mfma_f32_16x16x32_bf16 a[204:207], v[184:187], v[140:143], a[204:207]\n"
"v_mfma_f32_16x16x32_bf16 a[208:211], v[184:187], v[144:147], a[208:211]\n"
"v_mfma_f32_16x16x32_bf16 a[212:215], v[184:187], v[148:151], a[212:215]\n"
"v_mfma_f32_16x16x32_bf16 a[216:219], v[184:187], v[152:155], a[216:219]\n"
"v_mfma_f32_16x16x32_bf16 a[220:223], v[184:187], v[156:159], a[220:223]\n"
"v_mfma_f32_16x16x32_bf16 a[224:227], v[188:191], v[128:131], a[224:227]\n"
"v_mfma_f32_16x16x32_bf16 a[228:231], v[188:191], v[132:135], a[228:231]\n"
"v_mfma_f32_16x16x32_bf16 a[232:235], v[188:191], v[136:139], a[232:235]\n"
"v_mfma_f32_16x16x32_bf16 a[236:239], v[188:191], v[140:143], a[236:239]\n"
"v_mfma_f32_16x16x32_bf16 a[240:243], v[188:191], v[144:147], a[240:243]\n"
"v_mfma_f32_16x16x32_bf16 a[244:247], v[188:191], v[148:151], a[244:247]\n"
"v_mfma_f32_16x16x32_bf16 a[248:251], v[188:191], v[152:155], a[248:251]\n"
"v_mfma_f32_16x16x32_bf16 a[252:255], v[188:191], v[156:159], a[252:255]\n"
"s_waitcnt vmcnt(0) lgkmcnt(0)\n"
"s_barrier\n"
"s_cmp_lg_u32 s73, 0\n"
"s_cbranch_scc1 L_fl_kvq_%=\n"
"s_lshl_b32 s76, s71, 1\n"
"s_and_b32 s77, s72, 1\n"
"s_add_u32 s76, s76, s77\n"
"s_lshl_b32 s76, s76, 1\n"
"s_add_u32 s76, s76, s82\n"
"s_lshl_b32 s76, s76, 3\n"
"s_lshr_b32 s77, s72, 2\n"
"s_xor_b32 s77, s77, 0\n"
"s_add_u32 s76, s76, s77\n"
"s_lshl_b32 s76, s76, 2\n"
"s_add_u32 s76, s76, 126879744\n"
"v_mov_b32 v1, s55\n"
"v_mov_b32 v2, 0\n"
"s_mov_b64 exec, 1\n"
"buffer_store_dword v1, v2, s[44:47], s76 offen sc1\n"
"s_mov_b64 exec, -1\n"
"L_fl_kvq_%=:\n"
"v_xor_b32 v4, 0x10000, v4\n"
"v_xor_b32 v5, 0x10000, v5\n"
"v_xor_b32 v6, 0x10000, v6\n"
"v_xor_b32 v7, 0x10000, v7\n"
"v_mfma_f32_16x16x32_bf16 a[0:3], v[224:227], v[192:195], a[0:3]\n"
"v_mfma_f32_16x16x32_bf16 a[4:7], v[224:227], v[196:199], a[4:7]\n"
"ds_read_b128 v[128:131], v4 offset:0\n"
"v_mfma_f32_16x16x32_bf16 a[8:11], v[224:227], v[200:203], a[8:11]\n"
"v_mfma_f32_16x16x32_bf16 a[12:15], v[224:227], v[204:207], a[12:15]\n"
"ds_read_b64_tr_b16 v[160:161], v6 offset:0\n"
"v_mfma_f32_16x16x32_bf16 a[16:19], v[224:227], v[208:211], a[16:19]\n"
"v_mfma_f32_16x16x32_bf16 a[20:23], v[224:227], v[212:215], a[20:23]\n"
"ds_read_b64_tr_b16 v[162:163], v6 offset:2048\n"
"v_mfma_f32_16x16x32_bf16 a[24:27], v[224:227], v[216:219], a[24:27]\n"
"v_mfma_f32_16x16x32_bf16 a[28:31], v[224:227], v[220:223], a[28:31]\n"
"ds_read_b128 v[132:135], v4 offset:2048\n"
"v_mfma_f32_16x16x32_bf16 a[32:35], v[228:231], v[192:195], a[32:35]\n"
"v_mfma_f32_16x16x32_bf16 a[36:39], v[228:231], v[196:199], a[36:39]\n"
"ds_read_b64_tr_b16 v[164:165], v7 offset:0\n"
"v_mfma_f32_16x16x32_bf16 a[40:43], v[228:231], v[200:203], a[40:43]\n"
"v_mfma_f32_16x16x32_bf16 a[44:47], v[228:231], v[204:207], a[44:47]\n"
"ds_read_b64_tr_b16 v[166:167], v7 offset:2048\n"
"v_mfma_f32_16x16x32_bf16 a[48:51], v[228:231], v[208:211], a[48:51]\n"
"v_mfma_f32_16x16x32_bf16 a[52:55], v[228:231], v[212:215], a[52:55]\n"
"ds_read_b128 v[136:139], v4 offset:4096\n"
"v_mfma_f32_16x16x32_bf16 a[56:59], v[228:231], v[216:219], a[56:59]\n"
"v_mfma_f32_16x16x32_bf16 a[60:63], v[228:231], v[220:223], a[60:63]\n"
"ds_read_b64_tr_b16 v[168:169], v6 offset:256\n"
"v_mfma_f32_16x16x32_bf16 a[64:67], v[232:235], v[192:195], a[64:67]\n"
"v_mfma_f32_16x16x32_bf16 a[68:71], v[232:235], v[196:199], a[68:71]\n"
"ds_read_b64_tr_b16 v[170:171], v6 offset:2304\n"
"v_mfma_f32_16x16x32_bf16 a[72:75], v[232:235], v[200:203], a[72:75]\n"
"v_mfma_f32_16x16x32_bf16 a[76:79], v[232:235], v[204:207], a[76:79]\n"
"ds_read_b128 v[140:143], v4 offset:6144\n"
"v_mfma_f32_16x16x32_bf16 a[80:83], v[232:235], v[208:211], a[80:83]\n"
"v_mfma_f32_16x16x32_bf16 a[84:87], v[232:235], v[212:215], a[84:87]\n"
"ds_read_b64_tr_b16 v[172:173], v7 offset:256\n"
"v_mfma_f32_16x16x32_bf16 a[88:91], v[232:235], v[216:219], a[88:91]\n"
"v_mfma_f32_16x16x32_bf16 a[92:95], v[232:235], v[220:223], a[92:95]\n"
"ds_read_b64_tr_b16 v[174:175], v7 offset:2304\n"
"v_mfma_f32_16x16x32_bf16 a[96:99], v[236:239], v[192:195], a[96:99]\n"
"v_mfma_f32_16x16x32_bf16 a[100:103], v[236:239], v[196:199], a[100:103]\n"
"ds_read_b128 v[144:147], v4 offset:8192\n"
"v_mfma_f32_16x16x32_bf16 a[104:107], v[236:239], v[200:203], a[104:107]\n"
"v_mfma_f32_16x16x32_bf16 a[108:111], v[236:239], v[204:207], a[108:111]\n"
"ds_read_b64_tr_b16 v[176:177], v6 offset:512\n"
"v_mfma_f32_16x16x32_bf16 a[112:115], v[236:239], v[208:211], a[112:115]\n"
"v_mfma_f32_16x16x32_bf16 a[116:119], v[236:239], v[212:215], a[116:119]\n"
"ds_read_b64_tr_b16 v[178:179], v6 offset:2560\n"
"v_mfma_f32_16x16x32_bf16 a[120:123], v[236:239], v[216:219], a[120:123]\n"
"v_mfma_f32_16x16x32_bf16 a[124:127], v[236:239], v[220:223], a[124:127]\n"
"ds_read_b128 v[148:151], v4 offset:10240\n"
"v_mfma_f32_16x16x32_bf16 a[128:131], v[240:243], v[192:195], a[128:131]\n"
"v_mfma_f32_16x16x32_bf16 a[132:135], v[240:243], v[196:199], a[132:135]\n"
"ds_read_b64_tr_b16 v[180:181], v7 offset:512\n"
"v_mfma_f32_16x16x32_bf16 a[136:139], v[240:243], v[200:203], a[136:139]\n"
"v_mfma_f32_16x16x32_bf16 a[140:143], v[240:243], v[204:207], a[140:143]\n"
"ds_read_b64_tr_b16 v[182:183], v7 offset:2560\n"
"v_mfma_f32_16x16x32_bf16 a[144:147], v[240:243], v[208:211], a[144:147]\n"
"v_mfma_f32_16x16x32_bf16 a[148:151], v[240:243], v[212:215], a[148:151]\n"
"ds_read_b128 v[152:155], v4 offset:12288\n"
"v_mfma_f32_16x16x32_bf16 a[152:155], v[240:243], v[216:219], a[152:155]\n"
"v_mfma_f32_16x16x32_bf16 a[156:159], v[240:243], v[220:223], a[156:159]\n"
"ds_read_b64_tr_b16 v[184:185], v6 offset:768\n"
"v_mfma_f32_16x16x32_bf16 a[160:163], v[244:247], v[192:195], a[160:163]\n"
"v_mfma_f32_16x16x32_bf16 a[164:167], v[244:247], v[196:199], a[164:167]\n"
"ds_read_b64_tr_b16 v[186:187], v6 offset:2816\n"
"v_mfma_f32_16x16x32_bf16 a[168:171], v[244:247], v[200:203], a[168:171]\n"
"v_mfma_f32_16x16x32_bf16 a[172:175], v[244:247], v[204:207], a[172:175]\n"
"ds_read_b128 v[156:159], v4 offset:14336\n"
"v_mfma_f32_16x16x32_bf16 a[176:179], v[244:247], v[208:211], a[176:179]\n"
"v_mfma_f32_16x16x32_bf16 a[180:183], v[244:247], v[212:215], a[180:183]\n"
"ds_read_b64_tr_b16 v[188:189], v7 offset:768\n"
"v_mfma_f32_16x16x32_bf16 a[184:187], v[244:247], v[216:219], a[184:187]\n"
"v_mfma_f32_16x16x32_bf16 a[188:191], v[244:247], v[220:223], a[188:191]\n"
"ds_read_b64_tr_b16 v[190:191], v7 offset:2816\n"
"v_mfma_f32_16x16x32_bf16 a[192:195], v[248:251], v[192:195], a[192:195]\n"
"v_mfma_f32_16x16x32_bf16 a[196:199], v[248:251], v[196:199], a[196:199]\n"
"v_mfma_f32_16x16x32_bf16 a[200:203], v[248:251], v[200:203], a[200:203]\n"
"v_mfma_f32_16x16x32_bf16 a[204:207], v[248:251], v[204:207], a[204:207]\n"
"v_mfma_f32_16x16x32_bf16 a[208:211], v[248:251], v[208:211], a[208:211]\n"
"v_mfma_f32_16x16x32_bf16 a[212:215], v[248:251], v[212:215], a[212:215]\n"
"v_mfma_f32_16x16x32_bf16 a[216:219], v[248:251], v[216:219], a[216:219]\n"
"v_mfma_f32_16x16x32_bf16 a[220:223], v[248:251], v[220:223], a[220:223]\n"
"v_mfma_f32_16x16x32_bf16 a[224:227], v[252:255], v[192:195], a[224:227]\n"
"v_mfma_f32_16x16x32_bf16 a[228:231], v[252:255], v[196:199], a[228:231]\n"
"v_mfma_f32_16x16x32_bf16 a[232:235], v[252:255], v[200:203], a[232:235]\n"
"v_mfma_f32_16x16x32_bf16 a[236:239], v[252:255], v[204:207], a[236:239]\n"
"v_mfma_f32_16x16x32_bf16 a[240:243], v[252:255], v[208:211], a[240:243]\n"
"v_mfma_f32_16x16x32_bf16 a[244:247], v[252:255], v[212:215], a[244:247]\n"
"v_mfma_f32_16x16x32_bf16 a[248:251], v[252:255], v[216:219], a[248:251]\n"
"v_mfma_f32_16x16x32_bf16 a[252:255], v[252:255], v[220:223], a[252:255]\n"
"s_waitcnt lgkmcnt(0)\n"
"v_mfma_f32_16x16x32_bf16 a[0:3], v[160:163], v[128:131], a[0:3]\n"
"v_mfma_f32_16x16x32_bf16 a[4:7], v[160:163], v[132:135], a[4:7]\n"
"ds_read_b128 v[192:195], v5 offset:0\n"
"v_mfma_f32_16x16x32_bf16 a[8:11], v[160:163], v[136:139], a[8:11]\n"
"v_mfma_f32_16x16x32_bf16 a[12:15], v[160:163], v[140:143], a[12:15]\n"
"ds_read_b64_tr_b16 v[224:225], v6 offset:16384\n"
"v_mfma_f32_16x16x32_bf16 a[16:19], v[160:163], v[144:147], a[16:19]\n"
"v_mfma_f32_16x16x32_bf16 a[20:23], v[160:163], v[148:151], a[20:23]\n"
"ds_read_b64_tr_b16 v[226:227], v6 offset:18432\n"
"v_mfma_f32_16x16x32_bf16 a[24:27], v[160:163], v[152:155], a[24:27]\n"
"v_mfma_f32_16x16x32_bf16 a[28:31], v[160:163], v[156:159], a[28:31]\n"
"ds_read_b128 v[196:199], v5 offset:2048\n"
"v_mfma_f32_16x16x32_bf16 a[32:35], v[164:167], v[128:131], a[32:35]\n"
"v_mfma_f32_16x16x32_bf16 a[36:39], v[164:167], v[132:135], a[36:39]\n"
"ds_read_b64_tr_b16 v[228:229], v7 offset:16384\n"
"v_mfma_f32_16x16x32_bf16 a[40:43], v[164:167], v[136:139], a[40:43]\n"
"v_mfma_f32_16x16x32_bf16 a[44:47], v[164:167], v[140:143], a[44:47]\n"
"ds_read_b64_tr_b16 v[230:231], v7 offset:18432\n"
"v_mfma_f32_16x16x32_bf16 a[48:51], v[164:167], v[144:147], a[48:51]\n"
"v_mfma_f32_16x16x32_bf16 a[52:55], v[164:167], v[148:151], a[52:55]\n"
"ds_read_b128 v[200:203], v5 offset:4096\n"
"v_mfma_f32_16x16x32_bf16 a[56:59], v[164:167], v[152:155], a[56:59]\n"
"v_mfma_f32_16x16x32_bf16 a[60:63], v[164:167], v[156:159], a[60:63]\n"
"ds_read_b64_tr_b16 v[232:233], v6 offset:16640\n"
"v_mfma_f32_16x16x32_bf16 a[64:67], v[168:171], v[128:131], a[64:67]\n"
"v_mfma_f32_16x16x32_bf16 a[68:71], v[168:171], v[132:135], a[68:71]\n"
"ds_read_b64_tr_b16 v[234:235], v6 offset:18688\n"
"v_mfma_f32_16x16x32_bf16 a[72:75], v[168:171], v[136:139], a[72:75]\n"
"v_mfma_f32_16x16x32_bf16 a[76:79], v[168:171], v[140:143], a[76:79]\n"
"ds_read_b128 v[204:207], v5 offset:6144\n"
"v_mfma_f32_16x16x32_bf16 a[80:83], v[168:171], v[144:147], a[80:83]\n"
"v_mfma_f32_16x16x32_bf16 a[84:87], v[168:171], v[148:151], a[84:87]\n"
"ds_read_b64_tr_b16 v[236:237], v7 offset:16640\n"
"v_mfma_f32_16x16x32_bf16 a[88:91], v[168:171], v[152:155], a[88:91]\n"
"v_mfma_f32_16x16x32_bf16 a[92:95], v[168:171], v[156:159], a[92:95]\n"
"ds_read_b64_tr_b16 v[238:239], v7 offset:18688\n"
"v_mfma_f32_16x16x32_bf16 a[96:99], v[172:175], v[128:131], a[96:99]\n"
"v_mfma_f32_16x16x32_bf16 a[100:103], v[172:175], v[132:135], a[100:103]\n"
"ds_read_b128 v[208:211], v5 offset:8192\n"
"v_mfma_f32_16x16x32_bf16 a[104:107], v[172:175], v[136:139], a[104:107]\n"
"v_mfma_f32_16x16x32_bf16 a[108:111], v[172:175], v[140:143], a[108:111]\n"
"ds_read_b64_tr_b16 v[240:241], v6 offset:16896\n"
"v_mfma_f32_16x16x32_bf16 a[112:115], v[172:175], v[144:147], a[112:115]\n"
"v_mfma_f32_16x16x32_bf16 a[116:119], v[172:175], v[148:151], a[116:119]\n"
"ds_read_b64_tr_b16 v[242:243], v6 offset:18944\n"
"v_mfma_f32_16x16x32_bf16 a[120:123], v[172:175], v[152:155], a[120:123]\n"
"v_mfma_f32_16x16x32_bf16 a[124:127], v[172:175], v[156:159], a[124:127]\n"
"ds_read_b128 v[212:215], v5 offset:10240\n"
"v_mfma_f32_16x16x32_bf16 a[128:131], v[176:179], v[128:131], a[128:131]\n"
"v_mfma_f32_16x16x32_bf16 a[132:135], v[176:179], v[132:135], a[132:135]\n"
"ds_read_b64_tr_b16 v[244:245], v7 offset:16896\n"
"v_mfma_f32_16x16x32_bf16 a[136:139], v[176:179], v[136:139], a[136:139]\n"
"v_mfma_f32_16x16x32_bf16 a[140:143], v[176:179], v[140:143], a[140:143]\n"
"ds_read_b64_tr_b16 v[246:247], v7 offset:18944\n"
"v_mfma_f32_16x16x32_bf16 a[144:147], v[176:179], v[144:147], a[144:147]\n"
"v_mfma_f32_16x16x32_bf16 a[148:151], v[176:179], v[148:151], a[148:151]\n"
"ds_read_b128 v[216:219], v5 offset:12288\n"
"v_mfma_f32_16x16x32_bf16 a[152:155], v[176:179], v[152:155], a[152:155]\n"
"v_mfma_f32_16x16x32_bf16 a[156:159], v[176:179], v[156:159], a[156:159]\n"
"ds_read_b64_tr_b16 v[248:249], v6 offset:17152\n"
"v_mfma_f32_16x16x32_bf16 a[160:163], v[180:183], v[128:131], a[160:163]\n"
"v_mfma_f32_16x16x32_bf16 a[164:167], v[180:183], v[132:135], a[164:167]\n"
"ds_read_b64_tr_b16 v[250:251], v6 offset:19200\n"
"v_mfma_f32_16x16x32_bf16 a[168:171], v[180:183], v[136:139], a[168:171]\n"
"v_mfma_f32_16x16x32_bf16 a[172:175], v[180:183], v[140:143], a[172:175]\n"
"ds_read_b128 v[220:223], v5 offset:14336\n"
"v_mfma_f32_16x16x32_bf16 a[176:179], v[180:183], v[144:147], a[176:179]\n"
"v_mfma_f32_16x16x32_bf16 a[180:183], v[180:183], v[148:151], a[180:183]\n"
"ds_read_b64_tr_b16 v[252:253], v7 offset:17152\n"
"v_mfma_f32_16x16x32_bf16 a[184:187], v[180:183], v[152:155], a[184:187]\n"
"v_mfma_f32_16x16x32_bf16 a[188:191], v[180:183], v[156:159], a[188:191]\n"
"ds_read_b64_tr_b16 v[254:255], v7 offset:19200\n"
"v_mfma_f32_16x16x32_bf16 a[192:195], v[184:187], v[128:131], a[192:195]\n"
"v_mfma_f32_16x16x32_bf16 a[196:199], v[184:187], v[132:135], a[196:199]\n"
"v_mfma_f32_16x16x32_bf16 a[200:203], v[184:187], v[136:139], a[200:203]\n"
"v_mfma_f32_16x16x32_bf16 a[204:207], v[184:187], v[140:143], a[204:207]\n"
"v_mfma_f32_16x16x32_bf16 a[208:211], v[184:187], v[144:147], a[208:211]\n"
"v_mfma_f32_16x16x32_bf16 a[212:215], v[184:187], v[148:151], a[212:215]\n"
"v_mfma_f32_16x16x32_bf16 a[216:219], v[184:187], v[152:155], a[216:219]\n"
"v_mfma_f32_16x16x32_bf16 a[220:223], v[184:187], v[156:159], a[220:223]\n"
"v_mfma_f32_16x16x32_bf16 a[224:227], v[188:191], v[128:131], a[224:227]\n"
"v_mfma_f32_16x16x32_bf16 a[228:231], v[188:191], v[132:135], a[228:231]\n"
"v_mfma_f32_16x16x32_bf16 a[232:235], v[188:191], v[136:139], a[232:235]\n"
"v_mfma_f32_16x16x32_bf16 a[236:239], v[188:191], v[140:143], a[236:239]\n"
"v_mfma_f32_16x16x32_bf16 a[240:243], v[188:191], v[144:147], a[240:243]\n"
"v_mfma_f32_16x16x32_bf16 a[244:247], v[188:191], v[148:151], a[244:247]\n"
"v_mfma_f32_16x16x32_bf16 a[248:251], v[188:191], v[152:155], a[248:251]\n"
"v_mfma_f32_16x16x32_bf16 a[252:255], v[188:191], v[156:159], a[252:255]\n"
"s_waitcnt lgkmcnt(0)\n"
"v_mfma_f32_16x16x32_bf16 a[0:3], v[224:227], v[192:195], a[0:3]\n"
"v_mfma_f32_16x16x32_bf16 a[4:7], v[224:227], v[196:199], a[4:7]\n"
"v_mfma_f32_16x16x32_bf16 a[8:11], v[224:227], v[200:203], a[8:11]\n"
"v_mfma_f32_16x16x32_bf16 a[12:15], v[224:227], v[204:207], a[12:15]\n"
"v_mfma_f32_16x16x32_bf16 a[16:19], v[224:227], v[208:211], a[16:19]\n"
"v_mfma_f32_16x16x32_bf16 a[20:23], v[224:227], v[212:215], a[20:23]\n"
"v_mfma_f32_16x16x32_bf16 a[24:27], v[224:227], v[216:219], a[24:27]\n"
"v_mfma_f32_16x16x32_bf16 a[28:31], v[224:227], v[220:223], a[28:31]\n"
"v_mfma_f32_16x16x32_bf16 a[32:35], v[228:231], v[192:195], a[32:35]\n"
"v_mfma_f32_16x16x32_bf16 a[36:39], v[228:231], v[196:199], a[36:39]\n"
"v_mfma_f32_16x16x32_bf16 a[40:43], v[228:231], v[200:203], a[40:43]\n"
"v_mfma_f32_16x16x32_bf16 a[44:47], v[228:231], v[204:207], a[44:47]\n"
"v_mfma_f32_16x16x32_bf16 a[48:51], v[228:231], v[208:211], a[48:51]\n"
"v_mfma_f32_16x16x32_bf16 a[52:55], v[228:231], v[212:215], a[52:55]\n"
"v_mfma_f32_16x16x32_bf16 a[56:59], v[228:231], v[216:219], a[56:59]\n"
"v_mfma_f32_16x16x32_bf16 a[60:63], v[228:231], v[220:223], a[60:63]\n"
"v_mfma_f32_16x16x32_bf16 a[64:67], v[232:235], v[192:195], a[64:67]\n"
"v_mfma_f32_16x16x32_bf16 a[68:71], v[232:235], v[196:199], a[68:71]\n"
"v_mfma_f32_16x16x32_bf16 a[72:75], v[232:235], v[200:203], a[72:75]\n"
"v_mfma_f32_16x16x32_bf16 a[76:79], v[232:235], v[204:207], a[76:79]\n"
"v_mfma_f32_16x16x32_bf16 a[80:83], v[232:235], v[208:211], a[80:83]\n"
"v_mfma_f32_16x16x32_bf16 a[84:87], v[232:235], v[212:215], a[84:87]\n"
"v_mfma_f32_16x16x32_bf16 a[88:91], v[232:235], v[216:219], a[88:91]\n"
"v_mfma_f32_16x16x32_bf16 a[92:95], v[232:235], v[220:223], a[92:95]\n"
"v_mfma_f32_16x16x32_bf16 a[96:99], v[236:239], v[192:195], a[96:99]\n"
"v_mfma_f32_16x16x32_bf16 a[100:103], v[236:239], v[196:199], a[100:103]\n"
"v_mfma_f32_16x16x32_bf16 a[104:107], v[236:239], v[200:203], a[104:107]\n"
"v_mfma_f32_16x16x32_bf16 a[108:111], v[236:239], v[204:207], a[108:111]\n"
"v_mfma_f32_16x16x32_bf16 a[112:115], v[236:239], v[208:211], a[112:115]\n"
"v_mfma_f32_16x16x32_bf16 a[116:119], v[236:239], v[212:215], a[116:119]\n"
"v_mfma_f32_16x16x32_bf16 a[120:123], v[236:239], v[216:219], a[120:123]\n"
"v_mfma_f32_16x16x32_bf16 a[124:127], v[236:239], v[220:223], a[124:127]\n"
"v_mfma_f32_16x16x32_bf16 a[128:131], v[240:243], v[192:195], a[128:131]\n"
"v_mfma_f32_16x16x32_bf16 a[132:135], v[240:243], v[196:199], a[132:135]\n"
"v_mfma_f32_16x16x32_bf16 a[136:139], v[240:243], v[200:203], a[136:139]\n"
"v_mfma_f32_16x16x32_bf16 a[140:143], v[240:243], v[204:207], a[140:143]\n"
"v_mfma_f32_16x16x32_bf16 a[144:147], v[240:243], v[208:211], a[144:147]\n"
"v_mfma_f32_16x16x32_bf16 a[148:151], v[240:243], v[212:215], a[148:151]\n"
"v_mfma_f32_16x16x32_bf16 a[152:155], v[240:243], v[216:219], a[152:155]\n"
"v_mfma_f32_16x16x32_bf16 a[156:159], v[240:243], v[220:223], a[156:159]\n"
"v_mfma_f32_16x16x32_bf16 a[160:163], v[244:247], v[192:195], a[160:163]\n"
"v_mfma_f32_16x16x32_bf16 a[164:167], v[244:247], v[196:199], a[164:167]\n"
"v_mfma_f32_16x16x32_bf16 a[168:171], v[244:247], v[200:203], a[168:171]\n"
"v_mfma_f32_16x16x32_bf16 a[172:175], v[244:247], v[204:207], a[172:175]\n"
"v_mfma_f32_16x16x32_bf16 a[176:179], v[244:247], v[208:211], a[176:179]\n"
"v_mfma_f32_16x16x32_bf16 a[180:183], v[244:247], v[212:215], a[180:183]\n"
"v_mfma_f32_16x16x32_bf16 a[184:187], v[244:247], v[216:219], a[184:187]\n"
"v_mfma_f32_16x16x32_bf16 a[188:191], v[244:247], v[220:223], a[188:191]\n"
"v_mfma_f32_16x16x32_bf16 a[192:195], v[248:251], v[192:195], a[192:195]\n"
"v_mfma_f32_16x16x32_bf16 a[196:199], v[248:251], v[196:199], a[196:199]\n"
"v_mfma_f32_16x16x32_bf16 a[200:203], v[248:251], v[200:203], a[200:203]\n"
"v_mfma_f32_16x16x32_bf16 a[204:207], v[248:251], v[204:207], a[204:207]\n"
"v_mfma_f32_16x16x32_bf16 a[208:211], v[248:251], v[208:211], a[208:211]\n"
"v_mfma_f32_16x16x32_bf16 a[212:215], v[248:251], v[212:215], a[212:215]\n"
"v_mfma_f32_16x16x32_bf16 a[216:219], v[248:251], v[216:219], a[216:219]\n"
"v_mfma_f32_16x16x32_bf16 a[220:223], v[248:251], v[220:223], a[220:223]\n"
"v_mfma_f32_16x16x32_bf16 a[224:227], v[252:255], v[192:195], a[224:227]\n"
"v_mfma_f32_16x16x32_bf16 a[228:231], v[252:255], v[196:199], a[228:231]\n"
"v_mfma_f32_16x16x32_bf16 a[232:235], v[252:255], v[200:203], a[232:235]\n"
"v_mfma_f32_16x16x32_bf16 a[236:239], v[252:255], v[204:207], a[236:239]\n"
"v_mfma_f32_16x16x32_bf16 a[240:243], v[252:255], v[208:211], a[240:243]\n"
"v_mfma_f32_16x16x32_bf16 a[244:247], v[252:255], v[212:215], a[244:247]\n"
"v_mfma_f32_16x16x32_bf16 a[248:251], v[252:255], v[216:219], a[248:251]\n"
"v_mfma_f32_16x16x32_bf16 a[252:255], v[252:255], v[220:223], a[252:255]\n"
"s_nop 15\n"
"s_mov_b32 s83, s53\n"
"s_mov_b32 s76, s70\n"
"s_mov_b32 s77, s70\n"
"v_accvgpr_read_b32 v192, a0\n"
"v_accvgpr_read_b32 v193, a1\n"
"v_accvgpr_read_b32 v194, a2\n"
"v_accvgpr_read_b32 v195, a3\n"
"v_accvgpr_read_b32 v196, a32\n"
"v_accvgpr_read_b32 v197, a33\n"
"v_accvgpr_read_b32 v198, a34\n"
"v_accvgpr_read_b32 v199, a35\n"
"v_pk_mul_f32 v[192:193], v[192:193], s[76:77]\n"
"v_pk_mul_f32 v[194:195], v[194:195], s[76:77]\n"
"v_pk_mul_f32 v[196:197], v[196:197], s[76:77]\n"
"v_pk_mul_f32 v[198:199], v[198:199], s[76:77]\n"
"v_cvt_pk_bf16_f32 v200, v192, v193\n"
"v_cvt_pk_bf16_f32 v201, v194, v195\n"
"v_cvt_pk_bf16_f32 v202, v196, v197\n"
"v_cvt_pk_bf16_f32 v203, v198, v199\n"
"buffer_store_dwordx4 v[200:203], v8, s[44:47], s83 offen offset:0 sc1\n"
"v_accvgpr_read_b32 v208, a64\n"
"v_accvgpr_read_b32 v209, a65\n"
"v_accvgpr_read_b32 v210, a66\n"
"v_accvgpr_read_b32 v211, a67\n"
"v_accvgpr_read_b32 v212, a96\n"
"v_accvgpr_read_b32 v213, a97\n"
"v_accvgpr_read_b32 v214, a98\n"
"v_accvgpr_read_b32 v215, a99\n"
"v_pk_mul_f32 v[208:209], v[208:209], s[76:77]\n"
"v_pk_mul_f32 v[210:211], v[210:211], s[76:77]\n"
"v_pk_mul_f32 v[212:213], v[212:213], s[76:77]\n"
"v_pk_mul_f32 v[214:215], v[214:215], s[76:77]\n"
"v_cvt_pk_bf16_f32 v216, v208, v209\n"
"v_cvt_pk_bf16_f32 v217, v210, v211\n"
"v_cvt_pk_bf16_f32 v218, v212, v213\n"
"v_cvt_pk_bf16_f32 v219, v214, v215\n"
"buffer_store_dwordx4 v[216:219], v8, s[44:47], s83 offen offset:1024 sc1\n"
"v_accvgpr_read_b32 v224, a128\n"
"v_accvgpr_read_b32 v225, a129\n"
"v_accvgpr_read_b32 v226, a130\n"
"v_accvgpr_read_b32 v227, a131\n"
"v_accvgpr_read_b32 v228, a160\n"
"v_accvgpr_read_b32 v229, a161\n"
"v_accvgpr_read_b32 v230, a162\n"
"v_accvgpr_read_b32 v231, a163\n"
"v_pk_mul_f32 v[224:225], v[224:225], s[76:77]\n"
"v_pk_mul_f32 v[226:227], v[226:227], s[76:77]\n"
"v_pk_mul_f32 v[228:229], v[228:229], s[76:77]\n"
"v_pk_mul_f32 v[230:231], v[230:231], s[76:77]\n"
"v_cvt_pk_bf16_f32 v232, v224, v225\n"
"v_cvt_pk_bf16_f32 v233, v226, v227\n"
"v_cvt_pk_bf16_f32 v234, v228, v229\n"
"v_cvt_pk_bf16_f32 v235, v230, v231\n"
"buffer_store_dwordx4 v[232:235], v8, s[44:47], s83 offen offset:2048 sc1\n"
"v_accvgpr_read_b32 v240, a192\n"
"v_accvgpr_read_b32 v241, a193\n"
"v_accvgpr_read_b32 v242, a194\n"
"v_accvgpr_read_b32 v243, a195\n"
"v_accvgpr_read_b32 v244, a224\n"
"v_accvgpr_read_b32 v245, a225\n"
"v_accvgpr_read_b32 v246, a226\n"
"v_accvgpr_read_b32 v247, a227\n"
"v_pk_mul_f32 v[240:241], v[240:241], s[76:77]\n"
"v_pk_mul_f32 v[242:243], v[242:243], s[76:77]\n"
"v_pk_mul_f32 v[244:245], v[244:245], s[76:77]\n"
"v_pk_mul_f32 v[246:247], v[246:247], s[76:77]\n"
"v_cvt_pk_bf16_f32 v248, v240, v241\n"
"v_cvt_pk_bf16_f32 v249, v242, v243\n"
"v_cvt_pk_bf16_f32 v250, v244, v245\n"
"v_cvt_pk_bf16_f32 v251, v246, v247\n"
"buffer_store_dwordx4 v[248:251], v8, s[44:47], s83 offen offset:3072 sc1\n"
"s_add_u32 s83, s83, 8192\n"
"v_accvgpr_read_b32 v192, a4\n"
"v_accvgpr_read_b32 v193, a5\n"
"v_accvgpr_read_b32 v194, a6\n"
"v_accvgpr_read_b32 v195, a7\n"
"v_accvgpr_read_b32 v196, a36\n"
"v_accvgpr_read_b32 v197, a37\n"
"v_accvgpr_read_b32 v198, a38\n"
"v_accvgpr_read_b32 v199, a39\n"
"v_pk_mul_f32 v[192:193], v[192:193], s[76:77]\n"
"v_pk_mul_f32 v[194:195], v[194:195], s[76:77]\n"
"v_pk_mul_f32 v[196:197], v[196:197], s[76:77]\n"
"v_pk_mul_f32 v[198:199], v[198:199], s[76:77]\n"
"v_cvt_pk_bf16_f32 v200, v192, v193\n"
"v_cvt_pk_bf16_f32 v201, v194, v195\n"
"v_cvt_pk_bf16_f32 v202, v196, v197\n"
"v_cvt_pk_bf16_f32 v203, v198, v199\n"
"buffer_store_dwordx4 v[200:203], v8, s[44:47], s83 offen offset:0 sc1\n"
"v_accvgpr_read_b32 v208, a68\n"
"v_accvgpr_read_b32 v209, a69\n"
"v_accvgpr_read_b32 v210, a70\n"
"v_accvgpr_read_b32 v211, a71\n"
"v_accvgpr_read_b32 v212, a100\n"
"v_accvgpr_read_b32 v213, a101\n"
"v_accvgpr_read_b32 v214, a102\n"
"v_accvgpr_read_b32 v215, a103\n"
"v_pk_mul_f32 v[208:209], v[208:209], s[76:77]\n"
"v_pk_mul_f32 v[210:211], v[210:211], s[76:77]\n"
"v_pk_mul_f32 v[212:213], v[212:213], s[76:77]\n"
"v_pk_mul_f32 v[214:215], v[214:215], s[76:77]\n"
"v_cvt_pk_bf16_f32 v216, v208, v209\n"
"v_cvt_pk_bf16_f32 v217, v210, v211\n"
"v_cvt_pk_bf16_f32 v218, v212, v213\n"
"v_cvt_pk_bf16_f32 v219, v214, v215\n"
"buffer_store_dwordx4 v[216:219], v8, s[44:47], s83 offen offset:1024 sc1\n"
"v_accvgpr_read_b32 v224, a132\n"
"v_accvgpr_read_b32 v225, a133\n"
"v_accvgpr_read_b32 v226, a134\n"
"v_accvgpr_read_b32 v227, a135\n"
"v_accvgpr_read_b32 v228, a164\n"
"v_accvgpr_read_b32 v229, a165\n"
"v_accvgpr_read_b32 v230, a166\n"
"v_accvgpr_read_b32 v231, a167\n"
"v_pk_mul_f32 v[224:225], v[224:225], s[76:77]\n"
"v_pk_mul_f32 v[226:227], v[226:227], s[76:77]\n"
"v_pk_mul_f32 v[228:229], v[228:229], s[76:77]\n"
"v_pk_mul_f32 v[230:231], v[230:231], s[76:77]\n"
"v_cvt_pk_bf16_f32 v232, v224, v225\n"
"v_cvt_pk_bf16_f32 v233, v226, v227\n"
"v_cvt_pk_bf16_f32 v234, v228, v229\n"
"v_cvt_pk_bf16_f32 v235, v230, v231\n"
"buffer_store_dwordx4 v[232:235], v8, s[44:47], s83 offen offset:2048 sc1\n"
"v_accvgpr_read_b32 v240, a196\n"
"v_accvgpr_read_b32 v241, a197\n"
"v_accvgpr_read_b32 v242, a198\n"
"v_accvgpr_read_b32 v243, a199\n"
"v_accvgpr_read_b32 v244, a228\n"
"v_accvgpr_read_b32 v245, a229\n"
"v_accvgpr_read_b32 v246, a230\n"
"v_accvgpr_read_b32 v247, a231\n"
"v_pk_mul_f32 v[240:241], v[240:241], s[76:77]\n"
"v_pk_mul_f32 v[242:243], v[242:243], s[76:77]\n"
"v_pk_mul_f32 v[244:245], v[244:245], s[76:77]\n"
"v_pk_mul_f32 v[246:247], v[246:247], s[76:77]\n"
"v_cvt_pk_bf16_f32 v248, v240, v241\n"
"v_cvt_pk_bf16_f32 v249, v242, v243\n"
"v_cvt_pk_bf16_f32 v250, v244, v245\n"
"v_cvt_pk_bf16_f32 v251, v246, v247\n"
"buffer_store_dwordx4 v[248:251], v8, s[44:47], s83 offen offset:3072 sc1\n"
"s_add_u32 s83, s83, 8192\n"
"v_accvgpr_read_b32 v192, a8\n"
"v_accvgpr_read_b32 v193, a9\n"
"v_accvgpr_read_b32 v194, a10\n"
"v_accvgpr_read_b32 v195, a11\n"
"v_accvgpr_read_b32 v196, a40\n"
"v_accvgpr_read_b32 v197, a41\n"
"v_accvgpr_read_b32 v198, a42\n"
"v_accvgpr_read_b32 v199, a43\n"
"v_pk_mul_f32 v[192:193], v[192:193], s[76:77]\n"
"v_pk_mul_f32 v[194:195], v[194:195], s[76:77]\n"
"v_pk_mul_f32 v[196:197], v[196:197], s[76:77]\n"
"v_pk_mul_f32 v[198:199], v[198:199], s[76:77]\n"
"v_cvt_pk_bf16_f32 v200, v192, v193\n"
"v_cvt_pk_bf16_f32 v201, v194, v195\n"
"v_cvt_pk_bf16_f32 v202, v196, v197\n"
"v_cvt_pk_bf16_f32 v203, v198, v199\n"
"buffer_store_dwordx4 v[200:203], v8, s[44:47], s83 offen offset:0 sc1\n"
"v_accvgpr_read_b32 v208, a72\n"
"v_accvgpr_read_b32 v209, a73\n"
"v_accvgpr_read_b32 v210, a74\n"
"v_accvgpr_read_b32 v211, a75\n"
"v_accvgpr_read_b32 v212, a104\n"
"v_accvgpr_read_b32 v213, a105\n"
"v_accvgpr_read_b32 v214, a106\n"
"v_accvgpr_read_b32 v215, a107\n"
"v_pk_mul_f32 v[208:209], v[208:209], s[76:77]\n"
"v_pk_mul_f32 v[210:211], v[210:211], s[76:77]\n"
"v_pk_mul_f32 v[212:213], v[212:213], s[76:77]\n"
"v_pk_mul_f32 v[214:215], v[214:215], s[76:77]\n"
"v_cvt_pk_bf16_f32 v216, v208, v209\n"
"v_cvt_pk_bf16_f32 v217, v210, v211\n"
"v_cvt_pk_bf16_f32 v218, v212, v213\n"
"v_cvt_pk_bf16_f32 v219, v214, v215\n"
"buffer_store_dwordx4 v[216:219], v8, s[44:47], s83 offen offset:1024 sc1\n"
"v_accvgpr_read_b32 v224, a136\n"
"v_accvgpr_read_b32 v225, a137\n"
"v_accvgpr_read_b32 v226, a138\n"
"v_accvgpr_read_b32 v227, a139\n"
"v_accvgpr_read_b32 v228, a168\n"
"v_accvgpr_read_b32 v229, a169\n"
"v_accvgpr_read_b32 v230, a170\n"
"v_accvgpr_read_b32 v231, a171\n"
"v_pk_mul_f32 v[224:225], v[224:225], s[76:77]\n"
"v_pk_mul_f32 v[226:227], v[226:227], s[76:77]\n"
"v_pk_mul_f32 v[228:229], v[228:229], s[76:77]\n"
"v_pk_mul_f32 v[230:231], v[230:231], s[76:77]\n"
"v_cvt_pk_bf16_f32 v232, v224, v225\n"
"v_cvt_pk_bf16_f32 v233, v226, v227\n"
"v_cvt_pk_bf16_f32 v234, v228, v229\n"
"v_cvt_pk_bf16_f32 v235, v230, v231\n"
"buffer_store_dwordx4 v[232:235], v8, s[44:47], s83 offen offset:2048 sc1\n"
"v_accvgpr_read_b32 v240, a200\n"
"v_accvgpr_read_b32 v241, a201\n"
"v_accvgpr_read_b32 v242, a202\n"
"v_accvgpr_read_b32 v243, a203\n"
"v_accvgpr_read_b32 v244, a232\n"
"v_accvgpr_read_b32 v245, a233\n"
"v_accvgpr_read_b32 v246, a234\n"
"v_accvgpr_read_b32 v247, a235\n"
"v_pk_mul_f32 v[240:241], v[240:241], s[76:77]\n"
"v_pk_mul_f32 v[242:243], v[242:243], s[76:77]\n"
"v_pk_mul_f32 v[244:245], v[244:245], s[76:77]\n"
"v_pk_mul_f32 v[246:247], v[246:247], s[76:77]\n"
"v_cvt_pk_bf16_f32 v248, v240, v241\n"
"v_cvt_pk_bf16_f32 v249, v242, v243\n"
"v_cvt_pk_bf16_f32 v250, v244, v245\n"
"v_cvt_pk_bf16_f32 v251, v246, v247\n"
"buffer_store_dwordx4 v[248:251], v8, s[44:47], s83 offen offset:3072 sc1\n"
"s_add_u32 s83, s83, 8192\n"
"v_accvgpr_read_b32 v192, a12\n"
"v_accvgpr_read_b32 v193, a13\n"
"v_accvgpr_read_b32 v194, a14\n"
"v_accvgpr_read_b32 v195, a15\n"
"v_accvgpr_read_b32 v196, a44\n"
"v_accvgpr_read_b32 v197, a45\n"
"v_accvgpr_read_b32 v198, a46\n"
"v_accvgpr_read_b32 v199, a47\n"
"v_pk_mul_f32 v[192:193], v[192:193], s[76:77]\n"
"v_pk_mul_f32 v[194:195], v[194:195], s[76:77]\n"
"v_pk_mul_f32 v[196:197], v[196:197], s[76:77]\n"
"v_pk_mul_f32 v[198:199], v[198:199], s[76:77]\n"
"v_cvt_pk_bf16_f32 v200, v192, v193\n"
"v_cvt_pk_bf16_f32 v201, v194, v195\n"
"v_cvt_pk_bf16_f32 v202, v196, v197\n"
"v_cvt_pk_bf16_f32 v203, v198, v199\n"
"buffer_store_dwordx4 v[200:203], v8, s[44:47], s83 offen offset:0 sc1\n"
"v_accvgpr_read_b32 v208, a76\n"
"v_accvgpr_read_b32 v209, a77\n"
"v_accvgpr_read_b32 v210, a78\n"
"v_accvgpr_read_b32 v211, a79\n"
"v_accvgpr_read_b32 v212, a108\n"
"v_accvgpr_read_b32 v213, a109\n"
"v_accvgpr_read_b32 v214, a110\n"
"v_accvgpr_read_b32 v215, a111\n"
"v_pk_mul_f32 v[208:209], v[208:209], s[76:77]\n"
"v_pk_mul_f32 v[210:211], v[210:211], s[76:77]\n"
"v_pk_mul_f32 v[212:213], v[212:213], s[76:77]\n"
"v_pk_mul_f32 v[214:215], v[214:215], s[76:77]\n"
"v_cvt_pk_bf16_f32 v216, v208, v209\n"
"v_cvt_pk_bf16_f32 v217, v210, v211\n"
"v_cvt_pk_bf16_f32 v218, v212, v213\n"
"v_cvt_pk_bf16_f32 v219, v214, v215\n"
"buffer_store_dwordx4 v[216:219], v8, s[44:47], s83 offen offset:1024 sc1\n"
"v_accvgpr_read_b32 v224, a140\n"
"v_accvgpr_read_b32 v225, a141\n"
"v_accvgpr_read_b32 v226, a142\n"
"v_accvgpr_read_b32 v227, a143\n"
"v_accvgpr_read_b32 v228, a172\n"
"v_accvgpr_read_b32 v229, a173\n"
"v_accvgpr_read_b32 v230, a174\n"
"v_accvgpr_read_b32 v231, a175\n"
"v_pk_mul_f32 v[224:225], v[224:225], s[76:77]\n"
"v_pk_mul_f32 v[226:227], v[226:227], s[76:77]\n"
"v_pk_mul_f32 v[228:229], v[228:229], s[76:77]\n"
"v_pk_mul_f32 v[230:231], v[230:231], s[76:77]\n"
"v_cvt_pk_bf16_f32 v232, v224, v225\n"
"v_cvt_pk_bf16_f32 v233, v226, v227\n"
"v_cvt_pk_bf16_f32 v234, v228, v229\n"
"v_cvt_pk_bf16_f32 v235, v230, v231\n"
"buffer_store_dwordx4 v[232:235], v8, s[44:47], s83 offen offset:2048 sc1\n"
"v_accvgpr_read_b32 v240, a204\n"
"v_accvgpr_read_b32 v241, a205\n"
"v_accvgpr_read_b32 v242, a206\n"
"v_accvgpr_read_b32 v243, a207\n"
"v_accvgpr_read_b32 v244, a236\n"
"v_accvgpr_read_b32 v245, a237\n"
"v_accvgpr_read_b32 v246, a238\n"
"v_accvgpr_read_b32 v247, a239\n"
"v_pk_mul_f32 v[240:241], v[240:241], s[76:77]\n"
"v_pk_mul_f32 v[242:243], v[242:243], s[76:77]\n"
"v_pk_mul_f32 v[244:245], v[244:245], s[76:77]\n"
"v_pk_mul_f32 v[246:247], v[246:247], s[76:77]\n"
"v_cvt_pk_bf16_f32 v248, v240, v241\n"
"v_cvt_pk_bf16_f32 v249, v242, v243\n"
"v_cvt_pk_bf16_f32 v250, v244, v245\n"
"v_cvt_pk_bf16_f32 v251, v246, v247\n"
"buffer_store_dwordx4 v[248:251], v8, s[44:47], s83 offen offset:3072 sc1\n"
"s_add_u32 s83, s83, 8192\n"
"v_accvgpr_read_b32 v192, a16\n"
"v_accvgpr_read_b32 v193, a17\n"
"v_accvgpr_read_b32 v194, a18\n"
"v_accvgpr_read_b32 v195, a19\n"
"v_accvgpr_read_b32 v196, a48\n"
"v_accvgpr_read_b32 v197, a49\n"
"v_accvgpr_read_b32 v198, a50\n"
"v_accvgpr_read_b32 v199, a51\n"
"v_pk_mul_f32 v[192:193], v[192:193], s[76:77]\n"
"v_pk_mul_f32 v[194:195], v[194:195], s[76:77]\n"
"v_pk_mul_f32 v[196:197], v[196:197], s[76:77]\n"
"v_pk_mul_f32 v[198:199], v[198:199], s[76:77]\n"
"v_cvt_pk_bf16_f32 v200, v192, v193\n"
"v_cvt_pk_bf16_f32 v201, v194, v195\n"
"v_cvt_pk_bf16_f32 v202, v196, v197\n"
"v_cvt_pk_bf16_f32 v203, v198, v199\n"
"buffer_store_dwordx4 v[200:203], v8, s[44:47], s83 offen offset:0 sc1\n"
"v_accvgpr_read_b32 v208, a80\n"
"v_accvgpr_read_b32 v209, a81\n"
"v_accvgpr_read_b32 v210, a82\n"
"v_accvgpr_read_b32 v211, a83\n"
"v_accvgpr_read_b32 v212, a112\n"
"v_accvgpr_read_b32 v213, a113\n"
"v_accvgpr_read_b32 v214, a114\n"
"v_accvgpr_read_b32 v215, a115\n"
"v_pk_mul_f32 v[208:209], v[208:209], s[76:77]\n"
"v_pk_mul_f32 v[210:211], v[210:211], s[76:77]\n"
"v_pk_mul_f32 v[212:213], v[212:213], s[76:77]\n"
"v_pk_mul_f32 v[214:215], v[214:215], s[76:77]\n"
"v_cvt_pk_bf16_f32 v216, v208, v209\n"
"v_cvt_pk_bf16_f32 v217, v210, v211\n"
"v_cvt_pk_bf16_f32 v218, v212, v213\n"
"v_cvt_pk_bf16_f32 v219, v214, v215\n"
"buffer_store_dwordx4 v[216:219], v8, s[44:47], s83 offen offset:1024 sc1\n"
"v_accvgpr_read_b32 v224, a144\n"
"v_accvgpr_read_b32 v225, a145\n"
"v_accvgpr_read_b32 v226, a146\n"
"v_accvgpr_read_b32 v227, a147\n"
"v_accvgpr_read_b32 v228, a176\n"
"v_accvgpr_read_b32 v229, a177\n"
"v_accvgpr_read_b32 v230, a178\n"
"v_accvgpr_read_b32 v231, a179\n"
"v_pk_mul_f32 v[224:225], v[224:225], s[76:77]\n"
"v_pk_mul_f32 v[226:227], v[226:227], s[76:77]\n"
"v_pk_mul_f32 v[228:229], v[228:229], s[76:77]\n"
"v_pk_mul_f32 v[230:231], v[230:231], s[76:77]\n"
"v_cvt_pk_bf16_f32 v232, v224, v225\n"
"v_cvt_pk_bf16_f32 v233, v226, v227\n"
"v_cvt_pk_bf16_f32 v234, v228, v229\n"
"v_cvt_pk_bf16_f32 v235, v230, v231\n"
"buffer_store_dwordx4 v[232:235], v8, s[44:47], s83 offen offset:2048 sc1\n"
"v_accvgpr_read_b32 v240, a208\n"
"v_accvgpr_read_b32 v241, a209\n"
"v_accvgpr_read_b32 v242, a210\n"
"v_accvgpr_read_b32 v243, a211\n"
"v_accvgpr_read_b32 v244, a240\n"
"v_accvgpr_read_b32 v245, a241\n"
"v_accvgpr_read_b32 v246, a242\n"
"v_accvgpr_read_b32 v247, a243\n"
"v_pk_mul_f32 v[240:241], v[240:241], s[76:77]\n"
"v_pk_mul_f32 v[242:243], v[242:243], s[76:77]\n"
"v_pk_mul_f32 v[244:245], v[244:245], s[76:77]\n"
"v_pk_mul_f32 v[246:247], v[246:247], s[76:77]\n"
"v_cvt_pk_bf16_f32 v248, v240, v241\n"
"v_cvt_pk_bf16_f32 v249, v242, v243\n"
"v_cvt_pk_bf16_f32 v250, v244, v245\n"
"v_cvt_pk_bf16_f32 v251, v246, v247\n"
"buffer_store_dwordx4 v[248:251], v8, s[44:47], s83 offen offset:3072 sc1\n"
"s_add_u32 s83, s83, 8192\n"
"v_accvgpr_read_b32 v192, a20\n"
"v_accvgpr_read_b32 v193, a21\n"
"v_accvgpr_read_b32 v194, a22\n"
"v_accvgpr_read_b32 v195, a23\n"
"v_accvgpr_read_b32 v196, a52\n"
"v_accvgpr_read_b32 v197, a53\n"
"v_accvgpr_read_b32 v198, a54\n"
"v_accvgpr_read_b32 v199, a55\n"
"v_pk_mul_f32 v[192:193], v[192:193], s[76:77]\n"
"v_pk_mul_f32 v[194:195], v[194:195], s[76:77]\n"
"v_pk_mul_f32 v[196:197], v[196:197], s[76:77]\n"
"v_pk_mul_f32 v[198:199], v[198:199], s[76:77]\n"
"v_cvt_pk_bf16_f32 v200, v192, v193\n"
"v_cvt_pk_bf16_f32 v201, v194, v195\n"
"v_cvt_pk_bf16_f32 v202, v196, v197\n"
"v_cvt_pk_bf16_f32 v203, v198, v199\n"
"buffer_store_dwordx4 v[200:203], v8, s[44:47], s83 offen offset:0 sc1\n"
"v_accvgpr_read_b32 v208, a84\n"
"v_accvgpr_read_b32 v209, a85\n"
"v_accvgpr_read_b32 v210, a86\n"
"v_accvgpr_read_b32 v211, a87\n"
"v_accvgpr_read_b32 v212, a116\n"
"v_accvgpr_read_b32 v213, a117\n"
"v_accvgpr_read_b32 v214, a118\n"
"v_accvgpr_read_b32 v215, a119\n"
"v_pk_mul_f32 v[208:209], v[208:209], s[76:77]\n"
"v_pk_mul_f32 v[210:211], v[210:211], s[76:77]\n"
"v_pk_mul_f32 v[212:213], v[212:213], s[76:77]\n"
"v_pk_mul_f32 v[214:215], v[214:215], s[76:77]\n"
"v_cvt_pk_bf16_f32 v216, v208, v209\n"
"v_cvt_pk_bf16_f32 v217, v210, v211\n"
"v_cvt_pk_bf16_f32 v218, v212, v213\n"
"v_cvt_pk_bf16_f32 v219, v214, v215\n"
"buffer_store_dwordx4 v[216:219], v8, s[44:47], s83 offen offset:1024 sc1\n"
"v_accvgpr_read_b32 v224, a148\n"
"v_accvgpr_read_b32 v225, a149\n"
"v_accvgpr_read_b32 v226, a150\n"
"v_accvgpr_read_b32 v227, a151\n"
"v_accvgpr_read_b32 v228, a180\n"
"v_accvgpr_read_b32 v229, a181\n"
"v_accvgpr_read_b32 v230, a182\n"
"v_accvgpr_read_b32 v231, a183\n"
"v_pk_mul_f32 v[224:225], v[224:225], s[76:77]\n"
"v_pk_mul_f32 v[226:227], v[226:227], s[76:77]\n"
"v_pk_mul_f32 v[228:229], v[228:229], s[76:77]\n"
"v_pk_mul_f32 v[230:231], v[230:231], s[76:77]\n"
"v_cvt_pk_bf16_f32 v232, v224, v225\n"
"v_cvt_pk_bf16_f32 v233, v226, v227\n"
"v_cvt_pk_bf16_f32 v234, v228, v229\n"
"v_cvt_pk_bf16_f32 v235, v230, v231\n"
"buffer_store_dwordx4 v[232:235], v8, s[44:47], s83 offen offset:2048 sc1\n"
"v_accvgpr_read_b32 v240, a212\n"
"v_accvgpr_read_b32 v241, a213\n"
"v_accvgpr_read_b32 v242, a214\n"
"v_accvgpr_read_b32 v243, a215\n"
"v_accvgpr_read_b32 v244, a244\n"
"v_accvgpr_read_b32 v245, a245\n"
"v_accvgpr_read_b32 v246, a246\n"
"v_accvgpr_read_b32 v247, a247\n"
"v_pk_mul_f32 v[240:241], v[240:241], s[76:77]\n"
"v_pk_mul_f32 v[242:243], v[242:243], s[76:77]\n"
"v_pk_mul_f32 v[244:245], v[244:245], s[76:77]\n"
"v_pk_mul_f32 v[246:247], v[246:247], s[76:77]\n"
"v_cvt_pk_bf16_f32 v248, v240, v241\n"
"v_cvt_pk_bf16_f32 v249, v242, v243\n"
"v_cvt_pk_bf16_f32 v250, v244, v245\n"
"v_cvt_pk_bf16_f32 v251, v246, v247\n"
"buffer_store_dwordx4 v[248:251], v8, s[44:47], s83 offen offset:3072 sc1\n"
"s_add_u32 s83, s83, 8192\n"
"v_accvgpr_read_b32 v192, a24\n"
"v_accvgpr_read_b32 v193, a25\n"
"v_accvgpr_read_b32 v194, a26\n"
"v_accvgpr_read_b32 v195, a27\n"
"v_accvgpr_read_b32 v196, a56\n"
"v_accvgpr_read_b32 v197, a57\n"
"v_accvgpr_read_b32 v198, a58\n"
"v_accvgpr_read_b32 v199, a59\n"
"v_pk_mul_f32 v[192:193], v[192:193], s[76:77]\n"
"v_pk_mul_f32 v[194:195], v[194:195], s[76:77]\n"
"v_pk_mul_f32 v[196:197], v[196:197], s[76:77]\n"
"v_pk_mul_f32 v[198:199], v[198:199], s[76:77]\n"
"v_cvt_pk_bf16_f32 v200, v192, v193\n"
"v_cvt_pk_bf16_f32 v201, v194, v195\n"
"v_cvt_pk_bf16_f32 v202, v196, v197\n"
"v_cvt_pk_bf16_f32 v203, v198, v199\n"
"buffer_store_dwordx4 v[200:203], v8, s[44:47], s83 offen offset:0 sc1\n"
"v_accvgpr_read_b32 v208, a88\n"
"v_accvgpr_read_b32 v209, a89\n"
"v_accvgpr_read_b32 v210, a90\n"
"v_accvgpr_read_b32 v211, a91\n"
"v_accvgpr_read_b32 v212, a120\n"
"v_accvgpr_read_b32 v213, a121\n"
"v_accvgpr_read_b32 v214, a122\n"
"v_accvgpr_read_b32 v215, a123\n"
"v_pk_mul_f32 v[208:209], v[208:209], s[76:77]\n"
"v_pk_mul_f32 v[210:211], v[210:211], s[76:77]\n"
"v_pk_mul_f32 v[212:213], v[212:213], s[76:77]\n"
"v_pk_mul_f32 v[214:215], v[214:215], s[76:77]\n"
"v_cvt_pk_bf16_f32 v216, v208, v209\n"
"v_cvt_pk_bf16_f32 v217, v210, v211\n"
"v_cvt_pk_bf16_f32 v218, v212, v213\n"
"v_cvt_pk_bf16_f32 v219, v214, v215\n"
"buffer_store_dwordx4 v[216:219], v8, s[44:47], s83 offen offset:1024 sc1\n"
"v_accvgpr_read_b32 v224, a152\n"
"v_accvgpr_read_b32 v225, a153\n"
"v_accvgpr_read_b32 v226, a154\n"
"v_accvgpr_read_b32 v227, a155\n"
"v_accvgpr_read_b32 v228, a184\n"
"v_accvgpr_read_b32 v229, a185\n"
"v_accvgpr_read_b32 v230, a186\n"
"v_accvgpr_read_b32 v231, a187\n"
"v_pk_mul_f32 v[224:225], v[224:225], s[76:77]\n"
"v_pk_mul_f32 v[226:227], v[226:227], s[76:77]\n"
"v_pk_mul_f32 v[228:229], v[228:229], s[76:77]\n"
"v_pk_mul_f32 v[230:231], v[230:231], s[76:77]\n"
"v_cvt_pk_bf16_f32 v232, v224, v225\n"
"v_cvt_pk_bf16_f32 v233, v226, v227\n"
"v_cvt_pk_bf16_f32 v234, v228, v229\n"
"v_cvt_pk_bf16_f32 v235, v230, v231\n"
"buffer_store_dwordx4 v[232:235], v8, s[44:47], s83 offen offset:2048 sc1\n"
"v_accvgpr_read_b32 v240, a216\n"
"v_accvgpr_read_b32 v241, a217\n"
"v_accvgpr_read_b32 v242, a218\n"
"v_accvgpr_read_b32 v243, a219\n"
"v_accvgpr_read_b32 v244, a248\n"
"v_accvgpr_read_b32 v245, a249\n"
"v_accvgpr_read_b32 v246, a250\n"
"v_accvgpr_read_b32 v247, a251\n"
"v_pk_mul_f32 v[240:241], v[240:241], s[76:77]\n"
"v_pk_mul_f32 v[242:243], v[242:243], s[76:77]\n"
"v_pk_mul_f32 v[244:245], v[244:245], s[76:77]\n"
"v_pk_mul_f32 v[246:247], v[246:247], s[76:77]\n"
"v_cvt_pk_bf16_f32 v248, v240, v241\n"
"v_cvt_pk_bf16_f32 v249, v242, v243\n"
"v_cvt_pk_bf16_f32 v250, v244, v245\n"
"v_cvt_pk_bf16_f32 v251, v246, v247\n"
"buffer_store_dwordx4 v[248:251], v8, s[44:47], s83 offen offset:3072 sc1\n"
"s_add_u32 s83, s83, 8192\n"
"v_accvgpr_read_b32 v192, a28\n"
"v_accvgpr_read_b32 v193, a29\n"
"v_accvgpr_read_b32 v194, a30\n"
"v_accvgpr_read_b32 v195, a31\n"
"v_accvgpr_read_b32 v196, a60\n"
"v_accvgpr_read_b32 v197, a61\n"
"v_accvgpr_read_b32 v198, a62\n"
"v_accvgpr_read_b32 v199, a63\n"
"v_pk_mul_f32 v[192:193], v[192:193], s[76:77]\n"
"v_pk_mul_f32 v[194:195], v[194:195], s[76:77]\n"
"v_pk_mul_f32 v[196:197], v[196:197], s[76:77]\n"
"v_pk_mul_f32 v[198:199], v[198:199], s[76:77]\n"
"v_cvt_pk_bf16_f32 v200, v192, v193\n"
"v_cvt_pk_bf16_f32 v201, v194, v195\n"
"v_cvt_pk_bf16_f32 v202, v196, v197\n"
"v_cvt_pk_bf16_f32 v203, v198, v199\n"
"buffer_store_dwordx4 v[200:203], v8, s[44:47], s83 offen offset:0 sc1\n"
"v_accvgpr_read_b32 v208, a92\n"
"v_accvgpr_read_b32 v209, a93\n"
"v_accvgpr_read_b32 v210, a94\n"
"v_accvgpr_read_b32 v211, a95\n"
"v_accvgpr_read_b32 v212, a124\n"
"v_accvgpr_read_b32 v213, a125\n"
"v_accvgpr_read_b32 v214, a126\n"
"v_accvgpr_read_b32 v215, a127\n"
"v_pk_mul_f32 v[208:209], v[208:209], s[76:77]\n"
"v_pk_mul_f32 v[210:211], v[210:211], s[76:77]\n"
"v_pk_mul_f32 v[212:213], v[212:213], s[76:77]\n"
"v_pk_mul_f32 v[214:215], v[214:215], s[76:77]\n"
"v_cvt_pk_bf16_f32 v216, v208, v209\n"
"v_cvt_pk_bf16_f32 v217, v210, v211\n"
"v_cvt_pk_bf16_f32 v218, v212, v213\n"
"v_cvt_pk_bf16_f32 v219, v214, v215\n"
"buffer_store_dwordx4 v[216:219], v8, s[44:47], s83 offen offset:1024 sc1\n"
"v_accvgpr_read_b32 v224, a156\n"
"v_accvgpr_read_b32 v225, a157\n"
"v_accvgpr_read_b32 v226, a158\n"
"v_accvgpr_read_b32 v227, a159\n"
"v_accvgpr_read_b32 v228, a188\n"
"v_accvgpr_read_b32 v229, a189\n"
"v_accvgpr_read_b32 v230, a190\n"
"v_accvgpr_read_b32 v231, a191\n"
"v_pk_mul_f32 v[224:225], v[224:225], s[76:77]\n"
"v_pk_mul_f32 v[226:227], v[226:227], s[76:77]\n"
"v_pk_mul_f32 v[228:229], v[228:229], s[76:77]\n"
"v_pk_mul_f32 v[230:231], v[230:231], s[76:77]\n"
"v_cvt_pk_bf16_f32 v232, v224, v225\n"
"v_cvt_pk_bf16_f32 v233, v226, v227\n"
"v_cvt_pk_bf16_f32 v234, v228, v229\n"
"v_cvt_pk_bf16_f32 v235, v230, v231\n"
"buffer_store_dwordx4 v[232:235], v8, s[44:47], s83 offen offset:2048 sc1\n"
"v_accvgpr_read_b32 v240, a220\n"
"v_accvgpr_read_b32 v241, a221\n"
"v_accvgpr_read_b32 v242, a222\n"
"v_accvgpr_read_b32 v243, a223\n"
"v_accvgpr_read_b32 v244, a252\n"
"v_accvgpr_read_b32 v245, a253\n"
"v_accvgpr_read_b32 v246, a254\n"
"v_accvgpr_read_b32 v247, a255\n"
"v_pk_mul_f32 v[240:241], v[240:241], s[76:77]\n"
"v_pk_mul_f32 v[242:243], v[242:243], s[76:77]\n"
"v_pk_mul_f32 v[244:245], v[244:245], s[76:77]\n"
"v_pk_mul_f32 v[246:247], v[246:247], s[76:77]\n"
"v_cvt_pk_bf16_f32 v248, v240, v241\n"
"v_cvt_pk_bf16_f32 v249, v242, v243\n"
"v_cvt_pk_bf16_f32 v250, v244, v245\n"
"v_cvt_pk_bf16_f32 v251, v246, v247\n"
"buffer_store_dwordx4 v[248:251], v8, s[44:47], s83 offen offset:3072 sc1\n"
"s_add_u32 s83, s83, 8192\n"
"s_waitcnt vmcnt(0)\n"
"s_barrier\n"
"s_cmp_lg_u32 s73, 0\n"
"s_cbranch_scc1 L_fl_q_%=\n"
"s_lshl_b32 s76, s71, 1\n"
"s_and_b32 s77, s72, 1\n"
"s_add_u32 s76, s76, s77\n"
"s_lshl_b32 s76, s76, 3\n"
"s_lshr_b32 s77, s72, 1\n"
"s_add_u32 s76, s76, s77\n"
"s_lshl_b32 s76, s76, 2\n"
"s_add_u32 s76, s76, 126880768\n"
"v_mov_b32 v1, s55\n"
"v_mov_b32 v2, 0\n"
"s_mov_b64 exec, 1\n"
"buffer_store_dword v1, v2, s[44:47], s76 offen sc1\n"
"s_mov_b64 exec, -1\n"
"L_fl_q_%=:\n"
"s_branch L_end_%=\n"
"L_B_tile_%=:\n"
"v_add_u32 v56, 0, v4\n"
"v_xor_b32 v57, 64, v56\n"
"v_add_u32 v58, 0, v5\n"
"v_add_u32 v59, 0, v6\n"
"v_add_u32 v60, 0, v7\n"
"s_waitcnt vmcnt(22)\n"
"s_barrier\n"
"ds_read_b64_tr_b16 v[168:169], v58 offset:0\n"
"ds_read_b64_tr_b16 v[170:171], v58 offset:1536\n"
"ds_read_b64_tr_b16 v[172:173], v59 offset:0\n"
"ds_read_b64_tr_b16 v[174:175], v59 offset:1536\n"
"ds_read_b64_tr_b16 v[176:177], v60 offset:0\n"
"ds_read_b64_tr_b16 v[178:179], v60 offset:1536\n"
"ds_read_b128 v[128:131], v56 offset:0\n"
"ds_read_b128 v[132:135], v56 offset:2048\n"
"ds_read_b128 v[136:139], v56 offset:4096\n"
"ds_read_b128 v[140:143], v56 offset:6144\n"
"ds_read_b128 v[144:147], v56 offset:8192\n"
"ds_read_b128 v[148:151], v56 offset:10240\n"
"ds_read_b128 v[152:155], v56 offset:12288\n"
"ds_read_b128 v[156:159], v56 offset:14336\n"
"ds_read_b128 v[160:163], v56 offset:16384\n"
"s_waitcnt lgkmcnt(0)\n"
"v_mfma_f32_16x16x32_bf16 a[0:3], v[168:171], v[128:131], 0\n"
"ds_read_b64_tr_b16 v[232:233], v58 offset:12288\n"
"v_mfma_f32_16x16x32_bf16 a[4:7], v[168:171], v[132:135], 0\n"
"ds_read_b64_tr_b16 v[234:235], v58 offset:13824\n"
"v_mfma_f32_16x16x32_bf16 a[8:11], v[168:171], v[136:139], 0\n"
"ds_read_b64_tr_b16 v[236:237], v59 offset:12288\n"
"buffer_store_dwordx4 v[64:67], v8, s[44:47], s83 offen offset:0 sc1\n"
"v_mfma_f32_16x16x32_bf16 a[12:15], v[168:171], v[140:143], 0\n"
"ds_read_b64_tr_b16 v[238:239], v59 offset:13824\n"
"v_mfma_f32_16x16x32_bf16 a[16:19], v[168:171], v[144:147], 0\n"
"ds_read_b64_tr_b16 v[240:241], v60 offset:12288\n"
"v_mfma_f32_16x16x32_bf16 a[20:23], v[168:171], v[148:151], 0\n"
"ds_read_b64_tr_b16 v[242:243], v60 offset:13824\n"
"v_mfma_f32_16x16x32_bf16 a[24:27], v[168:171], v[152:155], 0\n"
"ds_read_b128 v[192:195], v57 offset:0\n"
"v_mfma_f32_16x16x32_bf16 a[28:31], v[168:171], v[156:159], 0\n"
"ds_read_b128 v[196:199], v57 offset:2048\n"
"v_mfma_f32_16x16x32_bf16 a[32:35], v[168:171], v[160:163], 0\n"
"ds_read_b128 v[200:203], v57 offset:4096\n"
"v_mfma_f32_16x16x32_bf16 a[36:39], v[172:175], v[128:131], 0\n"
"ds_read_b128 v[204:207], v57 offset:6144\n"
"v_mfma_f32_16x16x32_bf16 a[40:43], v[172:175], v[132:135], 0\n"
"ds_read_b128 v[208:211], v57 offset:8192\n"
"v_mfma_f32_16x16x32_bf16 a[44:47], v[172:175], v[136:139], 0\n"
"ds_read_b128 v[212:215], v57 offset:10240\n"
"buffer_store_dwordx4 v[68:71], v8, s[44:47], s83 offen offset:1024 sc1\n"
"v_mfma_f32_16x16x32_bf16 a[48:51], v[172:175], v[140:143], 0\n"
"ds_read_b128 v[216:219], v57 offset:12288\n"
"v_mfma_f32_16x16x32_bf16 a[52:55], v[172:175], v[144:147], 0\n"
"ds_read_b128 v[220:223], v57 offset:14336\n"
"v_mfma_f32_16x16x32_bf16 a[56:59], v[172:175], v[148:151], 0\n"
"ds_read_b128 v[224:227], v57 offset:16384\n"
"v_mfma_f32_16x16x32_bf16 a[60:63], v[172:175], v[152:155], 0\n"
"v_mfma_f32_16x16x32_bf16 a[64:67], v[172:175], v[156:159], 0\n"
"v_mfma_f32_16x16x32_bf16 a[68:71], v[172:175], v[160:163], 0\n"
"v_mfma_f32_16x16x32_bf16 a[72:75], v[176:179], v[128:131], 0\n"
"v_mfma_f32_16x16x32_bf16 a[76:79], v[176:179], v[132:135], 0\n"
"v_mfma_f32_16x16x32_bf16 a[80:83], v[176:179], v[136:139], 0\n"
"buffer_store_dwordx4 v[72:75], v8, s[44:47], s83 offen offset:2048 sc1\n"
"v_mfma_f32_16x16x32_bf16 a[84:87], v[176:179], v[140:143], 0\n"
"v_mfma_f32_16x16x32_bf16 a[88:91], v[176:179], v[144:147], 0\n"
"v_mfma_f32_16x16x32_bf16 a[92:95], v[176:179], v[148:151], 0\n"
"v_mfma_f32_16x16x32_bf16 a[96:99], v[176:179], v[152:155], 0\n"
"v_mfma_f32_16x16x32_bf16 a[100:103], v[176:179], v[156:159], 0\n"
"v_mfma_f32_16x16x32_bf16 a[104:107], v[176:179], v[160:163], 0\n"
"s_waitcnt vmcnt(14) lgkmcnt(0)\n"
"s_barrier\n"
"v_add_u32 v56, 45056, v4\n"
"v_xor_b32 v57, 64, v56\n"
"v_add_u32 v58, 45056, v5\n"
"v_add_u32 v59, 45056, v6\n"
"v_add_u32 v60, 45056, v7\n"
"v_mfma_f32_16x16x32_bf16 a[0:3], v[232:235], v[192:195], a[0:3]\n"
"ds_read_b64_tr_b16 v[168:169], v58 offset:0\n"
"v_mfma_f32_16x16x32_bf16 a[4:7], v[232:235], v[196:199], a[4:7]\n"
"ds_read_b64_tr_b16 v[170:171], v58 offset:1536\n"
"v_mfma_f32_16x16x32_bf16 a[8:11], v[232:235], v[200:203], a[8:11]\n"
"ds_read_b64_tr_b16 v[172:173], v59 offset:0\n"
"buffer_store_dwordx4 v[76:79], v8, s[44:47], s83 offen offset:3072 sc1\n"
"s_add_u32 s83, s83, s98\n"
"v_mfma_f32_16x16x32_bf16 a[12:15], v[232:235], v[204:207], a[12:15]\n"
"ds_read_b64_tr_b16 v[174:175], v59 offset:1536\n"
"v_mfma_f32_16x16x32_bf16 a[16:19], v[232:235], v[208:211], a[16:19]\n"
"ds_read_b64_tr_b16 v[176:177], v60 offset:0\n"
"v_mfma_f32_16x16x32_bf16 a[20:23], v[232:235], v[212:215], a[20:23]\n"
"ds_read_b64_tr_b16 v[178:179], v60 offset:1536\n"
"v_mfma_f32_16x16x32_bf16 a[24:27], v[232:235], v[216:219], a[24:27]\n"
"ds_read_b128 v[128:131], v56 offset:0\n"
"v_mfma_f32_16x16x32_bf16 a[28:31], v[232:235], v[220:223], a[28:31]\n"
"ds_read_b128 v[132:135], v56 offset:2048\n"
"v_mfma_f32_16x16x32_bf16 a[32:35], v[232:235], v[224:227], a[32:35]\n"
"ds_read_b128 v[136:139], v56 offset:4096\n"
"v_mfma_f32_16x16x32_bf16 a[36:39], v[236:239], v[192:195], a[36:39]\n"
"ds_read_b128 v[140:143], v56 offset:6144\n"
"v_mfma_f32_16x16x32_bf16 a[40:43], v[236:239], v[196:199], a[40:43]\n"
"ds_read_b128 v[144:147], v56 offset:8192\n"
"v_mfma_f32_16x16x32_bf16 a[44:47], v[236:239], v[200:203], a[44:47]\n"
"ds_read_b128 v[148:151], v56 offset:10240\n"
"buffer_store_dwordx4 v[80:83], v8, s[44:47], s83 offen offset:0 sc1\n"
"v_mfma_f32_16x16x32_bf16 a[48:51], v[236:239], v[204:207], a[48:51]\n"
"ds_read_b128 v[152:155], v56 offset:12288\n"
"v_mfma_f32_16x16x32_bf16 a[52:55], v[236:239], v[208:211], a[52:55]\n"
"ds_read_b128 v[156:159], v56 offset:14336\n"
"v_mfma_f32_16x16x32_bf16 a[56:59], v[236:239], v[212:215], a[56:59]\n"
"ds_read_b128 v[160:163], v56 offset:16384\n"
"v_mfma_f32_16x16x32_bf16 a[60:63], v[236:239], v[216:219], a[60:63]\n"
"v_mfma_f32_16x16x32_bf16 a[64:67], v[236:239], v[220:223], a[64:67]\n"
"v_mfma_f32_16x16x32_bf16 a[68:71], v[236:239], v[224:227], a[68:71]\n"
"v_mfma_f32_16x16x32_bf16 a[72:75], v[240:243], v[192:195], a[72:75]\n"
"v_mfma_f32_16x16x32_bf16 a[76:79], v[240:243], v[196:199], a[76:79]\n"
"v_mfma_f32_16x16x32_bf16 a[80:83], v[240:243], v[200:203], a[80:83]\n"
"v_mfma_f32_16x16x32_bf16 a[84:87], v[240:243], v[204:207], a[84:87]\n"
"v_mfma_f32_16x16x32_bf16 a[88:91], v[240:243], v[208:211], a[88:91]\n"
"v_mfma_f32_16x16x32_bf16 a[92:95], v[240:243], v[212:215], a[92:95]\n"
"v_mfma_f32_16x16x32_bf16 a[96:99], v[240:243], v[216:219], a[96:99]\n"
"v_mfma_f32_16x16x32_bf16 a[100:103], v[240:243], v[220:223], a[100:103]\n"
"v_mfma_f32_16x16x32_bf16 a[104:107], v[240:243], v[224:227], a[104:107]\n"
"s_waitcnt lgkmcnt(0)\n"
"v_mfma_f32_16x16x32_bf16 a[0:3], v[168:171], v[128:131], a[0:3]\n"
"ds_read_b64_tr_b16 v[232:233], v58 offset:12288\n"
"s_add_u32 m0, s52, 0\n"
"s_nop 0\n"
"buffer_load_dwordx4 v32, s[56:59], s48 offen lds\n"
"v_mfma_f32_16x16x32_bf16 a[4:7], v[168:171], v[132:135], a[4:7]\n"
"ds_read_b64_tr_b16 v[234:235], v58 offset:13824\n"
"v_mfma_f32_16x16x32_bf16 a[8:11], v[168:171], v[136:139], a[8:11]\n"
"ds_read_b64_tr_b16 v[236:237], v59 offset:12288\n"
"buffer_store_dwordx4 v[84:87], v8, s[44:47], s83 offen offset:1024 sc1\n"
"v_mfma_f32_16x16x32_bf16 a[12:15], v[168:171], v[140:143], a[12:15]\n"
"ds_read_b64_tr_b16 v[238:239], v59 offset:13824\n"
"v_mfma_f32_16x16x32_bf16 a[16:19], v[168:171], v[144:147], a[16:19]\n"
"ds_read_b64_tr_b16 v[240:241], v60 offset:12288\n"
"s_add_u32 m0, s52, 4096\n"
"s_nop 0\n"
"buffer_load_dwordx4 v33, s[56:59], s48 offen lds\n"
"v_mfma_f32_16x16x32_bf16 a[20:23], v[168:171], v[148:151], a[20:23]\n"
"ds_read_b64_tr_b16 v[242:243], v60 offset:13824\n"
"v_mfma_f32_16x16x32_bf16 a[24:27], v[168:171], v[152:155], a[24:27]\n"
"ds_read_b128 v[192:195], v57 offset:0\n"
"v_mfma_f32_16x16x32_bf16 a[28:31], v[168:171], v[156:159], a[28:31]\n"
"ds_read_b128 v[196:199], v57 offset:2048\n"
"v_mfma_f32_16x16x32_bf16 a[32:35], v[168:171], v[160:163], a[32:35]\n"
"ds_read_b128 v[200:203], v57 offset:4096\n"
"s_add_u32 m0, s52, 8192\n"
"s_nop 0\n"
"buffer_load_dwordx4 v34, s[56:59], s48 offen lds\n"
"v_mfma_f32_16x16x32_bf16 a[36:39], v[172:175], v[128:131], a[36:39]\n"
"ds_read_b128 v[204:207], v57 offset:6144\n"
"v_mfma_f32_16x16x32_bf16 a[40:43], v[172:175], v[132:135], a[40:43]\n"
"ds_read_b128 v[208:211], v57 offset:8192\n"
"v_mfma_f32_16x16x32_bf16 a[44:47], v[172:175], v[136:139], a[44:47]\n"
"ds_read_b128 v[212:215], v57 offset:10240\n"
"buffer_store_dwordx4 v[88:91], v8, s[44:47], s83 offen offset:2048 sc1\n"
"v_mfma_f32_16x16x32_bf16 a[48:51], v[172:175], v[140:143], a[48:51]\n"
"ds_read_b128 v[216:219], v57 offset:12288\n"
"v_mfma_f32_16x16x32_bf16 a[52:55], v[172:175], v[144:147], a[52:55]\n"
"ds_read_b128 v[220:223], v57 offset:14336\n"
"s_add_u32 m0, s52, 12288\n"
"s_nop 0\n"
"buffer_load_dwordx4 v35, s[56:59], s48 offen lds\n"
"v_mfma_f32_16x16x32_bf16 a[56:59], v[172:175], v[148:151], a[56:59]\n"
"ds_read_b128 v[224:227], v57 offset:16384\n"
"v_mfma_f32_16x16x32_bf16 a[60:63], v[172:175], v[152:155], a[60:63]\n"
"v_mfma_f32_16x16x32_bf16 a[64:67], v[172:175], v[156:159], a[64:67]\n"
"v_mfma_f32_16x16x32_bf16 a[68:71], v[172:175], v[160:163], a[68:71]\n"
"s_add_u32 m0, s52, 16384\n"
"s_nop 0\n"
"buffer_load_dwordx4 v36, s[56:59], s48 offen lds\n"
"v_mfma_f32_16x16x32_bf16 a[72:75], v[176:179], v[128:131], a[72:75]\n"
"v_mfma_f32_16x16x32_bf16 a[76:79], v[176:179], v[132:135], a[76:79]\n"
"v_mfma_f32_16x16x32_bf16 a[80:83], v[176:179], v[136:139], a[80:83]\n"
"buffer_store_dwordx4 v[92:95], v8, s[44:47], s83 offen offset:3072 sc1\n"
"s_add_u32 s83, s83, s99\n"
"v_mfma_f32_16x16x32_bf16 a[84:87], v[176:179], v[140:143], a[84:87]\n"
"s_add_u32 m0, s52, 20480\n"
"s_nop 0\n"
"buffer_load_dwordx4 v40, s[60:63], s49 offen lds\n"
"v_mfma_f32_16x16x32_bf16 a[88:91], v[176:179], v[144:147], a[88:91]\n"
"v_mfma_f32_16x16x32_bf16 a[92:95], v[176:179], v[148:151], a[92:95]\n"
"v_mfma_f32_16x16x32_bf16 a[96:99], v[176:179], v[152:155], a[96:99]\n"
"v_mfma_f32_16x16x32_bf16 a[100:103], v[176:179], v[156:159], a[100:103]\n"
"v_mfma_f32_16x16x32_bf16 a[104:107], v[176:179], v[160:163], a[104:107]\n"
"s_waitcnt vmcnt(14) lgkmcnt(0)\n"
"s_barrier\n"
"v_add_u32 v56, 90112, v4\n"
"v_xor_b32 v57, 64, v56\n"
"v_add_u32 v58, 90112, v5\n"
"v_add_u32 v59, 90112, v6\n"
"v_add_u32 v60, 90112, v7\n"
"v_mfma_f32_16x16x32_bf16 a[0:3], v[232:235], v[192:195], a[0:3]\n"
"ds_read_b64_tr_b16 v[168:169], v58 offset:0\n"
"s_add_u32 m0, s52, 24576\n"
"s_nop 0\n"
"buffer_load_dwordx4 v41, s[60:63], s49 offen lds\n"
"v_mfma_f32_16x16x32_bf16 a[4:7], v[232:235], v[196:199], a[4:7]\n"
"ds_read_b64_tr_b16 v[170:171], v58 offset:1536\n"
"v_mfma_f32_16x16x32_bf16 a[8:11], v[232:235], v[200:203], a[8:11]\n"
"ds_read_b64_tr_b16 v[172:173], v59 offset:0\n"
"buffer_store_dwordx4 v[96:99], v8, s[44:47], s83 offen offset:0 sc1\n"
"v_mfma_f32_16x16x32_bf16 a[12:15], v[232:235], v[204:207], a[12:15]\n"
"ds_read_b64_tr_b16 v[174:175], v59 offset:1536\n"
"v_mfma_f32_16x16x32_bf16 a[16:19], v[232:235], v[208:211], a[16:19]\n"
"ds_read_b64_tr_b16 v[176:177], v60 offset:0\n"
"v_mfma_f32_16x16x32_bf16 a[20:23], v[232:235], v[212:215], a[20:23]\n"
"ds_read_b64_tr_b16 v[178:179], v60 offset:1536\n"
"s_add_u32 m0, s52, 28672\n"
"s_nop 0\n"
"buffer_load_dwordx4 v42, s[60:63], s49 offen lds\n"
"v_mfma_f32_16x16x32_bf16 a[24:27], v[232:235], v[216:219], a[24:27]\n"
"ds_read_b128 v[128:131], v56 offset:0\n"
"v_mfma_f32_16x16x32_bf16 a[28:31], v[232:235], v[220:223], a[28:31]\n"
"ds_read_b128 v[132:135], v56 offset:2048\n"
"v_mfma_f32_16x16x32_bf16 a[32:35], v[232:235], v[224:227], a[32:35]\n"
"ds_read_b128 v[136:139], v56 offset:4096\n"
"v_mfma_f32_16x16x32_bf16 a[36:39], v[236:239], v[192:195], a[36:39]\n"
"ds_read_b128 v[140:143], v56 offset:6144\n"
"v_mfma_f32_16x16x32_bf16 a[40:43], v[236:239], v[196:199], a[40:43]\n"
"ds_read_b128 v[144:147], v56 offset:8192\n"
"s_add_u32 m0, s52, 32768\n"
"s_nop 0\n"
"buffer_load_dwordx4 v43, s[60:63], s49 offen lds\n"
"v_mfma_f32_16x16x32_bf16 a[44:47], v[236:239], v[200:203], a[44:47]\n"
"ds_read_b128 v[148:151], v56 offset:10240\n"
"buffer_store_dwordx4 v[100:103], v8, s[44:47], s83 offen offset:1024 sc1\n"
"v_mfma_f32_16x16x32_bf16 a[48:51], v[236:239], v[204:207], a[48:51]\n"
"ds_read_b128 v[152:155], v56 offset:12288\n"
"v_mfma_f32_16x16x32_bf16 a[52:55], v[236:239], v[208:211], a[52:55]\n"
"ds_read_b128 v[156:159], v56 offset:14336\n"
"v_mfma_f32_16x16x32_bf16 a[56:59], v[236:239], v[212:215], a[56:59]\n"
"ds_read_b128 v[160:163], v56 offset:16384\n"
"v_mfma_f32_16x16x32_bf16 a[60:63], v[236:239], v[216:219], a[60:63]\n"
"s_add_u32 m0, s52, 36864\n"
"s_nop 0\n"
"buffer_load_dwordx4 v44, s[60:63], s49 offen lds\n"
"v_mfma_f32_16x16x32_bf16 a[64:67], v[236:239], v[220:223], a[64:67]\n"
"v_mfma_f32_16x16x32_bf16 a[68:71], v[236:239], v[224:227], a[68:71]\n"
"v_mfma_f32_16x16x32_bf16 a[72:75], v[240:243], v[192:195], a[72:75]\n"
"v_mfma_f32_16x16x32_bf16 a[76:79], v[240:243], v[196:199], a[76:79]\n"
"v_mfma_f32_16x16x32_bf16 a[80:83], v[240:243], v[200:203], a[80:83]\n"
"s_add_u32 m0, s52, 40960\n"
"s_nop 0\n"
"buffer_load_dwordx4 v45, s[60:63], s49 offen lds\n"
"s_add_u32 s48, s48, 128\n"
"s_add_u32 s49, s49, 172032\n"
"v_mfma_f32_16x16x32_bf16 a[84:87], v[240:243], v[204:207], a[84:87]\n"
"v_mfma_f32_16x16x32_bf16 a[88:91], v[240:243], v[208:211], a[88:91]\n"
"v_mfma_f32_16x16x32_bf16 a[92:95], v[240:243], v[212:215], a[92:95]\n"
"v_mfma_f32_16x16x32_bf16 a[96:99], v[240:243], v[216:219], a[96:99]\n"
"v_mfma_f32_16x16x32_bf16 a[100:103], v[240:243], v[220:223], a[100:103]\n"
"v_mfma_f32_16x16x32_bf16 a[104:107], v[240:243], v[224:227], a[104:107]\n"
"s_waitcnt lgkmcnt(0)\n"
"v_mfma_f32_16x16x32_bf16 a[0:3], v[168:171], v[128:131], a[0:3]\n"
"ds_read_b64_tr_b16 v[232:233], v58 offset:12288\n"
"s_add_u32 m0, s52, 45056\n"
"s_nop 0\n"
"buffer_load_dwordx4 v32, s[56:59], s48 offen lds\n"
"v_mfma_f32_16x16x32_bf16 a[4:7], v[168:171], v[132:135], a[4:7]\n"
"ds_read_b64_tr_b16 v[234:235], v58 offset:13824\n"
"v_mfma_f32_16x16x32_bf16 a[8:11], v[168:171], v[136:139], a[8:11]\n"
"ds_read_b64_tr_b16 v[236:237], v59 offset:12288\n"
"buffer_store_dwordx4 v[104:107], v8, s[44:47], s83 offen offset:2048 sc1\n"
"v_mfma_f32_16x16x32_bf16 a[12:15], v[168:171], v[140:143], a[12:15]\n"
"ds_read_b64_tr_b16 v[238:239], v59 offset:13824\n"
"v_mfma_f32_16x16x32_bf16 a[16:19], v[168:171], v[144:147], a[16:19]\n"
"ds_read_b64_tr_b16 v[240:241], v60 offset:12288\n"
"s_add_u32 m0, s52, 49152\n"
"s_nop 0\n"
"buffer_load_dwordx4 v33, s[56:59], s48 offen lds\n"
"v_mfma_f32_16x16x32_bf16 a[20:23], v[168:171], v[148:151], a[20:23]\n"
"ds_read_b64_tr_b16 v[242:243], v60 offset:13824\n"
"v_mfma_f32_16x16x32_bf16 a[24:27], v[168:171], v[152:155], a[24:27]\n"
"ds_read_b128 v[192:195], v57 offset:0\n"
"v_mfma_f32_16x16x32_bf16 a[28:31], v[168:171], v[156:159], a[28:31]\n"
"ds_read_b128 v[196:199], v57 offset:2048\n"
"v_mfma_f32_16x16x32_bf16 a[32:35], v[168:171], v[160:163], a[32:35]\n"
"ds_read_b128 v[200:203], v57 offset:4096\n"
"s_add_u32 m0, s52, 53248\n"
"s_nop 0\n"
"buffer_load_dwordx4 v34, s[56:59], s48 offen lds\n"
"v_mfma_f32_16x16x32_bf16 a[36:39], v[172:175], v[128:131], a[36:39]\n"
"ds_read_b128 v[204:207], v57 offset:6144\n"
"v_mfma_f32_16x16x32_bf16 a[40:43], v[172:175], v[132:135], a[40:43]\n"
"ds_read_b128 v[208:211], v57 offset:8192\n"
"v_mfma_f32_16x16x32_bf16 a[44:47], v[172:175], v[136:139], a[44:47]\n"
"ds_read_b128 v[212:215], v57 offset:10240\n"
"buffer_store_dwordx4 v[108:111], v8, s[44:47], s83 offen offset:3072 sc1\n"
"s_add_u32 s83, s83, s98\n"
"v_mfma_f32_16x16x32_bf16 a[48:51], v[172:175], v[140:143], a[48:51]\n"
"ds_read_b128 v[216:219], v57 offset:12288\n"
"v_mfma_f32_16x16x32_bf16 a[52:55], v[172:175], v[144:147], a[52:55]\n"
"ds_read_b128 v[220:223], v57 offset:14336\n"
"s_add_u32 m0, s52, 57344\n"
"s_nop 0\n"
"buffer_load_dwordx4 v35, s[56:59], s48 offen lds\n"
"v_mfma_f32_16x16x32_bf16 a[56:59], v[172:175], v[148:151], a[56:59]\n"
"ds_read_b128 v[224:227], v57 offset:16384\n"
"v_mfma_f32_16x16x32_bf16 a[60:63], v[172:175], v[152:155], a[60:63]\n"
"v_mfma_f32_16x16x32_bf16 a[64:67], v[172:175], v[156:159], a[64:67]\n"
"v_mfma_f32_16x16x32_bf16 a[68:71], v[172:175], v[160:163], a[68:71]\n"
"s_add_u32 m0, s52, 61440\n"
"s_nop 0\n"
"buffer_load_dwordx4 v36, s[56:59], s48 offen lds\n"
"v_mfma_f32_16x16x32_bf16 a[72:75], v[176:179], v[128:131], a[72:75]\n"
"v_mfma_f32_16x16x32_bf16 a[76:79], v[176:179], v[132:135], a[76:79]\n"
"v_mfma_f32_16x16x32_bf16 a[80:83], v[176:179], v[136:139], a[80:83]\n"
"buffer_store_dwordx4 v[112:115], v8, s[44:47], s83 offen offset:0 sc1\n"
"v_mfma_f32_16x16x32_bf16 a[84:87], v[176:179], v[140:143], a[84:87]\n"
"s_add_u32 m0, s52, 65536\n"
"s_nop 0\n"
"buffer_load_dwordx4 v40, s[60:63], s49 offen lds\n"
"v_mfma_f32_16x16x32_bf16 a[88:91], v[176:179], v[144:147], a[88:91]\n"
"v_mfma_f32_16x16x32_bf16 a[92:95], v[176:179], v[148:151], a[92:95]\n"
"v_mfma_f32_16x16x32_bf16 a[96:99], v[176:179], v[152:155], a[96:99]\n"
"v_mfma_f32_16x16x32_bf16 a[100:103], v[176:179], v[156:159], a[100:103]\n"
"v_mfma_f32_16x16x32_bf16 a[104:107], v[176:179], v[160:163], a[104:107]\n"
"s_waitcnt vmcnt(9) lgkmcnt(0)\n"
"s_barrier\n"
"v_add_u32 v56, 0, v4\n"
"v_xor_b32 v57, 64, v56\n"
"v_add_u32 v58, 0, v5\n"
"v_add_u32 v59, 0, v6\n"
"v_add_u32 v60, 0, v7\n"
"v_mfma_f32_16x16x32_bf16 a[0:3], v[232:235], v[192:195], a[0:3]\n"
"ds_read_b64_tr_b16 v[168:169], v58 offset:0\n"
"s_add_u32 m0, s52, 69632\n"
"s_nop 0\n"
"buffer_load_dwordx4 v41, s[60:63], s49 offen lds\n"
"v_mfma_f32_16x16x32_bf16 a[4:7], v[232:235], v[196:199], a[4:7]\n"
"ds_read_b64_tr_b16 v[170:171], v58 offset:1536\n"
"v_mfma_f32_16x16x32_bf16 a[8:11], v[232:235], v[200:203], a[8:11]\n"
"ds_read_b64_tr_b16 v[172:173], v59 offset:0\n"
"buffer_store_dwordx4 v[116:119], v8, s[44:47], s83 offen offset:1024 sc1\n"
"v_mfma_f32_16x16x32_bf16 a[12:15], v[232:235], v[204:207], a[12:15]\n"
"ds_read_b64_tr_b16 v[174:175], v59 offset:1536\n"
"v_mfma_f32_16x16x32_bf16 a[16:19], v[232:235], v[208:211], a[16:19]\n"
"ds_read_b64_tr_b16 v[176:177], v60 offset:0\n"
"v_mfma_f32_16x16x32_bf16 a[20:23], v[232:235], v[212:215], a[20:23]\n"
"ds_read_b64_tr_b16 v[178:179], v60 offset:1536\n"
"s_add_u32 m0, s52, 73728\n"
"s_nop 0\n"
"buffer_load_dwordx4 v42, s[60:63], s49 offen lds\n"
"v_mfma_f32_16x16x32_bf16 a[24:27], v[232:235], v[216:219], a[24:27]\n"
"ds_read_b128 v[128:131], v56 offset:0\n"
"v_mfma_f32_16x16x32_bf16 a[28:31], v[232:235], v[220:223], a[28:31]\n"
"ds_read_b128 v[132:135], v56 offset:2048\n"
"v_mfma_f32_16x16x32_bf16 a[32:35], v[232:235], v[224:227], a[32:35]\n"
"ds_read_b128 v[136:139], v56 offset:4096\n"
"v_mfma_f32_16x16x32_bf16 a[36:39], v[236:239], v[192:195], a[36:39]\n"
"ds_read_b128 v[140:143], v56 offset:6144\n"
"v_mfma_f32_16x16x32_bf16 a[40:43], v[236:239], v[196:199], a[40:43]\n"
"ds_read_b128 v[144:147], v56 offset:8192\n"
"s_add_u32 m0, s52, 77824\n"
"s_nop 0\n"
"buffer_load_dwordx4 v43, s[60:63], s49 offen lds\n"
"v_mfma_f32_16x16x32_bf16 a[44:47], v[236:239], v[200:203], a[44:47]\n"
"ds_read_b128 v[148:151], v56 offset:10240\n"
"buffer_store_dwordx4 v[120:123], v8, s[44:47], s83 offen offset:2048 sc1\n"
"v_mfma_f32_16x16x32_bf16 a[48:51], v[236:239], v[204:207], a[48:51]\n"
"ds_read_b128 v[152:155], v56 offset:12288\n"
"v_mfma_f32_16x16x32_bf16 a[52:55], v[236:239], v[208:211], a[52:55]\n"
"ds_read_b128 v[156:159], v56 offset:14336\n"
"v_mfma_f32_16x16x32_bf16 a[56:59], v[236:239], v[212:215], a[56:59]\n"
"ds_read_b128 v[160:163], v56 offset:16384\n"
"v_mfma_f32_16x16x32_bf16 a[60:63], v[236:239], v[216:219], a[60:63]\n"
"s_add_u32 m0, s52, 81920\n"
"s_nop 0\n"
"buffer_load_dwordx4 v44, s[60:63], s49 offen lds\n"
"v_mfma_f32_16x16x32_bf16 a[64:67], v[236:239], v[220:223], a[64:67]\n"
"v_mfma_f32_16x16x32_bf16 a[68:71], v[236:239], v[224:227], a[68:71]\n"
"v_mfma_f32_16x16x32_bf16 a[72:75], v[240:243], v[192:195], a[72:75]\n"
"v_mfma_f32_16x16x32_bf16 a[76:79], v[240:243], v[196:199], a[76:79]\n"
"v_mfma_f32_16x16x32_bf16 a[80:83], v[240:243], v[200:203], a[80:83]\n"
"s_add_u32 m0, s52, 86016\n"
"s_nop 0\n"
"buffer_load_dwordx4 v45, s[60:63], s49 offen lds\n"
"s_add_u32 s48, s48, 128\n"
"s_add_u32 s49, s49, 172032\n"
"v_mfma_f32_16x16x32_bf16 a[84:87], v[240:243], v[204:207], a[84:87]\n"
"v_mfma_f32_16x16x32_bf16 a[88:91], v[240:243], v[208:211], a[88:91]\n"
"v_mfma_f32_16x16x32_bf16 a[92:95], v[240:243], v[212:215], a[92:95]\n"
"v_mfma_f32_16x16x32_bf16 a[96:99], v[240:243], v[216:219], a[96:99]\n"
"v_mfma_f32_16x16x32_bf16 a[100:103], v[240:243], v[220:223], a[100:103]\n"
"v_mfma_f32_16x16x32_bf16 a[104:107], v[240:243], v[224:227], a[104:107]\n"
"s_waitcnt lgkmcnt(0)\n"
"v_mfma_f32_16x16x32_bf16 a[0:3], v[168:171], v[128:131], a[0:3]\n"
"ds_read_b64_tr_b16 v[232:233], v58 offset:12288\n"
"s_add_u32 m0, s52, 90112\n"
"s_nop 0\n"
"buffer_load_dwordx4 v32, s[56:59], s48 offen lds\n"
"v_mfma_f32_16x16x32_bf16 a[4:7], v[168:171], v[132:135], a[4:7]\n"
"ds_read_b64_tr_b16 v[234:235], v58 offset:13824\n"
"v_mfma_f32_16x16x32_bf16 a[8:11], v[168:171], v[136:139], a[8:11]\n"
"ds_read_b64_tr_b16 v[236:237], v59 offset:12288\n"
"buffer_store_dwordx4 v[124:127], v8, s[44:47], s83 offen offset:3072 sc1\n"
"s_add_u32 s83, s83, s99\n"
"v_mfma_f32_16x16x32_bf16 a[12:15], v[168:171], v[140:143], a[12:15]\n"
"ds_read_b64_tr_b16 v[238:239], v59 offset:13824\n"
"v_mfma_f32_16x16x32_bf16 a[16:19], v[168:171], v[144:147], a[16:19]\n"
"ds_read_b64_tr_b16 v[240:241], v60 offset:12288\n"
"s_add_u32 m0, s52, 94208\n"
"s_nop 0\n"
"buffer_load_dwordx4 v33, s[56:59], s48 offen lds\n"
"v_mfma_f32_16x16x32_bf16 a[20:23], v[168:171], v[148:151], a[20:23]\n"
"ds_read_b64_tr_b16 v[242:243], v60 offset:13824\n"
"v_mfma_f32_16x16x32_bf16 a[24:27], v[168:171], v[152:155], a[24:27]\n"
"ds_read_b128 v[192:195], v57 offset:0\n"
"v_mfma_f32_16x16x32_bf16 a[28:31], v[168:171], v[156:159], a[28:31]\n"
"ds_read_b128 v[196:199], v57 offset:2048\n"
"v_mfma_f32_16x16x32_bf16 a[32:35], v[168:171], v[160:163], a[32:35]\n"
"ds_read_b128 v[200:203], v57 offset:4096\n"
"s_add_u32 m0, s52, 98304\n"
"s_nop 0\n"
"buffer_load_dwordx4 v34, s[56:59], s48 offen lds\n"
"v_mfma_f32_16x16x32_bf16 a[36:39], v[172:175], v[128:131], a[36:39]\n"
"ds_read_b128 v[204:207], v57 offset:6144\n"
"v_mfma_f32_16x16x32_bf16 a[40:43], v[172:175], v[132:135], a[40:43]\n"
"ds_read_b128 v[208:211], v57 offset:8192\n"
"v_mfma_f32_16x16x32_bf16 a[44:47], v[172:175], v[136:139], a[44:47]\n"
"ds_read_b128 v[212:215], v57 offset:10240\n"
"buffer_store_dwordx4 a[192:195], v8, s[44:47], s83 offen offset:0 sc1\n"
"v_mfma_f32_16x16x32_bf16 a[48:51], v[172:175], v[140:143], a[48:51]\n"
"ds_read_b128 v[216:219], v57 offset:12288\n"
"v_mfma_f32_16x16x32_bf16 a[52:55], v[172:175], v[144:147], a[52:55]\n"
"ds_read_b128 v[220:223], v57 offset:14336\n"
"s_add_u32 m0, s52, 102400\n"
"s_nop 0\n"
"buffer_load_dwordx4 v35, s[56:59], s48 offen lds\n"
"v_mfma_f32_16x16x32_bf16 a[56:59], v[172:175], v[148:151], a[56:59]\n"
"ds_read_b128 v[224:227], v57 offset:16384\n"
"v_mfma_f32_16x16x32_bf16 a[60:63], v[172:175], v[152:155], a[60:63]\n"
"v_mfma_f32_16x16x32_bf16 a[64:67], v[172:175], v[156:159], a[64:67]\n"
"v_mfma_f32_16x16x32_bf16 a[68:71], v[172:175], v[160:163], a[68:71]\n"
"s_add_u32 m0, s52, 106496\n"
"s_nop 0\n"
"buffer_load_dwordx4 v36, s[56:59], s48 offen lds\n"
"v_mfma_f32_16x16x32_bf16 a[72:75], v[176:179], v[128:131], a[72:75]\n"
"v_mfma_f32_16x16x32_bf16 a[76:79], v[176:179], v[132:135], a[76:79]\n"
"v_mfma_f32_16x16x32_bf16 a[80:83], v[176:179], v[136:139], a[80:83]\n"
"buffer_store_dwordx4 a[196:199], v8, s[44:47], s83 offen offset:1024 sc1\n"
"v_mfma_f32_16x16x32_bf16 a[84:87], v[176:179], v[140:143], a[84:87]\n"
"s_add_u32 m0, s52, 110592\n"
"s_nop 0\n"
"buffer_load_dwordx4 v40, s[60:63], s49 offen lds\n"
"v_mfma_f32_16x16x32_bf16 a[88:91], v[176:179], v[144:147], a[88:91]\n"
"v_mfma_f32_16x16x32_bf16 a[92:95], v[176:179], v[148:151], a[92:95]\n"
"v_mfma_f32_16x16x32_bf16 a[96:99], v[176:179], v[152:155], a[96:99]\n"
"v_mfma_f32_16x16x32_bf16 a[100:103], v[176:179], v[156:159], a[100:103]\n"
"v_mfma_f32_16x16x32_bf16 a[104:107], v[176:179], v[160:163], a[104:107]\n"
"s_waitcnt vmcnt(9) lgkmcnt(0)\n"
"s_barrier\n"
"v_add_u32 v56, 45056, v4\n"
"v_xor_b32 v57, 64, v56\n"
"v_add_u32 v58, 45056, v5\n"
"v_add_u32 v59, 45056, v6\n"
"v_add_u32 v60, 45056, v7\n"
"v_mfma_f32_16x16x32_bf16 a[0:3], v[232:235], v[192:195], a[0:3]\n"
"ds_read_b64_tr_b16 v[168:169], v58 offset:0\n"
"s_add_u32 m0, s52, 114688\n"
"s_nop 0\n"
"buffer_load_dwordx4 v41, s[60:63], s49 offen lds\n"
"v_mfma_f32_16x16x32_bf16 a[4:7], v[232:235], v[196:199], a[4:7]\n"
"ds_read_b64_tr_b16 v[170:171], v58 offset:1536\n"
"v_mfma_f32_16x16x32_bf16 a[8:11], v[232:235], v[200:203], a[8:11]\n"
"ds_read_b64_tr_b16 v[172:173], v59 offset:0\n"
"buffer_store_dwordx4 a[200:203], v8, s[44:47], s83 offen offset:2048 sc1\n"
"v_mfma_f32_16x16x32_bf16 a[12:15], v[232:235], v[204:207], a[12:15]\n"
"ds_read_b64_tr_b16 v[174:175], v59 offset:1536\n"
"v_mfma_f32_16x16x32_bf16 a[16:19], v[232:235], v[208:211], a[16:19]\n"
"ds_read_b64_tr_b16 v[176:177], v60 offset:0\n"
"v_mfma_f32_16x16x32_bf16 a[20:23], v[232:235], v[212:215], a[20:23]\n"
"ds_read_b64_tr_b16 v[178:179], v60 offset:1536\n"
"s_add_u32 m0, s52, 118784\n"
"s_nop 0\n"
"buffer_load_dwordx4 v42, s[60:63], s49 offen lds\n"
"v_mfma_f32_16x16x32_bf16 a[24:27], v[232:235], v[216:219], a[24:27]\n"
"ds_read_b128 v[128:131], v56 offset:0\n"
"v_mfma_f32_16x16x32_bf16 a[28:31], v[232:235], v[220:223], a[28:31]\n"
"ds_read_b128 v[132:135], v56 offset:2048\n"
"v_mfma_f32_16x16x32_bf16 a[32:35], v[232:235], v[224:227], a[32:35]\n"
"ds_read_b128 v[136:139], v56 offset:4096\n"
"v_mfma_f32_16x16x32_bf16 a[36:39], v[236:239], v[192:195], a[36:39]\n"
"ds_read_b128 v[140:143], v56 offset:6144\n"
"v_mfma_f32_16x16x32_bf16 a[40:43], v[236:239], v[196:199], a[40:43]\n"
"ds_read_b128 v[144:147], v56 offset:8192\n"
"s_add_u32 m0, s52, 122880\n"
"s_nop 0\n"
"buffer_load_dwordx4 v43, s[60:63], s49 offen lds\n"
"v_mfma_f32_16x16x32_bf16 a[44:47], v[236:239], v[200:203], a[44:47]\n"
"ds_read_b128 v[148:151], v56 offset:10240\n"
"buffer_store_dwordx4 a[204:207], v8, s[44:47], s83 offen offset:3072 sc1\n"
"s_add_u32 s83, s83, s98\n"
"v_mfma_f32_16x16x32_bf16 a[48:51], v[236:239], v[204:207], a[48:51]\n"
"ds_read_b128 v[152:155], v56 offset:12288\n"
"v_mfma_f32_16x16x32_bf16 a[52:55], v[236:239], v[208:211], a[52:55]\n"
"ds_read_b128 v[156:159], v56 offset:14336\n"
"v_mfma_f32_16x16x32_bf16 a[56:59], v[236:239], v[212:215], a[56:59]\n"
"ds_read_b128 v[160:163], v56 offset:16384\n"
"v_mfma_f32_16x16x32_bf16 a[60:63], v[236:239], v[216:219], a[60:63]\n"
"s_add_u32 m0, s52, 126976\n"
"s_nop 0\n"
"buffer_load_dwordx4 v44, s[60:63], s49 offen lds\n"
"v_mfma_f32_16x16x32_bf16 a[64:67], v[236:239], v[220:223], a[64:67]\n"
"v_mfma_f32_16x16x32_bf16 a[68:71], v[236:239], v[224:227], a[68:71]\n"
"v_mfma_f32_16x16x32_bf16 a[72:75], v[240:243], v[192:195], a[72:75]\n"
"v_mfma_f32_16x16x32_bf16 a[76:79], v[240:243], v[196:199], a[76:79]\n"
"v_mfma_f32_16x16x32_bf16 a[80:83], v[240:243], v[200:203], a[80:83]\n"
"s_add_u32 m0, s52, 131072\n"
"s_nop 0\n"
"buffer_load_dwordx4 v45, s[60:63], s49 offen lds\n"
"s_add_u32 s48, s48, 128\n"
"s_add_u32 s49, s49, 172032\n"
"v_mfma_f32_16x16x32_bf16 a[84:87], v[240:243], v[204:207], a[84:87]\n"
"v_mfma_f32_16x16x32_bf16 a[88:91], v[240:243], v[208:211], a[88:91]\n"
"v_mfma_f32_16x16x32_bf16 a[92:95], v[240:243], v[212:215], a[92:95]\n"
"v_mfma_f32_16x16x32_bf16 a[96:99], v[240:243], v[216:219], a[96:99]\n"
"v_mfma_f32_16x16x32_bf16 a[100:103], v[240:243], v[220:223], a[100:103]\n"
"v_mfma_f32_16x16x32_bf16 a[104:107], v[240:243], v[224:227], a[104:107]\n"
"s_waitcnt lgkmcnt(0)\n"
"v_mfma_f32_16x16x32_bf16 a[0:3], v[168:171], v[128:131], a[0:3]\n"
"ds_read_b64_tr_b16 v[232:233], v58 offset:12288\n"
"s_add_u32 m0, s52, 0\n"
"s_nop 0\n"
"buffer_load_dwordx4 v32, s[56:59], s48 offen lds\n"
"v_mfma_f32_16x16x32_bf16 a[4:7], v[168:171], v[132:135], a[4:7]\n"
"ds_read_b64_tr_b16 v[234:235], v58 offset:13824\n"
"v_mfma_f32_16x16x32_bf16 a[8:11], v[168:171], v[136:139], a[8:11]\n"
"ds_read_b64_tr_b16 v[236:237], v59 offset:12288\n"
"buffer_store_dwordx4 a[208:211], v8, s[44:47], s83 offen offset:0 sc1\n"
"v_mfma_f32_16x16x32_bf16 a[12:15], v[168:171], v[140:143], a[12:15]\n"
"ds_read_b64_tr_b16 v[238:239], v59 offset:13824\n"
"v_mfma_f32_16x16x32_bf16 a[16:19], v[168:171], v[144:147], a[16:19]\n"
"ds_read_b64_tr_b16 v[240:241], v60 offset:12288\n"
"s_add_u32 m0, s52, 4096\n"
"s_nop 0\n"
"buffer_load_dwordx4 v33, s[56:59], s48 offen lds\n"
"v_mfma_f32_16x16x32_bf16 a[20:23], v[168:171], v[148:151], a[20:23]\n"
"ds_read_b64_tr_b16 v[242:243], v60 offset:13824\n"
"v_mfma_f32_16x16x32_bf16 a[24:27], v[168:171], v[152:155], a[24:27]\n"
"ds_read_b128 v[192:195], v57 offset:0\n"
"v_mfma_f32_16x16x32_bf16 a[28:31], v[168:171], v[156:159], a[28:31]\n"
"ds_read_b128 v[196:199], v57 offset:2048\n"
"v_mfma_f32_16x16x32_bf16 a[32:35], v[168:171], v[160:163], a[32:35]\n"
"ds_read_b128 v[200:203], v57 offset:4096\n"
"s_add_u32 m0, s52, 8192\n"
"s_nop 0\n"
"buffer_load_dwordx4 v34, s[56:59], s48 offen lds\n"
"v_mfma_f32_16x16x32_bf16 a[36:39], v[172:175], v[128:131], a[36:39]\n"
"ds_read_b128 v[204:207], v57 offset:6144\n"
"v_mfma_f32_16x16x32_bf16 a[40:43], v[172:175], v[132:135], a[40:43]\n"
"ds_read_b128 v[208:211], v57 offset:8192\n"
"v_mfma_f32_16x16x32_bf16 a[44:47], v[172:175], v[136:139], a[44:47]\n"
"ds_read_b128 v[212:215], v57 offset:10240\n"
"buffer_store_dwordx4 a[212:215], v8, s[44:47], s83 offen offset:1024 sc1\n"
"v_mfma_f32_16x16x32_bf16 a[48:51], v[172:175], v[140:143], a[48:51]\n"
"ds_read_b128 v[216:219], v57 offset:12288\n"
"v_mfma_f32_16x16x32_bf16 a[52:55], v[172:175], v[144:147], a[52:55]\n"
"ds_read_b128 v[220:223], v57 offset:14336\n"
"s_add_u32 m0, s52, 12288\n"
"s_nop 0\n"
"buffer_load_dwordx4 v35, s[56:59], s48 offen lds\n"
"v_mfma_f32_16x16x32_bf16 a[56:59], v[172:175], v[148:151], a[56:59]\n"
"ds_read_b128 v[224:227], v57 offset:16384\n"
"v_mfma_f32_16x16x32_bf16 a[60:63], v[172:175], v[152:155], a[60:63]\n"
"v_mfma_f32_16x16x32_bf16 a[64:67], v[172:175], v[156:159], a[64:67]\n"
"v_mfma_f32_16x16x32_bf16 a[68:71], v[172:175], v[160:163], a[68:71]\n"
"s_add_u32 m0, s52, 16384\n"
"s_nop 0\n"
"buffer_load_dwordx4 v36, s[56:59], s48 offen lds\n"
"v_mfma_f32_16x16x32_bf16 a[72:75], v[176:179], v[128:131], a[72:75]\n"
"v_mfma_f32_16x16x32_bf16 a[76:79], v[176:179], v[132:135], a[76:79]\n"
"v_mfma_f32_16x16x32_bf16 a[80:83], v[176:179], v[136:139], a[80:83]\n"
"buffer_store_dwordx4 a[216:219], v8, s[44:47], s83 offen offset:2048 sc1\n"
"v_mfma_f32_16x16x32_bf16 a[84:87], v[176:179], v[140:143], a[84:87]\n"
"s_add_u32 m0, s52, 20480\n"
"s_nop 0\n"
"buffer_load_dwordx4 v40, s[60:63], s49 offen lds\n"
"v_mfma_f32_16x16x32_bf16 a[88:91], v[176:179], v[144:147], a[88:91]\n"
"v_mfma_f32_16x16x32_bf16 a[92:95], v[176:179], v[148:151], a[92:95]\n"
"v_mfma_f32_16x16x32_bf16 a[96:99], v[176:179], v[152:155], a[96:99]\n"
"v_mfma_f32_16x16x32_bf16 a[100:103], v[176:179], v[156:159], a[100:103]\n"
"v_mfma_f32_16x16x32_bf16 a[104:107], v[176:179], v[160:163], a[104:107]\n"
"s_waitcnt vmcnt(9) lgkmcnt(0)\n"
"s_barrier\n"
"v_add_u32 v56, 90112, v4\n"
"v_xor_b32 v57, 64, v56\n"
"v_add_u32 v58, 90112, v5\n"
"v_add_u32 v59, 90112, v6\n"
"v_add_u32 v60, 90112, v7\n"
"v_mfma_f32_16x16x32_bf16 a[0:3], v[232:235], v[192:195], a[0:3]\n"
"ds_read_b64_tr_b16 v[168:169], v58 offset:0\n"
"s_add_u32 m0, s52, 24576\n"
"s_nop 0\n"
"buffer_load_dwordx4 v41, s[60:63], s49 offen lds\n"
"v_mfma_f32_16x16x32_bf16 a[4:7], v[232:235], v[196:199], a[4:7]\n"
"ds_read_b64_tr_b16 v[170:171], v58 offset:1536\n"
"v_mfma_f32_16x16x32_bf16 a[8:11], v[232:235], v[200:203], a[8:11]\n"
"ds_read_b64_tr_b16 v[172:173], v59 offset:0\n"
"buffer_store_dwordx4 a[220:223], v8, s[44:47], s83 offen offset:3072 sc1\n"
"s_add_u32 s83, s83, s99\n"
"v_mfma_f32_16x16x32_bf16 a[12:15], v[232:235], v[204:207], a[12:15]\n"
"ds_read_b64_tr_b16 v[174:175], v59 offset:1536\n"
"v_mfma_f32_16x16x32_bf16 a[16:19], v[232:235], v[208:211], a[16:19]\n"
"ds_read_b64_tr_b16 v[176:177], v60 offset:0\n"
"v_mfma_f32_16x16x32_bf16 a[20:23], v[232:235], v[212:215], a[20:23]\n"
"ds_read_b64_tr_b16 v[178:179], v60 offset:1536\n"
"s_add_u32 m0, s52, 28672\n"
"s_nop 0\n"
"buffer_load_dwordx4 v42, s[60:63], s49 offen lds\n"
"v_mfma_f32_16x16x32_bf16 a[24:27], v[232:235], v[216:219], a[24:27]\n"
"ds_read_b128 v[128:131], v56 offset:0\n"
"v_mfma_f32_16x16x32_bf16 a[28:31], v[232:235], v[220:223], a[28:31]\n"
"ds_read_b128 v[132:135], v56 offset:2048\n"
"v_mfma_f32_16x16x32_bf16 a[32:35], v[232:235], v[224:227], a[32:35]\n"
"ds_read_b128 v[136:139], v56 offset:4096\n"
"v_mfma_f32_16x16x32_bf16 a[36:39], v[236:239], v[192:195], a[36:39]\n"
"ds_read_b128 v[140:143], v56 offset:6144\n"
"v_mfma_f32_16x16x32_bf16 a[40:43], v[236:239], v[196:199], a[40:43]\n"
"ds_read_b128 v[144:147], v56 offset:8192\n"
"s_add_u32 m0, s52, 32768\n"
"s_nop 0\n"
"buffer_load_dwordx4 v43, s[60:63], s49 offen lds\n"
"v_mfma_f32_16x16x32_bf16 a[44:47], v[236:239], v[200:203], a[44:47]\n"
"ds_read_b128 v[148:151], v56 offset:10240\n"
"buffer_store_dwordx4 a[224:227], v8, s[44:47], s83 offen offset:0 sc1\n"
"v_mfma_f32_16x16x32_bf16 a[48:51], v[236:239], v[204:207], a[48:51]\n"
"ds_read_b128 v[152:155], v56 offset:12288\n"
"v_mfma_f32_16x16x32_bf16 a[52:55], v[236:239], v[208:211], a[52:55]\n"
"ds_read_b128 v[156:159], v56 offset:14336\n"
"v_mfma_f32_16x16x32_bf16 a[56:59], v[236:239], v[212:215], a[56:59]\n"
"ds_read_b128 v[160:163], v56 offset:16384\n"
"v_mfma_f32_16x16x32_bf16 a[60:63], v[236:239], v[216:219], a[60:63]\n"
"s_add_u32 m0, s52, 36864\n"
"s_nop 0\n"
"buffer_load_dwordx4 v44, s[60:63], s49 offen lds\n"
"v_mfma_f32_16x16x32_bf16 a[64:67], v[236:239], v[220:223], a[64:67]\n"
"v_mfma_f32_16x16x32_bf16 a[68:71], v[236:239], v[224:227], a[68:71]\n"
"v_mfma_f32_16x16x32_bf16 a[72:75], v[240:243], v[192:195], a[72:75]\n"
"v_mfma_f32_16x16x32_bf16 a[76:79], v[240:243], v[196:199], a[76:79]\n"
"v_mfma_f32_16x16x32_bf16 a[80:83], v[240:243], v[200:203], a[80:83]\n"
"s_add_u32 m0, s52, 40960\n"
"s_nop 0\n"
"buffer_load_dwordx4 v45, s[60:63], s49 offen lds\n"
"s_add_u32 s48, s48, 128\n"
"s_add_u32 s49, s49, 172032\n"
"v_mfma_f32_16x16x32_bf16 a[84:87], v[240:243], v[204:207], a[84:87]\n"
"v_mfma_f32_16x16x32_bf16 a[88:91], v[240:243], v[208:211], a[88:91]\n"
"v_mfma_f32_16x16x32_bf16 a[92:95], v[240:243], v[212:215], a[92:95]\n"
"v_mfma_f32_16x16x32_bf16 a[96:99], v[240:243], v[216:219], a[96:99]\n"
"v_mfma_f32_16x16x32_bf16 a[100:103], v[240:243], v[220:223], a[100:103]\n"
"v_mfma_f32_16x16x32_bf16 a[104:107], v[240:243], v[224:227], a[104:107]\n"
"s_waitcnt lgkmcnt(0)\n"
"v_mfma_f32_16x16x32_bf16 a[0:3], v[168:171], v[128:131], a[0:3]\n"
"ds_read_b64_tr_b16 v[232:233], v58 offset:12288\n"
"v_mfma_f32_16x16x32_bf16 a[4:7], v[168:171], v[132:135], a[4:7]\n"
"ds_read_b64_tr_b16 v[234:235], v58 offset:13824\n"
"v_mfma_f32_16x16x32_bf16 a[8:11], v[168:171], v[136:139], a[8:11]\n"
"ds_read_b64_tr_b16 v[236:237], v59 offset:12288\n"
"buffer_store_dwordx4 a[228:231], v8, s[44:47], s83 offen offset:1024 sc1\n"
"v_mfma_f32_16x16x32_bf16 a[12:15], v[168:171], v[140:143], a[12:15]\n"
"ds_read_b64_tr_b16 v[238:239], v59 offset:13824\n"
"v_mfma_f32_16x16x32_bf16 a[16:19], v[168:171], v[144:147], a[16:19]\n"
"ds_read_b64_tr_b16 v[240:241], v60 offset:12288\n"
"v_mfma_f32_16x16x32_bf16 a[20:23], v[168:171], v[148:151], a[20:23]\n"
"ds_read_b64_tr_b16 v[242:243], v60 offset:13824\n"
"v_mfma_f32_16x16x32_bf16 a[24:27], v[168:171], v[152:155], a[24:27]\n"
"ds_read_b128 v[192:195], v57 offset:0\n"
"v_mfma_f32_16x16x32_bf16 a[28:31], v[168:171], v[156:159], a[28:31]\n"
"ds_read_b128 v[196:199], v57 offset:2048\n"
"v_mfma_f32_16x16x32_bf16 a[32:35], v[168:171], v[160:163], a[32:35]\n"
"ds_read_b128 v[200:203], v57 offset:4096\n"
"v_mfma_f32_16x16x32_bf16 a[36:39], v[172:175], v[128:131], a[36:39]\n"
"ds_read_b128 v[204:207], v57 offset:6144\n"
"v_mfma_f32_16x16x32_bf16 a[40:43], v[172:175], v[132:135], a[40:43]\n"
"ds_read_b128 v[208:211], v57 offset:8192\n"
"v_mfma_f32_16x16x32_bf16 a[44:47], v[172:175], v[136:139], a[44:47]\n"
"ds_read_b128 v[212:215], v57 offset:10240\n"
"buffer_store_dwordx4 a[232:235], v8, s[44:47], s83 offen offset:2048 sc1\n"
"v_mfma_f32_16x16x32_bf16 a[48:51], v[172:175], v[140:143], a[48:51]\n"
"ds_read_b128 v[216:219], v57 offset:12288\n"
"v_mfma_f32_16x16x32_bf16 a[52:55], v[172:175], v[144:147], a[52:55]\n"
"ds_read_b128 v[220:223], v57 offset:14336\n"
"v_mfma_f32_16x16x32_bf16 a[56:59], v[172:175], v[148:151], a[56:59]\n"
"ds_read_b128 v[224:227], v57 offset:16384\n"
"v_mfma_f32_16x16x32_bf16 a[60:63], v[172:175], v[152:155], a[60:63]\n"
"v_mfma_f32_16x16x32_bf16 a[64:67], v[172:175], v[156:159], a[64:67]\n"
"v_mfma_f32_16x16x32_bf16 a[68:71], v[172:175], v[160:163], a[68:71]\n"
"v_mfma_f32_16x16x32_bf16 a[72:75], v[176:179], v[128:131], a[72:75]\n"
"v_mfma_f32_16x16x32_bf16 a[76:79], v[176:179], v[132:135], a[76:79]\n"
"v_mfma_f32_16x16x32_bf16 a[80:83], v[176:179], v[136:139], a[80:83]\n"
"buffer_store_dwordx4 a[236:239], v8, s[44:47], s83 offen offset:3072 sc1\n"
"s_add_u32 s83, s83, s98\n"
"v_mfma_f32_16x16x32_bf16 a[84:87], v[176:179], v[140:143], a[84:87]\n"
"v_mfma_f32_16x16x32_bf16 a[88:91], v[176:179], v[144:147], a[88:91]\n"
"v_mfma_f32_16x16x32_bf16 a[92:95], v[176:179], v[148:151], a[92:95]\n"
"v_mfma_f32_16x16x32_bf16 a[96:99], v[176:179], v[152:155], a[96:99]\n"
"v_mfma_f32_16x16x32_bf16 a[100:103], v[176:179], v[156:159], a[100:103]\n"
"v_mfma_f32_16x16x32_bf16 a[104:107], v[176:179], v[160:163], a[104:107]\n"
"s_waitcnt vmcnt(3) lgkmcnt(0)\n"
"s_barrier\n"
"v_add_u32 v56, 0, v4\n"
"v_xor_b32 v57, 64, v56\n"
"v_add_u32 v58, 0, v5\n"
"v_add_u32 v59, 0, v6\n"
"v_add_u32 v60, 0, v7\n"
"v_mfma_f32_16x16x32_bf16 a[0:3], v[232:235], v[192:195], a[0:3]\n"
"ds_read_b64_tr_b16 v[168:169], v58 offset:0\n"
"v_mfma_f32_16x16x32_bf16 a[4:7], v[232:235], v[196:199], a[4:7]\n"
"ds_read_b64_tr_b16 v[170:171], v58 offset:1536\n"
"v_mfma_f32_16x16x32_bf16 a[8:11], v[232:235], v[200:203], a[8:11]\n"
"ds_read_b64_tr_b16 v[172:173], v59 offset:0\n"
"buffer_store_dwordx4 a[240:243], v8, s[44:47], s83 offen offset:0 sc1\n"
"v_mfma_f32_16x16x32_bf16 a[12:15], v[232:235], v[204:207], a[12:15]\n"
"ds_read_b64_tr_b16 v[174:175], v59 offset:1536\n"
"v_mfma_f32_16x16x32_bf16 a[16:19], v[232:235], v[208:211], a[16:19]\n"
"ds_read_b64_tr_b16 v[176:177], v60 offset:0\n"
"v_mfma_f32_16x16x32_bf16 a[20:23], v[232:235], v[212:215], a[20:23]\n"
"ds_read_b64_tr_b16 v[178:179], v60 offset:1536\n"
"v_mfma_f32_16x16x32_bf16 a[24:27], v[232:235], v[216:219], a[24:27]\n"
"ds_read_b128 v[128:131], v56 offset:0\n"
"v_mfma_f32_16x16x32_bf16 a[28:31], v[232:235], v[220:223], a[28:31]\n"
"ds_read_b128 v[132:135], v56 offset:2048\n"
"v_mfma_f32_16x16x32_bf16 a[32:35], v[232:235], v[224:227], a[32:35]\n"
"ds_read_b128 v[136:139], v56 offset:4096\n"
"v_mfma_f32_16x16x32_bf16 a[36:39], v[236:239], v[192:195], a[36:39]\n"
"ds_read_b128 v[140:143], v56 offset:6144\n"
"v_mfma_f32_16x16x32_bf16 a[40:43], v[236:239], v[196:199], a[40:43]\n"
"ds_read_b128 v[144:147], v56 offset:8192\n"
"v_mfma_f32_16x16x32_bf16 a[44:47], v[236:239], v[200:203], a[44:47]\n"
"ds_read_b128 v[148:151], v56 offset:10240\n"
"buffer_store_dwordx4 a[244:247], v8, s[44:47], s83 offen offset:1024 sc1\n"
"v_mfma_f32_16x16x32_bf16 a[48:51], v[236:239], v[204:207], a[48:51]\n"
"ds_read_b128 v[152:155], v56 offset:12288\n"
"v_mfma_f32_16x16x32_bf16 a[52:55], v[236:239], v[208:211], a[52:55]\n"
"ds_read_b128 v[156:159], v56 offset:14336\n"
"v_mfma_f32_16x16x32_bf16 a[56:59], v[236:239], v[212:215], a[56:59]\n"
"ds_read_b128 v[160:163], v56 offset:16384\n"
"v_mfma_f32_16x16x32_bf16 a[60:63], v[236:239], v[216:219], a[60:63]\n"
"v_mfma_f32_16x16x32_bf16 a[64:67], v[236:239], v[220:223], a[64:67]\n"
"v_mfma_f32_16x16x32_bf16 a[68:71], v[236:239], v[224:227], a[68:71]\n"
"v_mfma_f32_16x16x32_bf16 a[72:75], v[240:243], v[192:195], a[72:75]\n"
"v_mfma_f32_16x16x32_bf16 a[76:79], v[240:243], v[196:199], a[76:79]\n"
"v_mfma_f32_16x16x32_bf16 a[80:83], v[240:243], v[200:203], a[80:83]\n"
"v_mfma_f32_16x16x32_bf16 a[84:87], v[240:243], v[204:207], a[84:87]\n"
"v_mfma_f32_16x16x32_bf16 a[88:91], v[240:243], v[208:211], a[88:91]\n"
"v_mfma_f32_16x16x32_bf16 a[92:95], v[240:243], v[212:215], a[92:95]\n"
"v_mfma_f32_16x16x32_bf16 a[96:99], v[240:243], v[216:219], a[96:99]\n"
"v_mfma_f32_16x16x32_bf16 a[100:103], v[240:243], v[220:223], a[100:103]\n"
"v_mfma_f32_16x16x32_bf16 a[104:107], v[240:243], v[224:227], a[104:107]\n"
"s_waitcnt lgkmcnt(0)\n"
"v_mfma_f32_16x16x32_bf16 a[0:3], v[168:171], v[128:131], a[0:3]\n"
"ds_read_b64_tr_b16 v[232:233], v58 offset:12288\n"
"v_mfma_f32_16x16x32_bf16 a[4:7], v[168:171], v[132:135], a[4:7]\n"
"ds_read_b64_tr_b16 v[234:235], v58 offset:13824\n"
"v_mfma_f32_16x16x32_bf16 a[8:11], v[168:171], v[136:139], a[8:11]\n"
"ds_read_b64_tr_b16 v[236:237], v59 offset:12288\n"
"buffer_store_dwordx4 a[248:251], v8, s[44:47], s83 offen offset:2048 sc1\n"
"v_mfma_f32_16x16x32_bf16 a[12:15], v[168:171], v[140:143], a[12:15]\n"
"ds_read_b64_tr_b16 v[238:239], v59 offset:13824\n"
"v_mfma_f32_16x16x32_bf16 a[16:19], v[168:171], v[144:147], a[16:19]\n"
"ds_read_b64_tr_b16 v[240:241], v60 offset:12288\n"
"v_mfma_f32_16x16x32_bf16 a[20:23], v[168:171], v[148:151], a[20:23]\n"
"ds_read_b64_tr_b16 v[242:243], v60 offset:13824\n"
"v_mfma_f32_16x16x32_bf16 a[24:27], v[168:171], v[152:155], a[24:27]\n"
"ds_read_b128 v[192:195], v57 offset:0\n"
"v_mfma_f32_16x16x32_bf16 a[28:31], v[168:171], v[156:159], a[28:31]\n"
"ds_read_b128 v[196:199], v57 offset:2048\n"
"v_mfma_f32_16x16x32_bf16 a[32:35], v[168:171], v[160:163], a[32:35]\n"
"ds_read_b128 v[200:203], v57 offset:4096\n"
"v_mfma_f32_16x16x32_bf16 a[36:39], v[172:175], v[128:131], a[36:39]\n"
"ds_read_b128 v[204:207], v57 offset:6144\n"
"v_mfma_f32_16x16x32_bf16 a[40:43], v[172:175], v[132:135], a[40:43]\n"
"ds_read_b128 v[208:211], v57 offset:8192\n"
"v_mfma_f32_16x16x32_bf16 a[44:47], v[172:175], v[136:139], a[44:47]\n"
"ds_read_b128 v[212:215], v57 offset:10240\n"
"buffer_store_dwordx4 a[252:255], v8, s[44:47], s83 offen offset:3072 sc1\n"
"v_mfma_f32_16x16x32_bf16 a[48:51], v[172:175], v[140:143], a[48:51]\n"
"ds_read_b128 v[216:219], v57 offset:12288\n"
"v_mfma_f32_16x16x32_bf16 a[52:55], v[172:175], v[144:147], a[52:55]\n"
"ds_read_b128 v[220:223], v57 offset:14336\n"
"v_mfma_f32_16x16x32_bf16 a[56:59], v[172:175], v[148:151], a[56:59]\n"
"ds_read_b128 v[224:227], v57 offset:16384\n"
"v_mfma_f32_16x16x32_bf16 a[60:63], v[172:175], v[152:155], a[60:63]\n"
"v_mfma_f32_16x16x32_bf16 a[64:67], v[172:175], v[156:159], a[64:67]\n"
"v_mfma_f32_16x16x32_bf16 a[68:71], v[172:175], v[160:163], a[68:71]\n"
"v_mfma_f32_16x16x32_bf16 a[72:75], v[176:179], v[128:131], a[72:75]\n"
"v_mfma_f32_16x16x32_bf16 a[76:79], v[176:179], v[132:135], a[76:79]\n"
"v_mfma_f32_16x16x32_bf16 a[80:83], v[176:179], v[136:139], a[80:83]\n"
"v_mfma_f32_16x16x32_bf16 a[84:87], v[176:179], v[140:143], a[84:87]\n"
"v_mfma_f32_16x16x32_bf16 a[88:91], v[176:179], v[144:147], a[88:91]\n"
"v_mfma_f32_16x16x32_bf16 a[92:95], v[176:179], v[148:151], a[92:95]\n"
"v_mfma_f32_16x16x32_bf16 a[96:99], v[176:179], v[152:155], a[96:99]\n"
"v_mfma_f32_16x16x32_bf16 a[100:103], v[176:179], v[156:159], a[100:103]\n"
"v_mfma_f32_16x16x32_bf16 a[104:107], v[176:179], v[160:163], a[104:107]\n"
"s_waitcnt lgkmcnt(0)\n"
"s_barrier\n"
"v_mfma_f32_16x16x32_bf16 a[0:3], v[232:235], v[192:195], a[0:3]\n"
"v_mfma_f32_16x16x32_bf16 a[4:7], v[232:235], v[196:199], a[4:7]\n"
"v_mfma_f32_16x16x32_bf16 a[8:11], v[232:235], v[200:203], a[8:11]\n"
"v_mfma_f32_16x16x32_bf16 a[12:15], v[232:235], v[204:207], a[12:15]\n"
"v_mfma_f32_16x16x32_bf16 a[16:19], v[232:235], v[208:211], a[16:19]\n"
"v_mfma_f32_16x16x32_bf16 a[20:23], v[232:235], v[212:215], a[20:23]\n"
"v_mfma_f32_16x16x32_bf16 a[24:27], v[232:235], v[216:219], a[24:27]\n"
"v_mfma_f32_16x16x32_bf16 a[28:31], v[232:235], v[220:223], a[28:31]\n"
"v_mfma_f32_16x16x32_bf16 a[32:35], v[232:235], v[224:227], a[32:35]\n"
"v_mfma_f32_16x16x32_bf16 a[36:39], v[236:239], v[192:195], a[36:39]\n"
"v_mfma_f32_16x16x32_bf16 a[40:43], v[236:239], v[196:199], a[40:43]\n"
"v_mfma_f32_16x16x32_bf16 a[44:47], v[236:239], v[200:203], a[44:47]\n"
"v_mfma_f32_16x16x32_bf16 a[48:51], v[236:239], v[204:207], a[48:51]\n"
"v_mfma_f32_16x16x32_bf16 a[52:55], v[236:239], v[208:211], a[52:55]\n"
"v_mfma_f32_16x16x32_bf16 a[56:59], v[236:239], v[212:215], a[56:59]\n"
"v_mfma_f32_16x16x32_bf16 a[60:63], v[236:239], v[216:219], a[60:63]\n"
"v_mfma_f32_16x16x32_bf16 a[64:67], v[236:239], v[220:223], a[64:67]\n"
"v_mfma_f32_16x16x32_bf16 a[68:71], v[236:239], v[224:227], a[68:71]\n"
"v_mfma_f32_16x16x32_bf16 a[72:75], v[240:243], v[192:195], a[72:75]\n"
"v_mfma_f32_16x16x32_bf16 a[76:79], v[240:243], v[196:199], a[76:79]\n"
"v_mfma_f32_16x16x32_bf16 a[80:83], v[240:243], v[200:203], a[80:83]\n"
"v_mfma_f32_16x16x32_bf16 a[84:87], v[240:243], v[204:207], a[84:87]\n"
"v_mfma_f32_16x16x32_bf16 a[88:91], v[240:243], v[208:211], a[88:91]\n"
"v_mfma_f32_16x16x32_bf16 a[92:95], v[240:243], v[212:215], a[92:95]\n"
"v_mfma_f32_16x16x32_bf16 a[96:99], v[240:243], v[216:219], a[96:99]\n"
"v_mfma_f32_16x16x32_bf16 a[100:103], v[240:243], v[220:223], a[100:103]\n"
"v_mfma_f32_16x16x32_bf16 a[104:107], v[240:243], v[224:227], a[104:107]\n"
"s_waitcnt vmcnt(0)\n"
"s_barrier\n"
"s_cmp_lg_u32 s73, 0\n"
"s_cbranch_scc1 L_fl_kvhi_%=\n"
"s_lshl_b32 s76, s71, 1\n"
"s_and_b32 s77, s72, 1\n"
"s_add_u32 s76, s76, s77\n"
"s_lshl_b32 s76, s76, 1\n"
"s_add_u32 s76, s76, s82\n"
"s_lshl_b32 s76, s76, 3\n"
"s_lshr_b32 s77, s72, 2\n"
"s_xor_b32 s77, s77, 0\n"
"s_add_u32 s76, s76, s77\n"
"s_lshl_b32 s76, s76, 2\n"
"s_add_u32 s76, s76, 126879744\n"
"v_mov_b32 v1, s55\n"
"v_mov_b32 v2, 0\n"
"s_mov_b64 exec, 1\n"
"buffer_store_dword v1, v2, s[44:47], s76 offen sc1\n"
"s_mov_b64 exec, -1\n"
"L_fl_kvhi_%=:\n"
"s_nop 7\n"
"v_and_b32 v1, 15, v0\n"
"v_mul_u32_u24 v1, 400, v1\n"
"v_lshrrev_b32 v2, 4, v0\n"
"v_lshlrev_b32 v2, 2, v2\n"
"s_mul_i32 s76, s73, 48\n"
"v_add_u32 v2, s76, v2\n"
"v_add_u32 v3, 0, v2\n"
"v_mul_u32_u24 v11, 147, v3\n"
"v_lshrrev_b32 v11, 12, v11\n"
"v_mul_u32_u24 v12, 28, v11\n"
"v_sub_u32 v3, v3, v12\n"
"v_lshlrev_b32 v3, 1, v3\n"
"v_lshl_add_u32 v3, v11, 6, v3\n"
"v_add_u32 v13, v1, v3\n"
"v_add_u32 v3, 16, v2\n"
"v_mul_u32_u24 v11, 147, v3\n"
"v_lshrrev_b32 v11, 12, v11\n"
"v_mul_u32_u24 v12, 28, v11\n"
"v_sub_u32 v3, v3, v12\n"
"v_lshlrev_b32 v3, 1, v3\n"
"v_lshl_add_u32 v3, v11, 6, v3\n"
"v_add_u32 v14, v1, v3\n"
"v_add_u32 v3, 32, v2\n"
"v_mul_u32_u24 v11, 147, v3\n"
"v_lshrrev_b32 v11, 12, v11\n"
"v_mul_u32_u24 v12, 28, v11\n"
"v_sub_u32 v3, v3, v12\n"
"v_lshlrev_b32 v3, 1, v3\n"
"v_lshl_add_u32 v3, v11, 6, v3\n"
"v_add_u32 v15, v1, v3\n"
"v_accvgpr_read_b32 v64, a0\n"
"v_accvgpr_read_b32 v65, a1\n"
"v_accvgpr_read_b32 v66, a2\n"
"v_accvgpr_read_b32 v67, a3\n"
"v_cvt_pk_bf16_f32 v68, v64, v65\n"
"v_cvt_pk_bf16_f32 v69, v66, v67\n"
"ds_write_b64 v13, v[68:69] offset:0\n"
"v_accvgpr_read_b32 v72, a4\n"
"v_accvgpr_read_b32 v73, a5\n"
"v_accvgpr_read_b32 v74, a6\n"
"v_accvgpr_read_b32 v75, a7\n"
"v_cvt_pk_bf16_f32 v76, v72, v73\n"
"v_cvt_pk_bf16_f32 v77, v74, v75\n"
"ds_write_b64 v13, v[76:77] offset:6400\n"
"v_accvgpr_read_b32 v80, a8\n"
"v_accvgpr_read_b32 v81, a9\n"
"v_accvgpr_read_b32 v82, a10\n"
"v_accvgpr_read_b32 v83, a11\n"
"v_cvt_pk_bf16_f32 v84, v80, v81\n"
"v_cvt_pk_bf16_f32 v85, v82, v83\n"
"ds_write_b64 v13, v[84:85] offset:12800\n"
"v_accvgpr_read_b32 v88, a12\n"
"v_accvgpr_read_b32 v89, a13\n"
"v_accvgpr_read_b32 v90, a14\n"
"v_accvgpr_read_b32 v91, a15\n"
"v_cvt_pk_bf16_f32 v92, v88, v89\n"
"v_cvt_pk_bf16_f32 v93, v90, v91\n"
"ds_write_b64 v13, v[92:93] offset:19200\n"
"v_accvgpr_read_b32 v96, a16\n"
"v_accvgpr_read_b32 v97, a17\n"
"v_accvgpr_read_b32 v98, a18\n"
"v_accvgpr_read_b32 v99, a19\n"
"v_cvt_pk_bf16_f32 v100, v96, v97\n"
"v_cvt_pk_bf16_f32 v101, v98, v99\n"
"ds_write_b64 v13, v[100:101] offset:25600\n"
"v_accvgpr_read_b32 v104, a20\n"
"v_accvgpr_read_b32 v105, a21\n"
"v_accvgpr_read_b32 v106, a22\n"
"v_accvgpr_read_b32 v107, a23\n"
"v_cvt_pk_bf16_f32 v108, v104, v105\n"
"v_cvt_pk_bf16_f32 v109, v106, v107\n"
"ds_write_b64 v13, v[108:109] offset:32000\n"
"v_accvgpr_read_b32 v112, a24\n"
"v_accvgpr_read_b32 v113, a25\n"
"v_accvgpr_read_b32 v114, a26\n"
"v_accvgpr_read_b32 v115, a27\n"
"v_cvt_pk_bf16_f32 v116, v112, v113\n"
"v_cvt_pk_bf16_f32 v117, v114, v115\n"
"ds_write_b64 v13, v[116:117] offset:38400\n"
"v_accvgpr_read_b32 v120, a28\n"
"v_accvgpr_read_b32 v121, a29\n"
"v_accvgpr_read_b32 v122, a30\n"
"v_accvgpr_read_b32 v123, a31\n"
"v_cvt_pk_bf16_f32 v124, v120, v121\n"
"v_cvt_pk_bf16_f32 v125, v122, v123\n"
"ds_write_b64 v13, v[124:125] offset:44800\n"
"v_accvgpr_read_b32 v64, a32\n"
"v_accvgpr_read_b32 v65, a33\n"
"v_accvgpr_read_b32 v66, a34\n"
"v_accvgpr_read_b32 v67, a35\n"
"v_cvt_pk_bf16_f32 v68, v64, v65\n"
"v_cvt_pk_bf16_f32 v69, v66, v67\n"
"ds_write_b64 v13, v[68:69] offset:51200\n"
"v_accvgpr_read_b32 v72, a36\n"
"v_accvgpr_read_b32 v73, a37\n"
"v_accvgpr_read_b32 v74, a38\n"
"v_accvgpr_read_b32 v75, a39\n"
"v_cvt_pk_bf16_f32 v76, v72, v73\n"
"v_cvt_pk_bf16_f32 v77, v74, v75\n"
"ds_write_b64 v14, v[76:77] offset:0\n"
"v_accvgpr_read_b32 v80, a40\n"
"v_accvgpr_read_b32 v81, a41\n"
"v_accvgpr_read_b32 v82, a42\n"
"v_accvgpr_read_b32 v83, a43\n"
"v_cvt_pk_bf16_f32 v84, v80, v81\n"
"v_cvt_pk_bf16_f32 v85, v82, v83\n"
"ds_write_b64 v14, v[84:85] offset:6400\n"
"v_accvgpr_read_b32 v88, a44\n"
"v_accvgpr_read_b32 v89, a45\n"
"v_accvgpr_read_b32 v90, a46\n"
"v_accvgpr_read_b32 v91, a47\n"
"v_cvt_pk_bf16_f32 v92, v88, v89\n"
"v_cvt_pk_bf16_f32 v93, v90, v91\n"
"ds_write_b64 v14, v[92:93] offset:12800\n"
"v_accvgpr_read_b32 v96, a48\n"
"v_accvgpr_read_b32 v97, a49\n"
"v_accvgpr_read_b32 v98, a50\n"
"v_accvgpr_read_b32 v99, a51\n"
"v_cvt_pk_bf16_f32 v100, v96, v97\n"
"v_cvt_pk_bf16_f32 v101, v98, v99\n"
"ds_write_b64 v14, v[100:101] offset:19200\n"
"v_accvgpr_read_b32 v104, a52\n"
"v_accvgpr_read_b32 v105, a53\n"
"v_accvgpr_read_b32 v106, a54\n"
"v_accvgpr_read_b32 v107, a55\n"
"v_cvt_pk_bf16_f32 v108, v104, v105\n"
"v_cvt_pk_bf16_f32 v109, v106, v107\n"
"ds_write_b64 v14, v[108:109] offset:25600\n"
"v_accvgpr_read_b32 v112, a56\n"
"v_accvgpr_read_b32 v113, a57\n"
"v_accvgpr_read_b32 v114, a58\n"
"v_accvgpr_read_b32 v115, a59\n"
"v_cvt_pk_bf16_f32 v116, v112, v113\n"
"v_cvt_pk_bf16_f32 v117, v114, v115\n"
"ds_write_b64 v14, v[116:117] offset:32000\n"
"v_accvgpr_read_b32 v120, a60\n"
"v_accvgpr_read_b32 v121, a61\n"
"v_accvgpr_read_b32 v122, a62\n"
"v_accvgpr_read_b32 v123, a63\n"
"v_cvt_pk_bf16_f32 v124, v120, v121\n"
"v_cvt_pk_bf16_f32 v125, v122, v123\n"
"ds_write_b64 v14, v[124:125] offset:38400\n"
"v_accvgpr_read_b32 v64, a64\n"
"v_accvgpr_read_b32 v65, a65\n"
"v_accvgpr_read_b32 v66, a66\n"
"v_accvgpr_read_b32 v67, a67\n"
"v_cvt_pk_bf16_f32 v68, v64, v65\n"
"v_cvt_pk_bf16_f32 v69, v66, v67\n"
"ds_write_b64 v14, v[68:69] offset:44800\n"
"v_accvgpr_read_b32 v72, a68\n"
"v_accvgpr_read_b32 v73, a69\n"
"v_accvgpr_read_b32 v74, a70\n"
"v_accvgpr_read_b32 v75, a71\n"
"v_cvt_pk_bf16_f32 v76, v72, v73\n"
"v_cvt_pk_bf16_f32 v77, v74, v75\n"
"ds_write_b64 v14, v[76:77] offset:51200\n"
"s_cmp_eq_u32 s73, 3\n"
"s_cbranch_scc1 L_bld_%=\n"
"v_accvgpr_read_b32 v80, a72\n"
"v_accvgpr_read_b32 v81, a73\n"
"v_accvgpr_read_b32 v82, a74\n"
"v_accvgpr_read_b32 v83, a75\n"
"v_cvt_pk_bf16_f32 v84, v80, v81\n"
"v_cvt_pk_bf16_f32 v85, v82, v83\n"
"ds_write_b64 v15, v[84:85] offset:0\n"
"v_accvgpr_read_b32 v88, a76\n"
"v_accvgpr_read_b32 v89, a77\n"
"v_accvgpr_read_b32 v90, a78\n"
"v_accvgpr_read_b32 v91, a79\n"
"v_cvt_pk_bf16_f32 v92, v88, v89\n"
"v_cvt_pk_bf16_f32 v93, v90, v91\n"
"ds_write_b64 v15, v[92:93] offset:6400\n"
"v_accvgpr_read_b32 v96, a80\n"
"v_accvgpr_read_b32 v97, a81\n"
"v_accvgpr_read_b32 v98, a82\n"
"v_accvgpr_read_b32 v99, a83\n"
"v_cvt_pk_bf16_f32 v100, v96, v97\n"
"v_cvt_pk_bf16_f32 v101, v98, v99\n"
"ds_write_b64 v15, v[100:101] offset:12800\n"
"v_accvgpr_read_b32 v104, a84\n"
"v_accvgpr_read_b32 v105, a85\n"
"v_accvgpr_read_b32 v106, a86\n"
"v_accvgpr_read_b32 v107, a87\n"
"v_cvt_pk_bf16_f32 v108, v104, v105\n"
"v_cvt_pk_bf16_f32 v109, v106, v107\n"
"ds_write_b64 v15, v[108:109] offset:19200\n"
"v_accvgpr_read_b32 v112, a88\n"
"v_accvgpr_read_b32 v113, a89\n"
"v_accvgpr_read_b32 v114, a90\n"
"v_accvgpr_read_b32 v115, a91\n"
"v_cvt_pk_bf16_f32 v116, v112, v113\n"
"v_cvt_pk_bf16_f32 v117, v114, v115\n"
"ds_write_b64 v15, v[116:117] offset:25600\n"
"v_accvgpr_read_b32 v120, a92\n"
"v_accvgpr_read_b32 v121, a93\n"
"v_accvgpr_read_b32 v122, a94\n"
"v_accvgpr_read_b32 v123, a95\n"
"v_cvt_pk_bf16_f32 v124, v120, v121\n"
"v_cvt_pk_bf16_f32 v125, v122, v123\n"
"ds_write_b64 v15, v[124:125] offset:32000\n"
"v_accvgpr_read_b32 v64, a96\n"
"v_accvgpr_read_b32 v65, a97\n"
"v_accvgpr_read_b32 v66, a98\n"
"v_accvgpr_read_b32 v67, a99\n"
"v_cvt_pk_bf16_f32 v68, v64, v65\n"
"v_cvt_pk_bf16_f32 v69, v66, v67\n"
"ds_write_b64 v15, v[68:69] offset:38400\n"
"v_accvgpr_read_b32 v72, a100\n"
"v_accvgpr_read_b32 v73, a101\n"
"v_accvgpr_read_b32 v74, a102\n"
"v_accvgpr_read_b32 v75, a103\n"
"v_cvt_pk_bf16_f32 v76, v72, v73\n"
"v_cvt_pk_bf16_f32 v77, v74, v75\n"
"ds_write_b64 v15, v[76:77] offset:44800\n"
"v_accvgpr_read_b32 v80, a104\n"
"v_accvgpr_read_b32 v81, a105\n"
"v_accvgpr_read_b32 v82, a106\n"
"v_accvgpr_read_b32 v83, a107\n"
"v_cvt_pk_bf16_f32 v84, v80, v81\n"
"v_cvt_pk_bf16_f32 v85, v82, v83\n"
"ds_write_b64 v15, v[84:85] offset:51200\n"
"L_bld_%=:\n"
"s_waitcnt lgkmcnt(0)\n"
"s_barrier\n"
"s_branch L_end_%=\n"
"L_pfs_Vf_%=:\n"
"s_mov_b32 s34, 0\n"
"s_cmp_ge_u32 s35, 4\n"
"s_cbranch_scc1 L_pfl_Vf_%=\n"
"s_cmp_eq_u32 s35, 3\n"
"s_cbranch_scc1 L_pfw_Vf_%=\n"
"s_lshl_b32 s76, s72, 2\n"
"s_add_u32 s76, s76, s73\n"
"s_mul_i32 s76, s76, 3\n"
"s_add_u32 s76, s76, s35\n"
"s_lshl_b32 s76, s76, 6\n"
"v_add_u32 v1, s76, v0\n"
"v_lshlrev_b32 v2, 7, v1\n"
"v_subrev_u32 v3, 14336, v1\n"
"v_lshrrev_b32 v11, 3, v3\n"
"v_lshlrev_b32 v11, 13, v11\n"
"v_and_b32 v3, 7, v3\n"
"v_lshl_add_u32 v11, v3, 7, v11\n"
"s_lshl_b32 s77, s71, 10\n"
"v_add_u32 v11, s77, v11\n"
"v_subrev_u32 v3, 17920, v1\n"
"v_lshrrev_b32 v64, 3, v3\n"
"v_mul_u32_u24 v64, 2688, v64\n"
"v_and_b32 v3, 7, v3\n"
"v_lshrrev_b32 v65, 1, v3\n"
"v_min_u32 v65, 2, v65\n"
"v_mul_u32_u24 v65, 896, v65\n"
"v_and_b32 v3, 1, v3\n"
"v_mul_u32_u24 v3, 108, v3\n"
"v_add3_u32 v64, v64, v65, v3\n"
"s_mul_i32 s77, s71, 112\n"
"v_add_u32 v64, s77, v64\n"
"s_mov_b64 s[76:77], %4\n"
"v_mov_b32 v12, s76\n"
"v_mov_b32 v13, s77\n"
"v_mov_b32 v65, 0\n"
"s_mov_b64 s[76:77], %6\n"
"v_mov_b32 v14, s76\n"
"v_mov_b32 v15, s77\n"
"v_mov_b32 v3, 21504\n"
"v_cmp_gt_u32 vcc, v3, v1\n"
"s_nop 1\n"
"v_cndmask_b32 v12, v12, v14, vcc\n"
"v_cndmask_b32 v13, v13, v15, vcc\n"
"v_cndmask_b32 v65, v65, v64, vcc\n"
"s_mov_b64 s[76:77], %5\n"
"v_mov_b32 v14, s76\n"
"v_mov_b32 v15, s77\n"
"v_mov_b32 v3, 17920\n"
"v_cmp_gt_u32 vcc, v3, v1\n"
"s_nop 1\n"
"v_cndmask_b32 v12, v12, v14, vcc\n"
"v_cndmask_b32 v13, v13, v15, vcc\n"
"v_cndmask_b32 v65, v65, v11, vcc\n"
"s_mov_b64 s[76:77], %4\n"
"v_mov_b32 v14, s76\n"
"v_mov_b32 v15, s77\n"
"v_mov_b32 v3, 14336\n"
"v_cmp_gt_u32 vcc, v3, v1\n"
"s_nop 1\n"
"v_cndmask_b32 v12, v12, v14, vcc\n"
"v_cndmask_b32 v13, v13, v15, vcc\n"
"v_cndmask_b32 v65, v65, v2, vcc\n"
"v_mov_b32 v2, v65\n"
"v_add_co_u32 v48, vcc, v12, v2\n"
"s_nop 1\n"
"v_addc_co_u32 v49, vcc, 0, v13, vcc\n"
"s_add_u32 s35, s35, 1\n"
"L_pfl_Vf_%=:\n"
"global_load_dword v51, v[48:49], off\n"
"s_branch L_pfr_Vf_%=\n"
"L_pfw_Vf_%=:\n"
"s_and_b32 s76, s72, 15\n"
"s_lshl_b32 s76, s76, 3\n"
"s_add_u32 s76, s76, s71\n"
"s_lshl_b32 s76, s76, 2\n"
"s_add_u32 s76, s76, s73\n"
"s_lshl_b32 s76, s76, 6\n"
"v_add_u32 v1, s76, v0\n"
"v_mov_b32 v2, 31807\n"
"v_min_u32 v1, v1, v2\n"
"s_mov_b64 s[76:77], %10\n"
"v_mov_b32 v12, s76\n"
"v_mov_b32 v13, s77\n"
"s_mov_b64 s[76:77], %11\n"
"v_mov_b32 v14, s76\n"
"v_mov_b32 v15, s77\n"
"v_subrev_u32 v3, 28672, v1\n"
"v_mov_b32 v2, 28672\n"
"v_cmp_gt_u32 vcc, v2, v1\n"
"s_nop 1\n"
"v_cndmask_b32 v1, v3, v1, vcc\n"
"v_cndmask_b32 v12, v14, v12, vcc\n"
"v_cndmask_b32 v13, v15, v13, vcc\n"
"v_lshlrev_b32 v1, 7, v1\n"
"v_add_co_u32 v48, vcc, v12, v1\n"
"s_nop 1\n"
"v_addc_co_u32 v49, vcc, 0, v13, vcc\n"
"s_add_u32 s35, s35, 1\n"
"s_branch L_pfl_Vf_%=\n"
"L_pfs_Vl_%=:\n"
"s_mov_b32 s34, 0\n"
"s_cmp_ge_u32 s35, 4\n"
"s_cbranch_scc1 L_pfl_Vl_%=\n"
"s_cmp_eq_u32 s35, 3\n"
"s_cbranch_scc1 L_pfw_Vl_%=\n"
"s_lshl_b32 s76, s72, 2\n"
"s_add_u32 s76, s76, s73\n"
"s_mul_i32 s76, s76, 3\n"
"s_add_u32 s76, s76, s35\n"
"s_lshl_b32 s76, s76, 6\n"
"v_add_u32 v1, s76, v0\n"
"v_lshlrev_b32 v2, 7, v1\n"
"v_subrev_u32 v3, 14336, v1\n"
"v_lshrrev_b32 v11, 3, v3\n"
"v_lshlrev_b32 v11, 13, v11\n"
"v_and_b32 v3, 7, v3\n"
"v_lshl_add_u32 v11, v3, 7, v11\n"
"s_lshl_b32 s77, s71, 10\n"
"v_add_u32 v11, s77, v11\n"
"v_subrev_u32 v3, 17920, v1\n"
"v_lshrrev_b32 v64, 3, v3\n"
"v_mul_u32_u24 v64, 2688, v64\n"
"v_and_b32 v3, 7, v3\n"
"v_lshrrev_b32 v65, 1, v3\n"
"v_min_u32 v65, 2, v65\n"
"v_mul_u32_u24 v65, 896, v65\n"
"v_and_b32 v3, 1, v3\n"
"v_mul_u32_u24 v3, 108, v3\n"
"v_add3_u32 v64, v64, v65, v3\n"
"s_mul_i32 s77, s71, 112\n"
"v_add_u32 v64, s77, v64\n"
"s_mov_b64 s[76:77], %4\n"
"v_mov_b32 v12, s76\n"
"v_mov_b32 v13, s77\n"
"v_mov_b32 v65, 0\n"
"s_mov_b64 s[76:77], %6\n"
"v_mov_b32 v14, s76\n"
"v_mov_b32 v15, s77\n"
"v_mov_b32 v3, 21504\n"
"v_cmp_gt_u32 vcc, v3, v1\n"
"s_nop 1\n"
"v_cndmask_b32 v12, v12, v14, vcc\n"
"v_cndmask_b32 v13, v13, v15, vcc\n"
"v_cndmask_b32 v65, v65, v64, vcc\n"
"s_mov_b64 s[76:77], %5\n"
"v_mov_b32 v14, s76\n"
"v_mov_b32 v15, s77\n"
"v_mov_b32 v3, 17920\n"
"v_cmp_gt_u32 vcc, v3, v1\n"
"s_nop 1\n"
"v_cndmask_b32 v12, v12, v14, vcc\n"
"v_cndmask_b32 v13, v13, v15, vcc\n"
"v_cndmask_b32 v65, v65, v11, vcc\n"
"s_mov_b64 s[76:77], %4\n"
"v_mov_b32 v14, s76\n"
"v_mov_b32 v15, s77\n"
"v_mov_b32 v3, 14336\n"
"v_cmp_gt_u32 vcc, v3, v1\n"
"s_nop 1\n"
"v_cndmask_b32 v12, v12, v14, vcc\n"
"v_cndmask_b32 v13, v13, v15, vcc\n"
"v_cndmask_b32 v65, v65, v2, vcc\n"
"v_mov_b32 v2, v65\n"
"v_add_co_u32 v48, vcc, v12, v2\n"
"s_nop 1\n"
"v_addc_co_u32 v49, vcc, 0, v13, vcc\n"
"s_add_u32 s35, s35, 1\n"
"L_pfl_Vl_%=:\n"
"global_load_dword v51, v[48:49], off\n"
"s_branch L_pfr_Vl_%=\n"
"L_pfw_Vl_%=:\n"
"s_and_b32 s76, s72, 15\n"
"s_lshl_b32 s76, s76, 3\n"
"s_add_u32 s76, s76, s71\n"
"s_lshl_b32 s76, s76, 2\n"
"s_add_u32 s76, s76, s73\n"
"s_lshl_b32 s76, s76, 6\n"
"v_add_u32 v1, s76, v0\n"
"v_mov_b32 v2, 31807\n"
"v_min_u32 v1, v1, v2\n"
"s_mov_b64 s[76:77], %10\n"
"v_mov_b32 v12, s76\n"
"v_mov_b32 v13, s77\n"
"s_mov_b64 s[76:77], %11\n"
"v_mov_b32 v14, s76\n"
"v_mov_b32 v15, s77\n"
"v_subrev_u32 v3, 28672, v1\n"
"v_mov_b32 v2, 28672\n"
"v_cmp_gt_u32 vcc, v2, v1\n"
"s_nop 1\n"
"v_cndmask_b32 v1, v3, v1, vcc\n"
"v_cndmask_b32 v12, v14, v12, vcc\n"
"v_cndmask_b32 v13, v15, v13, vcc\n"
"v_lshlrev_b32 v1, 7, v1\n"
"v_add_co_u32 v48, vcc, v12, v1\n"
"s_nop 1\n"
"v_addc_co_u32 v49, vcc, 0, v13, vcc\n"
"s_add_u32 s35, s35, 1\n"
"s_branch L_pfl_Vl_%=\n"
"L_pfs_Kf_%=:\n"
"s_mov_b32 s34, 0\n"
"s_cmp_ge_u32 s35, 4\n"
"s_cbranch_scc1 L_pfl_Kf_%=\n"
"s_cmp_eq_u32 s35, 3\n"
"s_cbranch_scc1 L_pfw_Kf_%=\n"
"s_lshl_b32 s76, s72, 2\n"
"s_add_u32 s76, s76, s73\n"
"s_mul_i32 s76, s76, 3\n"
"s_add_u32 s76, s76, s35\n"
"s_lshl_b32 s76, s76, 6\n"
"v_add_u32 v1, s76, v0\n"
"v_lshlrev_b32 v2, 7, v1\n"
"v_subrev_u32 v3, 14336, v1\n"
"v_lshrrev_b32 v11, 3, v3\n"
"v_lshlrev_b32 v11, 13, v11\n"
"v_and_b32 v3, 7, v3\n"
"v_lshl_add_u32 v11, v3, 7, v11\n"
"s_lshl_b32 s77, s71, 10\n"
"v_add_u32 v11, s77, v11\n"
"v_subrev_u32 v3, 17920, v1\n"
"v_lshrrev_b32 v64, 3, v3\n"
"v_mul_u32_u24 v64, 2688, v64\n"
"v_and_b32 v3, 7, v3\n"
"v_lshrrev_b32 v65, 1, v3\n"
"v_min_u32 v65, 2, v65\n"
"v_mul_u32_u24 v65, 896, v65\n"
"v_and_b32 v3, 1, v3\n"
"v_mul_u32_u24 v3, 108, v3\n"
"v_add3_u32 v64, v64, v65, v3\n"
"s_mul_i32 s77, s71, 112\n"
"v_add_u32 v64, s77, v64\n"
"s_mov_b64 s[76:77], %4\n"
"v_mov_b32 v12, s76\n"
"v_mov_b32 v13, s77\n"
"v_mov_b32 v65, 0\n"
"s_mov_b64 s[76:77], %6\n"
"v_mov_b32 v14, s76\n"
"v_mov_b32 v15, s77\n"
"v_mov_b32 v3, 21504\n"
"v_cmp_gt_u32 vcc, v3, v1\n"
"s_nop 1\n"
"v_cndmask_b32 v12, v12, v14, vcc\n"
"v_cndmask_b32 v13, v13, v15, vcc\n"
"v_cndmask_b32 v65, v65, v64, vcc\n"
"s_mov_b64 s[76:77], %5\n"
"v_mov_b32 v14, s76\n"
"v_mov_b32 v15, s77\n"
"v_mov_b32 v3, 17920\n"
"v_cmp_gt_u32 vcc, v3, v1\n"
"s_nop 1\n"
"v_cndmask_b32 v12, v12, v14, vcc\n"
"v_cndmask_b32 v13, v13, v15, vcc\n"
"v_cndmask_b32 v65, v65, v11, vcc\n"
"s_mov_b64 s[76:77], %4\n"
"v_mov_b32 v14, s76\n"
"v_mov_b32 v15, s77\n"
"v_mov_b32 v3, 14336\n"
"v_cmp_gt_u32 vcc, v3, v1\n"
"s_nop 1\n"
"v_cndmask_b32 v12, v12, v14, vcc\n"
"v_cndmask_b32 v13, v13, v15, vcc\n"
"v_cndmask_b32 v65, v65, v2, vcc\n"
"v_mov_b32 v2, v65\n"
"v_add_co_u32 v48, vcc, v12, v2\n"
"s_nop 1\n"
"v_addc_co_u32 v49, vcc, 0, v13, vcc\n"
"s_add_u32 s35, s35, 1\n"
"L_pfl_Kf_%=:\n"
"global_load_dword v51, v[48:49], off\n"
"s_branch L_pfr_Kf_%=\n"
"L_pfw_Kf_%=:\n"
"s_and_b32 s76, s72, 15\n"
"s_lshl_b32 s76, s76, 3\n"
"s_add_u32 s76, s76, s71\n"
"s_lshl_b32 s76, s76, 2\n"
"s_add_u32 s76, s76, s73\n"
"s_lshl_b32 s76, s76, 6\n"
"v_add_u32 v1, s76, v0\n"
"v_mov_b32 v2, 31807\n"
"v_min_u32 v1, v1, v2\n"
"s_mov_b64 s[76:77], %10\n"
"v_mov_b32 v12, s76\n"
"v_mov_b32 v13, s77\n"
"s_mov_b64 s[76:77], %11\n"
"v_mov_b32 v14, s76\n"
"v_mov_b32 v15, s77\n"
"v_subrev_u32 v3, 28672, v1\n"
"v_mov_b32 v2, 28672\n"
"v_cmp_gt_u32 vcc, v2, v1\n"
"s_nop 1\n"
"v_cndmask_b32 v1, v3, v1, vcc\n"
"v_cndmask_b32 v12, v14, v12, vcc\n"
"v_cndmask_b32 v13, v15, v13, vcc\n"
"v_lshlrev_b32 v1, 7, v1\n"
"v_add_co_u32 v48, vcc, v12, v1\n"
"s_nop 1\n"
"v_addc_co_u32 v49, vcc, 0, v13, vcc\n"
"s_add_u32 s35, s35, 1\n"
"s_branch L_pfl_Kf_%=\n"
"L_pfs_Kl_%=:\n"
"s_mov_b32 s34, 0\n"
"s_cmp_ge_u32 s35, 4\n"
"s_cbranch_scc1 L_pfl_Kl_%=\n"
"s_cmp_eq_u32 s35, 3\n"
"s_cbranch_scc1 L_pfw_Kl_%=\n"
"s_lshl_b32 s76, s72, 2\n"
"s_add_u32 s76, s76, s73\n"
"s_mul_i32 s76, s76, 3\n"
"s_add_u32 s76, s76, s35\n"
"s_lshl_b32 s76, s76, 6\n"
"v_add_u32 v1, s76, v0\n"
"v_lshlrev_b32 v2, 7, v1\n"
"v_subrev_u32 v3, 14336, v1\n"
"v_lshrrev_b32 v11, 3, v3\n"
"v_lshlrev_b32 v11, 13, v11\n"
"v_and_b32 v3, 7, v3\n"
"v_lshl_add_u32 v11, v3, 7, v11\n"
"s_lshl_b32 s77, s71, 10\n"
"v_add_u32 v11, s77, v11\n"
"v_subrev_u32 v3, 17920, v1\n"
"v_lshrrev_b32 v64, 3, v3\n"
"v_mul_u32_u24 v64, 2688, v64\n"
"v_and_b32 v3, 7, v3\n"
"v_lshrrev_b32 v65, 1, v3\n"
"v_min_u32 v65, 2, v65\n"
"v_mul_u32_u24 v65, 896, v65\n"
"v_and_b32 v3, 1, v3\n"
"v_mul_u32_u24 v3, 108, v3\n"
"v_add3_u32 v64, v64, v65, v3\n"
"s_mul_i32 s77, s71, 112\n"
"v_add_u32 v64, s77, v64\n"
"s_mov_b64 s[76:77], %4\n"
"v_mov_b32 v12, s76\n"
"v_mov_b32 v13, s77\n"
"v_mov_b32 v65, 0\n"
"s_mov_b64 s[76:77], %6\n"
"v_mov_b32 v14, s76\n"
"v_mov_b32 v15, s77\n"
"v_mov_b32 v3, 21504\n"
"v_cmp_gt_u32 vcc, v3, v1\n"
"s_nop 1\n"
"v_cndmask_b32 v12, v12, v14, vcc\n"
"v_cndmask_b32 v13, v13, v15, vcc\n"
"v_cndmask_b32 v65, v65, v64, vcc\n"
"s_mov_b64 s[76:77], %5\n"
"v_mov_b32 v14, s76\n"
"v_mov_b32 v15, s77\n"
"v_mov_b32 v3, 17920\n"
"v_cmp_gt_u32 vcc, v3, v1\n"
"s_nop 1\n"
"v_cndmask_b32 v12, v12, v14, vcc\n"
"v_cndmask_b32 v13, v13, v15, vcc\n"
"v_cndmask_b32 v65, v65, v11, vcc\n"
"s_mov_b64 s[76:77], %4\n"
"v_mov_b32 v14, s76\n"
"v_mov_b32 v15, s77\n"
"v_mov_b32 v3, 14336\n"
"v_cmp_gt_u32 vcc, v3, v1\n"
"s_nop 1\n"
"v_cndmask_b32 v12, v12, v14, vcc\n"
"v_cndmask_b32 v13, v13, v15, vcc\n"
"v_cndmask_b32 v65, v65, v2, vcc\n"
"v_mov_b32 v2, v65\n"
"v_add_co_u32 v48, vcc, v12, v2\n"
"s_nop 1\n"
"v_addc_co_u32 v49, vcc, 0, v13, vcc\n"
"s_add_u32 s35, s35, 1\n"
"L_pfl_Kl_%=:\n"
"global_load_dword v51, v[48:49], off\n"
"s_branch L_pfr_Kl_%=\n"
"L_pfw_Kl_%=:\n"
"s_and_b32 s76, s72, 15\n"
"s_lshl_b32 s76, s76, 3\n"
"s_add_u32 s76, s76, s71\n"
"s_lshl_b32 s76, s76, 2\n"
"s_add_u32 s76, s76, s73\n"
"s_lshl_b32 s76, s76, 6\n"
"v_add_u32 v1, s76, v0\n"
"v_mov_b32 v2, 31807\n"
"v_min_u32 v1, v1, v2\n"
"s_mov_b64 s[76:77], %10\n"
"v_mov_b32 v12, s76\n"
"v_mov_b32 v13, s77\n"
"s_mov_b64 s[76:77], %11\n"
"v_mov_b32 v14, s76\n"
"v_mov_b32 v15, s77\n"
"v_subrev_u32 v3, 28672, v1\n"
"v_mov_b32 v2, 28672\n"
"v_cmp_gt_u32 vcc, v2, v1\n"
"s_nop 1\n"
"v_cndmask_b32 v1, v3, v1, vcc\n"
"v_cndmask_b32 v12, v14, v12, vcc\n"
"v_cndmask_b32 v13, v15, v13, vcc\n"
"v_lshlrev_b32 v1, 7, v1\n"
"v_add_co_u32 v48, vcc, v12, v1\n"
"s_nop 1\n"
"v_addc_co_u32 v49, vcc, 0, v13, vcc\n"
"s_add_u32 s35, s35, 1\n"
"s_branch L_pfl_Kl_%=\n"
"L_end_%=:\n"
"s_waitcnt vmcnt(0)\n"
"s_mov_b32 %0, s55\n"
"L_cpend_k1_%=:\n"
    : "=s"(tag) : "s"(ka[0]), "s"(ka[1]), "s"(ka[2]), "s"(ka[3]), "s"(ka[4]), "s"(ka[5]), "s"(ka[6]), "s"((int)blockIdx.x), "s"(wid), "s"(ka[8]), "s"(ka[9])
    : "v0", "v1", "v2", "v3", "v4", "v5", "v6", "v7", "v8", "v9", "v10", "v11", "v12", "v13", "v14", "v15", "v16", "v17", "v18", "v19", "v20", "v21", "v22", "v23", "v24", "v25", "v26", "v27", "v28", "v29", "v30", "v31", "v32", "v33", "v34", "v35", "v36", "v37", "v38", "v39", "v40", "v41", "v42", "v43", "v44", "v45", "v46", "v47", "v48", "v49", "v50", "v51", "v52", "v53", "v54", "v55", "v56", "v57", "v58", "v59", "v60", "v61", "v62", "v63", "v64", "v65", "v66", "v67", "v68", "v69", "v70", "v71", "v72", "v73", "v74", "v75", "v76", "v77", "v78", "v79", "v80", "v81", "v82", "v83", "v84", "v85", "v86", "v87", "v88", "v89", "v90", "v91", "v92", "v93", "v94", "v95", "v96", "v97", "v98", "v99", "v100", "v101", "v102", "v103", "v104", "v105", "v106", "v107", "v108", "v109", "v110", "v111", "v112", "v113", "v114", "v115", "v116", "v117", "v118", "v119", "v120", "v121", "v122", "v123", "v124", "v125", "v126", "v127", "v128", "v129", "v130", "v131", "v132", "v133", "v134", "v135", "v136", "v137", "v138", "v139", "v140", "v141", "v142", "v143", "v144", "v145", "v146", "v147", "v148", "v149", "v150", "v151", "v152", "v153", "v154", "v155", "v156", "v157", "v158", "v159", "v160", "v161", "v162", "v163", "v164", "v165", "v166", "v167", "v168", "v169", "v170", "v171", "v172", "v173", "v174", "v175", "v176", "v177", "v178", "v179", "v180", "v181", "v182", "v183", "v184", "v185", "v186", "v187", "v188", "v189", "v190", "v191", "v192", "v193", "v194", "v195", "v196", "v197", "v198", "v199", "v200", "v201", "v202", "v203", "v204", "v205", "v206", "v207", "v208", "v209", "v210", "v211", "v212", "v213", "v214", "v215", "v216", "v217", "v218", "v219", "v220", "v221", "v222", "v223", "v224", "v225", "v226", "v227", "v228", "v229", "v230", "v231", "v232", "v233", "v234", "v235", "v236", "v237", "v238", "v239", "v240", "v241", "v242", "v243", "v244", "v245", "v246", "v247", "v248", "v249", "v250", "v251", "v252", "v253", "v254", "v255", "a0", "a1", "a2", "a3", "a4", "a5", "a6", "a7", "a8", "a9", "a10", "a11", "a12", "a13", "a14", "a15", "a16", "a17", "a18", "a19", "a20", "a21", "a22", "a23", "a24", "a25", "a26", "a27", "a28", "a29", "a30", "a31", "a32", "a33", "a34", "a35", "a36", "a37", "a38", "a39", "a40", "a41", "a42", "a43", "a44", "a45", "a46", "a47", "a48", "a49", "a50", "a51", "a52", "a53", "a54", "a55", "a56", "a57", "a58", "a59", "a60", "a61", "a62", "a63", "a64", "a65", "a66", "a67", "a68", "a69", "a70", "a71", "a72", "a73", "a74", "a75", "a76", "a77", "a78", "a79", "a80", "a81", "a82", "a83", "a84", "a85", "a86", "a87", "a88", "a89", "a90", "a91", "a92", "a93", "a94", "a95", "a96", "a97", "a98", "a99", "a100", "a101", "a102", "a103", "a104", "a105", "a106", "a107", "a108", "a109", "a110", "a111", "a112", "a113", "a114", "a115", "a116", "a117", "a118", "a119", "a120", "a121", "a122", "a123", "a124", "a125", "a126", "a127", "a128", "a129", "a130", "a131", "a132", "a133", "a134", "a135", "a136", "a137", "a138", "a139", "a140", "a141", "a142", "a143", "a144", "a145", "a146", "a147", "a148", "a149", "a150", "a151", "a152", "a153", "a154", "a155", "a156", "a157", "a158", "a159", "a160", "a161", "a162", "a163", "a164", "a165", "a166", "a167", "a168", "a169", "a170", "a171", "a172", "a173", "a174", "a175", "a176", "a177", "a178", "a179", "a180", "a181", "a182", "a183", "a184", "a185", "a186", "a187", "a188", "a189", "a190", "a191", "a192", "a193", "a194", "a195", "a196", "a197", "a198", "a199", "a200", "a201", "a202", "a203", "a204", "a205", "a206", "a207", "a208", "a209", "a210", "a211", "a212", "a213", "a214", "a215", "a216", "a217", "a218", "a219", "a220", "a221", "a222", "a223", "a224", "a225", "a226", "a227", "a228", "a229", "a230", "a231", "a232", "a233", "a234", "a235", "a236", "a237", "a238", "a239", "a240", "a241", "a242", "a243", "a244", "a245", "a246", "a247", "a248", "a249", "a250", "a251", "a252", "a253", "a254", "a255", "s34", "s35", "s36", "s37", "s38", "s39", "s40", "s41", "s42", "s43", "s44", "s45", "s46", "s47", "s48", "s49", "s50", "s51", "s52", "s53", "s54", "s55", "s56", "s57", "s58", "s59", "s60", "s61", "s62", "s63", "s64", "s65", "s66", "s67", "s68", "s69", "s70", "s71", "s72", "s73", "s74", "s75", "s76", "s77", "s78", "s79", "s80", "s81", "s82", "s83", "s84", "s85", "s86", "s87", "s88", "s89", "s90", "s91", "s92", "s93", "s94", "s95", "s96", "s97", "s98", "s99", "vcc", "scc", "memory");
  asm volatile("" : "+s"(ka));
  const int idx = blockIdx.x >> 3;
  if (idx >= 16) {
    int lane; asm volatile("v_mbcnt_lo_u32_b32 %0, -1, 0\n\tv_mbcnt_hi_u32_b32 %0, -1, %0" : "=v"(lane));
    const int tok0 = 128 * (idx - 16) + 16 * (int)(blockIdx.x & 7) + 4 * wid;
    GateRegs gr; gate_load(gr, (const u16*)ARG(3), (const u16*)ARG(7), tok0, lane);
    local_attn(ARG(6), lds, idx - 16, (int)(blockIdx.x & 7), wid * 64 + lane);
    gate_finish(gr, ARG(6), tok0, lane);
    asm volatile("s_waitcnt vmcnt(0)" ::: "memory");
    __syncthreads();
    if (wid == 0 && lane == 0) st_dev32(ARG(6) + F_BL + 4 * ((idx - 16) * 8 + (blockIdx.x & 7)), (u32)tag);
  }
  TL_END1(ARG(6))
  asm volatile("" : "+s"(ka));
  asm volatile(
"v_mbcnt_lo_u32_b32 v0, -1, 0\n"
"v_mbcnt_hi_u32_b32 v0, -1, v0\n"
"s_mov_b64 s[36:37], %0\n"
"s_and_b32 s37, s37, 0xffff\n"
"s_mov_b32 s38, 268435456\n"
"s_mov_b32 s39, 0x20000\n"
"s_mov_b64 s[40:41], %0\n"
"s_add_u32 s40, s40, 16777216\n"
"s_addc_u32 s41, s41, 0\n"
"s_and_b32 s41, s41, 0xffff\n"
"s_mov_b32 s42, 33554432\n"
"s_mov_b32 s43, 0x20000\n"
"s_mov_b64 s[44:45], %0\n"
"s_add_u32 s44, s44, 33554432\n"
"s_addc_u32 s45, s45, 0\n"
"s_and_b32 s45, s45, 0xffff\n"
"s_mov_b32 s46, 16777216\n"
"s_mov_b32 s47, 0x20000\n"
"s_mov_b64 s[80:81], %0\n"
"s_mov_b32 s49, %2\n"
"s_mov_b32 s35, %8\n"
"s_and_b32 s96, %1, 7\n"
"s_lshr_b32 s97, %1, 3\n"
"s_and_b32 s48, s97, 1\n"
"s_lshl_b32 s96, s96, 1\n"
"s_add_u32 s48, s48, s96\n"
"s_lshr_b32 s97, s97, 1\n"
"s_lshr_b32 s50, s97, 3\n"
"s_xor_b32 s50, s50, 1\n"
"s_and_b32 s51, s97, 7\n"
"s_sub_u32 s52, 15, s51\n"
"s_lshl_b32 s68, s48, 3\n"
"s_add_u32 s68, s68, s51\n"
"s_lshl_b32 s96, s68, 4\n"
"s_add_u32 s96, s96, 126882816\n"
"s_add_u32 s80, s80, s96\n"
"s_addc_u32 s81, s81, 0\n"
"s_lshl_b32 s65, s49, 13\n"
"v_lshlrev_b32 v1, 4, v0\n"
"v_add_u32 v2, 0x1000, v1\n"
"v_mov_b32 v3, v1\n"
"v_add_u32 v4, 0x10000, v1\n"
"v_mov_b32 v9, 0xff800000\n"
"s_mov_b32 s66, 0x45800000\n"
"s_mov_b32 s67, 0xff7fffff\n"
"s_mov_b32 s53, s52\n"
"s_lshl_b32 s64, s48, 20\n"
"s_add_u32 s64, s64, s65\n"
"s_lshl_b32 s99, s52, 1\n"
"s_add_u32 s99, s99, 2\n"
"s_cmp_eq_u32 s50, 0\n"
"s_cbranch_scc0 L_initB_%=\n"
"s_mov_b32 s78, 0\n"
"s_min_u32 s79, s99, 18\n"
"s_mov_b32 s57, s79\n"
"s_mov_b32 s58, s79\n"
"s_cmp_le_u32 s99, 18\n"
"s_cselect_b32 s69, 2, 0\n"
"s_mov_b32 s59, 0x7ffffff0\n"
"s_branch L_initD_%=\n"
"L_initB_%=:\n"
"s_cmp_le_u32 s99, 18\n"
"s_cbranch_scc1 L_initS_%=\n"
"s_mov_b32 s78, 18\n"
"s_mov_b32 s79, s99\n"
"s_sub_u32 s57, s79, 18\n"
"s_lshl_b32 s96, s51, 1\n"
"s_add_u32 s96, s96, 2\n"
"s_add_u32 s58, s57, s96\n"
"s_add_u32 s59, s57, 3\n"
"s_mov_b32 s69, 1\n"
"s_branch L_initD_%=\n"
"L_initS_%=:\n"
"s_mov_b32 s53, s51\n"
"s_mov_b32 s78, 0\n"
"s_lshl_b32 s79, s51, 1\n"
"s_add_u32 s79, s79, 2\n"
"s_mov_b32 s57, s79\n"
"s_mov_b32 s58, s79\n"
"s_mov_b32 s59, 0x7ffffff0\n"
"s_mov_b32 s69, 2\n"
"L_initD_%=:\n"
"s_lshl_b32 s96, s53, 1\n"
"s_add_u32 s97, s78, 1\n"
"s_min_u32 s98, s79, s96\n"
"s_max_u32 s98, s98, s97\n"
"s_sub_u32 s55, s98, s97\n"
"s_sub_u32 s56, s79, s98\n"
"s_mov_b32 s54, s98\n"
"s_lshl_b32 s96, s78, 15\n"
"s_add_u32 s60, s64, s96\n"
"s_add_u32 s61, s60, 0x8000\n"
"s_cmp_eq_u32 s57, 1\n"
"s_cselect_b32 s61, s64, s61\n"
"s_mov_b32 s63, 1\n"
"s_mov_b32 s96, 0x7fffffff\n"
"s_cmp_gt_u32 s57, s63\n"
"s_cselect_b32 s97, s57, 0x7fffffff\n"
"s_min_u32 s96, s96, s97\n"
"s_cmp_gt_u32 s58, s63\n"
"s_cselect_b32 s97, s58, 0x7fffffff\n"
"s_min_u32 s96, s96, s97\n"
"s_cmp_gt_u32 s59, s63\n"
"s_cselect_b32 s97, s59, 0x7fffffff\n"
"s_min_u32 s96, s96, s97\n"
"s_sub_u32 s62, s96, s63\n"
"s_sub_u32 s62, s62, 1\n"
"s_mov_b32 s63, s96\n"
"s_mov_b32 s82, s78\n"
"s_lshl_b32 s79, s48, 20\n"
"s_lshl_b32 s96, s53, 16\n"
"s_add_u32 s79, s79, s96\n"
"s_lshl_b32 s96, s49, 14\n"
"s_add_u32 s79, s79, s96\n"
"v_and_b32 v10, 15, v0\n"
"v_lshrrev_b32 v11, 4, v0\n"
"v_lshlrev_b32 v11, 2, v11\n"
"v_sub_u32 v10, v10, v11\n"
"s_lshl_b32 s96, s53, 7\n"
"s_lshl_b32 s97, s49, 5\n"
"s_add_u32 s96, s96, s97\n"
"v_add_u32 v7, s96, v10\n"
"v_add_u32 v8, 16, v7\n"
"s_lshl_b32 s96, s48, 6\n"
"s_add_u32 s96, s96, 126879744\n"
"v_lshlrev_b32 v237, 2, v0\n"
"v_add_u32 v237, s96, v237\n"
"s_mov_b32 s99, 0\n"
"L_vpl_kv_%=:\n"
"buffer_load_dword v236, v237, s[36:39], 0 offen sc1\n"
"s_waitcnt vmcnt(0)\n"
"v_cmp_ne_u32 vcc, s35, v236\n"
"s_nop 4\n"
"s_and_b32 vcc_lo, vcc_lo, 65535\n"
"s_cbranch_scc0 L_vpd_kv_%=\n"
"s_sleep 1\n"
"s_add_u32 s99, s99, 1\n"
"s_cmp_lt_u32 s99, 0x200000\n"
"s_cbranch_scc1 L_vpl_kv_%=\n"
"L_vpd_kv_%=:\n"
"s_add_u32 m0, s65, 0\n"
"s_nop 0\n"
"buffer_load_dwordx4 v1, s[40:43], s60 offen offset:0 sc1 lds\n"
"buffer_load_dwordx4 v1, s[40:43], s60 offen offset:1024 sc1 lds\n"
"buffer_load_dwordx4 v1, s[40:43], s60 offen offset:2048 sc1 lds\n"
"buffer_load_dwordx4 v1, s[40:43], s60 offen offset:3072 sc1 lds\n"
"s_add_u32 m0, s65, 4096\n"
"s_nop 0\n"
"buffer_load_dwordx4 v2, s[40:43], s60 offen offset:0 sc1 lds\n"
"buffer_load_dwordx4 v2, s[40:43], s60 offen offset:1024 sc1 lds\n"
"buffer_load_dwordx4 v2, s[40:43], s60 offen offset:2048 sc1 lds\n"
"buffer_load_dwordx4 v2, s[40:43], s60 offen offset:3072 sc1 lds\n"
"s_add_u32 m0, s65, 32768\n"
"s_nop 0\n"
"buffer_load_dwordx4 v1, s[40:43], s61 offen offset:0 sc1 lds\n"
"buffer_load_dwordx4 v1, s[40:43], s61 offen offset:1024 sc1 lds\n"
"buffer_load_dwordx4 v1, s[40:43], s61 offen offset:2048 sc1 lds\n"
"buffer_load_dwordx4 v1, s[40:43], s61 offen offset:3072 sc1 lds\n"
"s_add_u32 m0, s65, 36864\n"
"s_nop 0\n"
"buffer_load_dwordx4 v2, s[40:43], s61 offen offset:0 sc1 lds\n"
"buffer_load_dwordx4 v2, s[40:43], s61 offen offset:1024 sc1 lds\n"
"buffer_load_dwordx4 v2, s[40:43], s61 offen offset:2048 sc1 lds\n"
"buffer_load_dwordx4 v2, s[40:43], s61 offen offset:3072 sc1 lds\n"
"s_lshl_b32 s96, s48, 3\n"
"s_lshr_b32 s97, s53, 1\n"
"s_add_u32 s97, s97, s96\n"
"s_lshl_b32 s97, s97, 2\n"
"s_add_u32 s97, s97, 126880768\n"
"s_lshr_b32 s98, s51, 1\n"
"s_add_u32 s98, s98, s96\n"
"s_lshl_b32 s98, s98, 2\n"
"s_add_u32 s98, s98, 126880768\n"
"s_cmp_eq_u32 s69, 1\n"
"s_cselect_b32 s98, s98, s97\n"
"v_mov_b32 v237, s97\n"
"v_mov_b32 v11, s98\n"
"v_cmp_lt_u32 vcc, 0, v0\n"
"s_nop 1\n"
"v_cndmask_b32 v237, v237, v11, vcc\n"
"s_mov_b32 s99, 0\n"
"L_vpl_st_%=:\n"
"buffer_load_dword v236, v237, s[36:39], 0 offen sc1\n"
"s_waitcnt vmcnt(0)\n"
"v_cmp_ne_u32 vcc, s35, v236\n"
"s_nop 4\n"
"s_and_b32 vcc_lo, vcc_lo, 3\n"
"s_cbranch_scc0 L_vpd_st_%=\n"
"s_sleep 1\n"
"s_add_u32 s99, s99, 1\n"
"s_cmp_lt_u32 s99, 0x200000\n"
"s_cbranch_scc1 L_vpl_st_%=\n"
"L_vpd_st_%=:\n"
"buffer_load_dwordx4 a[128:131], v1, s[36:39], s79 offen offset:0 sc1\n"
"buffer_load_dwordx4 a[132:135], v1, s[36:39], s79 offen offset:1024 sc1\n"
"buffer_load_dwordx4 a[136:139], v1, s[36:39], s79 offen offset:2048 sc1\n"
"buffer_load_dwordx4 a[140:143], v1, s[36:39], s79 offen offset:3072 sc1\n"
"buffer_load_dwordx4 a[144:147], v2, s[36:39], s79 offen offset:0 sc1\n"
"buffer_load_dwordx4 a[148:151], v2, s[36:39], s79 offen offset:1024 sc1\n"
"buffer_load_dwordx4 a[152:155], v2, s[36:39], s79 offen offset:2048 sc1\n"
"buffer_load_dwordx4 a[156:159], v2, s[36:39], s79 offen offset:3072 sc1\n"
"s_add_u32 s99, s79, 8192\n"
"buffer_load_dwordx4 a[160:163], v1, s[36:39], s99 offen offset:0 sc1\n"
"buffer_load_dwordx4 a[164:167], v1, s[36:39], s99 offen offset:1024 sc1\n"
"buffer_load_dwordx4 a[168:171], v1, s[36:39], s99 offen offset:2048 sc1\n"
"buffer_load_dwordx4 a[172:175], v1, s[36:39], s99 offen offset:3072 sc1\n"
"buffer_load_dwordx4 a[176:179], v2, s[36:39], s99 offen offset:0 sc1\n"
"buffer_load_dwordx4 a[180:183], v2, s[36:39], s99 offen offset:1024 sc1\n"
"buffer_load_dwordx4 a[184:187], v2, s[36:39], s99 offen offset:2048 sc1\n"
"buffer_load_dwordx4 a[188:191], v2, s[36:39], s99 offen offset:3072 sc1\n"
"v_mov_b32 v236, 0\n"
"s_waitcnt vmcnt(0)\n"
"s_barrier\n"
"ds_read_b128 a[192:195], v3 offset:0\n"
"ds_read_b128 a[196:199], v3 offset:1024\n"
"ds_read_b128 a[200:203], v3 offset:2048\n"
"ds_read_b128 a[204:207], v3 offset:3072\n"
"ds_read_b128 a[208:211], v3 offset:4096\n"
"ds_read_b128 a[212:215], v3 offset:5120\n"
"ds_read_b128 a[216:219], v3 offset:6144\n"
"ds_read_b128 a[220:223], v3 offset:7168\n"
"ds_read_b128 a[224:227], v3 offset:8192\n"
"ds_read_b128 a[228:231], v3 offset:9216\n"
"ds_read_b128 a[232:235], v3 offset:10240\n"
"ds_read_b128 a[236:239], v3 offset:11264\n"
"ds_read_b128 a[240:243], v3 offset:12288\n"
"ds_read_b128 a[244:247], v3 offset:13312\n"
"L_cpa0_%=:\n"
"s_waitcnt lgkmcnt(6)\n"
"v_mfma_f32_16x16x32_bf16 v[32:35], a[192:195], a[128:131], 0\n"
"ds_read_b128 a[248:251], v3 offset:14336\n"
"v_accvgpr_write_b32 a0, 0\n"
"v_accvgpr_write_b32 a1, 0\n"
"v_mfma_f32_16x16x32_bf16 v[36:39], a[192:195], a[160:163], 0\n"
"v_accvgpr_write_b32 a2, 0\n"
"v_accvgpr_write_b32 a3, 0\n"
"v_mfma_f32_16x16x32_bf16 v[32:35], a[196:199], a[132:135], v[32:35]\n"
"ds_read_b128 a[252:255], v3 offset:15360\n"
"v_accvgpr_write_b32 a4, 0\n"
"v_accvgpr_write_b32 a5, 0\n"
"v_mfma_f32_16x16x32_bf16 v[36:39], a[196:199], a[164:167], v[36:39]\n"
"v_accvgpr_write_b32 a6, 0\n"
"v_accvgpr_write_b32 a7, 0\n"
"v_mfma_f32_16x16x32_bf16 v[32:35], a[200:203], a[136:139], v[32:35]\n"
"ds_read_b128 a[192:195], v3 offset:16384\n"
"v_accvgpr_write_b32 a8, 0\n"
"v_accvgpr_write_b32 a9, 0\n"
"v_mfma_f32_16x16x32_bf16 v[36:39], a[200:203], a[168:171], v[36:39]\n"
"v_accvgpr_write_b32 a10, 0\n"
"v_accvgpr_write_b32 a11, 0\n"
"v_mfma_f32_16x16x32_bf16 v[32:35], a[204:207], a[140:143], v[32:35]\n"
"ds_read_b128 a[196:199], v3 offset:17408\n"
"v_accvgpr_write_b32 a12, 0\n"
"v_accvgpr_write_b32 a13, 0\n"
"v_mfma_f32_16x16x32_bf16 v[36:39], a[204:207], a[172:175], v[36:39]\n"
"v_accvgpr_write_b32 a14, 0\n"
"v_accvgpr_write_b32 a15, 0\n"
"v_mfma_f32_16x16x32_bf16 v[32:35], a[208:211], a[144:147], v[32:35]\n"
"ds_read_b128 a[200:203], v3 offset:18432\n"
"v_accvgpr_write_b32 a16, 0\n"
"v_accvgpr_write_b32 a17, 0\n"
"v_mfma_f32_16x16x32_bf16 v[36:39], a[208:211], a[176:179], v[36:39]\n"
"v_accvgpr_write_b32 a18, 0\n"
"v_accvgpr_write_b32 a19, 0\n"
"v_mfma_f32_16x16x32_bf16 v[32:35], a[212:215], a[148:151], v[32:35]\n"
"ds_read_b128 a[204:207], v3 offset:19456\n"
"v_accvgpr_write_b32 a20, 0\n"
"v_accvgpr_write_b32 a21, 0\n"
"v_mfma_f32_16x16x32_bf16 v[36:39], a[212:215], a[180:183], v[36:39]\n"
"v_accvgpr_write_b32 a22, 0\n"
"v_accvgpr_write_b32 a23, 0\n"
"v_mfma_f32_16x16x32_bf16 v[32:35], a[216:219], a[152:155], v[32:35]\n"
"ds_read_b128 a[208:211], v3 offset:20480\n"
"v_accvgpr_write_b32 a24, 0\n"
"v_accvgpr_write_b32 a25, 0\n"
"v_mfma_f32_16x16x32_bf16 v[36:39], a[216:219], a[184:187], v[36:39]\n"
"v_accvgpr_write_b32 a26, 0\n"
"v_accvgpr_write_b32 a27, 0\n"
"v_mfma_f32_16x16x32_bf16 v[32:35], a[220:223], a[156:159], v[32:35]\n"
"ds_read_b128 a[212:215], v3 offset:21504\n"
"v_accvgpr_write_b32 a28, 0\n"
"v_accvgpr_write_b32 a29, 0\n"
"v_mfma_f32_16x16x32_bf16 v[36:39], a[220:223], a[188:191], v[36:39]\n"
"s_waitcnt lgkmcnt(6)\n"
"v_accvgpr_write_b32 a30, 0\n"
"v_accvgpr_write_b32 a31, 0\n"
"s_barrier\n"
"v_mfma_f32_16x16x32_bf16 v[40:43], a[224:227], a[128:131], 0\n"
"ds_read_b128 a[216:219], v3 offset:22528\n"
"v_accvgpr_write_b32 a32, 0\n"
"v_accvgpr_write_b32 a33, 0\n"
"s_add_u32 m0, s65, 65536\n"
"v_mfma_f32_16x16x32_bf16 v[44:47], a[224:227], a[160:163], 0\n"
"v_accvgpr_write_b32 a34, 0\n"
"v_accvgpr_write_b32 a35, 0\n"
"buffer_load_dwordx4 v1, s[44:47], s60 offen offset:0 sc1 lds\n"
"v_mfma_f32_16x16x32_bf16 v[40:43], a[228:231], a[132:135], v[40:43]\n"
"ds_read_b128 a[220:223], v3 offset:23552\n"
"v_accvgpr_write_b32 a36, 0\n"
"v_accvgpr_write_b32 a37, 0\n"
"v_mfma_f32_16x16x32_bf16 v[44:47], a[228:231], a[164:167], v[44:47]\n"
"v_accvgpr_write_b32 a38, 0\n"
"v_accvgpr_write_b32 a39, 0\n"
"v_mfma_f32_16x16x32_bf16 v[40:43], a[232:235], a[136:139], v[40:43]\n"
"ds_read_b128 a[224:227], v3 offset:24576\n"
"v_accvgpr_write_b32 a40, 0\n"
"v_accvgpr_write_b32 a41, 0\n"
"v_mfma_f32_16x16x32_bf16 v[44:47], a[232:235], a[168:171], v[44:47]\n"
"v_accvgpr_write_b32 a42, 0\n"
"v_accvgpr_write_b32 a43, 0\n"
"v_mfma_f32_16x16x32_bf16 v[40:43], a[236:239], a[140:143], v[40:43]\n"
"ds_read_b128 a[228:231], v3 offset:25600\n"
"v_accvgpr_write_b32 a44, 0\n"
"v_accvgpr_write_b32 a45, 0\n"
"buffer_load_dwordx4 v1, s[44:47], s60 offen offset:1024 sc1 lds\n"
"v_mfma_f32_16x16x32_bf16 v[44:47], a[236:239], a[172:175], v[44:47]\n"
"v_accvgpr_write_b32 a46, 0\n"
"v_accvgpr_write_b32 a47, 0\n"
"v_mfma_f32_16x16x32_bf16 v[40:43], a[240:243], a[144:147], v[40:43]\n"
"ds_read_b128 a[232:235], v3 offset:26624\n"
"v_accvgpr_write_b32 a48, 0\n"
"v_accvgpr_write_b32 a49, 0\n"
"v_mfma_f32_16x16x32_bf16 v[44:47], a[240:243], a[176:179], v[44:47]\n"
"v_accvgpr_write_b32 a50, 0\n"
"v_accvgpr_write_b32 a51, 0\n"
"v_mfma_f32_16x16x32_bf16 v[40:43], a[244:247], a[148:151], v[40:43]\n"
"ds_read_b128 a[236:239], v3 offset:27648\n"
"v_accvgpr_write_b32 a52, 0\n"
"v_accvgpr_write_b32 a53, 0\n"
"v_mfma_f32_16x16x32_bf16 v[44:47], a[244:247], a[180:183], v[44:47]\n"
"v_accvgpr_write_b32 a54, 0\n"
"v_accvgpr_write_b32 a55, 0\n"
"buffer_load_dwordx4 v1, s[44:47], s60 offen offset:2048 sc1 lds\n"
"v_mfma_f32_16x16x32_bf16 v[40:43], a[248:251], a[152:155], v[40:43]\n"
"ds_read_b128 a[240:243], v3 offset:28672\n"
"v_accvgpr_write_b32 a56, 0\n"
"v_accvgpr_write_b32 a57, 0\n"
"v_mfma_f32_16x16x32_bf16 v[44:47], a[248:251], a[184:187], v[44:47]\n"
"v_accvgpr_write_b32 a58, 0\n"
"v_accvgpr_write_b32 a59, 0\n"
"v_mfma_f32_16x16x32_bf16 v[40:43], a[252:255], a[156:159], v[40:43]\n"
"ds_read_b128 a[244:247], v3 offset:29696\n"
"v_accvgpr_write_b32 a60, 0\n"
"v_accvgpr_write_b32 a61, 0\n"
"v_mfma_f32_16x16x32_bf16 v[44:47], a[252:255], a[188:191], v[44:47]\n"
"s_waitcnt lgkmcnt(6)\n"
"v_accvgpr_write_b32 a62, 0\n"
"v_accvgpr_write_b32 a63, 0\n"
"v_mfma_f32_16x16x32_bf16 v[48:51], a[192:195], a[128:131], 0\n"
"ds_read_b128 a[248:251], v3 offset:30720\n"
"v_accvgpr_write_b32 a64, 0\n"
"v_accvgpr_write_b32 a65, 0\n"
"buffer_load_dwordx4 v1, s[44:47], s60 offen offset:3072 sc1 lds\n"
"v_mfma_f32_16x16x32_bf16 v[52:55], a[192:195], a[160:163], 0\n"
"v_accvgpr_write_b32 a66, 0\n"
"v_accvgpr_write_b32 a67, 0\n"
"v_mfma_f32_16x16x32_bf16 v[48:51], a[196:199], a[132:135], v[48:51]\n"
"ds_read_b128 a[252:255], v3 offset:31744\n"
"v_accvgpr_write_b32 a68, 0\n"
"v_accvgpr_write_b32 a69, 0\n"
"v_mfma_f32_16x16x32_bf16 v[52:55], a[196:199], a[164:167], v[52:55]\n"
"v_accvgpr_write_b32 a70, 0\n"
"v_accvgpr_write_b32 a71, 0\n"
"v_mfma_f32_16x16x32_bf16 v[48:51], a[200:203], a[136:139], v[48:51]\n"
"v_accvgpr_write_b32 a72, 0\n"
"v_accvgpr_write_b32 a73, 0\n"
"s_add_u32 m0, s65, 69632\n"
"v_mfma_f32_16x16x32_bf16 v[52:55], a[200:203], a[168:171], v[52:55]\n"
"v_accvgpr_write_b32 a74, 0\n"
"v_accvgpr_write_b32 a75, 0\n"
"buffer_load_dwordx4 v2, s[44:47], s60 offen offset:0 sc1 lds\n"
"v_mfma_f32_16x16x32_bf16 v[48:51], a[204:207], a[140:143], v[48:51]\n"
"v_accvgpr_write_b32 a76, 0\n"
"v_accvgpr_write_b32 a77, 0\n"
"v_mfma_f32_16x16x32_bf16 v[52:55], a[204:207], a[172:175], v[52:55]\n"
"v_accvgpr_write_b32 a78, 0\n"
"v_accvgpr_write_b32 a79, 0\n"
"v_mfma_f32_16x16x32_bf16 v[48:51], a[208:211], a[144:147], v[48:51]\n"
"v_accvgpr_write_b32 a80, 0\n"
"v_accvgpr_write_b32 a81, 0\n"
"v_mfma_f32_16x16x32_bf16 v[52:55], a[208:211], a[176:179], v[52:55]\n"
"v_accvgpr_write_b32 a82, 0\n"
"v_accvgpr_write_b32 a83, 0\n"
"v_mfma_f32_16x16x32_bf16 v[48:51], a[212:215], a[148:151], v[48:51]\n"
"v_accvgpr_write_b32 a84, 0\n"
"v_accvgpr_write_b32 a85, 0\n"
"buffer_load_dwordx4 v2, s[44:47], s60 offen offset:1024 sc1 lds\n"
"v_mfma_f32_16x16x32_bf16 v[52:55], a[212:215], a[180:183], v[52:55]\n"
"v_accvgpr_write_b32 a86, 0\n"
"v_accvgpr_write_b32 a87, 0\n"
"v_mfma_f32_16x16x32_bf16 v[48:51], a[216:219], a[152:155], v[48:51]\n"
"v_accvgpr_write_b32 a88, 0\n"
"v_accvgpr_write_b32 a89, 0\n"
"v_mfma_f32_16x16x32_bf16 v[52:55], a[216:219], a[184:187], v[52:55]\n"
"v_accvgpr_write_b32 a90, 0\n"
"v_accvgpr_write_b32 a91, 0\n"
"v_mfma_f32_16x16x32_bf16 v[48:51], a[220:223], a[156:159], v[48:51]\n"
"v_accvgpr_write_b32 a92, 0\n"
"v_accvgpr_write_b32 a93, 0\n"
"v_mfma_f32_16x16x32_bf16 v[52:55], a[220:223], a[188:191], v[52:55]\n"
"s_waitcnt lgkmcnt(0)\n"
"v_accvgpr_write_b32 a94, 0\n"
"v_accvgpr_write_b32 a95, 0\n"
"buffer_load_dwordx4 v2, s[44:47], s60 offen offset:2048 sc1 lds\n"
"v_mfma_f32_16x16x32_bf16 v[56:59], a[224:227], a[128:131], 0\n"
"v_accvgpr_write_b32 a96, 0\n"
"v_accvgpr_write_b32 a97, 0\n"
"v_mfma_f32_16x16x32_bf16 v[60:63], a[224:227], a[160:163], 0\n"
"v_accvgpr_write_b32 a98, 0\n"
"v_accvgpr_write_b32 a99, 0\n"
"v_mfma_f32_16x16x32_bf16 v[56:59], a[228:231], a[132:135], v[56:59]\n"
"v_accvgpr_write_b32 a100, 0\n"
"v_accvgpr_write_b32 a101, 0\n"
"v_mfma_f32_16x16x32_bf16 v[60:63], a[228:231], a[164:167], v[60:63]\n"
"v_accvgpr_write_b32 a102, 0\n"
"v_accvgpr_write_b32 a103, 0\n"
"v_mfma_f32_16x16x32_bf16 v[56:59], a[232:235], a[136:139], v[56:59]\n"
"v_accvgpr_write_b32 a104, 0\n"
"v_accvgpr_write_b32 a105, 0\n"
"buffer_load_dwordx4 v2, s[44:47], s60 offen offset:3072 sc1 lds\n"
"v_mfma_f32_16x16x32_bf16 v[60:63], a[232:235], a[168:171], v[60:63]\n"
"v_accvgpr_write_b32 a106, 0\n"
"v_accvgpr_write_b32 a107, 0\n"
"v_mfma_f32_16x16x32_bf16 v[56:59], a[236:239], a[140:143], v[56:59]\n"
"v_accvgpr_write_b32 a108, 0\n"
"v_accvgpr_write_b32 a109, 0\n"
"v_mfma_f32_16x16x32_bf16 v[60:63], a[236:239], a[172:175], v[60:63]\n"
"v_accvgpr_write_b32 a110, 0\n"
"v_accvgpr_write_b32 a111, 0\n"
"v_mfma_f32_16x16x32_bf16 v[56:59], a[240:243], a[144:147], v[56:59]\n"
"v_accvgpr_write_b32 a112, 0\n"
"v_accvgpr_write_b32 a113, 0\n"
"v_mfma_f32_16x16x32_bf16 v[60:63], a[240:243], a[176:179], v[60:63]\n"
"v_accvgpr_write_b32 a114, 0\n"
"v_accvgpr_write_b32 a115, 0\n"
"v_mfma_f32_16x16x32_bf16 v[56:59], a[244:247], a[148:151], v[56:59]\n"
"v_accvgpr_write_b32 a116, 0\n"
"v_accvgpr_write_b32 a117, 0\n"
"v_mfma_f32_16x16x32_bf16 v[60:63], a[244:247], a[180:183], v[60:63]\n"
"v_accvgpr_write_b32 a118, 0\n"
"v_accvgpr_write_b32 a119, 0\n"
"v_mfma_f32_16x16x32_bf16 v[56:59], a[248:251], a[152:155], v[56:59]\n"
"v_accvgpr_write_b32 a120, 0\n"
"v_accvgpr_write_b32 a121, 0\n"
"v_mfma_f32_16x16x32_bf16 v[60:63], a[248:251], a[184:187], v[60:63]\n"
"v_accvgpr_write_b32 a122, 0\n"
"v_accvgpr_write_b32 a123, 0\n"
"v_mfma_f32_16x16x32_bf16 v[56:59], a[252:255], a[156:159], v[56:59]\n"
"v_accvgpr_write_b32 a124, 0\n"
"v_accvgpr_write_b32 a125, 0\n"
"v_mfma_f32_16x16x32_bf16 v[60:63], a[252:255], a[188:191], v[60:63]\n"
"v_accvgpr_write_b32 a126, 0\n"
"v_accvgpr_write_b32 a127, 0\n"
"s_waitcnt vmcnt(8)\n"
"s_barrier\n"
"s_add_u32 s60, s61, 0x8000\n"
"s_sub_u32 s62, s62, 1\n"
"s_cbranch_scc1 L_ev_f0_%=\n"
"L_evr_f0_%=:\n"
"s_add_u32 m0, s65, 0\n"
"s_nop 0\n"
"buffer_load_dwordx4 v1, s[40:43], s60 offen offset:0 sc1 lds\n"
"buffer_load_dwordx4 v1, s[40:43], s60 offen offset:1024 sc1 lds\n"
"buffer_load_dwordx4 v1, s[40:43], s60 offen offset:2048 sc1 lds\n"
"buffer_load_dwordx4 v1, s[40:43], s60 offen offset:3072 sc1 lds\n"
"s_add_u32 m0, s65, 4096\n"
"s_nop 0\n"
"buffer_load_dwordx4 v2, s[40:43], s60 offen offset:0 sc1 lds\n"
"buffer_load_dwordx4 v2, s[40:43], s60 offen offset:1024 sc1 lds\n"
"buffer_load_dwordx4 v2, s[40:43], s60 offen offset:2048 sc1 lds\n"
"buffer_load_dwordx4 v2, s[40:43], s60 offen offset:3072 sc1 lds\n"
"ds_read_b128 a[192:195], v3 offset:32768\n"
"ds_read_b128 a[196:199], v3 offset:33792\n"
"ds_read_b128 a[200:203], v3 offset:34816\n"
"ds_read_b128 a[204:207], v3 offset:35840\n"
"ds_read_b128 a[208:211], v3 offset:36864\n"
"ds_read_b128 a[212:215], v3 offset:37888\n"
"ds_read_b128 a[216:219], v3 offset:38912\n"
"ds_read_b128 a[220:223], v3 offset:39936\n"
"ds_read_b128 a[224:227], v3 offset:40960\n"
"ds_read_b128 a[228:231], v3 offset:41984\n"
"ds_read_b128 a[232:235], v3 offset:43008\n"
"ds_read_b128 a[236:239], v3 offset:44032\n"
"ds_read_b128 a[240:243], v3 offset:45056\n"
"ds_read_b128 a[244:247], v3 offset:46080\n"
"v_mov_b32 v16, 0\n"
"v_mov_b32 v18, 0\n"
"v_mov_b32 v17, 0\n"
"v_mov_b32 v19, 0\n"
"s_lshl_b32 s96, s53, 1\n"
"s_cmp_lt_u32 s82, s96\n"
"s_cbranch_scc1 L_fnm_f0_%=\n"
"s_lshl_b32 s96, s82, 6\n"
"v_subrev_u32 v5, s96, v7\n"
"v_subrev_u32 v6, s96, v8\n"
"s_nop 7\n"
"v_cmp_gt_i32_e64 s[70:71], 0, v5\n"
"v_cmp_gt_i32_e64 s[72:73], 1, v5\n"
"v_cmp_gt_i32_e64 s[74:75], 2, v5\n"
"v_cndmask_b32_e64 v32, v32, v9, s[70:71]\n"
"v_cmp_gt_i32_e64 s[76:77], 3, v5\n"
"v_cndmask_b32_e64 v33, v33, v9, s[72:73]\n"
"v_cmp_gt_i32_e64 s[70:71], 0, v6\n"
"v_cndmask_b32_e64 v34, v34, v9, s[74:75]\n"
"v_cmp_gt_i32_e64 s[72:73], 1, v6\n"
"v_cndmask_b32_e64 v35, v35, v9, s[76:77]\n"
"v_cmp_gt_i32_e64 s[74:75], 2, v6\n"
"v_cndmask_b32_e64 v36, v36, v9, s[70:71]\n"
"v_cmp_gt_i32_e64 s[76:77], 3, v6\n"
"v_cndmask_b32_e64 v37, v37, v9, s[72:73]\n"
"v_cmp_gt_i32_e64 s[70:71], 16, v5\n"
"v_cndmask_b32_e64 v38, v38, v9, s[74:75]\n"
"v_cmp_gt_i32_e64 s[72:73], 17, v5\n"
"v_cndmask_b32_e64 v39, v39, v9, s[76:77]\n"
"v_cmp_gt_i32_e64 s[74:75], 18, v5\n"
"v_cndmask_b32_e64 v40, v40, v9, s[70:71]\n"
"v_cmp_gt_i32_e64 s[76:77], 19, v5\n"
"v_cndmask_b32_e64 v41, v41, v9, s[72:73]\n"
"v_cmp_gt_i32_e64 s[70:71], 16, v6\n"
"v_cndmask_b32_e64 v42, v42, v9, s[74:75]\n"
"v_cmp_gt_i32_e64 s[72:73], 17, v6\n"
"v_cndmask_b32_e64 v43, v43, v9, s[76:77]\n"
"v_cmp_gt_i32_e64 s[74:75], 18, v6\n"
"v_cndmask_b32_e64 v44, v44, v9, s[70:71]\n"
"v_cmp_gt_i32_e64 s[76:77], 19, v6\n"
"v_cndmask_b32_e64 v45, v45, v9, s[72:73]\n"
"v_cmp_gt_i32_e64 s[70:71], 32, v5\n"
"v_cndmask_b32_e64 v46, v46, v9, s[74:75]\n"
"v_cmp_gt_i32_e64 s[72:73], 33, v5\n"
"v_cndmask_b32_e64 v47, v47, v9, s[76:77]\n"
"v_cmp_gt_i32_e64 s[74:75], 34, v5\n"
"v_cndmask_b32_e64 v48, v48, v9, s[70:71]\n"
"v_cmp_gt_i32_e64 s[76:77], 35, v5\n"
"v_cndmask_b32_e64 v49, v49, v9, s[72:73]\n"
"v_cmp_gt_i32_e64 s[70:71], 32, v6\n"
"v_cndmask_b32_e64 v50, v50, v9, s[74:75]\n"
"v_cmp_gt_i32_e64 s[72:73], 33, v6\n"
"v_cndmask_b32_e64 v51, v51, v9, s[76:77]\n"
"v_cmp_gt_i32_e64 s[74:75], 34, v6\n"
"v_cndmask_b32_e64 v52, v52, v9, s[70:71]\n"
"v_cmp_gt_i32_e64 s[76:77], 35, v6\n"
"v_cndmask_b32_e64 v53, v53, v9, s[72:73]\n"
"v_cmp_gt_i32_e64 s[70:71], 48, v5\n"
"v_cndmask_b32_e64 v54, v54, v9, s[74:75]\n"
"v_cmp_gt_i32_e64 s[72:73], 49, v5\n"
"v_cndmask_b32_e64 v55, v55, v9, s[76:77]\n"
"v_cmp_gt_i32_e64 s[74:75], 50, v5\n"
"v_cndmask_b32_e64 v56, v56, v9, s[70:71]\n"
"v_cmp_gt_i32_e64 s[76:77], 51, v5\n"
"v_cndmask_b32_e64 v57, v57, v9, s[72:73]\n"
"v_cmp_gt_i32_e64 s[70:71], 48, v6\n"
"v_cndmask_b32_e64 v58, v58, v9, s[74:75]\n"
"v_cmp_gt_i32_e64 s[72:73], 49, v6\n"
"v_cndmask_b32_e64 v59, v59, v9, s[76:77]\n"
"v_cmp_gt_i32_e64 s[74:75], 50, v6\n"
"v_cndmask_b32_e64 v60, v60, v9, s[70:71]\n"
"v_cmp_gt_i32_e64 s[76:77], 51, v6\n"
"v_cndmask_b32_e64 v61, v61, v9, s[72:73]\n"
"s_nop 0\n"
"v_cndmask_b32_e64 v62, v62, v9, s[74:75]\n"
"s_nop 0\n"
"v_cndmask_b32_e64 v63, v63, v9, s[76:77]\n"
"L_fnm_f0_%=:\n"
"s_nop 7\n"
"v_max3_f32 v24, v32, v33, v34\n"
"v_max3_f32 v24, v24, v35, v40\n"
"v_max3_f32 v24, v24, v41, v42\n"
"v_max3_f32 v24, v24, v43, v48\n"
"v_max3_f32 v24, v24, v49, v50\n"
"v_max3_f32 v24, v24, v51, v56\n"
"v_max3_f32 v24, v24, v57, v58\n"
"v_max_f32 v24, v24, v59\n"
"v_max3_f32 v25, v36, v37, v38\n"
"v_max3_f32 v25, v25, v39, v44\n"
"v_max3_f32 v25, v25, v45, v46\n"
"v_max3_f32 v25, v25, v47, v52\n"
"v_max3_f32 v25, v25, v53, v54\n"
"v_max3_f32 v25, v25, v55, v60\n"
"v_max3_f32 v25, v25, v61, v62\n"
"v_max_f32 v25, v25, v63\n"
"v_mov_b32 v11, v24\n"
"s_nop 1\n"
"v_permlane16_swap_b32 v24, v11\n"
"s_nop 1\n"
"v_max_f32 v24, v24, v11\n"
"v_mov_b32 v11, v24\n"
"s_nop 1\n"
"v_permlane32_swap_b32 v24, v11\n"
"s_nop 1\n"
"v_max_f32 v24, v24, v11\n"
"v_max_f32 v24, s67, v24\n"
"v_mov_b32 v11, v25\n"
"s_nop 1\n"
"v_permlane16_swap_b32 v25, v11\n"
"s_nop 1\n"
"v_max_f32 v25, v25, v11\n"
"v_mov_b32 v11, v25\n"
"s_nop 1\n"
"v_permlane32_swap_b32 v25, v11\n"
"s_nop 1\n"
"v_max_f32 v25, v25, v11\n"
"v_max_f32 v25, s67, v25\n"
"v_add_f32 v16, v16, v24\n"
"v_sub_f32 v128, 0, v16\n"
"v_sub_f32 v129, 0, v16\n"
"v_sub_f32 v130, 0, v16\n"
"v_sub_f32 v131, 0, v16\n"
"v_add_f32 v17, v17, v25\n"
"v_sub_f32 v132, 0, v17\n"
"v_sub_f32 v133, 0, v17\n"
"v_sub_f32 v134, 0, v17\n"
"v_sub_f32 v135, 0, v17\n"
"v_sub_f32 v32, v32, v24\n"
"v_sub_f32 v33, v33, v24\n"
"v_sub_f32 v34, v34, v24\n"
"v_sub_f32 v35, v35, v24\n"
"v_sub_f32 v36, v36, v25\n"
"v_sub_f32 v37, v37, v25\n"
"v_sub_f32 v38, v38, v25\n"
"v_sub_f32 v39, v39, v25\n"
"v_sub_f32 v40, v40, v24\n"
"v_sub_f32 v41, v41, v24\n"
"v_sub_f32 v42, v42, v24\n"
"v_sub_f32 v43, v43, v24\n"
"v_sub_f32 v44, v44, v25\n"
"v_sub_f32 v45, v45, v25\n"
"v_sub_f32 v46, v46, v25\n"
"v_sub_f32 v47, v47, v25\n"
"v_sub_f32 v48, v48, v24\n"
"v_sub_f32 v49, v49, v24\n"
"v_sub_f32 v50, v50, v24\n"
"v_sub_f32 v51, v51, v24\n"
"v_sub_f32 v52, v52, v25\n"
"v_sub_f32 v53, v53, v25\n"
"v_sub_f32 v54, v54, v25\n"
"v_sub_f32 v55, v55, v25\n"
"v_sub_f32 v56, v56, v24\n"
"v_sub_f32 v57, v57, v24\n"
"v_sub_f32 v58, v58, v24\n"
"v_sub_f32 v59, v59, v24\n"
"v_sub_f32 v60, v60, v25\n"
"v_sub_f32 v61, v61, v25\n"
"v_sub_f32 v62, v62, v25\n"
"v_sub_f32 v63, v63, v25\n"
"v_exp_f32 v136, v32\n"
"v_exp_f32 v137, v33\n"
"v_exp_f32 v138, v34\n"
"v_exp_f32 v139, v35\n"
"v_exp_f32 v140, v36\n"
"v_exp_f32 v141, v37\n"
"v_exp_f32 v142, v38\n"
"v_exp_f32 v143, v39\n"
"v_exp_f32 v144, v40\n"
"v_exp_f32 v145, v41\n"
"v_exp_f32 v146, v42\n"
"v_exp_f32 v147, v43\n"
"v_exp_f32 v148, v44\n"
"v_exp_f32 v149, v45\n"
"v_exp_f32 v150, v46\n"
"v_exp_f32 v151, v47\n"
"v_exp_f32 v152, v48\n"
"v_exp_f32 v153, v49\n"
"v_exp_f32 v154, v50\n"
"v_exp_f32 v155, v51\n"
"v_exp_f32 v156, v52\n"
"v_exp_f32 v157, v53\n"
"v_exp_f32 v158, v54\n"
"v_exp_f32 v159, v55\n"
"v_exp_f32 v160, v56\n"
"v_exp_f32 v161, v57\n"
"v_exp_f32 v162, v58\n"
"v_exp_f32 v163, v59\n"
"v_exp_f32 v164, v60\n"
"s_nop 0\n"
"v_add_f32 v20, v136, v138\n"
"v_add_f32 v21, v137, v139\n"
"v_cvt_pk_bf16_f32 v96, v136, v137\n"
"v_cvt_pk_bf16_f32 v97, v138, v139\n"
"v_add_f32 v22, v140, v142\n"
"v_add_f32 v23, v141, v143\n"
"v_cvt_pk_bf16_f32 v100, v140, v141\n"
"v_cvt_pk_bf16_f32 v101, v142, v143\n"
"v_add_f32 v20, v20, v144\n"
"v_add_f32 v21, v21, v145\n"
"v_add_f32 v20, v20, v146\n"
"v_add_f32 v21, v21, v147\n"
"v_cvt_pk_bf16_f32 v98, v144, v145\n"
"v_cvt_pk_bf16_f32 v99, v146, v147\n"
"v_add_f32 v22, v22, v148\n"
"v_add_f32 v23, v23, v149\n"
"v_add_f32 v22, v22, v150\n"
"v_add_f32 v23, v23, v151\n"
"v_cvt_pk_bf16_f32 v102, v148, v149\n"
"v_cvt_pk_bf16_f32 v103, v150, v151\n"
"v_add_f32 v20, v20, v152\n"
"v_add_f32 v21, v21, v153\n"
"v_add_f32 v20, v20, v154\n"
"v_add_f32 v21, v21, v155\n"
"v_cvt_pk_bf16_f32 v104, v152, v153\n"
"v_cvt_pk_bf16_f32 v105, v154, v155\n"
"v_add_f32 v22, v22, v156\n"
"v_add_f32 v23, v23, v157\n"
"v_add_f32 v22, v22, v158\n"
"v_add_f32 v23, v23, v159\n"
"v_cvt_pk_bf16_f32 v108, v156, v157\n"
"v_cvt_pk_bf16_f32 v109, v158, v159\n"
"v_add_f32 v20, v20, v160\n"
"v_add_f32 v21, v21, v161\n"
"v_add_f32 v20, v20, v162\n"
"v_add_f32 v21, v21, v163\n"
"v_cvt_pk_bf16_f32 v106, v160, v161\n"
"v_cvt_pk_bf16_f32 v107, v162, v163\n"
"v_add_f32 v22, v22, v164\n"
"s_sub_u32 s55, s55, 1\n"
"s_cbranch_scc0 L_body10_%=\n"
"s_cmp_eq_u32 s56, 0\n"
"s_cbranch_scc1 L_last0_%=\n"
"s_sub_u32 s56, s56, 1\n"
"s_branch L_body11_%=\n"
"L_body00_%=:\n"
"s_waitcnt lgkmcnt(6)\n"
"v_mfma_f32_16x16x32_bf16 v[32:35], a[192:195], a[128:131], v[128:131]\n"
"ds_read_b128 a[248:251], v3 offset:14336\n"
"v_mfma_f32_16x16x32_bf16 v[36:39], a[192:195], a[160:163], v[132:135]\n"
"v_exp_f32 v165, v93\n"
"v_mfma_f32_16x16x32_bf16 v[32:35], a[196:199], a[132:135], v[32:35]\n"
"ds_read_b128 a[252:255], v3 offset:15360\n"
"v_add_f32 v23, v23, v165\n"
"v_mfma_f32_16x16x32_bf16 v[36:39], a[196:199], a[164:167], v[36:39]\n"
"v_cvt_pk_bf16_f32 v126, v164, v165\n"
"v_mfma_f32_16x16x32_bf16 v[32:35], a[200:203], a[136:139], v[32:35]\n"
"ds_read_b128 a[192:195], v3 offset:16384\n"
"v_mfma_f32_16x16x32_bf16 v[36:39], a[200:203], a[168:171], v[36:39]\n"
"v_exp_f32 v166, v94\n"
"v_mfma_f32_16x16x32_bf16 v[32:35], a[204:207], a[140:143], v[32:35]\n"
"ds_read_b128 a[196:199], v3 offset:17408\n"
"v_add_f32 v22, v22, v166\n"
"v_mfma_f32_16x16x32_bf16 v[36:39], a[204:207], a[172:175], v[36:39]\n"
"v_mfma_f32_16x16x32_bf16 v[32:35], a[208:211], a[144:147], v[32:35]\n"
"ds_read_b128 a[200:203], v3 offset:18432\n"
"v_mfma_f32_16x16x32_bf16 v[36:39], a[208:211], a[176:179], v[36:39]\n"
"v_exp_f32 v167, v95\n"
"v_mfma_f32_16x16x32_bf16 v[32:35], a[212:215], a[148:151], v[32:35]\n"
"ds_read_b128 a[204:207], v3 offset:19456\n"
"v_add_f32 v23, v23, v167\n"
"v_mfma_f32_16x16x32_bf16 v[36:39], a[212:215], a[180:183], v[36:39]\n"
"v_add_f32 v20, v20, v21\n"
"v_add_f32 v22, v22, v23\n"
"v_mfma_f32_16x16x32_bf16 v[32:35], a[216:219], a[152:155], v[32:35]\n"
"ds_read_b128 a[208:211], v3 offset:20480\n"
"v_cvt_pk_bf16_f32 v127, v166, v167\n"
"v_mfma_f32_16x16x32_bf16 v[36:39], a[216:219], a[184:187], v[36:39]\n"
"v_max_f32 v10, v20, v22\n"
"v_cmp_lt_f32 vcc, s66, v10\n"
"v_mfma_f32_16x16x32_bf16 v[32:35], a[220:223], a[156:159], v[32:35]\n"
"ds_read_b128 a[212:215], v3 offset:21504\n"
"v_mfma_f32_16x16x32_bf16 v[36:39], a[220:223], a[188:191], v[36:39]\n"
"s_waitcnt vmcnt(8) lgkmcnt(6)\n"
"s_barrier\n"
"v_mfma_f32_16x16x32_bf16 v[40:43], a[224:227], a[128:131], v[128:131]\n"
"ds_read_b128 a[216:219], v3 offset:22528\n"
"s_add_u32 m0, s65, 65536\n"
"v_mfma_f32_16x16x32_bf16 v[44:47], a[224:227], a[160:163], v[132:135]\n"
"buffer_load_dwordx4 v1, s[44:47], s60 offen offset:0 sc1 lds\n"
"v_mfma_f32_16x16x32_bf16 v[40:43], a[228:231], a[132:135], v[40:43]\n"
"ds_read_b128 a[220:223], v3 offset:23552\n"
"v_mfma_f32_16x16x32_bf16 v[44:47], a[228:231], a[164:167], v[44:47]\n"
"s_cbranch_vccnz L_tr_b00_%=\n"
"v_add_f32 v18, v18, v20\n"
"v_add_f32 v19, v19, v22\n"
"L_rt_b00_%=:\n"
"v_mfma_f32_16x16x32_bf16 v[40:43], a[232:235], a[136:139], v[40:43]\n"
"ds_read_b128 a[224:227], v3 offset:24576\n"
"v_mfma_f32_16x16x32_bf16 v[44:47], a[232:235], a[168:171], v[44:47]\n"
"v_exp_f32 v136, v32\n"
"v_mfma_f32_16x16x32_bf16 v[40:43], a[236:239], a[140:143], v[40:43]\n"
"ds_read_b128 a[228:231], v3 offset:25600\n"
"v_mfma_f32_16x16x32_bf16 v[44:47], a[236:239], a[172:175], v[44:47]\n"
"v_exp_f32 v137, v33\n"
"v_mfma_f32_16x16x32_bf16 v[40:43], a[240:243], a[144:147], v[40:43]\n"
"ds_read_b128 a[232:235], v3 offset:26624\n"
"v_cvt_pk_bf16_f32 v96, v136, v137\n"
"v_mfma_f32_16x16x32_bf16 v[44:47], a[240:243], a[176:179], v[44:47]\n"
"buffer_load_dwordx4 v1, s[44:47], s60 offen offset:1024 sc1 lds\n"
"v_mfma_f32_16x16x32_bf16 v[40:43], a[244:247], a[148:151], v[40:43]\n"
"ds_read_b128 a[236:239], v3 offset:27648\n"
"v_mfma_f32_16x16x32_bf16 v[44:47], a[244:247], a[180:183], v[44:47]\n"
"v_exp_f32 v138, v34\n"
"v_mfma_f32_16x16x32_bf16 v[40:43], a[248:251], a[152:155], v[40:43]\n"
"ds_read_b128 a[240:243], v3 offset:28672\n"
"v_add_f32 v20, v136, v138\n"
"v_mfma_f32_16x16x32_bf16 v[44:47], a[248:251], a[184:187], v[44:47]\n"
"v_exp_f32 v139, v35\n"
"v_mfma_f32_16x16x32_bf16 v[40:43], a[252:255], a[156:159], v[40:43]\n"
"ds_read_b128 a[244:247], v3 offset:29696\n"
"v_mfma_f32_16x16x32_bf16 v[44:47], a[252:255], a[188:191], v[44:47]\n"
"s_waitcnt lgkmcnt(6)\n"
"v_add_f32 v21, v137, v139\n"
"v_mfma_f32_16x16x32_bf16 v[48:51], a[192:195], a[128:131], v[128:131]\n"
"ds_read_b128 a[248:251], v3 offset:30720\n"
"v_cvt_pk_bf16_f32 v97, v138, v139\n"
"v_mfma_f32_16x16x32_bf16 v[52:55], a[192:195], a[160:163], v[132:135]\n"
"buffer_load_dwordx4 v1, s[44:47], s60 offen offset:2048 sc1 lds\n"
"v_mfma_f32_16x16x32_bf16 v[48:51], a[196:199], a[132:135], v[48:51]\n"
"ds_read_b128 a[252:255], v3 offset:31744\n"
"v_mfma_f32_16x16x32_bf16 v[52:55], a[196:199], a[164:167], v[52:55]\n"
"v_exp_f32 v140, v36\n"
"v_mfma_f32_16x16x32_bf16 v[48:51], a[200:203], a[136:139], v[48:51]\n"
"ds_read_b128 a[192:195], v4 offset:32768\n"
"v_mfma_f32_16x16x32_bf16 v[52:55], a[200:203], a[168:171], v[52:55]\n"
"v_exp_f32 v141, v37\n"
"v_mfma_f32_16x16x32_bf16 v[48:51], a[204:207], a[140:143], v[48:51]\n"
"ds_read_b128 a[196:199], v4 offset:33792\n"
"v_mfma_f32_16x16x32_bf16 v[52:55], a[204:207], a[172:175], v[52:55]\n"
"v_mfma_f32_16x16x32_bf16 v[48:51], a[208:211], a[144:147], v[48:51]\n"
"ds_read_b128 a[200:203], v4 offset:34816\n"
"v_cvt_pk_bf16_f32 v100, v140, v141\n"
"v_mfma_f32_16x16x32_bf16 v[52:55], a[208:211], a[176:179], v[52:55]\n"
"buffer_load_dwordx4 v1, s[44:47], s60 offen offset:3072 sc1 lds\n"
"v_mfma_f32_16x16x32_bf16 v[48:51], a[212:215], a[148:151], v[48:51]\n"
"ds_read_b128 a[204:207], v4 offset:35840\n"
"v_mfma_f32_16x16x32_bf16 v[52:55], a[212:215], a[180:183], v[52:55]\n"
"v_exp_f32 v142, v38\n"
"v_mfma_f32_16x16x32_bf16 v[48:51], a[216:219], a[152:155], v[48:51]\n"
"ds_read_b128 a[208:211], v4 offset:36864\n"
"v_add_f32 v22, v140, v142\n"
"v_mfma_f32_16x16x32_bf16 v[52:55], a[216:219], a[184:187], v[52:55]\n"
"v_exp_f32 v143, v39\n"
"v_mfma_f32_16x16x32_bf16 v[48:51], a[220:223], a[156:159], v[48:51]\n"
"ds_read_b128 a[212:215], v4 offset:37888\n"
"v_add_f32 v23, v141, v143\n"
"v_mfma_f32_16x16x32_bf16 v[52:55], a[220:223], a[188:191], v[52:55]\n"
"s_waitcnt lgkmcnt(6)\n"
"v_cvt_pk_bf16_f32 v101, v142, v143\n"
"v_mfma_f32_16x16x32_bf16 v[56:59], a[224:227], a[128:131], v[128:131]\n"
"ds_read_b128 a[216:219], v4 offset:38912\n"
"s_add_u32 m0, s65, 69632\n"
"v_mfma_f32_16x16x32_bf16 v[60:63], a[224:227], a[160:163], v[132:135]\n"
"buffer_load_dwordx4 v2, s[44:47], s60 offen offset:0 sc1 lds\n"
"v_mfma_f32_16x16x32_bf16 v[56:59], a[228:231], a[132:135], v[56:59]\n"
"ds_read_b128 a[220:223], v4 offset:39936\n"
"v_mfma_f32_16x16x32_bf16 v[60:63], a[228:231], a[164:167], v[60:63]\n"
"v_exp_f32 v144, v40\n"
"v_mfma_f32_16x16x32_bf16 v[56:59], a[232:235], a[136:139], v[56:59]\n"
"ds_read_b128 a[224:227], v4 offset:40960\n"
"v_add_f32 v20, v20, v144\n"
"v_mfma_f32_16x16x32_bf16 v[60:63], a[232:235], a[168:171], v[60:63]\n"
"v_exp_f32 v145, v41\n"
"v_mfma_f32_16x16x32_bf16 v[56:59], a[236:239], a[140:143], v[56:59]\n"
"ds_read_b128 a[228:231], v4 offset:41984\n"
"v_add_f32 v21, v21, v145\n"
"v_mfma_f32_16x16x32_bf16 v[60:63], a[236:239], a[172:175], v[60:63]\n"
"v_cvt_pk_bf16_f32 v98, v144, v145\n"
"v_mfma_f32_16x16x32_bf16 v[56:59], a[240:243], a[144:147], v[56:59]\n"
"ds_read_b128 a[232:235], v4 offset:43008\n"
"v_mfma_f32_16x16x32_bf16 v[60:63], a[240:243], a[176:179], v[60:63]\n"
"buffer_load_dwordx4 v2, s[44:47], s60 offen offset:1024 sc1 lds\n"
"v_mfma_f32_16x16x32_bf16 v[56:59], a[244:247], a[148:151], v[56:59]\n"
"ds_read_b128 a[236:239], v4 offset:44032\n"
"v_mfma_f32_16x16x32_bf16 v[60:63], a[244:247], a[180:183], v[60:63]\n"
"v_exp_f32 v146, v42\n"
"v_mfma_f32_16x16x32_bf16 v[56:59], a[248:251], a[152:155], v[56:59]\n"
"ds_read_b128 a[240:243], v4 offset:45056\n"
"v_add_f32 v20, v20, v146\n"
"v_mfma_f32_16x16x32_bf16 v[60:63], a[248:251], a[184:187], v[60:63]\n"
"v_exp_f32 v147, v43\n"
"v_mfma_f32_16x16x32_bf16 v[56:59], a[252:255], a[156:159], v[56:59]\n"
"ds_read_b128 a[244:247], v4 offset:46080\n"
"v_add_f32 v21, v21, v147\n"
"v_mfma_f32_16x16x32_bf16 v[60:63], a[252:255], a[188:191], v[60:63]\n"
"s_waitcnt lgkmcnt(6)\n"
"v_cvt_pk_bf16_f32 v99, v146, v147\n"
"v_mfma_f32_16x16x32_bf16 a[0:3], a[192:195], v[112:115], a[0:3]\n"
"ds_read_b128 a[248:251], v4 offset:47104\n"
"v_mfma_f32_16x16x32_bf16 a[4:7], a[192:195], v[116:119], a[4:7]\n"
"buffer_load_dwordx4 v2, s[44:47], s60 offen offset:2048 sc1 lds\n"
"v_mfma_f32_16x16x32_bf16 a[8:11], a[196:199], v[112:115], a[8:11]\n"
"ds_read_b128 a[252:255], v4 offset:48128\n"
"v_mfma_f32_16x16x32_bf16 a[12:15], a[196:199], v[116:119], a[12:15]\n"
"v_exp_f32 v148, v44\n"
"v_mfma_f32_16x16x32_bf16 a[16:19], a[200:203], v[112:115], a[16:19]\n"
"ds_read_b128 a[192:195], v4 offset:49152\n"
"v_add_f32 v22, v22, v148\n"
"v_mfma_f32_16x16x32_bf16 a[20:23], a[200:203], v[116:119], a[20:23]\n"
"v_exp_f32 v149, v45\n"
"v_mfma_f32_16x16x32_bf16 a[24:27], a[204:207], v[112:115], a[24:27]\n"
"ds_read_b128 a[196:199], v4 offset:50176\n"
"v_add_f32 v23, v23, v149\n"
"v_mfma_f32_16x16x32_bf16 a[28:31], a[204:207], v[116:119], a[28:31]\n"
"v_cvt_pk_bf16_f32 v102, v148, v149\n"
"v_mfma_f32_16x16x32_bf16 a[32:35], a[208:211], v[112:115], a[32:35]\n"
"ds_read_b128 a[200:203], v4 offset:51200\n"
"v_mfma_f32_16x16x32_bf16 a[36:39], a[208:211], v[116:119], a[36:39]\n"
"buffer_load_dwordx4 v2, s[44:47], s60 offen offset:3072 sc1 lds\n"
"v_mfma_f32_16x16x32_bf16 a[40:43], a[212:215], v[112:115], a[40:43]\n"
"ds_read_b128 a[204:207], v4 offset:52224\n"
"v_mfma_f32_16x16x32_bf16 a[44:47], a[212:215], v[116:119], a[44:47]\n"
"v_exp_f32 v150, v46\n"
"v_mfma_f32_16x16x32_bf16 a[48:51], a[216:219], v[112:115], a[48:51]\n"
"ds_read_b128 a[208:211], v4 offset:53248\n"
"v_add_f32 v22, v22, v150\n"
"v_mfma_f32_16x16x32_bf16 a[52:55], a[216:219], v[116:119], a[52:55]\n"
"s_add_u32 s60, s61, 0x8000\n"
"s_sub_u32 s62, s62, 1\n"
"s_cbranch_scc1 L_ev_b00_%=\n"
"L_evr_b00_%=:\n"
"v_mfma_f32_16x16x32_bf16 a[56:59], a[220:223], v[112:115], a[56:59]\n"
"ds_read_b128 a[212:215], v4 offset:54272\n"
"v_mfma_f32_16x16x32_bf16 a[60:63], a[220:223], v[116:119], a[60:63]\n"
"s_waitcnt vmcnt(8) lgkmcnt(6)\n"
"s_barrier\n"
"v_mfma_f32_16x16x32_bf16 a[64:67], a[224:227], v[112:115], a[64:67]\n"
"ds_read_b128 a[216:219], v4 offset:55296\n"
"s_add_u32 m0, s65, 0\n"
"v_mfma_f32_16x16x32_bf16 a[68:71], a[224:227], v[116:119], a[68:71]\n"
"buffer_load_dwordx4 v1, s[40:43], s60 offen offset:0 sc1 lds\n"
"v_mfma_f32_16x16x32_bf16 a[72:75], a[228:231], v[112:115], a[72:75]\n"
"ds_read_b128 a[220:223], v4 offset:56320\n"
"v_mfma_f32_16x16x32_bf16 a[76:79], a[228:231], v[116:119], a[76:79]\n"
"v_exp_f32 v151, v47\n"
"v_mfma_f32_16x16x32_bf16 a[80:83], a[232:235], v[112:115], a[80:83]\n"
"ds_read_b128 a[224:227], v4 offset:57344\n"
"v_add_f32 v23, v23, v151\n"
"v_mfma_f32_16x16x32_bf16 a[84:87], a[232:235], v[116:119], a[84:87]\n"
"v_cvt_pk_bf16_f32 v103, v150, v151\n"
"v_mfma_f32_16x16x32_bf16 a[88:91], a[236:239], v[112:115], a[88:91]\n"
"ds_read_b128 a[228:231], v4 offset:58368\n"
"v_mfma_f32_16x16x32_bf16 a[92:95], a[236:239], v[116:119], a[92:95]\n"
"buffer_load_dwordx4 v1, s[40:43], s60 offen offset:1024 sc1 lds\n"
"v_exp_f32 v152, v48\n"
"v_mfma_f32_16x16x32_bf16 a[96:99], a[240:243], v[112:115], a[96:99]\n"
"ds_read_b128 a[232:235], v4 offset:59392\n"
"v_add_f32 v20, v20, v152\n"
"v_mfma_f32_16x16x32_bf16 a[100:103], a[240:243], v[116:119], a[100:103]\n"
"v_exp_f32 v153, v49\n"
"v_mfma_f32_16x16x32_bf16 a[104:107], a[244:247], v[112:115], a[104:107]\n"
"ds_read_b128 a[236:239], v4 offset:60416\n"
"v_add_f32 v21, v21, v153\n"
"v_mfma_f32_16x16x32_bf16 a[108:111], a[244:247], v[116:119], a[108:111]\n"
"v_cvt_pk_bf16_f32 v104, v152, v153\n"
"v_mfma_f32_16x16x32_bf16 a[112:115], a[248:251], v[112:115], a[112:115]\n"
"ds_read_b128 a[240:243], v4 offset:61440\n"
"v_mfma_f32_16x16x32_bf16 a[116:119], a[248:251], v[116:119], a[116:119]\n"
"buffer_load_dwordx4 v1, s[40:43], s60 offen offset:2048 sc1 lds\n"
"v_mfma_f32_16x16x32_bf16 a[120:123], a[252:255], v[112:115], a[120:123]\n"
"ds_read_b128 a[244:247], v4 offset:62464\n"
"v_exp_f32 v154, v50\n"
"v_mfma_f32_16x16x32_bf16 a[124:127], a[252:255], v[116:119], a[124:127]\n"
"s_waitcnt lgkmcnt(6)\n"
"v_add_f32 v20, v20, v154\n"
"v_mfma_f32_16x16x32_bf16 a[0:3], a[192:195], v[120:123], a[0:3]\n"
"ds_read_b128 a[248:251], v4 offset:63488\n"
"v_mfma_f32_16x16x32_bf16 a[4:7], a[192:195], v[124:127], a[4:7]\n"
"v_exp_f32 v155, v51\n"
"v_mfma_f32_16x16x32_bf16 a[8:11], a[196:199], v[120:123], a[8:11]\n"
"ds_read_b128 a[252:255], v4 offset:64512\n"
"v_add_f32 v21, v21, v155\n"
"v_mfma_f32_16x16x32_bf16 a[12:15], a[196:199], v[124:127], a[12:15]\n"
"buffer_load_dwordx4 v1, s[40:43], s60 offen offset:3072 sc1 lds\n"
"v_cvt_pk_bf16_f32 v105, v154, v155\n"
"v_mfma_f32_16x16x32_bf16 a[16:19], a[200:203], v[120:123], a[16:19]\n"
"ds_read_b128 a[192:195], v3 offset:32768\n"
"v_mfma_f32_16x16x32_bf16 a[20:23], a[200:203], v[124:127], a[20:23]\n"
"v_exp_f32 v156, v52\n"
"v_mfma_f32_16x16x32_bf16 a[24:27], a[204:207], v[120:123], a[24:27]\n"
"ds_read_b128 a[196:199], v3 offset:33792\n"
"v_add_f32 v22, v22, v156\n"
"v_mfma_f32_16x16x32_bf16 a[28:31], a[204:207], v[124:127], a[28:31]\n"
"v_exp_f32 v157, v53\n"
"v_mfma_f32_16x16x32_bf16 a[32:35], a[208:211], v[120:123], a[32:35]\n"
"ds_read_b128 a[200:203], v3 offset:34816\n"
"s_add_u32 m0, s65, 4096\n"
"v_mfma_f32_16x16x32_bf16 a[36:39], a[208:211], v[124:127], a[36:39]\n"
"buffer_load_dwordx4 v2, s[40:43], s60 offen offset:0 sc1 lds\n"
"v_add_f32 v23, v23, v157\n"
"v_mfma_f32_16x16x32_bf16 a[40:43], a[212:215], v[120:123], a[40:43]\n"
"ds_read_b128 a[204:207], v3 offset:35840\n"
"v_cvt_pk_bf16_f32 v108, v156, v157\n"
"v_mfma_f32_16x16x32_bf16 a[44:47], a[212:215], v[124:127], a[44:47]\n"
"v_exp_f32 v158, v54\n"
"v_mfma_f32_16x16x32_bf16 a[48:51], a[216:219], v[120:123], a[48:51]\n"
"ds_read_b128 a[208:211], v3 offset:36864\n"
"v_add_f32 v22, v22, v158\n"
"v_mfma_f32_16x16x32_bf16 a[52:55], a[216:219], v[124:127], a[52:55]\n"
"v_exp_f32 v159, v55\n"
"v_mfma_f32_16x16x32_bf16 a[56:59], a[220:223], v[120:123], a[56:59]\n"
"ds_read_b128 a[212:215], v3 offset:37888\n"
"v_add_f32 v23, v23, v159\n"
"v_mfma_f32_16x16x32_bf16 a[60:63], a[220:223], v[124:127], a[60:63]\n"
"s_waitcnt lgkmcnt(6)\n"
"buffer_load_dwordx4 v2, s[40:43], s60 offen offset:1024 sc1 lds\n"
"v_mfma_f32_16x16x32_bf16 a[64:67], a[224:227], v[120:123], a[64:67]\n"
"ds_read_b128 a[216:219], v3 offset:38912\n"
"v_cvt_pk_bf16_f32 v109, v158, v159\n"
"v_mfma_f32_16x16x32_bf16 a[68:71], a[224:227], v[124:127], a[68:71]\n"
"v_exp_f32 v160, v56\n"
"v_mfma_f32_16x16x32_bf16 a[72:75], a[228:231], v[120:123], a[72:75]\n"
"ds_read_b128 a[220:223], v3 offset:39936\n"
"v_add_f32 v20, v20, v160\n"
"v_mfma_f32_16x16x32_bf16 a[76:79], a[228:231], v[124:127], a[76:79]\n"
"v_exp_f32 v161, v57\n"
"v_mfma_f32_16x16x32_bf16 a[80:83], a[232:235], v[120:123], a[80:83]\n"
"ds_read_b128 a[224:227], v3 offset:40960\n"
"v_add_f32 v21, v21, v161\n"
"v_mfma_f32_16x16x32_bf16 a[84:87], a[232:235], v[124:127], a[84:87]\n"
"buffer_load_dwordx4 v2, s[40:43], s60 offen offset:2048 sc1 lds\n"
"v_cvt_pk_bf16_f32 v106, v160, v161\n"
"v_mfma_f32_16x16x32_bf16 a[88:91], a[236:239], v[120:123], a[88:91]\n"
"ds_read_b128 a[228:231], v3 offset:41984\n"
"v_mfma_f32_16x16x32_bf16 a[92:95], a[236:239], v[124:127], a[92:95]\n"
"v_exp_f32 v162, v58\n"
"v_mfma_f32_16x16x32_bf16 a[96:99], a[240:243], v[120:123], a[96:99]\n"
"ds_read_b128 a[232:235], v3 offset:43008\n"
"v_add_f32 v20, v20, v162\n"
"v_mfma_f32_16x16x32_bf16 a[100:103], a[240:243], v[124:127], a[100:103]\n"
"v_exp_f32 v163, v59\n"
"v_mfma_f32_16x16x32_bf16 a[104:107], a[244:247], v[120:123], a[104:107]\n"
"ds_read_b128 a[236:239], v3 offset:44032\n"
"v_add_f32 v21, v21, v163\n"
"v_mfma_f32_16x16x32_bf16 a[108:111], a[244:247], v[124:127], a[108:111]\n"
"buffer_load_dwordx4 v2, s[40:43], s60 offen offset:3072 sc1 lds\n"
"v_cvt_pk_bf16_f32 v107, v162, v163\n"
"v_mfma_f32_16x16x32_bf16 a[112:115], a[248:251], v[120:123], a[112:115]\n"
"ds_read_b128 a[240:243], v3 offset:45056\n"
"v_mfma_f32_16x16x32_bf16 a[116:119], a[248:251], v[124:127], a[116:119]\n"
"v_exp_f32 v164, v60\n"
"v_mfma_f32_16x16x32_bf16 a[120:123], a[252:255], v[120:123], a[120:123]\n"
"ds_read_b128 a[244:247], v3 offset:46080\n"
"v_add_f32 v22, v22, v164\n"
"v_mfma_f32_16x16x32_bf16 a[124:127], a[252:255], v[124:127], a[124:127]\n"
"s_sub_u32 s55, s55, 1\n"
"s_cbranch_scc0 L_body10_%=\n"
"s_cmp_eq_u32 s56, 0\n"
"s_cbranch_scc1 L_last0_%=\n"
"s_sub_u32 s56, s56, 1\n"
"s_branch L_body11_%=\n"
"L_body01_%=:\n"
"s_waitcnt lgkmcnt(6)\n"
"v_mfma_f32_16x16x32_bf16 v[32:35], a[192:195], a[128:131], v[128:131]\n"
"ds_read_b128 a[248:251], v3 offset:14336\n"
"v_mfma_f32_16x16x32_bf16 v[36:39], a[192:195], a[160:163], v[132:135]\n"
"v_exp_f32 v165, v93\n"
"v_mfma_f32_16x16x32_bf16 v[32:35], a[196:199], a[132:135], v[32:35]\n"
"ds_read_b128 a[252:255], v3 offset:15360\n"
"v_add_f32 v23, v23, v165\n"
"v_mfma_f32_16x16x32_bf16 v[36:39], a[196:199], a[164:167], v[36:39]\n"
"v_cvt_pk_bf16_f32 v126, v164, v165\n"
"v_mfma_f32_16x16x32_bf16 v[32:35], a[200:203], a[136:139], v[32:35]\n"
"ds_read_b128 a[192:195], v3 offset:16384\n"
"v_mfma_f32_16x16x32_bf16 v[36:39], a[200:203], a[168:171], v[36:39]\n"
"v_exp_f32 v166, v94\n"
"v_mfma_f32_16x16x32_bf16 v[32:35], a[204:207], a[140:143], v[32:35]\n"
"ds_read_b128 a[196:199], v3 offset:17408\n"
"v_add_f32 v22, v22, v166\n"
"v_mfma_f32_16x16x32_bf16 v[36:39], a[204:207], a[172:175], v[36:39]\n"
"v_mfma_f32_16x16x32_bf16 v[32:35], a[208:211], a[144:147], v[32:35]\n"
"ds_read_b128 a[200:203], v3 offset:18432\n"
"v_mfma_f32_16x16x32_bf16 v[36:39], a[208:211], a[176:179], v[36:39]\n"
"v_exp_f32 v167, v95\n"
"v_mfma_f32_16x16x32_bf16 v[32:35], a[212:215], a[148:151], v[32:35]\n"
"ds_read_b128 a[204:207], v3 offset:19456\n"
"v_add_f32 v23, v23, v167\n"
"v_mfma_f32_16x16x32_bf16 v[36:39], a[212:215], a[180:183], v[36:39]\n"
"v_add_f32 v20, v20, v21\n"
"v_add_f32 v22, v22, v23\n"
"v_mfma_f32_16x16x32_bf16 v[32:35], a[216:219], a[152:155], v[32:35]\n"
"ds_read_b128 a[208:211], v3 offset:20480\n"
"v_cvt_pk_bf16_f32 v127, v166, v167\n"
"v_mfma_f32_16x16x32_bf16 v[36:39], a[216:219], a[184:187], v[36:39]\n"
"v_max_f32 v10, v20, v22\n"
"v_cmp_lt_f32 vcc, s66, v10\n"
"v_mfma_f32_16x16x32_bf16 v[32:35], a[220:223], a[156:159], v[32:35]\n"
"ds_read_b128 a[212:215], v3 offset:21504\n"
"v_mfma_f32_16x16x32_bf16 v[36:39], a[220:223], a[188:191], v[36:39]\n"
"s_waitcnt vmcnt(8) lgkmcnt(6)\n"
"s_barrier\n"
"v_mfma_f32_16x16x32_bf16 v[40:43], a[224:227], a[128:131], v[128:131]\n"
"ds_read_b128 a[216:219], v3 offset:22528\n"
"s_add_u32 m0, s65, 65536\n"
"v_mfma_f32_16x16x32_bf16 v[44:47], a[224:227], a[160:163], v[132:135]\n"
"buffer_load_dwordx4 v1, s[44:47], s60 offen offset:0 sc1 lds\n"
"s_lshl_b32 s96, s54, 6\n"
"v_subrev_u32 v5, s96, v7\n"
"v_subrev_u32 v6, s96, v8\n"
"v_mfma_f32_16x16x32_bf16 v[40:43], a[228:231], a[132:135], v[40:43]\n"
"ds_read_b128 a[220:223], v3 offset:23552\n"
"s_add_u32 s54, s54, 1\n"
"v_mfma_f32_16x16x32_bf16 v[44:47], a[228:231], a[164:167], v[44:47]\n"
"s_cbranch_vccnz L_tr_b01_%=\n"
"v_add_f32 v18, v18, v20\n"
"v_add_f32 v19, v19, v22\n"
"L_rt_b01_%=:\n"
"v_mfma_f32_16x16x32_bf16 v[40:43], a[232:235], a[136:139], v[40:43]\n"
"ds_read_b128 a[224:227], v3 offset:24576\n"
"v_cmp_gt_i32_e64 s[70:71], 0, v5\n"
"v_mfma_f32_16x16x32_bf16 v[44:47], a[232:235], a[168:171], v[44:47]\n"
"v_cmp_gt_i32_e64 s[72:73], 1, v5\n"
"v_cmp_gt_i32_e64 s[74:75], 2, v5\n"
"v_mfma_f32_16x16x32_bf16 v[40:43], a[236:239], a[140:143], v[40:43]\n"
"ds_read_b128 a[228:231], v3 offset:25600\n"
"v_cndmask_b32_e64 v32, v32, v9, s[70:71]\n"
"v_mfma_f32_16x16x32_bf16 v[44:47], a[236:239], a[172:175], v[44:47]\n"
"v_cndmask_b32_e64 v33, v33, v9, s[72:73]\n"
"v_cndmask_b32_e64 v34, v34, v9, s[74:75]\n"
"v_mfma_f32_16x16x32_bf16 v[40:43], a[240:243], a[144:147], v[40:43]\n"
"ds_read_b128 a[232:235], v3 offset:26624\n"
"v_cmp_gt_i32_e64 s[76:77], 3, v5\n"
"v_mfma_f32_16x16x32_bf16 v[44:47], a[240:243], a[176:179], v[44:47]\n"
"buffer_load_dwordx4 v1, s[44:47], s60 offen offset:1024 sc1 lds\n"
"v_cmp_gt_i32_e64 s[70:71], 0, v6\n"
"v_mfma_f32_16x16x32_bf16 v[40:43], a[244:247], a[148:151], v[40:43]\n"
"ds_read_b128 a[236:239], v3 offset:27648\n"
"v_cndmask_b32_e64 v35, v35, v9, s[76:77]\n"
"v_mfma_f32_16x16x32_bf16 v[44:47], a[244:247], a[180:183], v[44:47]\n"
"v_cndmask_b32_e64 v36, v36, v9, s[70:71]\n"
"v_cmp_gt_i32_e64 s[72:73], 1, v6\n"
"v_mfma_f32_16x16x32_bf16 v[40:43], a[248:251], a[152:155], v[40:43]\n"
"ds_read_b128 a[240:243], v3 offset:28672\n"
"v_cmp_gt_i32_e64 s[74:75], 2, v6\n"
"v_mfma_f32_16x16x32_bf16 v[44:47], a[248:251], a[184:187], v[44:47]\n"
"v_cndmask_b32_e64 v37, v37, v9, s[72:73]\n"
"v_mfma_f32_16x16x32_bf16 v[40:43], a[252:255], a[156:159], v[40:43]\n"
"ds_read_b128 a[244:247], v3 offset:29696\n"
"v_cndmask_b32_e64 v38, v38, v9, s[74:75]\n"
"v_mfma_f32_16x16x32_bf16 v[44:47], a[252:255], a[188:191], v[44:47]\n"
"s_waitcnt lgkmcnt(6)\n"
"v_cmp_gt_i32_e64 s[76:77], 3, v6\n"
"v_mfma_f32_16x16x32_bf16 v[48:51], a[192:195], a[128:131], v[128:131]\n"
"ds_read_b128 a[248:251], v3 offset:30720\n"
"v_mfma_f32_16x16x32_bf16 v[52:55], a[192:195], a[160:163], v[132:135]\n"
"buffer_load_dwordx4 v1, s[44:47], s60 offen offset:2048 sc1 lds\n"
"v_cndmask_b32_e64 v39, v39, v9, s[76:77]\n"
"v_mfma_f32_16x16x32_bf16 v[48:51], a[196:199], a[132:135], v[48:51]\n"
"ds_read_b128 a[252:255], v3 offset:31744\n"
"v_mfma_f32_16x16x32_bf16 v[52:55], a[196:199], a[164:167], v[52:55]\n"
"v_mfma_f32_16x16x32_bf16 v[48:51], a[200:203], a[136:139], v[48:51]\n"
"ds_read_b128 a[192:195], v4 offset:32768\n"
"v_cmp_gt_i32_e64 s[70:71], 16, v5\n"
"v_mfma_f32_16x16x32_bf16 v[52:55], a[200:203], a[168:171], v[52:55]\n"
"v_cmp_gt_i32_e64 s[72:73], 17, v5\n"
"v_cmp_gt_i32_e64 s[74:75], 18, v5\n"
"v_mfma_f32_16x16x32_bf16 v[48:51], a[204:207], a[140:143], v[48:51]\n"
"ds_read_b128 a[196:199], v4 offset:33792\n"
"v_cndmask_b32_e64 v40, v40, v9, s[70:71]\n"
"v_mfma_f32_16x16x32_bf16 v[52:55], a[204:207], a[172:175], v[52:55]\n"
"v_exp_f32 v136, v32\n"
"v_mfma_f32_16x16x32_bf16 v[48:51], a[208:211], a[144:147], v[48:51]\n"
"ds_read_b128 a[200:203], v4 offset:34816\n"
"v_cndmask_b32_e64 v41, v41, v9, s[72:73]\n"
"v_mfma_f32_16x16x32_bf16 v[52:55], a[208:211], a[176:179], v[52:55]\n"
"buffer_load_dwordx4 v1, s[44:47], s60 offen offset:3072 sc1 lds\n"
"v_cndmask_b32_e64 v42, v42, v9, s[74:75]\n"
"v_mfma_f32_16x16x32_bf16 v[48:51], a[212:215], a[148:151], v[48:51]\n"
"ds_read_b128 a[204:207], v4 offset:35840\n"
"v_cmp_gt_i32_e64 s[76:77], 19, v5\n"
"v_mfma_f32_16x16x32_bf16 v[52:55], a[212:215], a[180:183], v[52:55]\n"
"v_exp_f32 v137, v33\n"
"v_mfma_f32_16x16x32_bf16 v[48:51], a[216:219], a[152:155], v[48:51]\n"
"ds_read_b128 a[208:211], v4 offset:36864\n"
"v_exp_f32 v138, v34\n"
"v_exp_f32 v139, v35\n"
"v_exp_f32 v140, v36\n"
"v_mfma_f32_16x16x32_bf16 v[52:55], a[216:219], a[184:187], v[52:55]\n"
"v_exp_f32 v141, v37\n"
"v_cndmask_b32_e64 v43, v43, v9, s[76:77]\n"
"v_cmp_gt_i32_e64 s[70:71], 16, v6\n"
"v_mfma_f32_16x16x32_bf16 v[48:51], a[220:223], a[156:159], v[48:51]\n"
"ds_read_b128 a[212:215], v4 offset:37888\n"
"v_add_f32 v20, v136, v138\n"
"v_add_f32 v21, v137, v139\n"
"v_mfma_f32_16x16x32_bf16 v[52:55], a[220:223], a[188:191], v[52:55]\n"
"s_waitcnt lgkmcnt(6)\n"
"v_cvt_pk_bf16_f32 v96, v136, v137\n"
"v_cvt_pk_bf16_f32 v97, v138, v139\n"
"v_cndmask_b32_e64 v44, v44, v9, s[70:71]\n"
"v_cmp_gt_i32_e64 s[72:73], 17, v6\n"
"v_mfma_f32_16x16x32_bf16 v[56:59], a[224:227], a[128:131], v[128:131]\n"
"ds_read_b128 a[216:219], v4 offset:38912\n"
"s_add_u32 m0, s65, 69632\n"
"v_exp_f32 v142, v38\n"
"v_cvt_pk_bf16_f32 v100, v140, v141\n"
"v_mfma_f32_16x16x32_bf16 v[60:63], a[224:227], a[160:163], v[132:135]\n"
"buffer_load_dwordx4 v2, s[44:47], s60 offen offset:0 sc1 lds\n"
"v_add_f32 v22, v140, v142\n"
"v_cndmask_b32_e64 v45, v45, v9, s[72:73]\n"
"v_cmp_gt_i32_e64 s[74:75], 18, v6\n"
"v_mfma_f32_16x16x32_bf16 v[56:59], a[228:231], a[132:135], v[56:59]\n"
"ds_read_b128 a[220:223], v4 offset:39936\n"
"v_cmp_gt_i32_e64 s[76:77], 19, v6\n"
"v_mfma_f32_16x16x32_bf16 v[60:63], a[228:231], a[164:167], v[60:63]\n"
"v_exp_f32 v143, v39\n"
"v_cndmask_b32_e64 v46, v46, v9, s[74:75]\n"
"v_mfma_f32_16x16x32_bf16 v[56:59], a[232:235], a[136:139], v[56:59]\n"
"ds_read_b128 a[224:227], v4 offset:40960\n"
"v_add_f32 v23, v141, v143\n"
"v_mfma_f32_16x16x32_bf16 v[60:63], a[232:235], a[168:171], v[60:63]\n"
"v_cvt_pk_bf16_f32 v101, v142, v143\n"
"v_cndmask_b32_e64 v47, v47, v9, s[76:77]\n"
"v_cmp_gt_i32_e64 s[70:71], 32, v5\n"
"v_mfma_f32_16x16x32_bf16 v[56:59], a[236:239], a[140:143], v[56:59]\n"
"ds_read_b128 a[228:231], v4 offset:41984\n"
"v_cmp_gt_i32_e64 s[72:73], 33, v5\n"
"v_mfma_f32_16x16x32_bf16 v[60:63], a[236:239], a[172:175], v[60:63]\n"
"v_cndmask_b32_e64 v48, v48, v9, s[70:71]\n"
"v_cmp_gt_i32_e64 s[74:75], 34, v5\n"
"v_mfma_f32_16x16x32_bf16 v[56:59], a[240:243], a[144:147], v[56:59]\n"
"ds_read_b128 a[232:235], v4 offset:43008\n"
"v_cndmask_b32_e64 v49, v49, v9, s[72:73]\n"
"v_mfma_f32_16x16x32_bf16 v[60:63], a[240:243], a[176:179], v[60:63]\n"
"buffer_load_dwordx4 v2, s[44:47], s60 offen offset:1024 sc1 lds\n"
"v_cndmask_b32_e64 v50, v50, v9, s[74:75]\n"
"v_mfma_f32_16x16x32_bf16 v[56:59], a[244:247], a[148:151], v[56:59]\n"
"ds_read_b128 a[236:239], v4 offset:44032\n"
"v_cmp_gt_i32_e64 s[76:77], 35, v5\n"
"v_mfma_f32_16x16x32_bf16 v[60:63], a[244:247], a[180:183], v[60:63]\n"
"v_exp_f32 v144, v40\n"
"v_mfma_f32_16x16x32_bf16 v[56:59], a[248:251], a[152:155], v[56:59]\n"
"ds_read_b128 a[240:243], v4 offset:45056\n"
"v_exp_f32 v145, v41\n"
"v_mfma_f32_16x16x32_bf16 v[60:63], a[248:251], a[184:187], v[60:63]\n"
"v_add_f32 v20, v20, v144\n"
"v_add_f32 v21, v21, v145\n"
"v_cndmask_b32_e64 v51, v51, v9, s[76:77]\n"
"v_cmp_gt_i32_e64 s[70:71], 32, v6\n"
"v_mfma_f32_16x16x32_bf16 v[56:59], a[252:255], a[156:159], v[56:59]\n"
"ds_read_b128 a[244:247], v4 offset:46080\n"
"v_cvt_pk_bf16_f32 v98, v144, v145\n"
"v_mfma_f32_16x16x32_bf16 v[60:63], a[252:255], a[188:191], v[60:63]\n"
"s_waitcnt lgkmcnt(6)\n"
"v_cndmask_b32_e64 v52, v52, v9, s[70:71]\n"
"v_cmp_gt_i32_e64 s[72:73], 33, v6\n"
"v_mfma_f32_16x16x32_bf16 a[0:3], a[192:195], v[112:115], a[0:3]\n"
"ds_read_b128 a[248:251], v4 offset:47104\n"
"v_exp_f32 v146, v42\n"
"v_mfma_f32_16x16x32_bf16 a[4:7], a[192:195], v[116:119], a[4:7]\n"
"buffer_load_dwordx4 v2, s[44:47], s60 offen offset:2048 sc1 lds\n"
"v_add_f32 v20, v20, v146\n"
"v_cndmask_b32_e64 v53, v53, v9, s[72:73]\n"
"v_cmp_gt_i32_e64 s[74:75], 34, v6\n"
"v_mfma_f32_16x16x32_bf16 a[8:11], a[196:199], v[112:115], a[8:11]\n"
"ds_read_b128 a[252:255], v4 offset:48128\n"
"v_cmp_gt_i32_e64 s[76:77], 35, v6\n"
"v_mfma_f32_16x16x32_bf16 a[12:15], a[196:199], v[116:119], a[12:15]\n"
"v_exp_f32 v147, v43\n"
"v_cndmask_b32_e64 v54, v54, v9, s[74:75]\n"
"v_mfma_f32_16x16x32_bf16 a[16:19], a[200:203], v[112:115], a[16:19]\n"
"ds_read_b128 a[192:195], v4 offset:49152\n"
"v_add_f32 v21, v21, v147\n"
"v_mfma_f32_16x16x32_bf16 a[20:23], a[200:203], v[116:119], a[20:23]\n"
"v_cvt_pk_bf16_f32 v99, v146, v147\n"
"v_cndmask_b32_e64 v55, v55, v9, s[76:77]\n"
"v_cmp_gt_i32_e64 s[70:71], 48, v5\n"
"v_mfma_f32_16x16x32_bf16 a[24:27], a[204:207], v[112:115], a[24:27]\n"
"ds_read_b128 a[196:199], v4 offset:50176\n"
"v_cmp_gt_i32_e64 s[72:73], 49, v5\n"
"v_mfma_f32_16x16x32_bf16 a[28:31], a[204:207], v[116:119], a[28:31]\n"
"v_exp_f32 v148, v44\n"
"v_cndmask_b32_e64 v56, v56, v9, s[70:71]\n"
"v_mfma_f32_16x16x32_bf16 a[32:35], a[208:211], v[112:115], a[32:35]\n"
"ds_read_b128 a[200:203], v4 offset:51200\n"
"v_add_f32 v22, v22, v148\n"
"v_mfma_f32_16x16x32_bf16 a[36:39], a[208:211], v[116:119], a[36:39]\n"
"buffer_load_dwordx4 v2, s[44:47], s60 offen offset:3072 sc1 lds\n"
"v_cndmask_b32_e64 v57, v57, v9, s[72:73]\n"
"v_cmp_gt_i32_e64 s[74:75], 50, v5\n"
"v_mfma_f32_16x16x32_bf16 a[40:43], a[212:215], v[112:115], a[40:43]\n"
"ds_read_b128 a[204:207], v4 offset:52224\n"
"v_cmp_gt_i32_e64 s[76:77], 51, v5\n"
"v_mfma_f32_16x16x32_bf16 a[44:47], a[212:215], v[116:119], a[44:47]\n"
"v_exp_f32 v149, v45\n"
"v_cndmask_b32_e64 v58, v58, v9, s[74:75]\n"
"v_mfma_f32_16x16x32_bf16 a[48:51], a[216:219], v[112:115], a[48:51]\n"
"ds_read_b128 a[208:211], v4 offset:53248\n"
"v_add_f32 v23, v23, v149\n"
"v_mfma_f32_16x16x32_bf16 a[52:55], a[216:219], v[116:119], a[52:55]\n"
"s_add_u32 s60, s61, 0x8000\n"
"s_sub_u32 s62, s62, 1\n"
"s_cbranch_scc1 L_ev_b01_%=\n"
"L_evr_b01_%=:\n"
"v_cndmask_b32_e64 v59, v59, v9, s[76:77]\n"
"v_cmp_gt_i32_e64 s[70:71], 48, v6\n"
"v_mfma_f32_16x16x32_bf16 a[56:59], a[220:223], v[112:115], a[56:59]\n"
"ds_read_b128 a[212:215], v4 offset:54272\n"
"v_cvt_pk_bf16_f32 v102, v148, v149\n"
"v_mfma_f32_16x16x32_bf16 a[60:63], a[220:223], v[116:119], a[60:63]\n"
"s_waitcnt vmcnt(8) lgkmcnt(6)\n"
"s_barrier\n"
"v_exp_f32 v150, v46\n"
"v_cndmask_b32_e64 v60, v60, v9, s[70:71]\n"
"v_cmp_gt_i32_e64 s[72:73], 49, v6\n"
"v_mfma_f32_16x16x32_bf16 a[64:67], a[224:227], v[112:115], a[64:67]\n"
"ds_read_b128 a[216:219], v4 offset:55296\n"
"s_add_u32 m0, s65, 0\n"
"v_mfma_f32_16x16x32_bf16 a[68:71], a[224:227], v[116:119], a[68:71]\n"
"buffer_load_dwordx4 v1, s[40:43], s60 offen offset:0 sc1 lds\n"
"v_add_f32 v22, v22, v150\n"
"v_cndmask_b32_e64 v61, v61, v9, s[72:73]\n"
"v_cmp_gt_i32_e64 s[74:75], 50, v6\n"
"v_mfma_f32_16x16x32_bf16 a[72:75], a[228:231], v[112:115], a[72:75]\n"
"ds_read_b128 a[220:223], v4 offset:56320\n"
"v_cmp_gt_i32_e64 s[76:77], 51, v6\n"
"v_mfma_f32_16x16x32_bf16 a[76:79], a[228:231], v[116:119], a[76:79]\n"
"v_exp_f32 v151, v47\n"
"v_cndmask_b32_e64 v62, v62, v9, s[74:75]\n"
"v_mfma_f32_16x16x32_bf16 a[80:83], a[232:235], v[112:115], a[80:83]\n"
"ds_read_b128 a[224:227], v4 offset:57344\n"
"v_add_f32 v23, v23, v151\n"
"v_mfma_f32_16x16x32_bf16 a[84:87], a[232:235], v[116:119], a[84:87]\n"
"v_cvt_pk_bf16_f32 v103, v150, v151\n"
"v_cndmask_b32_e64 v63, v63, v9, s[76:77]\n"
"v_mfma_f32_16x16x32_bf16 a[88:91], a[236:239], v[112:115], a[88:91]\n"
"ds_read_b128 a[228:231], v4 offset:58368\n"
"v_mfma_f32_16x16x32_bf16 a[92:95], a[236:239], v[116:119], a[92:95]\n"
"buffer_load_dwordx4 v1, s[40:43], s60 offen offset:1024 sc1 lds\n"
"v_exp_f32 v152, v48\n"
"v_mfma_f32_16x16x32_bf16 a[96:99], a[240:243], v[112:115], a[96:99]\n"
"ds_read_b128 a[232:235], v4 offset:59392\n"
"v_add_f32 v20, v20, v152\n"
"v_mfma_f32_16x16x32_bf16 a[100:103], a[240:243], v[116:119], a[100:103]\n"
"v_exp_f32 v153, v49\n"
"v_mfma_f32_16x16x32_bf16 a[104:107], a[244:247], v[112:115], a[104:107]\n"
"ds_read_b128 a[236:239], v4 offset:60416\n"
"v_add_f32 v21, v21, v153\n"
"v_mfma_f32_16x16x32_bf16 a[108:111], a[244:247], v[116:119], a[108:111]\n"
"v_cvt_pk_bf16_f32 v104, v152, v153\n"
"v_mfma_f32_16x16x32_bf16 a[112:115], a[248:251], v[112:115], a[112:115]\n"
"ds_read_b128 a[240:243], v4 offset:61440\n"
"v_mfma_f32_16x16x32_bf16 a[116:119], a[248:251], v[116:119], a[116:119]\n"
"buffer_load_dwordx4 v1, s[40:43], s60 offen offset:2048 sc1 lds\n"
"v_mfma_f32_16x16x32_bf16 a[120:123], a[252:255], v[112:115], a[120:123]\n"
"ds_read_b128 a[244:247], v4 offset:62464\n"
"v_exp_f32 v154, v50\n"
"v_mfma_f32_16x16x32_bf16 a[124:127], a[252:255], v[116:119], a[124:127]\n"
"s_waitcnt lgkmcnt(6)\n"
"v_add_f32 v20, v20, v154\n"
"v_mfma_f32_16x16x32_bf16 a[0:3], a[192:195], v[120:123], a[0:3]\n"
"ds_read_b128 a[248:251], v4 offset:63488\n"
"v_mfma_f32_16x16x32_bf16 a[4:7], a[192:195], v[124:127], a[4:7]\n"
"v_exp_f32 v155, v51\n"
"v_mfma_f32_16x16x32_bf16 a[8:11], a[196:199], v[120:123], a[8:11]\n"
"ds_read_b128 a[252:255], v4 offset:64512\n"
"v_add_f32 v21, v21, v155\n"
"v_mfma_f32_16x16x32_bf16 a[12:15], a[196:199], v[124:127], a[12:15]\n"
"buffer_load_dwordx4 v1, s[40:43], s60 offen offset:3072 sc1 lds\n"
"v_cvt_pk_bf16_f32 v105, v154, v155\n"
"v_mfma_f32_16x16x32_bf16 a[16:19], a[200:203], v[120:123], a[16:19]\n"
"ds_read_b128 a[192:195], v3 offset:32768\n"
"v_mfma_f32_16x16x32_bf16 a[20:23], a[200:203], v[124:127], a[20:23]\n"
"v_exp_f32 v156, v52\n"
"v_mfma_f32_16x16x32_bf16 a[24:27], a[204:207], v[120:123], a[24:27]\n"
"ds_read_b128 a[196:199], v3 offset:33792\n"
"v_add_f32 v22, v22, v156\n"
"v_mfma_f32_16x16x32_bf16 a[28:31], a[204:207], v[124:127], a[28:31]\n"
"v_exp_f32 v157, v53\n"
"v_mfma_f32_16x16x32_bf16 a[32:35], a[208:211], v[120:123], a[32:35]\n"
"ds_read_b128 a[200:203], v3 offset:34816\n"
"s_add_u32 m0, s65, 4096\n"
"v_mfma_f32_16x16x32_bf16 a[36:39], a[208:211], v[124:127], a[36:39]\n"
"buffer_load_dwordx4 v2, s[40:43], s60 offen offset:0 sc1 lds\n"
"v_add_f32 v23, v23, v157\n"
"v_mfma_f32_16x16x32_bf16 a[40:43], a[212:215], v[120:123], a[40:43]\n"
"ds_read_b128 a[204:207], v3 offset:35840\n"
"v_cvt_pk_bf16_f32 v108, v156, v157\n"
"v_mfma_f32_16x16x32_bf16 a[44:47], a[212:215], v[124:127], a[44:47]\n"
"v_exp_f32 v158, v54\n"
"v_mfma_f32_16x16x32_bf16 a[48:51], a[216:219], v[120:123], a[48:51]\n"
"ds_read_b128 a[208:211], v3 offset:36864\n"
"v_add_f32 v22, v22, v158\n"
"v_mfma_f32_16x16x32_bf16 a[52:55], a[216:219], v[124:127], a[52:55]\n"
"v_exp_f32 v159, v55\n"
"v_mfma_f32_16x16x32_bf16 a[56:59], a[220:223], v[120:123], a[56:59]\n"
"ds_read_b128 a[212:215], v3 offset:37888\n"
"v_add_f32 v23, v23, v159\n"
"v_mfma_f32_16x16x32_bf16 a[60:63], a[220:223], v[124:127], a[60:63]\n"
"s_waitcnt lgkmcnt(6)\n"
"buffer_load_dwordx4 v2, s[40:43], s60 offen offset:1024 sc1 lds\n"
"v_mfma_f32_16x16x32_bf16 a[64:67], a[224:227], v[120:123], a[64:67]\n"
"ds_read_b128 a[216:219], v3 offset:38912\n"
"v_cvt_pk_bf16_f32 v109, v158, v159\n"
"v_mfma_f32_16x16x32_bf16 a[68:71], a[224:227], v[124:127], a[68:71]\n"
"v_exp_f32 v160, v56\n"
"v_mfma_f32_16x16x32_bf16 a[72:75], a[228:231], v[120:123], a[72:75]\n"
"ds_read_b128 a[220:223], v3 offset:39936\n"
"v_add_f32 v20, v20, v160\n"
"v_mfma_f32_16x16x32_bf16 a[76:79], a[228:231], v[124:127], a[76:79]\n"
"v_exp_f32 v161, v57\n"
"v_mfma_f32_16x16x32_bf16 a[80:83], a[232:235], v[120:123], a[80:83]\n"
"ds_read_b128 a[224:227], v3 offset:40960\n"
"v_add_f32 v21, v21, v161\n"
"v_mfma_f32_16x16x32_bf16 a[84:87], a[232:235], v[124:127], a[84:87]\n"
"buffer_load_dwordx4 v2, s[40:43], s60 offen offset:2048 sc1 lds\n"
"v_cvt_pk_bf16_f32 v106, v160, v161\n"
"v_mfma_f32_16x16x32_bf16 a[88:91], a[236:239], v[120:123], a[88:91]\n"
"ds_read_b128 a[228:231], v3 offset:41984\n"
"v_mfma_f32_16x16x32_bf16 a[92:95], a[236:239], v[124:127], a[92:95]\n"
"v_exp_f32 v162, v58\n"
"v_mfma_f32_16x16x32_bf16 a[96:99], a[240:243], v[120:123], a[96:99]\n"
"ds_read_b128 a[232:235], v3 offset:43008\n"
"v_add_f32 v20, v20, v162\n"
"v_mfma_f32_16x16x32_bf16 a[100:103], a[240:243], v[124:127], a[100:103]\n"
"v_exp_f32 v163, v59\n"
"v_mfma_f32_16x16x32_bf16 a[104:107], a[244:247], v[120:123], a[104:107]\n"
"ds_read_b128 a[236:239], v3 offset:44032\n"
"v_add_f32 v21, v21, v163\n"
"v_mfma_f32_16x16x32_bf16 a[108:111], a[244:247], v[124:127], a[108:111]\n"
"buffer_load_dwordx4 v2, s[40:43], s60 offen offset:3072 sc1 lds\n"
"v_cvt_pk_bf16_f32 v107, v162, v163\n"
"v_mfma_f32_16x16x32_bf16 a[112:115], a[248:251], v[120:123], a[112:115]\n"
"ds_read_b128 a[240:243], v3 offset:45056\n"
"v_mfma_f32_16x16x32_bf16 a[116:119], a[248:251], v[124:127], a[116:119]\n"
"v_exp_f32 v164, v60\n"
"v_mfma_f32_16x16x32_bf16 a[120:123], a[252:255], v[120:123], a[120:123]\n"
"ds_read_b128 a[244:247], v3 offset:46080\n"
"v_add_f32 v22, v22, v164\n"
"v_mfma_f32_16x16x32_bf16 a[124:127], a[252:255], v[124:127], a[124:127]\n"
"s_cmp_eq_u32 s56, 0\n"
"s_cbranch_scc1 L_last0_%=\n"
"s_sub_u32 s56, s56, 1\n"
"s_branch L_body11_%=\n"
"L_body10_%=:\n"
"s_waitcnt lgkmcnt(6)\n"
"v_mfma_f32_16x16x32_bf16 v[64:67], a[192:195], a[128:131], v[128:131]\n"
"ds_read_b128 a[248:251], v3 offset:47104\n"
"v_mfma_f32_16x16x32_bf16 v[68:71], a[192:195], a[160:163], v[132:135]\n"
"v_exp_f32 v165, v61\n"
"v_mfma_f32_16x16x32_bf16 v[64:67], a[196:199], a[132:135], v[64:67]\n"
"ds_read_b128 a[252:255], v3 offset:48128\n"
"v_add_f32 v23, v23, v165\n"
"v_mfma_f32_16x16x32_bf16 v[68:71], a[196:199], a[164:167], v[68:71]\n"
"v_cvt_pk_bf16_f32 v110, v164, v165\n"
"v_mfma_f32_16x16x32_bf16 v[64:67], a[200:203], a[136:139], v[64:67]\n"
"ds_read_b128 a[192:195], v3 offset:49152\n"
"v_mfma_f32_16x16x32_bf16 v[68:71], a[200:203], a[168:171], v[68:71]\n"
"v_exp_f32 v166, v62\n"
"v_mfma_f32_16x16x32_bf16 v[64:67], a[204:207], a[140:143], v[64:67]\n"
"ds_read_b128 a[196:199], v3 offset:50176\n"
"v_add_f32 v22, v22, v166\n"
"v_mfma_f32_16x16x32_bf16 v[68:71], a[204:207], a[172:175], v[68:71]\n"
"v_mfma_f32_16x16x32_bf16 v[64:67], a[208:211], a[144:147], v[64:67]\n"
"ds_read_b128 a[200:203], v3 offset:51200\n"
"v_mfma_f32_16x16x32_bf16 v[68:71], a[208:211], a[176:179], v[68:71]\n"
"v_exp_f32 v167, v63\n"
"v_mfma_f32_16x16x32_bf16 v[64:67], a[212:215], a[148:151], v[64:67]\n"
"ds_read_b128 a[204:207], v3 offset:52224\n"
"v_add_f32 v23, v23, v167\n"
"v_mfma_f32_16x16x32_bf16 v[68:71], a[212:215], a[180:183], v[68:71]\n"
"v_add_f32 v20, v20, v21\n"
"v_add_f32 v22, v22, v23\n"
"v_mfma_f32_16x16x32_bf16 v[64:67], a[216:219], a[152:155], v[64:67]\n"
"ds_read_b128 a[208:211], v3 offset:53248\n"
"v_cvt_pk_bf16_f32 v111, v166, v167\n"
"v_mfma_f32_16x16x32_bf16 v[68:71], a[216:219], a[184:187], v[68:71]\n"
"v_max_f32 v10, v20, v22\n"
"v_cmp_lt_f32 vcc, s66, v10\n"
"v_mfma_f32_16x16x32_bf16 v[64:67], a[220:223], a[156:159], v[64:67]\n"
"ds_read_b128 a[212:215], v3 offset:54272\n"
"v_mfma_f32_16x16x32_bf16 v[68:71], a[220:223], a[188:191], v[68:71]\n"
"s_waitcnt vmcnt(8) lgkmcnt(6)\n"
"s_barrier\n"
"v_mfma_f32_16x16x32_bf16 v[72:75], a[224:227], a[128:131], v[128:131]\n"
"ds_read_b128 a[216:219], v3 offset:55296\n"
"s_add_u32 m0, s65, 98304\n"
"v_mfma_f32_16x16x32_bf16 v[76:79], a[224:227], a[160:163], v[132:135]\n"
"buffer_load_dwordx4 v1, s[44:47], s61 offen offset:0 sc1 lds\n"
"v_mfma_f32_16x16x32_bf16 v[72:75], a[228:231], a[132:135], v[72:75]\n"
"ds_read_b128 a[220:223], v3 offset:56320\n"
"v_mfma_f32_16x16x32_bf16 v[76:79], a[228:231], a[164:167], v[76:79]\n"
"s_cbranch_vccnz L_tr_b10_%=\n"
"v_add_f32 v18, v18, v20\n"
"v_add_f32 v19, v19, v22\n"
"L_rt_b10_%=:\n"
"v_mfma_f32_16x16x32_bf16 v[72:75], a[232:235], a[136:139], v[72:75]\n"
"ds_read_b128 a[224:227], v3 offset:57344\n"
"v_mfma_f32_16x16x32_bf16 v[76:79], a[232:235], a[168:171], v[76:79]\n"
"v_exp_f32 v136, v64\n"
"v_mfma_f32_16x16x32_bf16 v[72:75], a[236:239], a[140:143], v[72:75]\n"
"ds_read_b128 a[228:231], v3 offset:58368\n"
"v_mfma_f32_16x16x32_bf16 v[76:79], a[236:239], a[172:175], v[76:79]\n"
"v_exp_f32 v137, v65\n"
"v_mfma_f32_16x16x32_bf16 v[72:75], a[240:243], a[144:147], v[72:75]\n"
"ds_read_b128 a[232:235], v3 offset:59392\n"
"v_cvt_pk_bf16_f32 v112, v136, v137\n"
"v_mfma_f32_16x16x32_bf16 v[76:79], a[240:243], a[176:179], v[76:79]\n"
"buffer_load_dwordx4 v1, s[44:47], s61 offen offset:1024 sc1 lds\n"
"v_mfma_f32_16x16x32_bf16 v[72:75], a[244:247], a[148:151], v[72:75]\n"
"ds_read_b128 a[236:239], v3 offset:60416\n"
"v_mfma_f32_16x16x32_bf16 v[76:79], a[244:247], a[180:183], v[76:79]\n"
"v_exp_f32 v138, v66\n"
"v_mfma_f32_16x16x32_bf16 v[72:75], a[248:251], a[152:155], v[72:75]\n"
"ds_read_b128 a[240:243], v3 offset:61440\n"
"v_add_f32 v20, v136, v138\n"
"v_mfma_f32_16x16x32_bf16 v[76:79], a[248:251], a[184:187], v[76:79]\n"
"v_exp_f32 v139, v67\n"
"v_mfma_f32_16x16x32_bf16 v[72:75], a[252:255], a[156:159], v[72:75]\n"
"ds_read_b128 a[244:247], v3 offset:62464\n"
"v_mfma_f32_16x16x32_bf16 v[76:79], a[252:255], a[188:191], v[76:79]\n"
"s_waitcnt lgkmcnt(6)\n"
"v_add_f32 v21, v137, v139\n"
"v_mfma_f32_16x16x32_bf16 v[80:83], a[192:195], a[128:131], v[128:131]\n"
"ds_read_b128 a[248:251], v3 offset:63488\n"
"v_cvt_pk_bf16_f32 v113, v138, v139\n"
"v_mfma_f32_16x16x32_bf16 v[84:87], a[192:195], a[160:163], v[132:135]\n"
"buffer_load_dwordx4 v1, s[44:47], s61 offen offset:2048 sc1 lds\n"
"v_mfma_f32_16x16x32_bf16 v[80:83], a[196:199], a[132:135], v[80:83]\n"
"ds_read_b128 a[252:255], v3 offset:64512\n"
"v_mfma_f32_16x16x32_bf16 v[84:87], a[196:199], a[164:167], v[84:87]\n"
"v_exp_f32 v140, v68\n"
"v_mfma_f32_16x16x32_bf16 v[80:83], a[200:203], a[136:139], v[80:83]\n"
"ds_read_b128 a[192:195], v4 offset:0\n"
"v_mfma_f32_16x16x32_bf16 v[84:87], a[200:203], a[168:171], v[84:87]\n"
"v_exp_f32 v141, v69\n"
"v_mfma_f32_16x16x32_bf16 v[80:83], a[204:207], a[140:143], v[80:83]\n"
"ds_read_b128 a[196:199], v4 offset:1024\n"
"v_mfma_f32_16x16x32_bf16 v[84:87], a[204:207], a[172:175], v[84:87]\n"
"v_mfma_f32_16x16x32_bf16 v[80:83], a[208:211], a[144:147], v[80:83]\n"
"ds_read_b128 a[200:203], v4 offset:2048\n"
"v_cvt_pk_bf16_f32 v116, v140, v141\n"
"v_mfma_f32_16x16x32_bf16 v[84:87], a[208:211], a[176:179], v[84:87]\n"
"buffer_load_dwordx4 v1, s[44:47], s61 offen offset:3072 sc1 lds\n"
"v_mfma_f32_16x16x32_bf16 v[80:83], a[212:215], a[148:151], v[80:83]\n"
"ds_read_b128 a[204:207], v4 offset:3072\n"
"v_mfma_f32_16x16x32_bf16 v[84:87], a[212:215], a[180:183], v[84:87]\n"
"v_exp_f32 v142, v70\n"
"v_mfma_f32_16x16x32_bf16 v[80:83], a[216:219], a[152:155], v[80:83]\n"
"ds_read_b128 a[208:211], v4 offset:4096\n"
"v_add_f32 v22, v140, v142\n"
"v_mfma_f32_16x16x32_bf16 v[84:87], a[216:219], a[184:187], v[84:87]\n"
"v_exp_f32 v143, v71\n"
"v_mfma_f32_16x16x32_bf16 v[80:83], a[220:223], a[156:159], v[80:83]\n"
"ds_read_b128 a[212:215], v4 offset:5120\n"
"v_add_f32 v23, v141, v143\n"
"v_mfma_f32_16x16x32_bf16 v[84:87], a[220:223], a[188:191], v[84:87]\n"
"s_waitcnt lgkmcnt(6)\n"
"v_cvt_pk_bf16_f32 v117, v142, v143\n"
"v_mfma_f32_16x16x32_bf16 v[88:91], a[224:227], a[128:131], v[128:131]\n"
"ds_read_b128 a[216:219], v4 offset:6144\n"
"s_add_u32 m0, s65, 102400\n"
"v_mfma_f32_16x16x32_bf16 v[92:95], a[224:227], a[160:163], v[132:135]\n"
"buffer_load_dwordx4 v2, s[44:47], s61 offen offset:0 sc1 lds\n"
"v_mfma_f32_16x16x32_bf16 v[88:91], a[228:231], a[132:135], v[88:91]\n"
"ds_read_b128 a[220:223], v4 offset:7168\n"
"v_mfma_f32_16x16x32_bf16 v[92:95], a[228:231], a[164:167], v[92:95]\n"
"v_exp_f32 v144, v72\n"
"v_mfma_f32_16x16x32_bf16 v[88:91], a[232:235], a[136:139], v[88:91]\n"
"ds_read_b128 a[224:227], v4 offset:8192\n"
"v_add_f32 v20, v20, v144\n"
"v_mfma_f32_16x16x32_bf16 v[92:95], a[232:235], a[168:171], v[92:95]\n"
"v_exp_f32 v145, v73\n"
"v_mfma_f32_16x16x32_bf16 v[88:91], a[236:239], a[140:143], v[88:91]\n"
"ds_read_b128 a[228:231], v4 offset:9216\n"
"v_add_f32 v21, v21, v145\n"
"v_mfma_f32_16x16x32_bf16 v[92:95], a[236:239], a[172:175], v[92:95]\n"
"v_cvt_pk_bf16_f32 v114, v144, v145\n"
"v_mfma_f32_16x16x32_bf16 v[88:91], a[240:243], a[144:147], v[88:91]\n"
"ds_read_b128 a[232:235], v4 offset:10240\n"
"v_mfma_f32_16x16x32_bf16 v[92:95], a[240:243], a[176:179], v[92:95]\n"
"buffer_load_dwordx4 v2, s[44:47], s61 offen offset:1024 sc1 lds\n"
"v_mfma_f32_16x16x32_bf16 v[88:91], a[244:247], a[148:151], v[88:91]\n"
"ds_read_b128 a[236:239], v4 offset:11264\n"
"v_mfma_f32_16x16x32_bf16 v[92:95], a[244:247], a[180:183], v[92:95]\n"
"v_exp_f32 v146, v74\n"
"v_mfma_f32_16x16x32_bf16 v[88:91], a[248:251], a[152:155], v[88:91]\n"
"ds_read_b128 a[240:243], v4 offset:12288\n"
"v_add_f32 v20, v20, v146\n"
"v_mfma_f32_16x16x32_bf16 v[92:95], a[248:251], a[184:187], v[92:95]\n"
"v_exp_f32 v147, v75\n"
"v_mfma_f32_16x16x32_bf16 v[88:91], a[252:255], a[156:159], v[88:91]\n"
"ds_read_b128 a[244:247], v4 offset:13312\n"
"v_add_f32 v21, v21, v147\n"
"v_mfma_f32_16x16x32_bf16 v[92:95], a[252:255], a[188:191], v[92:95]\n"
"s_waitcnt lgkmcnt(6)\n"
"v_cvt_pk_bf16_f32 v115, v146, v147\n"
"v_mfma_f32_16x16x32_bf16 a[0:3], a[192:195], v[96:99], a[0:3]\n"
"ds_read_b128 a[248:251], v4 offset:14336\n"
"v_mfma_f32_16x16x32_bf16 a[4:7], a[192:195], v[100:103], a[4:7]\n"
"buffer_load_dwordx4 v2, s[44:47], s61 offen offset:2048 sc1 lds\n"
"v_mfma_f32_16x16x32_bf16 a[8:11], a[196:199], v[96:99], a[8:11]\n"
"ds_read_b128 a[252:255], v4 offset:15360\n"
"v_mfma_f32_16x16x32_bf16 a[12:15], a[196:199], v[100:103], a[12:15]\n"
"v_exp_f32 v148, v76\n"
"v_mfma_f32_16x16x32_bf16 a[16:19], a[200:203], v[96:99], a[16:19]\n"
"ds_read_b128 a[192:195], v4 offset:16384\n"
"v_add_f32 v22, v22, v148\n"
"v_mfma_f32_16x16x32_bf16 a[20:23], a[200:203], v[100:103], a[20:23]\n"
"v_exp_f32 v149, v77\n"
"v_mfma_f32_16x16x32_bf16 a[24:27], a[204:207], v[96:99], a[24:27]\n"
"ds_read_b128 a[196:199], v4 offset:17408\n"
"v_add_f32 v23, v23, v149\n"
"v_mfma_f32_16x16x32_bf16 a[28:31], a[204:207], v[100:103], a[28:31]\n"
"v_cvt_pk_bf16_f32 v118, v148, v149\n"
"v_mfma_f32_16x16x32_bf16 a[32:35], a[208:211], v[96:99], a[32:35]\n"
"ds_read_b128 a[200:203], v4 offset:18432\n"
"v_mfma_f32_16x16x32_bf16 a[36:39], a[208:211], v[100:103], a[36:39]\n"
"buffer_load_dwordx4 v2, s[44:47], s61 offen offset:3072 sc1 lds\n"
"v_mfma_f32_16x16x32_bf16 a[40:43], a[212:215], v[96:99], a[40:43]\n"
"ds_read_b128 a[204:207], v4 offset:19456\n"
"v_mfma_f32_16x16x32_bf16 a[44:47], a[212:215], v[100:103], a[44:47]\n"
"v_exp_f32 v150, v78\n"
"v_mfma_f32_16x16x32_bf16 a[48:51], a[216:219], v[96:99], a[48:51]\n"
"ds_read_b128 a[208:211], v4 offset:20480\n"
"v_add_f32 v22, v22, v150\n"
"v_mfma_f32_16x16x32_bf16 a[52:55], a[216:219], v[100:103], a[52:55]\n"
"s_add_u32 s61, s60, 0x8000\n"
"s_sub_u32 s62, s62, 1\n"
"s_cbranch_scc1 L_ev_b10_%=\n"
"L_evr_b10_%=:\n"
"v_mfma_f32_16x16x32_bf16 a[56:59], a[220:223], v[96:99], a[56:59]\n"
"ds_read_b128 a[212:215], v4 offset:21504\n"
"v_mfma_f32_16x16x32_bf16 a[60:63], a[220:223], v[100:103], a[60:63]\n"
"s_waitcnt vmcnt(8) lgkmcnt(6)\n"
"s_barrier\n"
"v_mfma_f32_16x16x32_bf16 a[64:67], a[224:227], v[96:99], a[64:67]\n"
"ds_read_b128 a[216:219], v4 offset:22528\n"
"s_add_u32 m0, s65, 32768\n"
"v_mfma_f32_16x16x32_bf16 a[68:71], a[224:227], v[100:103], a[68:71]\n"
"buffer_load_dwordx4 v1, s[40:43], s61 offen offset:0 sc1 lds\n"
"v_mfma_f32_16x16x32_bf16 a[72:75], a[228:231], v[96:99], a[72:75]\n"
"ds_read_b128 a[220:223], v4 offset:23552\n"
"v_mfma_f32_16x16x32_bf16 a[76:79], a[228:231], v[100:103], a[76:79]\n"
"v_exp_f32 v151, v79\n"
"v_mfma_f32_16x16x32_bf16 a[80:83], a[232:235], v[96:99], a[80:83]\n"
"ds_read_b128 a[224:227], v4 offset:24576\n"
"v_add_f32 v23, v23, v151\n"
"v_mfma_f32_16x16x32_bf16 a[84:87], a[232:235], v[100:103], a[84:87]\n"
"v_cvt_pk_bf16_f32 v119, v150, v151\n"
"v_mfma_f32_16x16x32_bf16 a[88:91], a[236:239], v[96:99], a[88:91]\n"
"ds_read_b128 a[228:231], v4 offset:25600\n"
"v_mfma_f32_16x16x32_bf16 a[92:95], a[236:239], v[100:103], a[92:95]\n"
"buffer_load_dwordx4 v1, s[40:43], s61 offen offset:1024 sc1 lds\n"
"v_exp_f32 v152, v80\n"
"v_mfma_f32_16x16x32_bf16 a[96:99], a[240:243], v[96:99], a[96:99]\n"
"ds_read_b128 a[232:235], v4 offset:26624\n"
"v_add_f32 v20, v20, v152\n"
"v_mfma_f32_16x16x32_bf16 a[100:103], a[240:243], v[100:103], a[100:103]\n"
"v_exp_f32 v153, v81\n"
"v_mfma_f32_16x16x32_bf16 a[104:107], a[244:247], v[96:99], a[104:107]\n"
"ds_read_b128 a[236:239], v4 offset:27648\n"
"v_add_f32 v21, v21, v153\n"
"v_mfma_f32_16x16x32_bf16 a[108:111], a[244:247], v[100:103], a[108:111]\n"
"v_cvt_pk_bf16_f32 v120, v152, v153\n"
"v_mfma_f32_16x16x32_bf16 a[112:115], a[248:251], v[96:99], a[112:115]\n"
"ds_read_b128 a[240:243], v4 offset:28672\n"
"v_mfma_f32_16x16x32_bf16 a[116:119], a[248:251], v[100:103], a[116:119]\n"
"buffer_load_dwordx4 v1, s[40:43], s61 offen offset:2048 sc1 lds\n"
"v_mfma_f32_16x16x32_bf16 a[120:123], a[252:255], v[96:99], a[120:123]\n"
"ds_read_b128 a[244:247], v4 offset:29696\n"
"v_exp_f32 v154, v82\n"
"v_mfma_f32_16x16x32_bf16 a[124:127], a[252:255], v[100:103], a[124:127]\n"
"s_waitcnt lgkmcnt(6)\n"
"v_add_f32 v20, v20, v154\n"
"v_mfma_f32_16x16x32_bf16 a[0:3], a[192:195], v[104:107], a[0:3]\n"
"ds_read_b128 a[248:251], v4 offset:30720\n"
"v_mfma_f32_16x16x32_bf16 a[4:7], a[192:195], v[108:111], a[4:7]\n"
"v_exp_f32 v155, v83\n"
"v_mfma_f32_16x16x32_bf16 a[8:11], a[196:199], v[104:107], a[8:11]\n"
"ds_read_b128 a[252:255], v4 offset:31744\n"
"v_add_f32 v21, v21, v155\n"
"v_mfma_f32_16x16x32_bf16 a[12:15], a[196:199], v[108:111], a[12:15]\n"
"buffer_load_dwordx4 v1, s[40:43], s61 offen offset:3072 sc1 lds\n"
"v_cvt_pk_bf16_f32 v121, v154, v155\n"
"v_mfma_f32_16x16x32_bf16 a[16:19], a[200:203], v[104:107], a[16:19]\n"
"ds_read_b128 a[192:195], v3 offset:0\n"
"v_mfma_f32_16x16x32_bf16 a[20:23], a[200:203], v[108:111], a[20:23]\n"
"v_exp_f32 v156, v84\n"
"v_mfma_f32_16x16x32_bf16 a[24:27], a[204:207], v[104:107], a[24:27]\n"
"ds_read_b128 a[196:199], v3 offset:1024\n"
"v_add_f32 v22, v22, v156\n"
"v_mfma_f32_16x16x32_bf16 a[28:31], a[204:207], v[108:111], a[28:31]\n"
"v_exp_f32 v157, v85\n"
"v_mfma_f32_16x16x32_bf16 a[32:35], a[208:211], v[104:107], a[32:35]\n"
"ds_read_b128 a[200:203], v3 offset:2048\n"
"s_add_u32 m0, s65, 36864\n"
"v_mfma_f32_16x16x32_bf16 a[36:39], a[208:211], v[108:111], a[36:39]\n"
"buffer_load_dwordx4 v2, s[40:43], s61 offen offset:0 sc1 lds\n"
"v_add_f32 v23, v23, v157\n"
"v_mfma_f32_16x16x32_bf16 a[40:43], a[212:215], v[104:107], a[40:43]\n"
"ds_read_b128 a[204:207], v3 offset:3072\n"
"v_cvt_pk_bf16_f32 v124, v156, v157\n"
"v_mfma_f32_16x16x32_bf16 a[44:47], a[212:215], v[108:111], a[44:47]\n"
"v_exp_f32 v158, v86\n"
"v_mfma_f32_16x16x32_bf16 a[48:51], a[216:219], v[104:107], a[48:51]\n"
"ds_read_b128 a[208:211], v3 offset:4096\n"
"v_add_f32 v22, v22, v158\n"
"v_mfma_f32_16x16x32_bf16 a[52:55], a[216:219], v[108:111], a[52:55]\n"
"v_exp_f32 v159, v87\n"
"v_mfma_f32_16x16x32_bf16 a[56:59], a[220:223], v[104:107], a[56:59]\n"
"ds_read_b128 a[212:215], v3 offset:5120\n"
"v_add_f32 v23, v23, v159\n"
"v_mfma_f32_16x16x32_bf16 a[60:63], a[220:223], v[108:111], a[60:63]\n"
"s_waitcnt lgkmcnt(6)\n"
"buffer_load_dwordx4 v2, s[40:43], s61 offen offset:1024 sc1 lds\n"
"v_mfma_f32_16x16x32_bf16 a[64:67], a[224:227], v[104:107], a[64:67]\n"
"ds_read_b128 a[216:219], v3 offset:6144\n"
"v_cvt_pk_bf16_f32 v125, v158, v159\n"
"v_mfma_f32_16x16x32_bf16 a[68:71], a[224:227], v[108:111], a[68:71]\n"
"v_exp_f32 v160, v88\n"
"v_mfma_f32_16x16x32_bf16 a[72:75], a[228:231], v[104:107], a[72:75]\n"
"ds_read_b128 a[220:223], v3 offset:7168\n"
"v_add_f32 v20, v20, v160\n"
"v_mfma_f32_16x16x32_bf16 a[76:79], a[228:231], v[108:111], a[76:79]\n"
"v_exp_f32 v161, v89\n"
"v_mfma_f32_16x16x32_bf16 a[80:83], a[232:235], v[104:107], a[80:83]\n"
"ds_read_b128 a[224:227], v3 offset:8192\n"
"v_add_f32 v21, v21, v161\n"
"v_mfma_f32_16x16x32_bf16 a[84:87], a[232:235], v[108:111], a[84:87]\n"
"buffer_load_dwordx4 v2, s[40:43], s61 offen offset:2048 sc1 lds\n"
"v_cvt_pk_bf16_f32 v122, v160, v161\n"
"v_mfma_f32_16x16x32_bf16 a[88:91], a[236:239], v[104:107], a[88:91]\n"
"ds_read_b128 a[228:231], v3 offset:9216\n"
"v_mfma_f32_16x16x32_bf16 a[92:95], a[236:239], v[108:111], a[92:95]\n"
"v_exp_f32 v162, v90\n"
"v_mfma_f32_16x16x32_bf16 a[96:99], a[240:243], v[104:107], a[96:99]\n"
"ds_read_b128 a[232:235], v3 offset:10240\n"
"v_add_f32 v20, v20, v162\n"
"v_mfma_f32_16x16x32_bf16 a[100:103], a[240:243], v[108:111], a[100:103]\n"
"v_exp_f32 v163, v91\n"
"v_mfma_f32_16x16x32_bf16 a[104:107], a[244:247], v[104:107], a[104:107]\n"
"ds_read_b128 a[236:239], v3 offset:11264\n"
"v_add_f32 v21, v21, v163\n"
"v_mfma_f32_16x16x32_bf16 a[108:111], a[244:247], v[108:111], a[108:111]\n"
"buffer_load_dwordx4 v2, s[40:43], s61 offen offset:3072 sc1 lds\n"
"v_cvt_pk_bf16_f32 v123, v162, v163\n"
"v_mfma_f32_16x16x32_bf16 a[112:115], a[248:251], v[104:107], a[112:115]\n"
"ds_read_b128 a[240:243], v3 offset:12288\n"
"v_mfma_f32_16x16x32_bf16 a[116:119], a[248:251], v[108:111], a[116:119]\n"
"v_exp_f32 v164, v92\n"
"v_mfma_f32_16x16x32_bf16 a[120:123], a[252:255], v[104:107], a[120:123]\n"
"ds_read_b128 a[244:247], v3 offset:13312\n"
"v_add_f32 v22, v22, v164\n"
"v_mfma_f32_16x16x32_bf16 a[124:127], a[252:255], v[108:111], a[124:127]\n"
"s_sub_u32 s55, s55, 1\n"
"s_cbranch_scc0 L_body00_%=\n"
"s_cmp_eq_u32 s56, 0\n"
"s_cbranch_scc1 L_last1_%=\n"
"s_sub_u32 s56, s56, 1\n"
"s_branch L_body01_%=\n"
"L_body11_%=:\n"
"s_waitcnt lgkmcnt(6)\n"
"v_mfma_f32_16x16x32_bf16 v[64:67], a[192:195], a[128:131], v[128:131]\n"
"ds_read_b128 a[248:251], v3 offset:47104\n"
"v_mfma_f32_16x16x32_bf16 v[68:71], a[192:195], a[160:163], v[132:135]\n"
"v_exp_f32 v165, v61\n"
"v_mfma_f32_16x16x32_bf16 v[64:67], a[196:199], a[132:135], v[64:67]\n"
"ds_read_b128 a[252:255], v3 offset:48128\n"
"v_add_f32 v23, v23, v165\n"
"v_mfma_f32_16x16x32_bf16 v[68:71], a[196:199], a[164:167], v[68:71]\n"
"v_cvt_pk_bf16_f32 v110, v164, v165\n"
"v_mfma_f32_16x16x32_bf16 v[64:67], a[200:203], a[136:139], v[64:67]\n"
"ds_read_b128 a[192:195], v3 offset:49152\n"
"v_mfma_f32_16x16x32_bf16 v[68:71], a[200:203], a[168:171], v[68:71]\n"
"v_exp_f32 v166, v62\n"
"v_mfma_f32_16x16x32_bf16 v[64:67], a[204:207], a[140:143], v[64:67]\n"
"ds_read_b128 a[196:199], v3 offset:50176\n"
"v_add_f32 v22, v22, v166\n"
"v_mfma_f32_16x16x32_bf16 v[68:71], a[204:207], a[172:175], v[68:71]\n"
"v_mfma_f32_16x16x32_bf16 v[64:67], a[208:211], a[144:147], v[64:67]\n"
"ds_read_b128 a[200:203], v3 offset:51200\n"
"v_mfma_f32_16x16x32_bf16 v[68:71], a[208:211], a[176:179], v[68:71]\n"
"v_exp_f32 v167, v63\n"
"v_mfma_f32_16x16x32_bf16 v[64:67], a[212:215], a[148:151], v[64:67]\n"
"ds_read_b128 a[204:207], v3 offset:52224\n"
"v_add_f32 v23, v23, v167\n"
"v_mfma_f32_16x16x32_bf16 v[68:71], a[212:215], a[180:183], v[68:71]\n"
"v_add_f32 v20, v20, v21\n"
"v_add_f32 v22, v22, v23\n"
"v_mfma_f32_16x16x32_bf16 v[64:67], a[216:219], a[152:155], v[64:67]\n"
"ds_read_b128 a[208:211], v3 offset:53248\n"
"v_cvt_pk_bf16_f32 v111, v166, v167\n"
"v_mfma_f32_16x16x32_bf16 v[68:71], a[216:219], a[184:187], v[68:71]\n"
"v_max_f32 v10, v20, v22\n"
"v_cmp_lt_f32 vcc, s66, v10\n"
"v_mfma_f32_16x16x32_bf16 v[64:67], a[220:223], a[156:159], v[64:67]\n"
"ds_read_b128 a[212:215], v3 offset:54272\n"
"v_mfma_f32_16x16x32_bf16 v[68:71], a[220:223], a[188:191], v[68:71]\n"
"s_waitcnt vmcnt(8) lgkmcnt(6)\n"
"s_barrier\n"
"v_mfma_f32_16x16x32_bf16 v[72:75], a[224:227], a[128:131], v[128:131]\n"
"ds_read_b128 a[216:219], v3 offset:55296\n"
"s_add_u32 m0, s65, 98304\n"
"v_mfma_f32_16x16x32_bf16 v[76:79], a[224:227], a[160:163], v[132:135]\n"
"buffer_load_dwordx4 v1, s[44:47], s61 offen offset:0 sc1 lds\n"
"s_lshl_b32 s96, s54, 6\n"
"v_subrev_u32 v5, s96, v7\n"
"v_subrev_u32 v6, s96, v8\n"
"v_mfma_f32_16x16x32_bf16 v[72:75], a[228:231], a[132:135], v[72:75]\n"
"ds_read_b128 a[220:223], v3 offset:56320\n"
"s_add_u32 s54, s54, 1\n"
"v_mfma_f32_16x16x32_bf16 v[76:79], a[228:231], a[164:167], v[76:79]\n"
"s_cbranch_vccnz L_tr_b11_%=\n"
"v_add_f32 v18, v18, v20\n"
"v_add_f32 v19, v19, v22\n"
"L_rt_b11_%=:\n"
"v_mfma_f32_16x16x32_bf16 v[72:75], a[232:235], a[136:139], v[72:75]\n"
"ds_read_b128 a[224:227], v3 offset:57344\n"
"v_cmp_gt_i32_e64 s[70:71], 0, v5\n"
"v_mfma_f32_16x16x32_bf16 v[76:79], a[232:235], a[168:171], v[76:79]\n"
"v_cmp_gt_i32_e64 s[72:73], 1, v5\n"
"v_cmp_gt_i32_e64 s[74:75], 2, v5\n"
"v_mfma_f32_16x16x32_bf16 v[72:75], a[236:239], a[140:143], v[72:75]\n"
"ds_read_b128 a[228:231], v3 offset:58368\n"
"v_cndmask_b32_e64 v64, v64, v9, s[70:71]\n"
"v_mfma_f32_16x16x32_bf16 v[76:79], a[236:239], a[172:175], v[76:79]\n"
"v_cndmask_b32_e64 v65, v65, v9, s[72:73]\n"
"v_cndmask_b32_e64 v66, v66, v9, s[74:75]\n"
"v_mfma_f32_16x16x32_bf16 v[72:75], a[240:243], a[144:147], v[72:75]\n"
"ds_read_b128 a[232:235], v3 offset:59392\n"
"v_cmp_gt_i32_e64 s[76:77], 3, v5\n"
"v_mfma_f32_16x16x32_bf16 v[76:79], a[240:243], a[176:179], v[76:79]\n"
"buffer_load_dwordx4 v1, s[44:47], s61 offen offset:1024 sc1 lds\n"
"v_cmp_gt_i32_e64 s[70:71], 0, v6\n"
"v_mfma_f32_16x16x32_bf16 v[72:75], a[244:247], a[148:151], v[72:75]\n"
"ds_read_b128 a[236:239], v3 offset:60416\n"
"v_cndmask_b32_e64 v67, v67, v9, s[76:77]\n"
"v_mfma_f32_16x16x32_bf16 v[76:79], a[244:247], a[180:183], v[76:79]\n"
"v_cndmask_b32_e64 v68, v68, v9, s[70:71]\n"
"v_cmp_gt_i32_e64 s[72:73], 1, v6\n"
"v_mfma_f32_16x16x32_bf16 v[72:75], a[248:251], a[152:155], v[72:75]\n"
"ds_read_b128 a[240:243], v3 offset:61440\n"
"v_cmp_gt_i32_e64 s[74:75], 2, v6\n"
"v_mfma_f32_16x16x32_bf16 v[76:79], a[248:251], a[184:187], v[76:79]\n"
"v_cndmask_b32_e64 v69, v69, v9, s[72:73]\n"
"v_mfma_f32_16x16x32_bf16 v[72:75], a[252:255], a[156:159], v[72:75]\n"
"ds_read_b128 a[244:247], v3 offset:62464\n"
"v_cndmask_b32_e64 v70, v70, v9, s[74:75]\n"
"v_mfma_f32_16x16x32_bf16 v[76:79], a[252:255], a[188:191], v[76:79]\n"
"s_waitcnt lgkmcnt(6)\n"
"v_cmp_gt_i32_e64 s[76:77], 3, v6\n"
"v_mfma_f32_16x16x32_bf16 v[80:83], a[192:195], a[128:131], v[128:131]\n"
"ds_read_b128 a[248:251], v3 offset:63488\n"
"v_mfma_f32_16x16x32_bf16 v[84:87], a[192:195], a[160:163], v[132:135]\n"
"buffer_load_dwordx4 v1, s[44:47], s61 offen offset:2048 sc1 lds\n"
"v_cndmask_b32_e64 v71, v71, v9, s[76:77]\n"
"v_mfma_f32_16x16x32_bf16 v[80:83], a[196:199], a[132:135], v[80:83]\n"
"ds_read_b128 a[252:255], v3 offset:64512\n"
"v_mfma_f32_16x16x32_bf16 v[84:87], a[196:199], a[164:167], v[84:87]\n"
"v_mfma_f32_16x16x32_bf16 v[80:83], a[200:203], a[136:139], v[80:83]\n"
"ds_read_b128 a[192:195], v4 offset:0\n"
"v_cmp_gt_i32_e64 s[70:71], 16, v5\n"
"v_mfma_f32_16x16x32_bf16 v[84:87], a[200:203], a[168:171], v[84:87]\n"
"v_cmp_gt_i32_e64 s[72:73], 17, v5\n"
"v_cmp_gt_i32_e64 s[74:75], 18, v5\n"
"v_mfma_f32_16x16x32_bf16 v[80:83], a[204:207], a[140:143], v[80:83]\n"
"ds_read_b128 a[196:199], v4 offset:1024\n"
"v_cndmask_b32_e64 v72, v72, v9, s[70:71]\n"
"v_mfma_f32_16x16x32_bf16 v[84:87], a[204:207], a[172:175], v[84:87]\n"
"v_exp_f32 v136, v64\n"
"v_mfma_f32_16x16x32_bf16 v[80:83], a[208:211], a[144:147], v[80:83]\n"
"ds_read_b128 a[200:203], v4 offset:2048\n"
"v_cndmask_b32_e64 v73, v73, v9, s[72:73]\n"
"v_mfma_f32_16x16x32_bf16 v[84:87], a[208:211], a[176:179], v[84:87]\n"
"buffer_load_dwordx4 v1, s[44:47], s61 offen offset:3072 sc1 lds\n"
"v_cndmask_b32_e64 v74, v74, v9, s[74:75]\n"
"v_mfma_f32_16x16x32_bf16 v[80:83], a[212:215], a[148:151], v[80:83]\n"
"ds_read_b128 a[204:207], v4 offset:3072\n"
"v_cmp_gt_i32_e64 s[76:77], 19, v5\n"
"v_mfma_f32_16x16x32_bf16 v[84:87], a[212:215], a[180:183], v[84:87]\n"
"v_exp_f32 v137, v65\n"
"v_mfma_f32_16x16x32_bf16 v[80:83], a[216:219], a[152:155], v[80:83]\n"
"ds_read_b128 a[208:211], v4 offset:4096\n"
"v_exp_f32 v138, v66\n"
"v_exp_f32 v139, v67\n"
"v_exp_f32 v140, v68\n"
"v_mfma_f32_16x16x32_bf16 v[84:87], a[216:219], a[184:187], v[84:87]\n"
"v_exp_f32 v141, v69\n"
"v_cndmask_b32_e64 v75, v75, v9, s[76:77]\n"
"v_cmp_gt_i32_e64 s[70:71], 16, v6\n"
"v_mfma_f32_16x16x32_bf16 v[80:83], a[220:223], a[156:159], v[80:83]\n"
"ds_read_b128 a[212:215], v4 offset:5120\n"
"v_add_f32 v20, v136, v138\n"
"v_add_f32 v21, v137, v139\n"
"v_mfma_f32_16x16x32_bf16 v[84:87], a[220:223], a[188:191], v[84:87]\n"
"s_waitcnt lgkmcnt(6)\n"
"v_cvt_pk_bf16_f32 v112, v136, v137\n"
"v_cvt_pk_bf16_f32 v113, v138, v139\n"
"v_cndmask_b32_e64 v76, v76, v9, s[70:71]\n"
"v_cmp_gt_i32_e64 s[72:73], 17, v6\n"
"v_mfma_f32_16x16x32_bf16 v[88:91], a[224:227], a[128:131], v[128:131]\n"
"ds_read_b128 a[216:219], v4 offset:6144\n"
"s_add_u32 m0, s65, 102400\n"
"v_exp_f32 v142, v70\n"
"v_cvt_pk_bf16_f32 v116, v140, v141\n"
"v_mfma_f32_16x16x32_bf16 v[92:95], a[224:227], a[160:163], v[132:135]\n"
"buffer_load_dwordx4 v2, s[44:47], s61 offen offset:0 sc1 lds\n"
"v_add_f32 v22, v140, v142\n"
"v_cndmask_b32_e64 v77, v77, v9, s[72:73]\n"
"v_cmp_gt_i32_e64 s[74:75], 18, v6\n"
"v_mfma_f32_16x16x32_bf16 v[88:91], a[228:231], a[132:135], v[88:91]\n"
"ds_read_b128 a[220:223], v4 offset:7168\n"
"v_cmp_gt_i32_e64 s[76:77], 19, v6\n"
"v_mfma_f32_16x16x32_bf16 v[92:95], a[228:231], a[164:167], v[92:95]\n"
"v_exp_f32 v143, v71\n"
"v_cndmask_b32_e64 v78, v78, v9, s[74:75]\n"
"v_mfma_f32_16x16x32_bf16 v[88:91], a[232:235], a[136:139], v[88:91]\n"
"ds_read_b128 a[224:227], v4 offset:8192\n"
"v_add_f32 v23, v141, v143\n"
"v_mfma_f32_16x16x32_bf16 v[92:95], a[232:235], a[168:171], v[92:95]\n"
"v_cvt_pk_bf16_f32 v117, v142, v143\n"
"v_cndmask_b32_e64 v79, v79, v9, s[76:77]\n"
"v_cmp_gt_i32_e64 s[70:71], 32, v5\n"
"v_mfma_f32_16x16x32_bf16 v[88:91], a[236:239], a[140:143], v[88:91]\n"
"ds_read_b128 a[228:231], v4 offset:9216\n"
"v_cmp_gt_i32_e64 s[72:73], 33, v5\n"
"v_mfma_f32_16x16x32_bf16 v[92:95], a[236:239], a[172:175], v[92:95]\n"
"v_cndmask_b32_e64 v80, v80, v9, s[70:71]\n"
"v_cmp_gt_i32_e64 s[74:75], 34, v5\n"
"v_mfma_f32_16x16x32_bf16 v[88:91], a[240:243], a[144:147], v[88:91]\n"
"ds_read_b128 a[232:235], v4 offset:10240\n"
"v_cndmask_b32_e64 v81, v81, v9, s[72:73]\n"
"v_mfma_f32_16x16x32_bf16 v[92:95], a[240:243], a[176:179], v[92:95]\n"
"buffer_load_dwordx4 v2, s[44:47], s61 offen offset:1024 sc1 lds\n"
"v_cndmask_b32_e64 v82, v82, v9, s[74:75]\n"
"v_mfma_f32_16x16x32_bf16 v[88:91], a[244:247], a[148:151], v[88:91]\n"
"ds_read_b128 a[236:239], v4 offset:11264\n"
"v_cmp_gt_i32_e64 s[76:77], 35, v5\n"
"v_mfma_f32_16x16x32_bf16 v[92:95], a[244:247], a[180:183], v[92:95]\n"
"v_exp_f32 v144, v72\n"
"v_mfma_f32_16x16x32_bf16 v[88:91], a[248:251], a[152:155], v[88:91]\n"
"ds_read_b128 a[240:243], v4 offset:12288\n"
"v_exp_f32 v145, v73\n"
"v_mfma_f32_16x16x32_bf16 v[92:95], a[248:251], a[184:187], v[92:95]\n"
"v_add_f32 v20, v20, v144\n"
"v_add_f32 v21, v21, v145\n"
"v_cndmask_b32_e64 v83, v83, v9, s[76:77]\n"
"v_cmp_gt_i32_e64 s[70:71], 32, v6\n"
"v_mfma_f32_16x16x32_bf16 v[88:91], a[252:255], a[156:159], v[88:91]\n"
"ds_read_b128 a[244:247], v4 offset:13312\n"
"v_cvt_pk_bf16_f32 v114, v144, v145\n"
"v_mfma_f32_16x16x32_bf16 v[92:95], a[252:255], a[188:191], v[92:95]\n"
"s_waitcnt lgkmcnt(6)\n"
"v_cndmask_b32_e64 v84, v84, v9, s[70:71]\n"
"v_cmp_gt_i32_e64 s[72:73], 33, v6\n"
"v_mfma_f32_16x16x32_bf16 a[0:3], a[192:195], v[96:99], a[0:3]\n"
"ds_read_b128 a[248:251], v4 offset:14336\n"
"v_exp_f32 v146, v74\n"
"v_mfma_f32_16x16x32_bf16 a[4:7], a[192:195], v[100:103], a[4:7]\n"
"buffer_load_dwordx4 v2, s[44:47], s61 offen offset:2048 sc1 lds\n"
"v_add_f32 v20, v20, v146\n"
"v_cndmask_b32_e64 v85, v85, v9, s[72:73]\n"
"v_cmp_gt_i32_e64 s[74:75], 34, v6\n"
"v_mfma_f32_16x16x32_bf16 a[8:11], a[196:199], v[96:99], a[8:11]\n"
"ds_read_b128 a[252:255], v4 offset:15360\n"
"v_cmp_gt_i32_e64 s[76:77], 35, v6\n"
"v_mfma_f32_16x16x32_bf16 a[12:15], a[196:199], v[100:103], a[12:15]\n"
"v_exp_f32 v147, v75\n"
"v_cndmask_b32_e64 v86, v86, v9, s[74:75]\n"
"v_mfma_f32_16x16x32_bf16 a[16:19], a[200:203], v[96:99], a[16:19]\n"
"ds_read_b128 a[192:195], v4 offset:16384\n"
"v_add_f32 v21, v21, v147\n"
"v_mfma_f32_16x16x32_bf16 a[20:23], a[200:203], v[100:103], a[20:23]\n"
"v_cvt_pk_bf16_f32 v115, v146, v147\n"
"v_cndmask_b32_e64 v87, v87, v9, s[76:77]\n"
"v_cmp_gt_i32_e64 s[70:71], 48, v5\n"
"v_mfma_f32_16x16x32_bf16 a[24:27], a[204:207], v[96:99], a[24:27]\n"
"ds_read_b128 a[196:199], v4 offset:17408\n"
"v_cmp_gt_i32_e64 s[72:73], 49, v5\n"
"v_mfma_f32_16x16x32_bf16 a[28:31], a[204:207], v[100:103], a[28:31]\n"
"v_exp_f32 v148, v76\n"
"v_cndmask_b32_e64 v88, v88, v9, s[70:71]\n"
"v_mfma_f32_16x16x32_bf16 a[32:35], a[208:211], v[96:99], a[32:35]\n"
"ds_read_b128 a[200:203], v4 offset:18432\n"
"v_add_f32 v22, v22, v148\n"
"v_mfma_f32_16x16x32_bf16 a[36:39], a[208:211], v[100:103], a[36:39]\n"
"buffer_load_dwordx4 v2, s[44:47], s61 offen offset:3072 sc1 lds\n"
"v_cndmask_b32_e64 v89, v89, v9, s[72:73]\n"
"v_cmp_gt_i32_e64 s[74:75], 50, v5\n"
"v_mfma_f32_16x16x32_bf16 a[40:43], a[212:215], v[96:99], a[40:43]\n"
"ds_read_b128 a[204:207], v4 offset:19456\n"
"v_cmp_gt_i32_e64 s[76:77], 51, v5\n"
"v_mfma_f32_16x16x32_bf16 a[44:47], a[212:215], v[100:103], a[44:47]\n"
"v_exp_f32 v149, v77\n"
"v_cndmask_b32_e64 v90, v90, v9, s[74:75]\n"
"v_mfma_f32_16x16x32_bf16 a[48:51], a[216:219], v[96:99], a[48:51]\n"
"ds_read_b128 a[208:211], v4 offset:20480\n"
"v_add_f32 v23, v23, v149\n"
"v_mfma_f32_16x16x32_bf16 a[52:55], a[216:219], v[100:103], a[52:55]\n"
"s_add_u32 s61, s60, 0x8000\n"
"s_sub_u32 s62, s62, 1\n"
"s_cbranch_scc1 L_ev_b11_%=\n"
"L_evr_b11_%=:\n"
"v_cndmask_b32_e64 v91, v91, v9, s[76:77]\n"
"v_cmp_gt_i32_e64 s[70:71], 48, v6\n"
"v_mfma_f32_16x16x32_bf16 a[56:59], a[220:223], v[96:99], a[56:59]\n"
"ds_read_b128 a[212:215], v4 offset:21504\n"
"v_cvt_pk_bf16_f32 v118, v148, v149\n"
"v_mfma_f32_16x16x32_bf16 a[60:63], a[220:223], v[100:103], a[60:63]\n"
"s_waitcnt vmcnt(8) lgkmcnt(6)\n"
"s_barrier\n"
"v_exp_f32 v150, v78\n"
"v_cndmask_b32_e64 v92, v92, v9, s[70:71]\n"
"v_cmp_gt_i32_e64 s[72:73], 49, v6\n"
"v_mfma_f32_16x16x32_bf16 a[64:67], a[224:227], v[96:99], a[64:67]\n"
"ds_read_b128 a[216:219], v4 offset:22528\n"
"s_add_u32 m0, s65, 32768\n"
"v_mfma_f32_16x16x32_bf16 a[68:71], a[224:227], v[100:103], a[68:71]\n"
"buffer_load_dwordx4 v1, s[40:43], s61 offen offset:0 sc1 lds\n"
"v_add_f32 v22, v22, v150\n"
"v_cndmask_b32_e64 v93, v93, v9, s[72:73]\n"
"v_cmp_gt_i32_e64 s[74:75], 50, v6\n"
"v_mfma_f32_16x16x32_bf16 a[72:75], a[228:231], v[96:99], a[72:75]\n"
"ds_read_b128 a[220:223], v4 offset:23552\n"
"v_cmp_gt_i32_e64 s[76:77], 51, v6\n"
"v_mfma_f32_16x16x32_bf16 a[76:79], a[228:231], v[100:103], a[76:79]\n"
"v_exp_f32 v151, v79\n"
"v_cndmask_b32_e64 v94, v94, v9, s[74:75]\n"
"v_mfma_f32_16x16x32_bf16 a[80:83], a[232:235], v[96:99], a[80:83]\n"
"ds_read_b128 a[224:227], v4 offset:24576\n"
"v_add_f32 v23, v23, v151\n"
"v_mfma_f32_16x16x32_bf16 a[84:87], a[232:235], v[100:103], a[84:87]\n"
"v_cvt_pk_bf16_f32 v119, v150, v151\n"
"v_cndmask_b32_e64 v95, v95, v9, s[76:77]\n"
"v_mfma_f32_16x16x32_bf16 a[88:91], a[236:239], v[96:99], a[88:91]\n"
"ds_read_b128 a[228:231], v4 offset:25600\n"
"v_mfma_f32_16x16x32_bf16 a[92:95], a[236:239], v[100:103], a[92:95]\n"
"buffer_load_dwordx4 v1, s[40:43], s61 offen offset:1024 sc1 lds\n"
"v_exp_f32 v152, v80\n"
"v_mfma_f32_16x16x32_bf16 a[96:99], a[240:243], v[96:99], a[96:99]\n"
"ds_read_b128 a[232:235], v4 offset:26624\n"
"v_add_f32 v20, v20, v152\n"
"v_mfma_f32_16x16x32_bf16 a[100:103], a[240:243], v[100:103], a[100:103]\n"
"v_exp_f32 v153, v81\n"
"v_mfma_f32_16x16x32_bf16 a[104:107], a[244:247], v[96:99], a[104:107]\n"
"ds_read_b128 a[236:239], v4 offset:27648\n"
"v_add_f32 v21, v21, v153\n"
"v_mfma_f32_16x16x32_bf16 a[108:111], a[244:247], v[100:103], a[108:111]\n"
"v_cvt_pk_bf16_f32 v120, v152, v153\n"
"v_mfma_f32_16x16x32_bf16 a[112:115], a[248:251], v[96:99], a[112:115]\n"
"ds_read_b128 a[240:243], v4 offset:28672\n"
"v_mfma_f32_16x16x32_bf16 a[116:119], a[248:251], v[100:103], a[116:119]\n"
"buffer_load_dwordx4 v1, s[40:43], s61 offen offset:2048 sc1 lds\n"
"v_mfma_f32_16x16x32_bf16 a[120:123], a[252:255], v[96:99], a[120:123]\n"
"ds_read_b128 a[244:247], v4 offset:29696\n"
"v_exp_f32 v154, v82\n"
"v_mfma_f32_16x16x32_bf16 a[124:127], a[252:255], v[100:103], a[124:127]\n"
"s_waitcnt lgkmcnt(6)\n"
"v_add_f32 v20, v20, v154\n"
"v_mfma_f32_16x16x32_bf16 a[0:3], a[192:195], v[104:107], a[0:3]\n"
"ds_read_b128 a[248:251], v4 offset:30720\n"
"v_mfma_f32_16x16x32_bf16 a[4:7], a[192:195], v[108:111], a[4:7]\n"
"v_exp_f32 v155, v83\n"
"v_mfma_f32_16x16x32_bf16 a[8:11], a[196:199], v[104:107], a[8:11]\n"
"ds_read_b128 a[252:255], v4 offset:31744\n"
"v_add_f32 v21, v21, v155\n"
"v_mfma_f32_16x16x32_bf16 a[12:15], a[196:199], v[108:111], a[12:15]\n"
"buffer_load_dwordx4 v1, s[40:43], s61 offen offset:3072 sc1 lds\n"
"v_cvt_pk_bf16_f32 v121, v154, v155\n"
"v_mfma_f32_16x16x32_bf16 a[16:19], a[200:203], v[104:107], a[16:19]\n"
"ds_read_b128 a[192:195], v3 offset:0\n"
"v_mfma_f32_16x16x32_bf16 a[20:23], a[200:203], v[108:111], a[20:23]\n"
"v_exp_f32 v156, v84\n"
"v_mfma_f32_16x16x32_bf16 a[24:27], a[204:207], v[104:107], a[24:27]\n"
"ds_read_b128 a[196:199], v3 offset:1024\n"
"v_add_f32 v22, v22, v156\n"
"v_mfma_f32_16x16x32_bf16 a[28:31], a[204:207], v[108:111], a[28:31]\n"
"v_exp_f32 v157, v85\n"
"v_mfma_f32_16x16x32_bf16 a[32:35], a[208:211], v[104:107], a[32:35]\n"
"ds_read_b128 a[200:203], v3 offset:2048\n"
"s_add_u32 m0, s65, 36864\n"
"v_mfma_f32_16x16x32_bf16 a[36:39], a[208:211], v[108:111], a[36:39]\n"
"buffer_load_dwordx4 v2, s[40:43], s61 offen offset:0 sc1 lds\n"
"v_add_f32 v23, v23, v157\n"
"v_mfma_f32_16x16x32_bf16 a[40:43], a[212:215], v[104:107], a[40:43]\n"
"ds_read_b128 a[204:207], v3 offset:3072\n"
"v_cvt_pk_bf16_f32 v124, v156, v157\n"
"v_mfma_f32_16x16x32_bf16 a[44:47], a[212:215], v[108:111], a[44:47]\n"
"v_exp_f32 v158, v86\n"
"v_mfma_f32_16x16x32_bf16 a[48:51], a[216:219], v[104:107], a[48:51]\n"
"ds_read_b128 a[208:211], v3 offset:4096\n"
"v_add_f32 v22, v22, v158\n"
"v_mfma_f32_16x16x32_bf16 a[52:55], a[216:219], v[108:111], a[52:55]\n"
"v_exp_f32 v159, v87\n"
"v_mfma_f32_16x16x32_bf16 a[56:59], a[220:223], v[104:107], a[56:59]\n"
"ds_read_b128 a[212:215], v3 offset:5120\n"
"v_add_f32 v23, v23, v159\n"
"v_mfma_f32_16x16x32_bf16 a[60:63], a[220:223], v[108:111], a[60:63]\n"
"s_waitcnt lgkmcnt(6)\n"
"buffer_load_dwordx4 v2, s[40:43], s61 offen offset:1024 sc1 lds\n"
"v_mfma_f32_16x16x32_bf16 a[64:67], a[224:227], v[104:107], a[64:67]\n"
"ds_read_b128 a[216:219], v3 offset:6144\n"
"v_cvt_pk_bf16_f32 v125, v158, v159\n"
"v_mfma_f32_16x16x32_bf16 a[68:71], a[224:227], v[108:111], a[68:71]\n"
"v_exp_f32 v160, v88\n"
"v_mfma_f32_16x16x32_bf16 a[72:75], a[228:231], v[104:107], a[72:75]\n"
"ds_read_b128 a[220:223], v3 offset:7168\n"
"v_add_f32 v20, v20, v160\n"
"v_mfma_f32_16x16x32_bf16 a[76:79], a[228:231], v[108:111], a[76:79]\n"
"v_exp_f32 v161, v89\n"
"v_mfma_f32_16x16x32_bf16 a[80:83], a[232:235], v[104:107], a[80:83]\n"
"ds_read_b128 a[224:227], v3 offset:8192\n"
"v_add_f32 v21, v21, v161\n"
"v_mfma_f32_16x16x32_bf16 a[84:87], a[232:235], v[108:111], a[84:87]\n"
"buffer_load_dwordx4 v2, s[40:43], s61 offen offset:2048 sc1 lds\n"
"v_cvt_pk_bf16_f32 v122, v160, v161\n"
"v_mfma_f32_16x16x32_bf16 a[88:91], a[236:239], v[104:107], a[88:91]\n"
"ds_read_b128 a[228:231], v3 offset:9216\n"
"v_mfma_f32_16x16x32_bf16 a[92:95], a[236:239], v[108:111], a[92:95]\n"
"v_exp_f32 v162, v90\n"
"v_mfma_f32_16x16x32_bf16 a[96:99], a[240:243], v[104:107], a[96:99]\n"
"ds_read_b128 a[232:235], v3 offset:10240\n"
"v_add_f32 v20, v20, v162\n"
"v_mfma_f32_16x16x32_bf16 a[100:103], a[240:243], v[108:111], a[100:103]\n"
"v_exp_f32 v163, v91\n"
"v_mfma_f32_16x16x32_bf16 a[104:107], a[244:247], v[104:107], a[104:107]\n"
"ds_read_b128 a[236:239], v3 offset:11264\n"
"v_add_f32 v21, v21, v163\n"
"v_mfma_f32_16x16x32_bf16 a[108:111], a[244:247], v[108:111], a[108:111]\n"
"buffer_load_dwordx4 v2, s[40:43], s61 offen offset:3072 sc1 lds\n"
"v_cvt_pk_bf16_f32 v123, v162, v163\n"
"v_mfma_f32_16x16x32_bf16 a[112:115], a[248:251], v[104:107], a[112:115]\n"
"ds_read_b128 a[240:243], v3 offset:12288\n"
"v_mfma_f32_16x16x32_bf16 a[116:119], a[248:251], v[108:111], a[116:119]\n"
"v_exp_f32 v164, v92\n"
"v_mfma_f32_16x16x32_bf16 a[120:123], a[252:255], v[104:107], a[120:123]\n"
"ds_read_b128 a[244:247], v3 offset:13312\n"
"v_add_f32 v22, v22, v164\n"
"v_mfma_f32_16x16x32_bf16 a[124:127], a[252:255], v[108:111], a[124:127]\n"
"s_cmp_eq_u32 s56, 0\n"
"s_cbranch_scc1 L_last1_%=\n"
"s_sub_u32 s56, s56, 1\n"
"s_branch L_body01_%=\n"
"L_cpa1_%=:\n"
"L_cpb0_%=:\n"
"L_last0_%=:\n"
"s_cmp_eq_u32 s69, 1\n"
"s_cbranch_scc1 L_lastS0_%=\n"
"s_cmp_eq_u32 s69, 0\n"
"s_cbranch_scc1 L_lastM0_%=\n"
"s_waitcnt vmcnt(0) lgkmcnt(0)\n"
"s_barrier\n"
"ds_read_b128 a[192:195], v4 offset:0\n"
"ds_read_b128 a[196:199], v4 offset:1024\n"
"ds_read_b128 a[200:203], v4 offset:2048\n"
"ds_read_b128 a[204:207], v4 offset:3072\n"
"ds_read_b128 a[208:211], v4 offset:4096\n"
"ds_read_b128 a[212:215], v4 offset:5120\n"
"ds_read_b128 a[216:219], v4 offset:6144\n"
"ds_read_b128 a[220:223], v4 offset:7168\n"
"ds_read_b128 a[224:227], v4 offset:8192\n"
"ds_read_b128 a[228:231], v4 offset:9216\n"
"ds_read_b128 a[232:235], v4 offset:10240\n"
"ds_read_b128 a[236:239], v4 offset:11264\n"
"ds_read_b128 a[240:243], v4 offset:12288\n"
"ds_read_b128 a[244:247], v4 offset:13312\n"
"s_lshl_b32 s96, s53, 5\n"
"s_add_u32 s96, s96, 126881792\n"
"v_lshlrev_b32 v237, 2, v0\n"
"v_add_u32 v237, s96, v237\n"
"s_lshl_b32 s97, s68, 4\n"
"s_add_u32 s97, s97, 126882816\n"
"s_cmp_eq_u32 s69, 0\n"
"s_cselect_b32 s97, s97, s96\n"
"v_mov_b32 v14, s97\n"
"v_cmp_gt_u32 vcc, 8, v0\n"
"s_nop 1\n"
"v_cndmask_b32 v237, v14, v237, vcc\n"
"v_cmp_ne_u32 vcc, s35, v236\n"
"s_nop 4\n"
"s_and_b32 vcc_lo, vcc_lo, 511\n"
"s_cbranch_scc0 L_vpd_ll0_%=\n"
"s_mov_b32 s99, 0\n"
"L_vpl_ll0_%=:\n"
"buffer_load_dword v236, v237, s[36:39], 0 offen sc1\n"
"s_waitcnt vmcnt(0)\n"
"v_cmp_ne_u32 vcc, s35, v236\n"
"s_nop 4\n"
"s_and_b32 vcc_lo, vcc_lo, 511\n"
"s_cbranch_scc0 L_vpd_ll0_%=\n"
"s_sleep 1\n"
"s_add_u32 s99, s99, 1\n"
"s_cmp_lt_u32 s99, 0x200000\n"
"s_cbranch_scc1 L_vpl_ll0_%=\n"
"L_vpd_ll0_%=:\n"
"v_and_b32 v10, 15, v0\n"
"s_lshl_b32 s96, s53, 7\n"
"s_lshl_b32 s97, s49, 5\n"
"s_add_u32 s96, s96, s97\n"
"s_lshl_b32 s96, s96, 2\n"
"s_add_u32 s96, s96, 125829120\n"
"v_lshlrev_b32 v10, 2, v10\n"
"buffer_load_dword v28, v10, s[36:39], s96 offen sc1\n"
"buffer_load_dword v29, v10, s[36:39], s96 offen offset:64 sc1\n"
"v_exp_f32 v165, v61\n"
"s_nop 0\n"
"v_add_f32 v23, v23, v165\n"
"v_cvt_pk_bf16_f32 v110, v164, v165\n"
"v_exp_f32 v166, v62\n"
"s_nop 0\n"
"v_add_f32 v22, v22, v166\n"
"v_exp_f32 v167, v63\n"
"s_nop 0\n"
"v_add_f32 v23, v23, v167\n"
"v_add_f32 v20, v20, v21\n"
"v_add_f32 v22, v22, v23\n"
"v_cvt_pk_bf16_f32 v111, v166, v167\n"
"v_max_f32 v10, v20, v22\n"
"v_cmp_lt_f32 vcc, s66, v10\n"
"s_nop 4\n"
"s_cbranch_vccnz L_tr_l0_%=\n"
"v_add_f32 v18, v18, v20\n"
"v_add_f32 v19, v19, v22\n"
"L_rt_l0_%=:\n"
"s_nop 1\n"
"s_waitcnt lgkmcnt(6)\n"
"v_mfma_f32_16x16x32_bf16 a[0:3], a[192:195], v[96:99], a[0:3]\n"
"ds_read_b128 a[248:251], v4 offset:14336\n"
"s_and_b32 s54, %1, 7\n"
"s_mov_b32 s50, %2\n"
"s_lshr_b32 s96, %1, 3\n"
"s_and_b32 s52, s96, 1\n"
"s_mov_b32 s51, s53\n"
"s_mov_b64 s[40:41], %3\n"
"v_mfma_f32_16x16x32_bf16 a[4:7], a[192:195], v[100:103], a[4:7]\n"
"s_and_b32 s41, s41, 0xffff\n"
"s_mov_b32 s42, 3670016\n"
"s_mov_b32 s43, 0x20000\n"
"s_mov_b64 s[44:45], %4\n"
"s_and_b32 s45, s45, 0xffff\n"
"v_mfma_f32_16x16x32_bf16 a[8:11], a[196:199], v[96:99], a[8:11]\n"
"ds_read_b128 a[252:255], v4 offset:15360\n"
"s_mov_b32 s46, 401408\n"
"s_mov_b32 s47, 0x20000\n"
"s_mul_i32 s96, s50, 7\n"
"s_add_u32 s96, s96, 0\n"
"s_lshl_b32 s96, s96, 2\n"
"v_mfma_f32_16x16x32_bf16 a[12:15], a[196:199], v[100:103], a[12:15]\n"
"v_lshrrev_b32 v164, 4, v0\n"
"v_add_u32 v164, s96, v164\n"
"v_mul_u32_u24 v165, 147, v164\n"
"v_lshrrev_b32 v165, 10, v165\n"
"v_mul_u32_u24 v166, 7, v165\n"
"v_mfma_f32_16x16x32_bf16 a[16:19], a[200:203], v[96:99], a[16:19]\n"
"ds_read_b128 a[192:195], v4 offset:16384\n"
"v_sub_u32 v164, v164, v166\n"
"v_lshrrev_b32 v166, 2, v0\n"
"v_and_b32 v166, 3, v166\n"
"v_lshl_add_u32 v166, v165, 2, v166\n"
"v_mul_u32_u24 v166, 896, v166\n"
"v_mfma_f32_16x16x32_bf16 a[20:23], a[200:203], v[100:103], a[20:23]\n"
"v_lshl_add_u32 v166, v164, 6, v166\n"
"v_and_b32 v165, 1, v165\n"
"v_lshlrev_b32 v165, 1, v165\n"
"v_and_b32 v164, 3, v0\n"
"v_xor_b32 v165, v165, v164\n"
"v_mfma_f32_16x16x32_bf16 a[24:27], a[204:207], v[96:99], a[24:27]\n"
"ds_read_b128 a[196:199], v4 offset:17408\n"
"v_lshl_add_u32 v152, v165, 4, v166\n"
"s_mul_i32 s96, s50, 7\n"
"s_add_u32 s96, s96, 1\n"
"s_lshl_b32 s96, s96, 2\n"
"v_lshrrev_b32 v164, 4, v0\n"
"v_mfma_f32_16x16x32_bf16 a[28:31], a[204:207], v[100:103], a[28:31]\n"
"v_add_u32 v164, s96, v164\n"
"v_mul_u32_u24 v165, 147, v164\n"
"v_lshrrev_b32 v165, 10, v165\n"
"v_mul_u32_u24 v166, 7, v165\n"
"v_sub_u32 v164, v164, v166\n"
"v_lshrrev_b32 v166, 2, v0\n"
"v_mfma_f32_16x16x32_bf16 a[32:35], a[208:211], v[96:99], a[32:35]\n"
"ds_read_b128 a[200:203], v4 offset:18432\n"
"v_and_b32 v166, 3, v166\n"
"v_lshl_add_u32 v166, v165, 2, v166\n"
"v_mul_u32_u24 v166, 896, v166\n"
"v_lshl_add_u32 v166, v164, 6, v166\n"
"v_and_b32 v165, 1, v165\n"
"v_mfma_f32_16x16x32_bf16 a[36:39], a[208:211], v[100:103], a[36:39]\n"
"v_lshlrev_b32 v165, 1, v165\n"
"v_and_b32 v164, 3, v0\n"
"v_xor_b32 v165, v165, v164\n"
"v_lshl_add_u32 v153, v165, 4, v166\n"
"s_mul_i32 s96, s50, 7\n"
"v_mfma_f32_16x16x32_bf16 a[40:43], a[212:215], v[96:99], a[40:43]\n"
"ds_read_b128 a[204:207], v4 offset:19456\n"
"s_add_u32 s96, s96, 2\n"
"s_lshl_b32 s96, s96, 2\n"
"v_lshrrev_b32 v164, 4, v0\n"
"v_add_u32 v164, s96, v164\n"
"v_mul_u32_u24 v165, 147, v164\n"
"v_mfma_f32_16x16x32_bf16 a[44:47], a[212:215], v[100:103], a[44:47]\n"
"v_lshrrev_b32 v165, 10, v165\n"
"v_mul_u32_u24 v166, 7, v165\n"
"v_sub_u32 v164, v164, v166\n"
"v_lshrrev_b32 v166, 2, v0\n"
"v_and_b32 v166, 3, v166\n"
"v_mfma_f32_16x16x32_bf16 a[48:51], a[216:219], v[96:99], a[48:51]\n"
"ds_read_b128 a[208:211], v4 offset:20480\n"
"v_lshl_add_u32 v166, v165, 2, v166\n"
"v_mul_u32_u24 v166, 896, v166\n"
"v_lshl_add_u32 v166, v164, 6, v166\n"
"v_and_b32 v165, 1, v165\n"
"v_lshlrev_b32 v165, 1, v165\n"
"v_mfma_f32_16x16x32_bf16 a[52:55], a[216:219], v[100:103], a[52:55]\n"
"v_and_b32 v164, 3, v0\n"
"v_xor_b32 v165, v165, v164\n"
"v_lshl_add_u32 v154, v165, 4, v166\n"
"s_mul_i32 s96, s50, 7\n"
"s_add_u32 s96, s96, 3\n"
"v_mfma_f32_16x16x32_bf16 a[56:59], a[220:223], v[96:99], a[56:59]\n"
"ds_read_b128 a[212:215], v4 offset:21504\n"
"s_lshl_b32 s96, s96, 2\n"
"v_lshrrev_b32 v164, 4, v0\n"
"v_add_u32 v164, s96, v164\n"
"v_mul_u32_u24 v165, 147, v164\n"
"v_lshrrev_b32 v165, 10, v165\n"
"v_mul_u32_u24 v166, 7, v165\n"
"v_mfma_f32_16x16x32_bf16 a[60:63], a[220:223], v[100:103], a[60:63]\n"
"s_waitcnt lgkmcnt(6)\n"
"v_sub_u32 v164, v164, v166\n"
"v_lshrrev_b32 v166, 2, v0\n"
"v_and_b32 v166, 3, v166\n"
"v_lshl_add_u32 v166, v165, 2, v166\n"
"v_mul_u32_u24 v166, 896, v166\n"
"v_mfma_f32_16x16x32_bf16 a[64:67], a[224:227], v[96:99], a[64:67]\n"
"ds_read_b128 a[216:219], v4 offset:22528\n"
"v_lshl_add_u32 v166, v164, 6, v166\n"
"v_and_b32 v165, 1, v165\n"
"v_lshlrev_b32 v165, 1, v165\n"
"v_and_b32 v164, 3, v0\n"
"v_xor_b32 v165, v165, v164\n"
"v_mfma_f32_16x16x32_bf16 a[68:71], a[224:227], v[100:103], a[68:71]\n"
"v_lshl_add_u32 v155, v165, 4, v166\n"
"s_mul_i32 s96, s50, 7\n"
"s_add_u32 s96, s96, 4\n"
"s_lshl_b32 s96, s96, 2\n"
"v_lshrrev_b32 v164, 4, v0\n"
"v_mfma_f32_16x16x32_bf16 a[72:75], a[228:231], v[96:99], a[72:75]\n"
"ds_read_b128 a[220:223], v4 offset:23552\n"
"v_add_u32 v164, s96, v164\n"
"v_mul_u32_u24 v165, 147, v164\n"
"v_lshrrev_b32 v165, 10, v165\n"
"v_mul_u32_u24 v166, 7, v165\n"
"v_sub_u32 v164, v164, v166\n"
"v_mfma_f32_16x16x32_bf16 a[76:79], a[228:231], v[100:103], a[76:79]\n"
"v_lshrrev_b32 v166, 2, v0\n"
"v_and_b32 v166, 3, v166\n"
"v_lshl_add_u32 v166, v165, 2, v166\n"
"v_mul_u32_u24 v166, 896, v166\n"
"v_lshl_add_u32 v166, v164, 6, v166\n"
"v_mfma_f32_16x16x32_bf16 a[80:83], a[232:235], v[96:99], a[80:83]\n"
"ds_read_b128 a[224:227], v4 offset:24576\n"
"v_and_b32 v165, 1, v165\n"
"v_lshlrev_b32 v165, 1, v165\n"
"v_and_b32 v164, 3, v0\n"
"v_xor_b32 v165, v165, v164\n"
"v_lshl_add_u32 v156, v165, 4, v166\n"
"v_mfma_f32_16x16x32_bf16 a[84:87], a[232:235], v[100:103], a[84:87]\n"
"s_mul_i32 s96, s50, 7\n"
"s_add_u32 s96, s96, 5\n"
"s_lshl_b32 s96, s96, 2\n"
"v_lshrrev_b32 v164, 4, v0\n"
"v_add_u32 v164, s96, v164\n"
"v_mul_u32_u24 v165, 147, v164\n"
"v_mfma_f32_16x16x32_bf16 a[88:91], a[236:239], v[96:99], a[88:91]\n"
"ds_read_b128 a[228:231], v4 offset:25600\n"
"v_lshrrev_b32 v165, 10, v165\n"
"v_mul_u32_u24 v166, 7, v165\n"
"v_sub_u32 v164, v164, v166\n"
"v_lshrrev_b32 v166, 2, v0\n"
"v_and_b32 v166, 3, v166\n"
"v_mfma_f32_16x16x32_bf16 a[92:95], a[236:239], v[100:103], a[92:95]\n"
"v_lshl_add_u32 v166, v165, 2, v166\n"
"v_mul_u32_u24 v166, 896, v166\n"
"v_lshl_add_u32 v166, v164, 6, v166\n"
"v_and_b32 v165, 1, v165\n"
"v_lshlrev_b32 v165, 1, v165\n"
"v_mfma_f32_16x16x32_bf16 a[96:99], a[240:243], v[96:99], a[96:99]\n"
"ds_read_b128 a[232:235], v4 offset:26624\n"
"v_and_b32 v164, 3, v0\n"
"v_xor_b32 v165, v165, v164\n"
"v_lshl_add_u32 v157, v165, 4, v166\n"
"s_mul_i32 s96, s50, 7\n"
"s_add_u32 s96, s96, 6\n"
"v_mfma_f32_16x16x32_bf16 a[100:103], a[240:243], v[100:103], a[100:103]\n"
"s_lshl_b32 s96, s96, 2\n"
"v_lshrrev_b32 v164, 4, v0\n"
"v_add_u32 v164, s96, v164\n"
"v_mul_u32_u24 v165, 147, v164\n"
"v_lshrrev_b32 v165, 10, v165\n"
"v_mfma_f32_16x16x32_bf16 a[104:107], a[244:247], v[96:99], a[104:107]\n"
"ds_read_b128 a[236:239], v4 offset:27648\n"
"v_mul_u32_u24 v166, 7, v165\n"
"v_sub_u32 v164, v164, v166\n"
"v_lshrrev_b32 v166, 2, v0\n"
"v_and_b32 v166, 3, v166\n"
"v_lshl_add_u32 v166, v165, 2, v166\n"
"v_mfma_f32_16x16x32_bf16 a[108:111], a[244:247], v[100:103], a[108:111]\n"
"v_mul_u32_u24 v166, 896, v166\n"
"v_lshl_add_u32 v166, v164, 6, v166\n"
"v_and_b32 v165, 1, v165\n"
"v_lshlrev_b32 v165, 1, v165\n"
"v_and_b32 v164, 3, v0\n"
"v_mfma_f32_16x16x32_bf16 a[112:115], a[248:251], v[96:99], a[112:115]\n"
"ds_read_b128 a[240:243], v4 offset:28672\n"
"v_xor_b32 v165, v165, v164\n"
"v_lshl_add_u32 v158, v165, 4, v166\n"
"s_mul_i32 s56, s50, 7168\n"
"s_lshl_b32 s96, s54, 1\n"
"s_add_u32 s96, s96, s52\n"
"s_mul_i32 s55, s96, 229376\n"
"v_mfma_f32_16x16x32_bf16 a[116:119], a[248:251], v[100:103], a[116:119]\n"
"s_mul_i32 s96, s52, 448\n"
"s_add_u32 s55, s55, s96\n"
"s_lshl_b32 s96, s51, 3\n"
"s_lshl_b32 s97, s50, 1\n"
"s_add_u32 s96, s96, s97\n"
"v_mfma_f32_16x16x32_bf16 a[120:123], a[252:255], v[96:99], a[120:123]\n"
"ds_read_b128 a[244:247], v4 offset:29696\n"
"s_mul_i32 s57, s96, 142\n"
"s_lshl_b32 s57, s57, 10\n"
"s_add_u32 s57, s57, 58720256\n"
"s_lshl_b32 s97, s54, 11\n"
"s_add_u32 s97, s97, 131072\n"
"v_mfma_f32_16x16x32_bf16 a[124:127], a[252:255], v[100:103], a[124:127]\n"
"s_waitcnt lgkmcnt(6)\n"
"s_add_u32 s66, s97, s57\n"
"s_cmp_eq_u32 s54, 7\n"
"s_cselect_b32 s66, 0xf0000000, s66\n"
"s_add_u32 s67, s66, 145408\n"
"s_lshl_b32 s76, s96, 6\n"
"v_mfma_f32_16x16x32_bf16 a[0:3], a[192:195], v[104:107], a[0:3]\n"
"ds_read_b128 a[248:251], v4 offset:30720\n"
"s_add_u32 s76, s76, 125829120\n"
"v_and_b32 v164, 15, v0\n"
"v_lshlrev_b32 v167, 2, v164\n"
"s_lshl_b32 s96, s54, 1\n"
"s_xor_b32 s97, s52, 1\n"
"v_mfma_f32_16x16x32_bf16 a[4:7], a[192:195], v[108:111], a[4:7]\n"
"s_add_u32 s96, s96, s97\n"
"s_lshl_b32 s96, s96, 13\n"
"s_add_u32 s57, s57, s96\n"
"s_add_u32 s58, s57, 145408\n"
"v_lshrrev_b32 v164, 4, v0\n"
"v_mfma_f32_16x16x32_bf16 a[8:11], a[196:199], v[104:107], a[8:11]\n"
"ds_read_b128 a[252:255], v4 offset:31744\n"
"v_mul_u32_u24 v165, 1792, v164\n"
"v_and_b32 v166, 1, v164\n"
"v_lshl_add_u32 v165, v166, 5, v165\n"
"v_lshrrev_b32 v166, 2, v0\n"
"v_and_b32 v166, 3, v166\n"
"v_mfma_f32_16x16x32_bf16 a[12:15], a[196:199], v[108:111], a[12:15]\n"
"v_lshl_add_u32 v165, v166, 6, v165\n"
"v_and_b32 v166, 3, v0\n"
"v_lshl_add_u32 v160, v166, 3, v165\n"
"v_xor_b32 v161, 32, v160\n"
"s_mov_b32 s59, 0\n"
"v_mfma_f32_16x16x32_bf16 a[16:19], a[200:203], v[104:107], a[16:19]\n"
"v_mfma_f32_16x16x32_bf16 a[20:23], a[200:203], v[108:111], a[20:23]\n"
"s_mov_b32 m0, s56\n"
"s_nop 0\n"
"buffer_load_dwordx4 v152, s[40:43], s55 offen lds\n"
"v_mfma_f32_16x16x32_bf16 a[24:27], a[204:207], v[104:107], a[24:27]\n"
"s_add_u32 m0, m0, 0x400\n"
"s_nop 0\n"
"buffer_load_dwordx4 v153, s[40:43], s55 offen lds\n"
"v_mfma_f32_16x16x32_bf16 a[28:31], a[204:207], v[108:111], a[28:31]\n"
"v_mfma_f32_16x16x32_bf16 a[32:35], a[208:211], v[104:107], a[32:35]\n"
"s_add_u32 m0, m0, 0x400\n"
"s_nop 0\n"
"buffer_load_dwordx4 v154, s[40:43], s55 offen lds\n"
"v_mfma_f32_16x16x32_bf16 a[36:39], a[208:211], v[108:111], a[36:39]\n"
"v_mfma_f32_16x16x32_bf16 a[40:43], a[212:215], v[104:107], a[40:43]\n"
"s_add_u32 m0, m0, 0x400\n"
"s_nop 0\n"
"buffer_load_dwordx4 v155, s[40:43], s55 offen lds\n"
"v_mfma_f32_16x16x32_bf16 a[44:47], a[212:215], v[108:111], a[44:47]\n"
"v_mfma_f32_16x16x32_bf16 a[48:51], a[216:219], v[104:107], a[48:51]\n"
"s_add_u32 m0, m0, 0x400\n"
"s_nop 0\n"
"buffer_load_dwordx4 v156, s[40:43], s55 offen lds\n"
"v_mfma_f32_16x16x32_bf16 a[52:55], a[216:219], v[108:111], a[52:55]\n"
"v_mfma_f32_16x16x32_bf16 a[56:59], a[220:223], v[104:107], a[56:59]\n"
"s_add_u32 m0, m0, 0x400\n"
"s_nop 0\n"
"buffer_load_dwordx4 v157, s[40:43], s55 offen lds\n"
"v_mfma_f32_16x16x32_bf16 a[60:63], a[220:223], v[108:111], a[60:63]\n"
"s_waitcnt lgkmcnt(0)\n"
"v_mfma_f32_16x16x32_bf16 a[64:67], a[224:227], v[104:107], a[64:67]\n"
"s_add_u32 m0, m0, 0x400\n"
"s_nop 0\n"
"buffer_load_dwordx4 v158, s[40:43], s55 offen lds\n"
"s_add_u32 s55, s55, 57344\n"
"s_add_u32 s56, s56, 28672\n"
"s_sub_u32 s96, s56, 143360\n"
"s_cmp_ge_u32 s56, 143360\n"
"s_cselect_b32 s56, s96, s56\n"
"v_mfma_f32_16x16x32_bf16 a[68:71], a[224:227], v[108:111], a[68:71]\n"
"v_mfma_f32_16x16x32_bf16 a[72:75], a[228:231], v[104:107], a[72:75]\n"
"s_mov_b32 m0, s56\n"
"s_nop 0\n"
"buffer_load_dwordx4 v152, s[40:43], s55 offen lds\n"
"v_mfma_f32_16x16x32_bf16 a[76:79], a[228:231], v[108:111], a[76:79]\n"
"s_add_u32 m0, m0, 0x400\n"
"s_nop 0\n"
"buffer_load_dwordx4 v153, s[40:43], s55 offen lds\n"
"v_mfma_f32_16x16x32_bf16 a[80:83], a[232:235], v[104:107], a[80:83]\n"
"v_mfma_f32_16x16x32_bf16 a[84:87], a[232:235], v[108:111], a[84:87]\n"
"s_add_u32 m0, m0, 0x400\n"
"s_nop 0\n"
"buffer_load_dwordx4 v154, s[40:43], s55 offen lds\n"
"v_mfma_f32_16x16x32_bf16 a[88:91], a[236:239], v[104:107], a[88:91]\n"
"v_mfma_f32_16x16x32_bf16 a[92:95], a[236:239], v[108:111], a[92:95]\n"
"s_add_u32 m0, m0, 0x400\n"
"s_nop 0\n"
"buffer_load_dwordx4 v155, s[40:43], s55 offen lds\n"
"v_mfma_f32_16x16x32_bf16 a[96:99], a[240:243], v[104:107], a[96:99]\n"
"v_mfma_f32_16x16x32_bf16 a[100:103], a[240:243], v[108:111], a[100:103]\n"
"s_add_u32 m0, m0, 0x400\n"
"s_nop 0\n"
"buffer_load_dwordx4 v156, s[40:43], s55 offen lds\n"
"v_mfma_f32_16x16x32_bf16 a[104:107], a[244:247], v[104:107], a[104:107]\n"
"v_mfma_f32_16x16x32_bf16 a[108:111], a[244:247], v[108:111], a[108:111]\n"
"s_add_u32 m0, m0, 0x400\n"
"s_nop 0\n"
"buffer_load_dwordx4 v157, s[40:43], s55 offen lds\n"
"v_mfma_f32_16x16x32_bf16 a[112:115], a[248:251], v[104:107], a[112:115]\n"
"v_mfma_f32_16x16x32_bf16 a[116:119], a[248:251], v[108:111], a[116:119]\n"
"s_add_u32 m0, m0, 0x400\n"
"s_nop 0\n"
"buffer_load_dwordx4 v158, s[40:43], s55 offen lds\n"
"s_add_u32 s55, s55, 57344\n"
"s_add_u32 s56, s56, 28672\n"
"s_sub_u32 s96, s56, 143360\n"
"s_cmp_ge_u32 s56, 143360\n"
"s_cselect_b32 s56, s96, s56\n"
"v_mfma_f32_16x16x32_bf16 a[120:123], a[252:255], v[104:107], a[120:123]\n"
"v_mfma_f32_16x16x32_bf16 a[124:127], a[252:255], v[108:111], a[124:127]\n"
"s_nop 15\n"
"s_branch L_finF_%=\n"
"L_lastM0_%=:\n"
"s_waitcnt vmcnt(0) lgkmcnt(0)\n"
"s_barrier\n"
"ds_read_b128 a[192:195], v4 offset:0\n"
"ds_read_b128 a[196:199], v4 offset:1024\n"
"ds_read_b128 a[200:203], v4 offset:2048\n"
"ds_read_b128 a[204:207], v4 offset:3072\n"
"ds_read_b128 a[208:211], v4 offset:4096\n"
"ds_read_b128 a[212:215], v4 offset:5120\n"
"ds_read_b128 a[216:219], v4 offset:6144\n"
"ds_read_b128 a[220:223], v4 offset:7168\n"
"ds_read_b128 a[224:227], v4 offset:8192\n"
"ds_read_b128 a[228:231], v4 offset:9216\n"
"ds_read_b128 a[232:235], v4 offset:10240\n"
"ds_read_b128 a[236:239], v4 offset:11264\n"
"ds_read_b128 a[240:243], v4 offset:12288\n"
"ds_read_b128 a[244:247], v4 offset:13312\n"
"s_lshl_b32 s96, s53, 5\n"
"s_add_u32 s96, s96, 126881792\n"
"v_lshlrev_b32 v237, 2, v0\n"
"v_add_u32 v237, s96, v237\n"
"s_lshl_b32 s97, s68, 4\n"
"s_add_u32 s97, s97, 126882816\n"
"s_cmp_eq_u32 s69, 0\n"
"s_cselect_b32 s97, s97, s96\n"
"v_mov_b32 v14, s97\n"
"v_cmp_gt_u32 vcc, 8, v0\n"
"s_nop 1\n"
"v_cndmask_b32 v237, v14, v237, vcc\n"
"v_cmp_ne_u32 vcc, s35, v236\n"
"s_nop 4\n"
"s_and_b32 vcc_lo, vcc_lo, 511\n"
"s_cbranch_scc0 L_vpd_llm0_%=\n"
"s_mov_b32 s99, 0\n"
"L_vpl_llm0_%=:\n"
"buffer_load_dword v236, v237, s[36:39], 0 offen sc1\n"
"s_waitcnt vmcnt(0)\n"
"v_cmp_ne_u32 vcc, s35, v236\n"
"s_nop 4\n"
"s_and_b32 vcc_lo, vcc_lo, 511\n"
"s_cbranch_scc0 L_vpd_llm0_%=\n"
"s_sleep 1\n"
"s_add_u32 s99, s99, 1\n"
"s_cmp_lt_u32 s99, 0x200000\n"
"s_cbranch_scc1 L_vpl_llm0_%=\n"
"L_vpd_llm0_%=:\n"
"v_and_b32 v10, 15, v0\n"
"s_lshl_b32 s96, s53, 7\n"
"s_lshl_b32 s97, s49, 5\n"
"s_add_u32 s96, s96, s97\n"
"s_lshl_b32 s96, s96, 2\n"
"s_add_u32 s96, s96, 125829120\n"
"v_lshlrev_b32 v10, 2, v10\n"
"buffer_load_dword v28, v10, s[36:39], s96 offen sc1\n"
"buffer_load_dword v29, v10, s[36:39], s96 offen offset:64 sc1\n"
"s_lshl_b32 s78, s68, 16\n"
"s_lshl_b32 s96, s49, 14\n"
"s_add_u32 s78, s78, s96\n"
"s_add_u32 s78, s78, 79691776\n"
"s_lshl_b32 s98, s68, 12\n"
"s_lshl_b32 s96, s49, 10\n"
"s_add_u32 s98, s98, s96\n"
"s_add_u32 s98, s98, 97517568\n"
"s_add_u32 s99, s78, 8192\n"
"v_exp_f32 v165, v61\n"
"s_nop 0\n"
"v_add_f32 v23, v23, v165\n"
"v_cvt_pk_bf16_f32 v110, v164, v165\n"
"v_exp_f32 v166, v62\n"
"s_nop 0\n"
"v_add_f32 v22, v22, v166\n"
"v_exp_f32 v167, v63\n"
"s_nop 0\n"
"v_add_f32 v23, v23, v167\n"
"v_add_f32 v20, v20, v21\n"
"v_add_f32 v22, v22, v23\n"
"v_cvt_pk_bf16_f32 v111, v166, v167\n"
"v_max_f32 v10, v20, v22\n"
"v_cmp_lt_f32 vcc, s66, v10\n"
"s_nop 4\n"
"s_cbranch_vccnz L_tr_lm0_%=\n"
"v_add_f32 v18, v18, v20\n"
"v_add_f32 v19, v19, v22\n"
"L_rt_lm0_%=:\n"
"s_nop 1\n"
"s_waitcnt lgkmcnt(6)\n"
"v_mfma_f32_16x16x32_bf16 a[0:3], a[192:195], v[96:99], a[0:3]\n"
"ds_read_b128 a[248:251], v4 offset:14336\n"
"s_and_b32 s54, %1, 7\n"
"s_mov_b32 s50, %2\n"
"s_lshr_b32 s96, %1, 3\n"
"s_and_b32 s52, s96, 1\n"
"s_mov_b32 s51, s53\n"
"s_mov_b64 s[40:41], %3\n"
"v_mfma_f32_16x16x32_bf16 a[4:7], a[192:195], v[100:103], a[4:7]\n"
"buffer_load_dwordx4 v[232:235], v1, s[36:39], s98 offen sc1\n"
"s_and_b32 s41, s41, 0xffff\n"
"s_mov_b32 s42, 3670016\n"
"s_mov_b32 s43, 0x20000\n"
"s_mov_b64 s[44:45], %4\n"
"s_and_b32 s45, s45, 0xffff\n"
"v_mfma_f32_16x16x32_bf16 a[8:11], a[196:199], v[96:99], a[8:11]\n"
"ds_read_b128 a[252:255], v4 offset:15360\n"
"buffer_load_dwordx4 v[168:171], v1, s[36:39], s78 offen offset:0 sc1\n"
"s_mov_b32 s46, 401408\n"
"s_mov_b32 s47, 0x20000\n"
"s_mul_i32 s96, s50, 7\n"
"s_add_u32 s96, s96, 0\n"
"s_lshl_b32 s96, s96, 2\n"
"v_mfma_f32_16x16x32_bf16 a[12:15], a[196:199], v[100:103], a[12:15]\n"
"buffer_load_dwordx4 v[172:175], v1, s[36:39], s78 offen offset:1024 sc1\n"
"v_lshrrev_b32 v164, 4, v0\n"
"v_add_u32 v164, s96, v164\n"
"v_mul_u32_u24 v165, 147, v164\n"
"v_lshrrev_b32 v165, 10, v165\n"
"v_mul_u32_u24 v166, 7, v165\n"
"v_mfma_f32_16x16x32_bf16 a[16:19], a[200:203], v[96:99], a[16:19]\n"
"ds_read_b128 a[192:195], v4 offset:16384\n"
"v_sub_u32 v164, v164, v166\n"
"v_lshrrev_b32 v166, 2, v0\n"
"v_and_b32 v166, 3, v166\n"
"v_lshl_add_u32 v166, v165, 2, v166\n"
"v_mul_u32_u24 v166, 896, v166\n"
"v_mfma_f32_16x16x32_bf16 a[20:23], a[200:203], v[100:103], a[20:23]\n"
"buffer_load_dwordx4 v[176:179], v1, s[36:39], s78 offen offset:2048 sc1\n"
"v_lshl_add_u32 v166, v164, 6, v166\n"
"v_and_b32 v165, 1, v165\n"
"v_lshlrev_b32 v165, 1, v165\n"
"v_and_b32 v164, 3, v0\n"
"v_xor_b32 v165, v165, v164\n"
"v_mfma_f32_16x16x32_bf16 a[24:27], a[204:207], v[96:99], a[24:27]\n"
"ds_read_b128 a[196:199], v4 offset:17408\n"
"buffer_load_dwordx4 v[180:183], v1, s[36:39], s78 offen offset:3072 sc1\n"
"v_lshl_add_u32 v152, v165, 4, v166\n"
"s_mul_i32 s96, s50, 7\n"
"s_add_u32 s96, s96, 1\n"
"s_lshl_b32 s96, s96, 2\n"
"v_lshrrev_b32 v164, 4, v0\n"
"v_mfma_f32_16x16x32_bf16 a[28:31], a[204:207], v[100:103], a[28:31]\n"
"v_add_u32 v164, s96, v164\n"
"v_mul_u32_u24 v165, 147, v164\n"
"v_lshrrev_b32 v165, 10, v165\n"
"v_mul_u32_u24 v166, 7, v165\n"
"v_sub_u32 v164, v164, v166\n"
"v_lshrrev_b32 v166, 2, v0\n"
"v_mfma_f32_16x16x32_bf16 a[32:35], a[208:211], v[96:99], a[32:35]\n"
"ds_read_b128 a[200:203], v4 offset:18432\n"
"buffer_load_dwordx4 v[184:187], v2, s[36:39], s78 offen offset:0 sc1\n"
"v_and_b32 v166, 3, v166\n"
"v_lshl_add_u32 v166, v165, 2, v166\n"
"v_mul_u32_u24 v166, 896, v166\n"
"v_lshl_add_u32 v166, v164, 6, v166\n"
"v_and_b32 v165, 1, v165\n"
"v_mfma_f32_16x16x32_bf16 a[36:39], a[208:211], v[100:103], a[36:39]\n"
"buffer_load_dwordx4 v[188:191], v2, s[36:39], s78 offen offset:1024 sc1\n"
"v_lshlrev_b32 v165, 1, v165\n"
"v_and_b32 v164, 3, v0\n"
"v_xor_b32 v165, v165, v164\n"
"v_lshl_add_u32 v153, v165, 4, v166\n"
"s_mul_i32 s96, s50, 7\n"
"v_mfma_f32_16x16x32_bf16 a[40:43], a[212:215], v[96:99], a[40:43]\n"
"ds_read_b128 a[204:207], v4 offset:19456\n"
"buffer_load_dwordx4 v[192:195], v2, s[36:39], s78 offen offset:2048 sc1\n"
"s_add_u32 s96, s96, 2\n"
"s_lshl_b32 s96, s96, 2\n"
"v_lshrrev_b32 v164, 4, v0\n"
"v_add_u32 v164, s96, v164\n"
"v_mul_u32_u24 v165, 147, v164\n"
"v_mfma_f32_16x16x32_bf16 a[44:47], a[212:215], v[100:103], a[44:47]\n"
"v_lshrrev_b32 v165, 10, v165\n"
"v_mul_u32_u24 v166, 7, v165\n"
"v_sub_u32 v164, v164, v166\n"
"v_lshrrev_b32 v166, 2, v0\n"
"v_and_b32 v166, 3, v166\n"
"v_mfma_f32_16x16x32_bf16 a[48:51], a[216:219], v[96:99], a[48:51]\n"
"ds_read_b128 a[208:211], v4 offset:20480\n"
"buffer_load_dwordx4 v[196:199], v2, s[36:39], s78 offen offset:3072 sc1\n"
"v_lshl_add_u32 v166, v165, 2, v166\n"
"v_mul_u32_u24 v166, 896, v166\n"
"v_lshl_add_u32 v166, v164, 6, v166\n"
"v_and_b32 v165, 1, v165\n"
"v_lshlrev_b32 v165, 1, v165\n"
"v_mfma_f32_16x16x32_bf16 a[52:55], a[216:219], v[100:103], a[52:55]\n"
"buffer_load_dwordx4 v[200:203], v1, s[36:39], s99 offen offset:0 sc1\n"
"v_and_b32 v164, 3, v0\n"
"v_xor_b32 v165, v165, v164\n"
"v_lshl_add_u32 v154, v165, 4, v166\n"
"s_mul_i32 s96, s50, 7\n"
"s_add_u32 s96, s96, 3\n"
"v_mfma_f32_16x16x32_bf16 a[56:59], a[220:223], v[96:99], a[56:59]\n"
"ds_read_b128 a[212:215], v4 offset:21504\n"
"s_lshl_b32 s96, s96, 2\n"
"v_lshrrev_b32 v164, 4, v0\n"
"v_add_u32 v164, s96, v164\n"
"v_mul_u32_u24 v165, 147, v164\n"
"v_lshrrev_b32 v165, 10, v165\n"
"v_mul_u32_u24 v166, 7, v165\n"
"v_mfma_f32_16x16x32_bf16 a[60:63], a[220:223], v[100:103], a[60:63]\n"
"s_waitcnt lgkmcnt(6)\n"
"buffer_load_dwordx4 v[204:207], v1, s[36:39], s99 offen offset:1024 sc1\n"
"v_sub_u32 v164, v164, v166\n"
"v_lshrrev_b32 v166, 2, v0\n"
"v_and_b32 v166, 3, v166\n"
"v_lshl_add_u32 v166, v165, 2, v166\n"
"v_mul_u32_u24 v166, 896, v166\n"
"v_mfma_f32_16x16x32_bf16 a[64:67], a[224:227], v[96:99], a[64:67]\n"
"ds_read_b128 a[216:219], v4 offset:22528\n"
"buffer_load_dwordx4 v[208:211], v1, s[36:39], s99 offen offset:2048 sc1\n"
"v_lshl_add_u32 v166, v164, 6, v166\n"
"v_and_b32 v165, 1, v165\n"
"v_lshlrev_b32 v165, 1, v165\n"
"v_and_b32 v164, 3, v0\n"
"v_xor_b32 v165, v165, v164\n"
"v_mfma_f32_16x16x32_bf16 a[68:71], a[224:227], v[100:103], a[68:71]\n"
"buffer_load_dwordx4 v[212:215], v1, s[36:39], s99 offen offset:3072 sc1\n"
"v_lshl_add_u32 v155, v165, 4, v166\n"
"s_mul_i32 s96, s50, 7\n"
"s_add_u32 s96, s96, 4\n"
"s_lshl_b32 s96, s96, 2\n"
"v_lshrrev_b32 v164, 4, v0\n"
"v_mfma_f32_16x16x32_bf16 a[72:75], a[228:231], v[96:99], a[72:75]\n"
"ds_read_b128 a[220:223], v4 offset:23552\n"
"v_add_u32 v164, s96, v164\n"
"v_mul_u32_u24 v165, 147, v164\n"
"v_lshrrev_b32 v165, 10, v165\n"
"v_mul_u32_u24 v166, 7, v165\n"
"v_sub_u32 v164, v164, v166\n"
"v_mfma_f32_16x16x32_bf16 a[76:79], a[228:231], v[100:103], a[76:79]\n"
"buffer_load_dwordx4 v[216:219], v2, s[36:39], s99 offen offset:0 sc1\n"
"v_lshrrev_b32 v166, 2, v0\n"
"v_and_b32 v166, 3, v166\n"
"v_lshl_add_u32 v166, v165, 2, v166\n"
"v_mul_u32_u24 v166, 896, v166\n"
"v_lshl_add_u32 v166, v164, 6, v166\n"
"v_mfma_f32_16x16x32_bf16 a[80:83], a[232:235], v[96:99], a[80:83]\n"
"ds_read_b128 a[224:227], v4 offset:24576\n"
"buffer_load_dwordx4 v[220:223], v2, s[36:39], s99 offen offset:1024 sc1\n"
"v_and_b32 v165, 1, v165\n"
"v_lshlrev_b32 v165, 1, v165\n"
"v_and_b32 v164, 3, v0\n"
"v_xor_b32 v165, v165, v164\n"
"v_lshl_add_u32 v156, v165, 4, v166\n"
"v_mfma_f32_16x16x32_bf16 a[84:87], a[232:235], v[100:103], a[84:87]\n"
"s_mul_i32 s96, s50, 7\n"
"s_add_u32 s96, s96, 5\n"
"s_lshl_b32 s96, s96, 2\n"
"v_lshrrev_b32 v164, 4, v0\n"
"v_add_u32 v164, s96, v164\n"
"v_mul_u32_u24 v165, 147, v164\n"
"v_mfma_f32_16x16x32_bf16 a[88:91], a[236:239], v[96:99], a[88:91]\n"
"ds_read_b128 a[228:231], v4 offset:25600\n"
"buffer_load_dwordx4 v[224:227], v2, s[36:39], s99 offen offset:2048 sc1\n"
"v_lshrrev_b32 v165, 10, v165\n"
"v_mul_u32_u24 v166, 7, v165\n"
"v_sub_u32 v164, v164, v166\n"
"v_lshrrev_b32 v166, 2, v0\n"
"v_and_b32 v166, 3, v166\n"
"v_mfma_f32_16x16x32_bf16 a[92:95], a[236:239], v[100:103], a[92:95]\n"
"buffer_load_dwordx4 v[228:231], v2, s[36:39], s99 offen offset:3072 sc1\n"
"v_lshl_add_u32 v166, v165, 2, v166\n"
"v_mul_u32_u24 v166, 896, v166\n"
"v_lshl_add_u32 v166, v164, 6, v166\n"
"v_and_b32 v165, 1, v165\n"
"v_lshlrev_b32 v165, 1, v165\n"
"v_mfma_f32_16x16x32_bf16 a[96:99], a[240:243], v[96:99], a[96:99]\n"
"ds_read_b128 a[232:235], v4 offset:26624\n"
"v_and_b32 v164, 3, v0\n"
"v_xor_b32 v165, v165, v164\n"
"v_lshl_add_u32 v157, v165, 4, v166\n"
"s_mul_i32 s96, s50, 7\n"
"s_add_u32 s96, s96, 6\n"
"v_mfma_f32_16x16x32_bf16 a[100:103], a[240:243], v[100:103], a[100:103]\n"
"s_lshl_b32 s96, s96, 2\n"
"v_lshrrev_b32 v164, 4, v0\n"
"v_add_u32 v164, s96, v164\n"
"v_mul_u32_u24 v165, 147, v164\n"
"v_lshrrev_b32 v165, 10, v165\n"
"v_mfma_f32_16x16x32_bf16 a[104:107], a[244:247], v[96:99], a[104:107]\n"
"ds_read_b128 a[236:239], v4 offset:27648\n"
"v_mul_u32_u24 v166, 7, v165\n"
"v_sub_u32 v164, v164, v166\n"
"v_lshrrev_b32 v166, 2, v0\n"
"v_and_b32 v166, 3, v166\n"
"v_lshl_add_u32 v166, v165, 2, v166\n"
"v_mfma_f32_16x16x32_bf16 a[108:111], a[244:247], v[100:103], a[108:111]\n"
"v_mul_u32_u24 v166, 896, v166\n"
"v_lshl_add_u32 v166, v164, 6, v166\n"
"v_and_b32 v165, 1, v165\n"
"v_lshlrev_b32 v165, 1, v165\n"
"v_and_b32 v164, 3, v0\n"
"v_mfma_f32_16x16x32_bf16 a[112:115], a[248:251], v[96:99], a[112:115]\n"
"ds_read_b128 a[240:243], v4 offset:28672\n"
"v_xor_b32 v165, v165, v164\n"
"v_lshl_add_u32 v158, v165, 4, v166\n"
"s_mul_i32 s56, s50, 7168\n"
"s_lshl_b32 s96, s54, 1\n"
"s_add_u32 s96, s96, s52\n"
"s_mul_i32 s55, s96, 229376\n"
"v_mfma_f32_16x16x32_bf16 a[116:119], a[248:251], v[100:103], a[116:119]\n"
"s_mul_i32 s96, s52, 448\n"
"s_add_u32 s55, s55, s96\n"
"s_lshl_b32 s96, s51, 3\n"
"s_lshl_b32 s97, s50, 1\n"
"s_add_u32 s96, s96, s97\n"
"v_mfma_f32_16x16x32_bf16 a[120:123], a[252:255], v[96:99], a[120:123]\n"
"ds_read_b128 a[244:247], v4 offset:29696\n"
"s_mul_i32 s57, s96, 142\n"
"s_lshl_b32 s57, s57, 10\n"
"s_add_u32 s57, s57, 58720256\n"
"s_lshl_b32 s97, s54, 11\n"
"s_add_u32 s97, s97, 131072\n"
"v_mfma_f32_16x16x32_bf16 a[124:127], a[252:255], v[100:103], a[124:127]\n"
"s_waitcnt lgkmcnt(6)\n"
"s_add_u32 s66, s97, s57\n"
"s_cmp_eq_u32 s54, 7\n"
"s_cselect_b32 s66, 0xf0000000, s66\n"
"s_add_u32 s67, s66, 145408\n"
"s_lshl_b32 s76, s96, 6\n"
"v_mfma_f32_16x16x32_bf16 a[0:3], a[192:195], v[104:107], a[0:3]\n"
"ds_read_b128 a[248:251], v4 offset:30720\n"
"s_add_u32 s76, s76, 125829120\n"
"v_and_b32 v164, 15, v0\n"
"v_lshlrev_b32 v167, 2, v164\n"
"s_lshl_b32 s96, s54, 1\n"
"s_xor_b32 s97, s52, 1\n"
"v_mfma_f32_16x16x32_bf16 a[4:7], a[192:195], v[108:111], a[4:7]\n"
"s_add_u32 s96, s96, s97\n"
"s_lshl_b32 s96, s96, 13\n"
"s_add_u32 s57, s57, s96\n"
"s_add_u32 s58, s57, 145408\n"
"v_lshrrev_b32 v164, 4, v0\n"
"v_mfma_f32_16x16x32_bf16 a[8:11], a[196:199], v[104:107], a[8:11]\n"
"ds_read_b128 a[252:255], v4 offset:31744\n"
"v_mul_u32_u24 v165, 1792, v164\n"
"v_and_b32 v166, 1, v164\n"
"v_lshl_add_u32 v165, v166, 5, v165\n"
"v_lshrrev_b32 v166, 2, v0\n"
"v_and_b32 v166, 3, v166\n"
"v_mfma_f32_16x16x32_bf16 a[12:15], a[196:199], v[108:111], a[12:15]\n"
"v_lshl_add_u32 v165, v166, 6, v165\n"
"v_and_b32 v166, 3, v0\n"
"v_lshl_add_u32 v160, v166, 3, v165\n"
"v_xor_b32 v161, 32, v160\n"
"s_mov_b32 s59, 0\n"
"v_mfma_f32_16x16x32_bf16 a[16:19], a[200:203], v[104:107], a[16:19]\n"
"v_mfma_f32_16x16x32_bf16 a[20:23], a[200:203], v[108:111], a[20:23]\n"
"s_mov_b32 m0, s56\n"
"s_nop 0\n"
"buffer_load_dwordx4 v152, s[40:43], s55 offen lds\n"
"v_mfma_f32_16x16x32_bf16 a[24:27], a[204:207], v[104:107], a[24:27]\n"
"s_add_u32 m0, m0, 0x400\n"
"s_nop 0\n"
"buffer_load_dwordx4 v153, s[40:43], s55 offen lds\n"
"v_mfma_f32_16x16x32_bf16 a[28:31], a[204:207], v[108:111], a[28:31]\n"
"v_mfma_f32_16x16x32_bf16 a[32:35], a[208:211], v[104:107], a[32:35]\n"
"s_add_u32 m0, m0, 0x400\n"
"s_nop 0\n"
"buffer_load_dwordx4 v154, s[40:43], s55 offen lds\n"
"v_mfma_f32_16x16x32_bf16 a[36:39], a[208:211], v[108:111], a[36:39]\n"
"v_mfma_f32_16x16x32_bf16 a[40:43], a[212:215], v[104:107], a[40:43]\n"
"s_add_u32 m0, m0, 0x400\n"
"s_nop 0\n"
"buffer_load_dwordx4 v155, s[40:43], s55 offen lds\n"
"v_mfma_f32_16x16x32_bf16 a[44:47], a[212:215], v[108:111], a[44:47]\n"
"v_mfma_f32_16x16x32_bf16 a[48:51], a[216:219], v[104:107], a[48:51]\n"
"s_add_u32 m0, m0, 0x400\n"
"s_nop 0\n"
"buffer_load_dwordx4 v156, s[40:43], s55 offen lds\n"
"v_mfma_f32_16x16x32_bf16 a[52:55], a[216:219], v[108:111], a[52:55]\n"
"v_mfma_f32_16x16x32_bf16 a[56:59], a[220:223], v[104:107], a[56:59]\n"
"s_add_u32 m0, m0, 0x400\n"
"s_nop 0\n"
"buffer_load_dwordx4 v157, s[40:43], s55 offen lds\n"
"v_mfma_f32_16x16x32_bf16 a[60:63], a[220:223], v[108:111], a[60:63]\n"
"s_waitcnt lgkmcnt(0)\n"
"v_mfma_f32_16x16x32_bf16 a[64:67], a[224:227], v[104:107], a[64:67]\n"
"s_add_u32 m0, m0, 0x400\n"
"s_nop 0\n"
"buffer_load_dwordx4 v158, s[40:43], s55 offen lds\n"
"s_add_u32 s55, s55, 57344\n"
"s_add_u32 s56, s56, 28672\n"
"s_sub_u32 s96, s56, 143360\n"
"s_cmp_ge_u32 s56, 143360\n"
"s_cselect_b32 s56, s96, s56\n"
"v_mfma_f32_16x16x32_bf16 a[68:71], a[224:227], v[108:111], a[68:71]\n"
"v_mfma_f32_16x16x32_bf16 a[72:75], a[228:231], v[104:107], a[72:75]\n"
"s_mov_b32 m0, s56\n"
"s_nop 0\n"
"buffer_load_dwordx4 v152, s[40:43], s55 offen lds\n"
"v_mfma_f32_16x16x32_bf16 a[76:79], a[228:231], v[108:111], a[76:79]\n"
"s_add_u32 m0, m0, 0x400\n"
"s_nop 0\n"
"buffer_load_dwordx4 v153, s[40:43], s55 offen lds\n"
"v_mfma_f32_16x16x32_bf16 a[80:83], a[232:235], v[104:107], a[80:83]\n"
"v_mfma_f32_16x16x32_bf16 a[84:87], a[232:235], v[108:111], a[84:87]\n"
"s_add_u32 m0, m0, 0x400\n"
"s_nop 0\n"
"buffer_load_dwordx4 v154, s[40:43], s55 offen lds\n"
"v_mfma_f32_16x16x32_bf16 a[88:91], a[236:239], v[104:107], a[88:91]\n"
"v_mfma_f32_16x16x32_bf16 a[92:95], a[236:239], v[108:111], a[92:95]\n"
"s_add_u32 m0, m0, 0x400\n"
"s_nop 0\n"
"buffer_load_dwordx4 v155, s[40:43], s55 offen lds\n"
"v_mfma_f32_16x16x32_bf16 a[96:99], a[240:243], v[104:107], a[96:99]\n"
"v_mfma_f32_16x16x32_bf16 a[100:103], a[240:243], v[108:111], a[100:103]\n"
"s_add_u32 m0, m0, 0x400\n"
"s_nop 0\n"
"buffer_load_dwordx4 v156, s[40:43], s55 offen lds\n"
"v_mfma_f32_16x16x32_bf16 a[104:107], a[244:247], v[104:107], a[104:107]\n"
"v_mfma_f32_16x16x32_bf16 a[108:111], a[244:247], v[108:111], a[108:111]\n"
"s_add_u32 m0, m0, 0x400\n"
"s_nop 0\n"
"buffer_load_dwordx4 v157, s[40:43], s55 offen lds\n"
"v_mfma_f32_16x16x32_bf16 a[112:115], a[248:251], v[104:107], a[112:115]\n"
"v_mfma_f32_16x16x32_bf16 a[116:119], a[248:251], v[108:111], a[116:119]\n"
"s_add_u32 m0, m0, 0x400\n"
"s_nop 0\n"
"buffer_load_dwordx4 v158, s[40:43], s55 offen lds\n"
"s_add_u32 s55, s55, 57344\n"
"s_add_u32 s56, s56, 28672\n"
"s_sub_u32 s96, s56, 143360\n"
"s_cmp_ge_u32 s56, 143360\n"
"s_cselect_b32 s56, s96, s56\n"
"v_mfma_f32_16x16x32_bf16 a[120:123], a[252:255], v[104:107], a[120:123]\n"
"v_mfma_f32_16x16x32_bf16 a[124:127], a[252:255], v[108:111], a[124:127]\n"
"s_nop 15\n"
"s_branch L_finM_%=\n"
"L_lastS0_%=:\n"
"s_waitcnt vmcnt(8) lgkmcnt(0)\n"
"s_barrier\n"
"ds_read_b128 a[192:195], v4 offset:0\n"
"ds_read_b128 a[196:199], v4 offset:1024\n"
"ds_read_b128 a[200:203], v4 offset:2048\n"
"ds_read_b128 a[204:207], v4 offset:3072\n"
"ds_read_b128 a[208:211], v4 offset:4096\n"
"ds_read_b128 a[212:215], v4 offset:5120\n"
"ds_read_b128 a[216:219], v4 offset:6144\n"
"ds_read_b128 a[220:223], v4 offset:7168\n"
"ds_read_b128 a[224:227], v4 offset:8192\n"
"ds_read_b128 a[228:231], v4 offset:9216\n"
"ds_read_b128 a[232:235], v4 offset:10240\n"
"ds_read_b128 a[236:239], v4 offset:11264\n"
"ds_read_b128 a[240:243], v4 offset:12288\n"
"ds_read_b128 a[244:247], v4 offset:13312\n"
"s_mov_b32 s53, s51\n"
"s_lshl_b32 s79, s48, 20\n"
"s_lshl_b32 s96, s53, 16\n"
"s_add_u32 s79, s79, s96\n"
"s_lshl_b32 s96, s49, 14\n"
"s_add_u32 s79, s79, s96\n"
"v_and_b32 v10, 15, v0\n"
"v_lshrrev_b32 v11, 4, v0\n"
"v_lshlrev_b32 v11, 2, v11\n"
"v_sub_u32 v10, v10, v11\n"
"s_lshl_b32 s96, s53, 7\n"
"s_lshl_b32 s97, s49, 5\n"
"s_add_u32 s96, s96, s97\n"
"v_add_u32 v7, s96, v10\n"
"v_add_u32 v8, 16, v7\n"
"s_add_u32 s99, s79, 8192\n"
"v_exp_f32 v165, v61\n"
"s_nop 0\n"
"v_add_f32 v23, v23, v165\n"
"v_cvt_pk_bf16_f32 v110, v164, v165\n"
"v_exp_f32 v166, v62\n"
"s_nop 0\n"
"v_add_f32 v22, v22, v166\n"
"v_exp_f32 v167, v63\n"
"s_nop 0\n"
"v_add_f32 v23, v23, v167\n"
"v_add_f32 v20, v20, v21\n"
"v_add_f32 v22, v22, v23\n"
"v_cvt_pk_bf16_f32 v111, v166, v167\n"
"v_max_f32 v10, v20, v22\n"
"v_cmp_lt_f32 vcc, s66, v10\n"
"s_nop 4\n"
"s_cbranch_vccnz L_tr_ls0_%=\n"
"v_add_f32 v18, v18, v20\n"
"v_add_f32 v19, v19, v22\n"
"L_rt_ls0_%=:\n"
"s_nop 1\n"
"s_waitcnt lgkmcnt(6)\n"
"v_mfma_f32_16x16x32_bf16 a[0:3], a[192:195], v[96:99], a[0:3]\n"
"ds_read_b128 a[248:251], v4 offset:14336\n"
"v_mfma_f32_16x16x32_bf16 a[4:7], a[192:195], v[100:103], a[4:7]\n"
"s_add_u32 m0, s65, 98304\n"
"s_nop 0\n"
"buffer_load_dwordx4 v1, s[44:47], s61 offen offset:0 sc1 lds\n"
"v_mfma_f32_16x16x32_bf16 a[8:11], a[196:199], v[96:99], a[8:11]\n"
"ds_read_b128 a[252:255], v4 offset:15360\n"
"v_mfma_f32_16x16x32_bf16 a[12:15], a[196:199], v[100:103], a[12:15]\n"
"buffer_load_dwordx4 v1, s[44:47], s61 offen offset:1024 sc1 lds\n"
"v_mfma_f32_16x16x32_bf16 a[16:19], a[200:203], v[96:99], a[16:19]\n"
"ds_read_b128 a[192:195], v4 offset:16384\n"
"v_mfma_f32_16x16x32_bf16 a[20:23], a[200:203], v[100:103], a[20:23]\n"
"buffer_load_dwordx4 v1, s[44:47], s61 offen offset:2048 sc1 lds\n"
"v_mfma_f32_16x16x32_bf16 a[24:27], a[204:207], v[96:99], a[24:27]\n"
"ds_read_b128 a[196:199], v4 offset:17408\n"
"v_mfma_f32_16x16x32_bf16 a[28:31], a[204:207], v[100:103], a[28:31]\n"
"v_mfma_f32_16x16x32_bf16 a[32:35], a[208:211], v[96:99], a[32:35]\n"
"ds_read_b128 a[200:203], v4 offset:18432\n"
"buffer_load_dwordx4 v1, s[44:47], s61 offen offset:3072 sc1 lds\n"
"v_mfma_f32_16x16x32_bf16 a[36:39], a[208:211], v[100:103], a[36:39]\n"
"v_mfma_f32_16x16x32_bf16 a[40:43], a[212:215], v[96:99], a[40:43]\n"
"ds_read_b128 a[204:207], v4 offset:19456\n"
"s_add_u32 m0, s65, 102400\n"
"s_nop 0\n"
"buffer_load_dwordx4 v2, s[44:47], s61 offen offset:0 sc1 lds\n"
"v_mfma_f32_16x16x32_bf16 a[44:47], a[212:215], v[100:103], a[44:47]\n"
"v_mfma_f32_16x16x32_bf16 a[48:51], a[216:219], v[96:99], a[48:51]\n"
"ds_read_b128 a[208:211], v4 offset:20480\n"
"buffer_load_dwordx4 v2, s[44:47], s61 offen offset:1024 sc1 lds\n"
"v_mfma_f32_16x16x32_bf16 a[52:55], a[216:219], v[100:103], a[52:55]\n"
"v_mfma_f32_16x16x32_bf16 a[56:59], a[220:223], v[96:99], a[56:59]\n"
"ds_read_b128 a[212:215], v4 offset:21504\n"
"v_mfma_f32_16x16x32_bf16 a[60:63], a[220:223], v[100:103], a[60:63]\n"
"s_waitcnt lgkmcnt(6)\n"
"buffer_load_dwordx4 v2, s[44:47], s61 offen offset:2048 sc1 lds\n"
"v_mfma_f32_16x16x32_bf16 a[64:67], a[224:227], v[96:99], a[64:67]\n"
"ds_read_b128 a[216:219], v4 offset:22528\n"
"v_mfma_f32_16x16x32_bf16 a[68:71], a[224:227], v[100:103], a[68:71]\n"
"buffer_load_dwordx4 v2, s[44:47], s61 offen offset:3072 sc1 lds\n"
"v_mfma_f32_16x16x32_bf16 a[72:75], a[228:231], v[96:99], a[72:75]\n"
"ds_read_b128 a[220:223], v4 offset:23552\n"
"v_mfma_f32_16x16x32_bf16 a[76:79], a[228:231], v[100:103], a[76:79]\n"
"buffer_load_dwordx4 a[128:131], v1, s[36:39], s79 offen offset:0 sc1\n"
"v_mfma_f32_16x16x32_bf16 a[80:83], a[232:235], v[96:99], a[80:83]\n"
"ds_read_b128 a[224:227], v4 offset:24576\n"
"v_mfma_f32_16x16x32_bf16 a[84:87], a[232:235], v[100:103], a[84:87]\n"
"v_mfma_f32_16x16x32_bf16 a[88:91], a[236:239], v[96:99], a[88:91]\n"
"ds_read_b128 a[228:231], v4 offset:25600\n"
"buffer_load_dwordx4 a[132:135], v1, s[36:39], s79 offen offset:1024 sc1\n"
"v_mfma_f32_16x16x32_bf16 a[92:95], a[236:239], v[100:103], a[92:95]\n"
"v_mfma_f32_16x16x32_bf16 a[96:99], a[240:243], v[96:99], a[96:99]\n"
"ds_read_b128 a[232:235], v4 offset:26624\n"
"buffer_load_dwordx4 a[136:139], v1, s[36:39], s79 offen offset:2048 sc1\n"
"v_mfma_f32_16x16x32_bf16 a[100:103], a[240:243], v[100:103], a[100:103]\n"
"v_mfma_f32_16x16x32_bf16 a[104:107], a[244:247], v[96:99], a[104:107]\n"
"ds_read_b128 a[236:239], v4 offset:27648\n"
"buffer_load_dwordx4 a[140:143], v1, s[36:39], s79 offen offset:3072 sc1\n"
"v_mfma_f32_16x16x32_bf16 a[108:111], a[244:247], v[100:103], a[108:111]\n"
"v_mfma_f32_16x16x32_bf16 a[112:115], a[248:251], v[96:99], a[112:115]\n"
"ds_read_b128 a[240:243], v4 offset:28672\n"
"v_mfma_f32_16x16x32_bf16 a[116:119], a[248:251], v[100:103], a[116:119]\n"
"buffer_load_dwordx4 a[144:147], v2, s[36:39], s79 offen offset:0 sc1\n"
"v_mfma_f32_16x16x32_bf16 a[120:123], a[252:255], v[96:99], a[120:123]\n"
"ds_read_b128 a[244:247], v4 offset:29696\n"
"v_mfma_f32_16x16x32_bf16 a[124:127], a[252:255], v[100:103], a[124:127]\n"
"s_waitcnt lgkmcnt(6)\n"
"buffer_load_dwordx4 a[148:151], v2, s[36:39], s79 offen offset:1024 sc1\n"
"v_mfma_f32_16x16x32_bf16 a[0:3], a[192:195], v[104:107], a[0:3]\n"
"ds_read_b128 a[248:251], v4 offset:30720\n"
"v_mfma_f32_16x16x32_bf16 a[4:7], a[192:195], v[108:111], a[4:7]\n"
"buffer_load_dwordx4 a[152:155], v2, s[36:39], s79 offen offset:2048 sc1\n"
"v_mfma_f32_16x16x32_bf16 a[8:11], a[196:199], v[104:107], a[8:11]\n"
"ds_read_b128 a[252:255], v4 offset:31744\n"
"v_mfma_f32_16x16x32_bf16 a[12:15], a[196:199], v[108:111], a[12:15]\n"
"v_mfma_f32_16x16x32_bf16 a[16:19], a[200:203], v[104:107], a[16:19]\n"
"ds_read_b128 a[192:195], v3 offset:32768\n"
"buffer_load_dwordx4 a[156:159], v2, s[36:39], s79 offen offset:3072 sc1\n"
"v_mfma_f32_16x16x32_bf16 a[20:23], a[200:203], v[108:111], a[20:23]\n"
"v_mfma_f32_16x16x32_bf16 a[24:27], a[204:207], v[104:107], a[24:27]\n"
"ds_read_b128 a[196:199], v3 offset:33792\n"
"buffer_load_dwordx4 a[160:163], v1, s[36:39], s99 offen offset:0 sc1\n"
"v_mfma_f32_16x16x32_bf16 a[28:31], a[204:207], v[108:111], a[28:31]\n"
"v_mfma_f32_16x16x32_bf16 a[32:35], a[208:211], v[104:107], a[32:35]\n"
"ds_read_b128 a[200:203], v3 offset:34816\n"
"buffer_load_dwordx4 a[164:167], v1, s[36:39], s99 offen offset:1024 sc1\n"
"v_mfma_f32_16x16x32_bf16 a[36:39], a[208:211], v[108:111], a[36:39]\n"
"v_mfma_f32_16x16x32_bf16 a[40:43], a[212:215], v[104:107], a[40:43]\n"
"ds_read_b128 a[204:207], v3 offset:35840\n"
"v_mfma_f32_16x16x32_bf16 a[44:47], a[212:215], v[108:111], a[44:47]\n"
"buffer_load_dwordx4 a[168:171], v1, s[36:39], s99 offen offset:2048 sc1\n"
"v_mfma_f32_16x16x32_bf16 a[48:51], a[216:219], v[104:107], a[48:51]\n"
"ds_read_b128 a[208:211], v3 offset:36864\n"
"v_mfma_f32_16x16x32_bf16 a[52:55], a[216:219], v[108:111], a[52:55]\n"
"buffer_load_dwordx4 a[172:175], v1, s[36:39], s99 offen offset:3072 sc1\n"
"v_mfma_f32_16x16x32_bf16 a[56:59], a[220:223], v[104:107], a[56:59]\n"
"ds_read_b128 a[212:215], v3 offset:37888\n"
"v_mfma_f32_16x16x32_bf16 a[60:63], a[220:223], v[108:111], a[60:63]\n"
"s_waitcnt lgkmcnt(6)\n"
"buffer_load_dwordx4 a[176:179], v2, s[36:39], s99 offen offset:0 sc1\n"
"v_mfma_f32_16x16x32_bf16 a[64:67], a[224:227], v[104:107], a[64:67]\n"
"ds_read_b128 a[216:219], v3 offset:38912\n"
"v_mfma_f32_16x16x32_bf16 a[68:71], a[224:227], v[108:111], a[68:71]\n"
"v_mfma_f32_16x16x32_bf16 a[72:75], a[228:231], v[104:107], a[72:75]\n"
"ds_read_b128 a[220:223], v3 offset:39936\n"
"buffer_load_dwordx4 a[180:183], v2, s[36:39], s99 offen offset:1024 sc1\n"
"v_mfma_f32_16x16x32_bf16 a[76:79], a[228:231], v[108:111], a[76:79]\n"
"v_mfma_f32_16x16x32_bf16 a[80:83], a[232:235], v[104:107], a[80:83]\n"
"ds_read_b128 a[224:227], v3 offset:40960\n"
"buffer_load_dwordx4 a[184:187], v2, s[36:39], s99 offen offset:2048 sc1\n"
"v_mfma_f32_16x16x32_bf16 a[84:87], a[232:235], v[108:111], a[84:87]\n"
"v_mfma_f32_16x16x32_bf16 a[88:91], a[236:239], v[104:107], a[88:91]\n"
"ds_read_b128 a[228:231], v3 offset:41984\n"
"buffer_load_dwordx4 a[188:191], v2, s[36:39], s99 offen offset:3072 sc1\n"
"v_mfma_f32_16x16x32_bf16 a[92:95], a[236:239], v[108:111], a[92:95]\n"
"v_mfma_f32_16x16x32_bf16 a[96:99], a[240:243], v[104:107], a[96:99]\n"
"ds_read_b128 a[232:235], v3 offset:43008\n"
"v_mfma_f32_16x16x32_bf16 a[100:103], a[240:243], v[108:111], a[100:103]\n"
"v_mfma_f32_16x16x32_bf16 a[104:107], a[244:247], v[104:107], a[104:107]\n"
"ds_read_b128 a[236:239], v3 offset:44032\n"
"v_mfma_f32_16x16x32_bf16 a[108:111], a[244:247], v[108:111], a[108:111]\n"
"v_mfma_f32_16x16x32_bf16 a[112:115], a[248:251], v[104:107], a[112:115]\n"
"ds_read_b128 a[240:243], v3 offset:45056\n"
"v_mfma_f32_16x16x32_bf16 a[116:119], a[248:251], v[108:111], a[116:119]\n"
"v_mfma_f32_16x16x32_bf16 a[120:123], a[252:255], v[104:107], a[120:123]\n"
"ds_read_b128 a[244:247], v3 offset:46080\n"
"v_mfma_f32_16x16x32_bf16 a[124:127], a[252:255], v[108:111], a[124:127]\n"
"s_nop 15\n"
"s_lshl_b32 s78, s68, 16\n"
"s_lshl_b32 s96, s49, 14\n"
"s_add_u32 s78, s78, s96\n"
"s_add_u32 s78, s78, 79691776\n"
"s_lshl_b32 s98, s68, 12\n"
"s_lshl_b32 s96, s49, 10\n"
"s_add_u32 s98, s98, s96\n"
"s_add_u32 s98, s98, 97517568\n"
"buffer_store_dwordx4 v[16:19], v1, s[36:39], s98 offen sc1\n"
"v_accvgpr_read_b32 v136, a0\n"
"v_accvgpr_read_b32 v137, a1\n"
"v_accvgpr_read_b32 v138, a2\n"
"v_accvgpr_read_b32 v139, a3\n"
"v_accvgpr_read_b32 v140, a8\n"
"v_accvgpr_read_b32 v141, a9\n"
"v_accvgpr_read_b32 v142, a10\n"
"v_accvgpr_read_b32 v143, a11\n"
"v_cvt_pk_bf16_f32 v168, v136, v137\n"
"v_cvt_pk_bf16_f32 v169, v138, v139\n"
"v_cvt_pk_bf16_f32 v170, v140, v141\n"
"v_cvt_pk_bf16_f32 v171, v142, v143\n"
"buffer_store_dwordx4 v[168:171], v1, s[36:39], s78 offen offset:0 sc1\n"
"v_accvgpr_read_b32 v144, a16\n"
"v_accvgpr_read_b32 v145, a17\n"
"v_accvgpr_read_b32 v146, a18\n"
"v_accvgpr_read_b32 v147, a19\n"
"v_accvgpr_read_b32 v148, a24\n"
"v_accvgpr_read_b32 v149, a25\n"
"v_accvgpr_read_b32 v150, a26\n"
"v_accvgpr_read_b32 v151, a27\n"
"v_cvt_pk_bf16_f32 v172, v144, v145\n"
"v_cvt_pk_bf16_f32 v173, v146, v147\n"
"v_cvt_pk_bf16_f32 v174, v148, v149\n"
"v_cvt_pk_bf16_f32 v175, v150, v151\n"
"buffer_store_dwordx4 v[172:175], v1, s[36:39], s78 offen offset:1024 sc1\n"
"v_accvgpr_read_b32 v152, a32\n"
"v_accvgpr_read_b32 v153, a33\n"
"v_accvgpr_read_b32 v154, a34\n"
"v_accvgpr_read_b32 v155, a35\n"
"v_accvgpr_read_b32 v156, a40\n"
"v_accvgpr_read_b32 v157, a41\n"
"v_accvgpr_read_b32 v158, a42\n"
"v_accvgpr_read_b32 v159, a43\n"
"v_cvt_pk_bf16_f32 v176, v152, v153\n"
"v_cvt_pk_bf16_f32 v177, v154, v155\n"
"v_cvt_pk_bf16_f32 v178, v156, v157\n"
"v_cvt_pk_bf16_f32 v179, v158, v159\n"
"buffer_store_dwordx4 v[176:179], v1, s[36:39], s78 offen offset:2048 sc1\n"
"v_accvgpr_read_b32 v160, a48\n"
"v_accvgpr_read_b32 v161, a49\n"
"v_accvgpr_read_b32 v162, a50\n"
"v_accvgpr_read_b32 v163, a51\n"
"v_accvgpr_read_b32 v164, a56\n"
"v_accvgpr_read_b32 v165, a57\n"
"v_accvgpr_read_b32 v166, a58\n"
"v_accvgpr_read_b32 v167, a59\n"
"v_cvt_pk_bf16_f32 v180, v160, v161\n"
"v_cvt_pk_bf16_f32 v181, v162, v163\n"
"v_cvt_pk_bf16_f32 v182, v164, v165\n"
"v_cvt_pk_bf16_f32 v183, v166, v167\n"
"buffer_store_dwordx4 v[180:183], v1, s[36:39], s78 offen offset:3072 sc1\n"
"v_accvgpr_read_b32 v136, a64\n"
"v_accvgpr_read_b32 v137, a65\n"
"v_accvgpr_read_b32 v138, a66\n"
"v_accvgpr_read_b32 v139, a67\n"
"v_accvgpr_read_b32 v140, a72\n"
"v_accvgpr_read_b32 v141, a73\n"
"v_accvgpr_read_b32 v142, a74\n"
"v_accvgpr_read_b32 v143, a75\n"
"v_cvt_pk_bf16_f32 v168, v136, v137\n"
"v_cvt_pk_bf16_f32 v169, v138, v139\n"
"v_cvt_pk_bf16_f32 v170, v140, v141\n"
"v_cvt_pk_bf16_f32 v171, v142, v143\n"
"buffer_store_dwordx4 v[168:171], v2, s[36:39], s78 offen offset:0 sc1\n"
"v_accvgpr_read_b32 v144, a80\n"
"v_accvgpr_read_b32 v145, a81\n"
"v_accvgpr_read_b32 v146, a82\n"
"v_accvgpr_read_b32 v147, a83\n"
"v_accvgpr_read_b32 v148, a88\n"
"v_accvgpr_read_b32 v149, a89\n"
"v_accvgpr_read_b32 v150, a90\n"
"v_accvgpr_read_b32 v151, a91\n"
"v_cvt_pk_bf16_f32 v172, v144, v145\n"
"v_cvt_pk_bf16_f32 v173, v146, v147\n"
"v_cvt_pk_bf16_f32 v174, v148, v149\n"
"v_cvt_pk_bf16_f32 v175, v150, v151\n"
"buffer_store_dwordx4 v[172:175], v2, s[36:39], s78 offen offset:1024 sc1\n"
"v_accvgpr_read_b32 v152, a96\n"
"v_accvgpr_read_b32 v153, a97\n"
"v_accvgpr_read_b32 v154, a98\n"
"v_accvgpr_read_b32 v155, a99\n"
"v_accvgpr_read_b32 v156, a104\n"
"v_accvgpr_read_b32 v157, a105\n"
"v_accvgpr_read_b32 v158, a106\n"
"v_accvgpr_read_b32 v159, a107\n"
"v_cvt_pk_bf16_f32 v176, v152, v153\n"
"v_cvt_pk_bf16_f32 v177, v154, v155\n"
"v_cvt_pk_bf16_f32 v178, v156, v157\n"
"v_cvt_pk_bf16_f32 v179, v158, v159\n"
"buffer_store_dwordx4 v[176:179], v2, s[36:39], s78 offen offset:2048 sc1\n"
"v_accvgpr_read_b32 v160, a112\n"
"v_accvgpr_read_b32 v161, a113\n"
"v_accvgpr_read_b32 v162, a114\n"
"v_accvgpr_read_b32 v163, a115\n"
"v_accvgpr_read_b32 v164, a120\n"
"v_accvgpr_read_b32 v165, a121\n"
"v_accvgpr_read_b32 v166, a122\n"
"v_accvgpr_read_b32 v167, a123\n"
"v_cvt_pk_bf16_f32 v180, v160, v161\n"
"v_cvt_pk_bf16_f32 v181, v162, v163\n"
"v_cvt_pk_bf16_f32 v182, v164, v165\n"
"v_cvt_pk_bf16_f32 v183, v166, v167\n"
"buffer_store_dwordx4 v[180:183], v2, s[36:39], s78 offen offset:3072 sc1\n"
"v_accvgpr_read_b32 v136, a4\n"
"v_accvgpr_read_b32 v137, a5\n"
"v_accvgpr_read_b32 v138, a6\n"
"v_accvgpr_read_b32 v139, a7\n"
"v_accvgpr_read_b32 v140, a12\n"
"v_accvgpr_read_b32 v141, a13\n"
"v_accvgpr_read_b32 v142, a14\n"
"v_accvgpr_read_b32 v143, a15\n"
"v_cvt_pk_bf16_f32 v168, v136, v137\n"
"v_cvt_pk_bf16_f32 v169, v138, v139\n"
"v_cvt_pk_bf16_f32 v170, v140, v141\n"
"v_cvt_pk_bf16_f32 v171, v142, v143\n"
"s_add_u32 s78, s78, 8192\n"
"buffer_store_dwordx4 v[168:171], v1, s[36:39], s78 offen offset:0 sc1\n"
"v_accvgpr_read_b32 v144, a20\n"
"v_accvgpr_read_b32 v145, a21\n"
"v_accvgpr_read_b32 v146, a22\n"
"v_accvgpr_read_b32 v147, a23\n"
"v_accvgpr_read_b32 v148, a28\n"
"v_accvgpr_read_b32 v149, a29\n"
"v_accvgpr_read_b32 v150, a30\n"
"v_accvgpr_read_b32 v151, a31\n"
"v_cvt_pk_bf16_f32 v172, v144, v145\n"
"v_cvt_pk_bf16_f32 v173, v146, v147\n"
"v_cvt_pk_bf16_f32 v174, v148, v149\n"
"v_cvt_pk_bf16_f32 v175, v150, v151\n"
"buffer_store_dwordx4 v[172:175], v1, s[36:39], s78 offen offset:1024 sc1\n"
"v_accvgpr_read_b32 v152, a36\n"
"v_accvgpr_read_b32 v153, a37\n"
"v_accvgpr_read_b32 v154, a38\n"
"v_accvgpr_read_b32 v155, a39\n"
"v_accvgpr_read_b32 v156, a44\n"
"v_accvgpr_read_b32 v157, a45\n"
"v_accvgpr_read_b32 v158, a46\n"
"v_accvgpr_read_b32 v159, a47\n"
"v_cvt_pk_bf16_f32 v176, v152, v153\n"
"v_cvt_pk_bf16_f32 v177, v154, v155\n"
"v_cvt_pk_bf16_f32 v178, v156, v157\n"
"v_cvt_pk_bf16_f32 v179, v158, v159\n"
"buffer_store_dwordx4 v[176:179], v1, s[36:39], s78 offen offset:2048 sc1\n"
"v_accvgpr_read_b32 v160, a52\n"
"v_accvgpr_read_b32 v161, a53\n"
"v_accvgpr_read_b32 v162, a54\n"
"v_accvgpr_read_b32 v163, a55\n"
"v_accvgpr_read_b32 v164, a60\n"
"v_accvgpr_read_b32 v165, a61\n"
"v_accvgpr_read_b32 v166, a62\n"
"v_accvgpr_read_b32 v167, a63\n"
"v_cvt_pk_bf16_f32 v180, v160, v161\n"
"v_cvt_pk_bf16_f32 v181, v162, v163\n"
"v_cvt_pk_bf16_f32 v182, v164, v165\n"
"v_cvt_pk_bf16_f32 v183, v166, v167\n"
"buffer_store_dwordx4 v[180:183], v1, s[36:39], s78 offen offset:3072 sc1\n"
"v_accvgpr_read_b32 v136, a68\n"
"v_accvgpr_read_b32 v137, a69\n"
"v_accvgpr_read_b32 v138, a70\n"
"v_accvgpr_read_b32 v139, a71\n"
"v_accvgpr_read_b32 v140, a76\n"
"v_accvgpr_read_b32 v141, a77\n"
"v_accvgpr_read_b32 v142, a78\n"
"v_accvgpr_read_b32 v143, a79\n"
"v_cvt_pk_bf16_f32 v168, v136, v137\n"
"v_cvt_pk_bf16_f32 v169, v138, v139\n"
"v_cvt_pk_bf16_f32 v170, v140, v141\n"
"v_cvt_pk_bf16_f32 v171, v142, v143\n"
"buffer_store_dwordx4 v[168:171], v2, s[36:39], s78 offen offset:0 sc1\n"
"v_accvgpr_read_b32 v144, a84\n"
"v_accvgpr_read_b32 v145, a85\n"
"v_accvgpr_read_b32 v146, a86\n"
"v_accvgpr_read_b32 v147, a87\n"
"v_accvgpr_read_b32 v148, a92\n"
"v_accvgpr_read_b32 v149, a93\n"
"v_accvgpr_read_b32 v150, a94\n"
"v_accvgpr_read_b32 v151, a95\n"
"v_cvt_pk_bf16_f32 v172, v144, v145\n"
"v_cvt_pk_bf16_f32 v173, v146, v147\n"
"v_cvt_pk_bf16_f32 v174, v148, v149\n"
"v_cvt_pk_bf16_f32 v175, v150, v151\n"
"buffer_store_dwordx4 v[172:175], v2, s[36:39], s78 offen offset:1024 sc1\n"
"v_accvgpr_read_b32 v152, a100\n"
"v_accvgpr_read_b32 v153, a101\n"
"v_accvgpr_read_b32 v154, a102\n"
"v_accvgpr_read_b32 v155, a103\n"
"v_accvgpr_read_b32 v156, a108\n"
"v_accvgpr_read_b32 v157, a109\n"
"v_accvgpr_read_b32 v158, a110\n"
"v_accvgpr_read_b32 v159, a111\n"
"v_cvt_pk_bf16_f32 v176, v152, v153\n"
"v_cvt_pk_bf16_f32 v177, v154, v155\n"
"v_cvt_pk_bf16_f32 v178, v156, v157\n"
"v_cvt_pk_bf16_f32 v179, v158, v159\n"
"buffer_store_dwordx4 v[176:179], v2, s[36:39], s78 offen offset:2048 sc1\n"
"v_accvgpr_read_b32 v160, a116\n"
"v_accvgpr_read_b32 v161, a117\n"
"v_accvgpr_read_b32 v162, a118\n"
"v_accvgpr_read_b32 v163, a119\n"
"v_accvgpr_read_b32 v164, a124\n"
"v_accvgpr_read_b32 v165, a125\n"
"v_accvgpr_read_b32 v166, a126\n"
"v_accvgpr_read_b32 v167, a127\n"
"v_cvt_pk_bf16_f32 v180, v160, v161\n"
"v_cvt_pk_bf16_f32 v181, v162, v163\n"
"v_cvt_pk_bf16_f32 v182, v164, v165\n"
"v_cvt_pk_bf16_f32 v183, v166, v167\n"
"buffer_store_dwordx4 v[180:183], v2, s[36:39], s78 offen offset:3072 sc1\n"
"s_mov_b32 s69, 2\n"
"s_mov_b32 s82, 0\n"
"s_lshl_b32 s83, s51, 1\n"
"s_add_u32 s83, s83, 2\n"
"s_lshl_b32 s96, s53, 1\n"
"s_add_u32 s97, s82, 1\n"
"s_min_u32 s98, s83, s96\n"
"s_max_u32 s98, s98, s97\n"
"s_sub_u32 s55, s98, s97\n"
"s_sub_u32 s56, s83, s98\n"
"s_mov_b32 s54, s98\n"
"s_waitcnt vmcnt(17)\n"
"s_waitcnt lgkmcnt(6)\n"
"v_mfma_f32_16x16x32_bf16 v[64:67], a[192:195], a[128:131], 0\n"
"ds_read_b128 a[248:251], v3 offset:47104\n"
"v_accvgpr_write_b32 a0, 0\n"
"v_accvgpr_write_b32 a1, 0\n"
"v_mfma_f32_16x16x32_bf16 v[68:71], a[192:195], a[160:163], 0\n"
"v_accvgpr_write_b32 a2, 0\n"
"v_accvgpr_write_b32 a3, 0\n"
"v_mfma_f32_16x16x32_bf16 v[64:67], a[196:199], a[132:135], v[64:67]\n"
"ds_read_b128 a[252:255], v3 offset:48128\n"
"v_accvgpr_write_b32 a4, 0\n"
"v_accvgpr_write_b32 a5, 0\n"
"v_mfma_f32_16x16x32_bf16 v[68:71], a[196:199], a[164:167], v[68:71]\n"
"v_accvgpr_write_b32 a6, 0\n"
"v_accvgpr_write_b32 a7, 0\n"
"v_mfma_f32_16x16x32_bf16 v[64:67], a[200:203], a[136:139], v[64:67]\n"
"ds_read_b128 a[192:195], v3 offset:49152\n"
"v_accvgpr_write_b32 a8, 0\n"
"v_accvgpr_write_b32 a9, 0\n"
"v_mfma_f32_16x16x32_bf16 v[68:71], a[200:203], a[168:171], v[68:71]\n"
"v_accvgpr_write_b32 a10, 0\n"
"v_accvgpr_write_b32 a11, 0\n"
"v_mfma_f32_16x16x32_bf16 v[64:67], a[204:207], a[140:143], v[64:67]\n"
"ds_read_b128 a[196:199], v3 offset:50176\n"
"v_accvgpr_write_b32 a12, 0\n"
"v_accvgpr_write_b32 a13, 0\n"
"v_mfma_f32_16x16x32_bf16 v[68:71], a[204:207], a[172:175], v[68:71]\n"
"v_accvgpr_write_b32 a14, 0\n"
"v_accvgpr_write_b32 a15, 0\n"
"v_mfma_f32_16x16x32_bf16 v[64:67], a[208:211], a[144:147], v[64:67]\n"
"ds_read_b128 a[200:203], v3 offset:51200\n"
"v_accvgpr_write_b32 a16, 0\n"
"v_accvgpr_write_b32 a17, 0\n"
"v_mfma_f32_16x16x32_bf16 v[68:71], a[208:211], a[176:179], v[68:71]\n"
"v_accvgpr_write_b32 a18, 0\n"
"v_accvgpr_write_b32 a19, 0\n"
"v_mfma_f32_16x16x32_bf16 v[64:67], a[212:215], a[148:151], v[64:67]\n"
"ds_read_b128 a[204:207], v3 offset:52224\n"
"v_accvgpr_write_b32 a20, 0\n"
"v_accvgpr_write_b32 a21, 0\n"
"v_mfma_f32_16x16x32_bf16 v[68:71], a[212:215], a[180:183], v[68:71]\n"
"v_accvgpr_write_b32 a22, 0\n"
"v_accvgpr_write_b32 a23, 0\n"
"v_mfma_f32_16x16x32_bf16 v[64:67], a[216:219], a[152:155], v[64:67]\n"
"ds_read_b128 a[208:211], v3 offset:53248\n"
"v_accvgpr_write_b32 a24, 0\n"
"v_accvgpr_write_b32 a25, 0\n"
"v_mfma_f32_16x16x32_bf16 v[68:71], a[216:219], a[184:187], v[68:71]\n"
"v_accvgpr_write_b32 a26, 0\n"
"v_accvgpr_write_b32 a27, 0\n"
"v_mfma_f32_16x16x32_bf16 v[64:67], a[220:223], a[156:159], v[64:67]\n"
"ds_read_b128 a[212:215], v3 offset:54272\n"
"v_accvgpr_write_b32 a28, 0\n"
"v_accvgpr_write_b32 a29, 0\n"
"v_mfma_f32_16x16x32_bf16 v[68:71], a[220:223], a[188:191], v[68:71]\n"
"s_waitcnt lgkmcnt(6)\n"
"v_accvgpr_write_b32 a30, 0\n"
"v_accvgpr_write_b32 a31, 0\n"
"v_mfma_f32_16x16x32_bf16 v[72:75], a[224:227], a[128:131], 0\n"
"ds_read_b128 a[216:219], v3 offset:55296\n"
"v_accvgpr_write_b32 a32, 0\n"
"v_accvgpr_write_b32 a33, 0\n"
"v_mfma_f32_16x16x32_bf16 v[76:79], a[224:227], a[160:163], 0\n"
"v_accvgpr_write_b32 a34, 0\n"
"v_accvgpr_write_b32 a35, 0\n"
"v_mfma_f32_16x16x32_bf16 v[72:75], a[228:231], a[132:135], v[72:75]\n"
"ds_read_b128 a[220:223], v3 offset:56320\n"
"v_accvgpr_write_b32 a36, 0\n"
"v_accvgpr_write_b32 a37, 0\n"
"v_mfma_f32_16x16x32_bf16 v[76:79], a[228:231], a[164:167], v[76:79]\n"
"v_accvgpr_write_b32 a38, 0\n"
"v_accvgpr_write_b32 a39, 0\n"
"v_mfma_f32_16x16x32_bf16 v[72:75], a[232:235], a[136:139], v[72:75]\n"
"ds_read_b128 a[224:227], v3 offset:57344\n"
"v_accvgpr_write_b32 a40, 0\n"
"v_accvgpr_write_b32 a41, 0\n"
"v_mfma_f32_16x16x32_bf16 v[76:79], a[232:235], a[168:171], v[76:79]\n"
"v_accvgpr_write_b32 a42, 0\n"
"v_accvgpr_write_b32 a43, 0\n"
"v_mfma_f32_16x16x32_bf16 v[72:75], a[236:239], a[140:143], v[72:75]\n"
"ds_read_b128 a[228:231], v3 offset:58368\n"
"v_accvgpr_write_b32 a44, 0\n"
"v_accvgpr_write_b32 a45, 0\n"
"v_mfma_f32_16x16x32_bf16 v[76:79], a[236:239], a[172:175], v[76:79]\n"
"v_accvgpr_write_b32 a46, 0\n"
"v_accvgpr_write_b32 a47, 0\n"
"v_mfma_f32_16x16x32_bf16 v[72:75], a[240:243], a[144:147], v[72:75]\n"
"ds_read_b128 a[232:235], v3 offset:59392\n"
"v_accvgpr_write_b32 a48, 0\n"
"v_accvgpr_write_b32 a49, 0\n"
"v_mfma_f32_16x16x32_bf16 v[76:79], a[240:243], a[176:179], v[76:79]\n"
"v_accvgpr_write_b32 a50, 0\n"
"v_accvgpr_write_b32 a51, 0\n"
"v_mfma_f32_16x16x32_bf16 v[72:75], a[244:247], a[148:151], v[72:75]\n"
"ds_read_b128 a[236:239], v3 offset:60416\n"
"v_accvgpr_write_b32 a52, 0\n"
"v_accvgpr_write_b32 a53, 0\n"
"v_mfma_f32_16x16x32_bf16 v[76:79], a[244:247], a[180:183], v[76:79]\n"
"v_accvgpr_write_b32 a54, 0\n"
"v_accvgpr_write_b32 a55, 0\n"
"v_mfma_f32_16x16x32_bf16 v[72:75], a[248:251], a[152:155], v[72:75]\n"
"ds_read_b128 a[240:243], v3 offset:61440\n"
"v_accvgpr_write_b32 a56, 0\n"
"v_accvgpr_write_b32 a57, 0\n"
"v_mfma_f32_16x16x32_bf16 v[76:79], a[248:251], a[184:187], v[76:79]\n"
"v_accvgpr_write_b32 a58, 0\n"
"v_accvgpr_write_b32 a59, 0\n"
"v_mfma_f32_16x16x32_bf16 v[72:75], a[252:255], a[156:159], v[72:75]\n"
"ds_read_b128 a[244:247], v3 offset:62464\n"
"v_accvgpr_write_b32 a60, 0\n"
"v_accvgpr_write_b32 a61, 0\n"
"v_mfma_f32_16x16x32_bf16 v[76:79], a[252:255], a[188:191], v[76:79]\n"
"s_waitcnt lgkmcnt(6)\n"
"v_accvgpr_write_b32 a62, 0\n"
"v_accvgpr_write_b32 a63, 0\n"
"v_mfma_f32_16x16x32_bf16 v[80:83], a[192:195], a[128:131], 0\n"
"ds_read_b128 a[248:251], v3 offset:63488\n"
"v_accvgpr_write_b32 a64, 0\n"
"v_accvgpr_write_b32 a65, 0\n"
"v_mfma_f32_16x16x32_bf16 v[84:87], a[192:195], a[160:163], 0\n"
"v_accvgpr_write_b32 a66, 0\n"
"v_accvgpr_write_b32 a67, 0\n"
"v_mfma_f32_16x16x32_bf16 v[80:83], a[196:199], a[132:135], v[80:83]\n"
"ds_read_b128 a[252:255], v3 offset:64512\n"
"v_accvgpr_write_b32 a68, 0\n"
"v_accvgpr_write_b32 a69, 0\n"
"v_mfma_f32_16x16x32_bf16 v[84:87], a[196:199], a[164:167], v[84:87]\n"
"v_accvgpr_write_b32 a70, 0\n"
"v_accvgpr_write_b32 a71, 0\n"
"v_mfma_f32_16x16x32_bf16 v[80:83], a[200:203], a[136:139], v[80:83]\n"
"v_accvgpr_write_b32 a72, 0\n"
"v_accvgpr_write_b32 a73, 0\n"
"v_mfma_f32_16x16x32_bf16 v[84:87], a[200:203], a[168:171], v[84:87]\n"
"v_accvgpr_write_b32 a74, 0\n"
"v_accvgpr_write_b32 a75, 0\n"
"v_mfma_f32_16x16x32_bf16 v[80:83], a[204:207], a[140:143], v[80:83]\n"
"v_accvgpr_write_b32 a76, 0\n"
"v_accvgpr_write_b32 a77, 0\n"
"v_mfma_f32_16x16x32_bf16 v[84:87], a[204:207], a[172:175], v[84:87]\n"
"v_accvgpr_write_b32 a78, 0\n"
"v_accvgpr_write_b32 a79, 0\n"
"v_mfma_f32_16x16x32_bf16 v[80:83], a[208:211], a[144:147], v[80:83]\n"
"v_accvgpr_write_b32 a80, 0\n"
"v_accvgpr_write_b32 a81, 0\n"
"v_mfma_f32_16x16x32_bf16 v[84:87], a[208:211], a[176:179], v[84:87]\n"
"v_accvgpr_write_b32 a82, 0\n"
"v_accvgpr_write_b32 a83, 0\n"
"v_mfma_f32_16x16x32_bf16 v[80:83], a[212:215], a[148:151], v[80:83]\n"
"v_accvgpr_write_b32 a84, 0\n"
"v_accvgpr_write_b32 a85, 0\n"
"v_mfma_f32_16x16x32_bf16 v[84:87], a[212:215], a[180:183], v[84:87]\n"
"v_accvgpr_write_b32 a86, 0\n"
"v_accvgpr_write_b32 a87, 0\n"
"v_mfma_f32_16x16x32_bf16 v[80:83], a[216:219], a[152:155], v[80:83]\n"
"v_accvgpr_write_b32 a88, 0\n"
"v_accvgpr_write_b32 a89, 0\n"
"v_mfma_f32_16x16x32_bf16 v[84:87], a[216:219], a[184:187], v[84:87]\n"
"v_accvgpr_write_b32 a90, 0\n"
"v_accvgpr_write_b32 a91, 0\n"
"v_mfma_f32_16x16x32_bf16 v[80:83], a[220:223], a[156:159], v[80:83]\n"
"v_accvgpr_write_b32 a92, 0\n"
"v_accvgpr_write_b32 a93, 0\n"
"v_mfma_f32_16x16x32_bf16 v[84:87], a[220:223], a[188:191], v[84:87]\n"
"s_waitcnt lgkmcnt(0)\n"
"v_accvgpr_write_b32 a94, 0\n"
"v_accvgpr_write_b32 a95, 0\n"
"v_mfma_f32_16x16x32_bf16 v[88:91], a[224:227], a[128:131], 0\n"
"v_accvgpr_write_b32 a96, 0\n"
"v_accvgpr_write_b32 a97, 0\n"
"v_mfma_f32_16x16x32_bf16 v[92:95], a[224:227], a[160:163], 0\n"
"v_accvgpr_write_b32 a98, 0\n"
"v_accvgpr_write_b32 a99, 0\n"
"v_mfma_f32_16x16x32_bf16 v[88:91], a[228:231], a[132:135], v[88:91]\n"
"v_accvgpr_write_b32 a100, 0\n"
"v_accvgpr_write_b32 a101, 0\n"
"v_mfma_f32_16x16x32_bf16 v[92:95], a[228:231], a[164:167], v[92:95]\n"
"v_accvgpr_write_b32 a102, 0\n"
"v_accvgpr_write_b32 a103, 0\n"
"v_mfma_f32_16x16x32_bf16 v[88:91], a[232:235], a[136:139], v[88:91]\n"
"v_accvgpr_write_b32 a104, 0\n"
"v_accvgpr_write_b32 a105, 0\n"
"v_mfma_f32_16x16x32_bf16 v[92:95], a[232:235], a[168:171], v[92:95]\n"
"v_accvgpr_write_b32 a106, 0\n"
"v_accvgpr_write_b32 a107, 0\n"
"v_mfma_f32_16x16x32_bf16 v[88:91], a[236:239], a[140:143], v[88:91]\n"
"v_accvgpr_write_b32 a108, 0\n"
"v_accvgpr_write_b32 a109, 0\n"
"v_mfma_f32_16x16x32_bf16 v[92:95], a[236:239], a[172:175], v[92:95]\n"
"v_accvgpr_write_b32 a110, 0\n"
"v_accvgpr_write_b32 a111, 0\n"
"v_mfma_f32_16x16x32_bf16 v[88:91], a[240:243], a[144:147], v[88:91]\n"
"v_accvgpr_write_b32 a112, 0\n"
"v_accvgpr_write_b32 a113, 0\n"
"v_mfma_f32_16x16x32_bf16 v[92:95], a[240:243], a[176:179], v[92:95]\n"
"v_accvgpr_write_b32 a114, 0\n"
"v_accvgpr_write_b32 a115, 0\n"
"v_mfma_f32_16x16x32_bf16 v[88:91], a[244:247], a[148:151], v[88:91]\n"
"v_accvgpr_write_b32 a116, 0\n"
"v_accvgpr_write_b32 a117, 0\n"
"v_mfma_f32_16x16x32_bf16 v[92:95], a[244:247], a[180:183], v[92:95]\n"
"v_accvgpr_write_b32 a118, 0\n"
"v_accvgpr_write_b32 a119, 0\n"
"v_mfma_f32_16x16x32_bf16 v[88:91], a[248:251], a[152:155], v[88:91]\n"
"v_accvgpr_write_b32 a120, 0\n"
"v_accvgpr_write_b32 a121, 0\n"
"v_mfma_f32_16x16x32_bf16 v[92:95], a[248:251], a[184:187], v[92:95]\n"
"v_accvgpr_write_b32 a122, 0\n"
"v_accvgpr_write_b32 a123, 0\n"
"v_mfma_f32_16x16x32_bf16 v[88:91], a[252:255], a[156:159], v[88:91]\n"
"v_accvgpr_write_b32 a124, 0\n"
"v_accvgpr_write_b32 a125, 0\n"
"v_mfma_f32_16x16x32_bf16 v[92:95], a[252:255], a[188:191], v[92:95]\n"
"v_accvgpr_write_b32 a126, 0\n"
"v_accvgpr_write_b32 a127, 0\n"
"s_barrier\n"
"s_add_u32 s61, s60, 0x8000\n"
"s_sub_u32 s62, s62, 1\n"
"s_cbranch_scc1 L_ev_fs0_%=\n"
"L_evr_fs0_%=:\n"
"s_add_u32 m0, s65, 32768\n"
"s_nop 0\n"
"buffer_load_dwordx4 v1, s[40:43], s61 offen offset:0 sc1 lds\n"
"buffer_load_dwordx4 v1, s[40:43], s61 offen offset:1024 sc1 lds\n"
"buffer_load_dwordx4 v1, s[40:43], s61 offen offset:2048 sc1 lds\n"
"buffer_load_dwordx4 v1, s[40:43], s61 offen offset:3072 sc1 lds\n"
"s_add_u32 m0, s65, 36864\n"
"s_nop 0\n"
"buffer_load_dwordx4 v2, s[40:43], s61 offen offset:0 sc1 lds\n"
"buffer_load_dwordx4 v2, s[40:43], s61 offen offset:1024 sc1 lds\n"
"buffer_load_dwordx4 v2, s[40:43], s61 offen offset:2048 sc1 lds\n"
"buffer_load_dwordx4 v2, s[40:43], s61 offen offset:3072 sc1 lds\n"
"ds_read_b128 a[192:195], v3 offset:0\n"
"ds_read_b128 a[196:199], v3 offset:1024\n"
"ds_read_b128 a[200:203], v3 offset:2048\n"
"ds_read_b128 a[204:207], v3 offset:3072\n"
"ds_read_b128 a[208:211], v3 offset:4096\n"
"ds_read_b128 a[212:215], v3 offset:5120\n"
"ds_read_b128 a[216:219], v3 offset:6144\n"
"ds_read_b128 a[220:223], v3 offset:7168\n"
"ds_read_b128 a[224:227], v3 offset:8192\n"
"ds_read_b128 a[228:231], v3 offset:9216\n"
"ds_read_b128 a[232:235], v3 offset:10240\n"
"ds_read_b128 a[236:239], v3 offset:11264\n"
"ds_read_b128 a[240:243], v3 offset:12288\n"
"ds_read_b128 a[244:247], v3 offset:13312\n"
"v_mov_b32 v16, 0\n"
"v_mov_b32 v18, 0\n"
"v_mov_b32 v17, 0\n"
"v_mov_b32 v19, 0\n"
"s_lshl_b32 s96, s53, 1\n"
"s_cmp_lt_u32 s82, s96\n"
"s_cbranch_scc1 L_fnm_fs0_%=\n"
"s_lshl_b32 s96, s82, 6\n"
"v_subrev_u32 v5, s96, v7\n"
"v_subrev_u32 v6, s96, v8\n"
"s_nop 7\n"
"v_cmp_gt_i32_e64 s[70:71], 0, v5\n"
"v_cmp_gt_i32_e64 s[72:73], 1, v5\n"
"v_cmp_gt_i32_e64 s[74:75], 2, v5\n"
"v_cndmask_b32_e64 v64, v64, v9, s[70:71]\n"
"v_cmp_gt_i32_e64 s[76:77], 3, v5\n"
"v_cndmask_b32_e64 v65, v65, v9, s[72:73]\n"
"v_cmp_gt_i32_e64 s[70:71], 0, v6\n"
"v_cndmask_b32_e64 v66, v66, v9, s[74:75]\n"
"v_cmp_gt_i32_e64 s[72:73], 1, v6\n"
"v_cndmask_b32_e64 v67, v67, v9, s[76:77]\n"
"v_cmp_gt_i32_e64 s[74:75], 2, v6\n"
"v_cndmask_b32_e64 v68, v68, v9, s[70:71]\n"
"v_cmp_gt_i32_e64 s[76:77], 3, v6\n"
"v_cndmask_b32_e64 v69, v69, v9, s[72:73]\n"
"v_cmp_gt_i32_e64 s[70:71], 16, v5\n"
"v_cndmask_b32_e64 v70, v70, v9, s[74:75]\n"
"v_cmp_gt_i32_e64 s[72:73], 17, v5\n"
"v_cndmask_b32_e64 v71, v71, v9, s[76:77]\n"
"v_cmp_gt_i32_e64 s[74:75], 18, v5\n"
"v_cndmask_b32_e64 v72, v72, v9, s[70:71]\n"
"v_cmp_gt_i32_e64 s[76:77], 19, v5\n"
"v_cndmask_b32_e64 v73, v73, v9, s[72:73]\n"
"v_cmp_gt_i32_e64 s[70:71], 16, v6\n"
"v_cndmask_b32_e64 v74, v74, v9, s[74:75]\n"
"v_cmp_gt_i32_e64 s[72:73], 17, v6\n"
"v_cndmask_b32_e64 v75, v75, v9, s[76:77]\n"
"v_cmp_gt_i32_e64 s[74:75], 18, v6\n"
"v_cndmask_b32_e64 v76, v76, v9, s[70:71]\n"
"v_cmp_gt_i32_e64 s[76:77], 19, v6\n"
"v_cndmask_b32_e64 v77, v77, v9, s[72:73]\n"
"v_cmp_gt_i32_e64 s[70:71], 32, v5\n"
"v_cndmask_b32_e64 v78, v78, v9, s[74:75]\n"
"v_cmp_gt_i32_e64 s[72:73], 33, v5\n"
"v_cndmask_b32_e64 v79, v79, v9, s[76:77]\n"
"v_cmp_gt_i32_e64 s[74:75], 34, v5\n"
"v_cndmask_b32_e64 v80, v80, v9, s[70:71]\n"
"v_cmp_gt_i32_e64 s[76:77], 35, v5\n"
"v_cndmask_b32_e64 v81, v81, v9, s[72:73]\n"
"v_cmp_gt_i32_e64 s[70:71], 32, v6\n"
"v_cndmask_b32_e64 v82, v82, v9, s[74:75]\n"
"v_cmp_gt_i32_e64 s[72:73], 33, v6\n"
"v_cndmask_b32_e64 v83, v83, v9, s[76:77]\n"
"v_cmp_gt_i32_e64 s[74:75], 34, v6\n"
"v_cndmask_b32_e64 v84, v84, v9, s[70:71]\n"
"v_cmp_gt_i32_e64 s[76:77], 35, v6\n"
"v_cndmask_b32_e64 v85, v85, v9, s[72:73]\n"
"v_cmp_gt_i32_e64 s[70:71], 48, v5\n"
"v_cndmask_b32_e64 v86, v86, v9, s[74:75]\n"
"v_cmp_gt_i32_e64 s[72:73], 49, v5\n"
"v_cndmask_b32_e64 v87, v87, v9, s[76:77]\n"
"v_cmp_gt_i32_e64 s[74:75], 50, v5\n"
"v_cndmask_b32_e64 v88, v88, v9, s[70:71]\n"
"v_cmp_gt_i32_e64 s[76:77], 51, v5\n"
"v_cndmask_b32_e64 v89, v89, v9, s[72:73]\n"
"v_cmp_gt_i32_e64 s[70:71], 48, v6\n"
"v_cndmask_b32_e64 v90, v90, v9, s[74:75]\n"
"v_cmp_gt_i32_e64 s[72:73], 49, v6\n"
"v_cndmask_b32_e64 v91, v91, v9, s[76:77]\n"
"v_cmp_gt_i32_e64 s[74:75], 50, v6\n"
"v_cndmask_b32_e64 v92, v92, v9, s[70:71]\n"
"v_cmp_gt_i32_e64 s[76:77], 51, v6\n"
"v_cndmask_b32_e64 v93, v93, v9, s[72:73]\n"
"s_nop 0\n"
"v_cndmask_b32_e64 v94, v94, v9, s[74:75]\n"
"s_nop 0\n"
"v_cndmask_b32_e64 v95, v95, v9, s[76:77]\n"
"L_fnm_fs0_%=:\n"
"s_nop 7\n"
"v_max3_f32 v24, v64, v65, v66\n"
"v_max3_f32 v24, v24, v67, v72\n"
"v_max3_f32 v24, v24, v73, v74\n"
"v_max3_f32 v24, v24, v75, v80\n"
"v_max3_f32 v24, v24, v81, v82\n"
"v_max3_f32 v24, v24, v83, v88\n"
"v_max3_f32 v24, v24, v89, v90\n"
"v_max_f32 v24, v24, v91\n"
"v_max3_f32 v25, v68, v69, v70\n"
"v_max3_f32 v25, v25, v71, v76\n"
"v_max3_f32 v25, v25, v77, v78\n"
"v_max3_f32 v25, v25, v79, v84\n"
"v_max3_f32 v25, v25, v85, v86\n"
"v_max3_f32 v25, v25, v87, v92\n"
"v_max3_f32 v25, v25, v93, v94\n"
"v_max_f32 v25, v25, v95\n"
"v_mov_b32 v11, v24\n"
"s_nop 1\n"
"v_permlane16_swap_b32 v24, v11\n"
"s_nop 1\n"
"v_max_f32 v24, v24, v11\n"
"v_mov_b32 v11, v24\n"
"s_nop 1\n"
"v_permlane32_swap_b32 v24, v11\n"
"s_nop 1\n"
"v_max_f32 v24, v24, v11\n"
"v_max_f32 v24, s67, v24\n"
"v_mov_b32 v11, v25\n"
"s_nop 1\n"
"v_permlane16_swap_b32 v25, v11\n"
"s_nop 1\n"
"v_max_f32 v25, v25, v11\n"
"v_mov_b32 v11, v25\n"
"s_nop 1\n"
"v_permlane32_swap_b32 v25, v11\n"
"s_nop 1\n"
"v_max_f32 v25, v25, v11\n"
"v_max_f32 v25, s67, v25\n"
"v_add_f32 v16, v16, v24\n"
"v_sub_f32 v128, 0, v16\n"
"v_sub_f32 v129, 0, v16\n"
"v_sub_f32 v130, 0, v16\n"
"v_sub_f32 v131, 0, v16\n"
"v_add_f32 v17, v17, v25\n"
"v_sub_f32 v132, 0, v17\n"
"v_sub_f32 v133, 0, v17\n"
"v_sub_f32 v134, 0, v17\n"
"v_sub_f32 v135, 0, v17\n"
"v_sub_f32 v64, v64, v24\n"
"v_sub_f32 v65, v65, v24\n"
"v_sub_f32 v66, v66, v24\n"
"v_sub_f32 v67, v67, v24\n"
"v_sub_f32 v68, v68, v25\n"
"v_sub_f32 v69, v69, v25\n"
"v_sub_f32 v70, v70, v25\n"
"v_sub_f32 v71, v71, v25\n"
"v_sub_f32 v72, v72, v24\n"
"v_sub_f32 v73, v73, v24\n"
"v_sub_f32 v74, v74, v24\n"
"v_sub_f32 v75, v75, v24\n"
"v_sub_f32 v76, v76, v25\n"
"v_sub_f32 v77, v77, v25\n"
"v_sub_f32 v78, v78, v25\n"
"v_sub_f32 v79, v79, v25\n"
"v_sub_f32 v80, v80, v24\n"
"v_sub_f32 v81, v81, v24\n"
"v_sub_f32 v82, v82, v24\n"
"v_sub_f32 v83, v83, v24\n"
"v_sub_f32 v84, v84, v25\n"
"v_sub_f32 v85, v85, v25\n"
"v_sub_f32 v86, v86, v25\n"
"v_sub_f32 v87, v87, v25\n"
"v_sub_f32 v88, v88, v24\n"
"v_sub_f32 v89, v89, v24\n"
"v_sub_f32 v90, v90, v24\n"
"v_sub_f32 v91, v91, v24\n"
"v_sub_f32 v92, v92, v25\n"
"v_sub_f32 v93, v93, v25\n"
"v_sub_f32 v94, v94, v25\n"
"v_sub_f32 v95, v95, v25\n"
"v_exp_f32 v136, v64\n"
"v_exp_f32 v137, v65\n"
"v_exp_f32 v138, v66\n"
"v_exp_f32 v139, v67\n"
"v_exp_f32 v140, v68\n"
"v_exp_f32 v141, v69\n"
"v_exp_f32 v142, v70\n"
"v_exp_f32 v143, v71\n"
"v_exp_f32 v144, v72\n"
"v_exp_f32 v145, v73\n"
"v_exp_f32 v146, v74\n"
"v_exp_f32 v147, v75\n"
"v_exp_f32 v148, v76\n"
"v_exp_f32 v149, v77\n"
"v_exp_f32 v150, v78\n"
"v_exp_f32 v151, v79\n"
"v_exp_f32 v152, v80\n"
"v_exp_f32 v153, v81\n"
"v_exp_f32 v154, v82\n"
"v_exp_f32 v155, v83\n"
"v_exp_f32 v156, v84\n"
"v_exp_f32 v157, v85\n"
"v_exp_f32 v158, v86\n"
"v_exp_f32 v159, v87\n"
"v_exp_f32 v160, v88\n"
"v_exp_f32 v161, v89\n"
"v_exp_f32 v162, v90\n"
"v_exp_f32 v163, v91\n"
"v_exp_f32 v164, v92\n"
"s_nop 0\n"
"v_add_f32 v20, v136, v138\n"
"v_add_f32 v21, v137, v139\n"
"v_cvt_pk_bf16_f32 v112, v136, v137\n"
"v_cvt_pk_bf16_f32 v113, v138, v139\n"
"v_add_f32 v22, v140, v142\n"
"v_add_f32 v23, v141, v143\n"
"v_cvt_pk_bf16_f32 v116, v140, v141\n"
"v_cvt_pk_bf16_f32 v117, v142, v143\n"
"v_add_f32 v20, v20, v144\n"
"v_add_f32 v21, v21, v145\n"
"v_add_f32 v20, v20, v146\n"
"v_add_f32 v21, v21, v147\n"
"v_cvt_pk_bf16_f32 v114, v144, v145\n"
"v_cvt_pk_bf16_f32 v115, v146, v147\n"
"v_add_f32 v22, v22, v148\n"
"v_add_f32 v23, v23, v149\n"
"v_add_f32 v22, v22, v150\n"
"v_add_f32 v23, v23, v151\n"
"v_cvt_pk_bf16_f32 v118, v148, v149\n"
"v_cvt_pk_bf16_f32 v119, v150, v151\n"
"v_add_f32 v20, v20, v152\n"
"v_add_f32 v21, v21, v153\n"
"v_add_f32 v20, v20, v154\n"
"v_add_f32 v21, v21, v155\n"
"v_cvt_pk_bf16_f32 v120, v152, v153\n"
"v_cvt_pk_bf16_f32 v121, v154, v155\n"
"v_add_f32 v22, v22, v156\n"
"v_add_f32 v23, v23, v157\n"
"v_add_f32 v22, v22, v158\n"
"v_add_f32 v23, v23, v159\n"
"v_cvt_pk_bf16_f32 v124, v156, v157\n"
"v_cvt_pk_bf16_f32 v125, v158, v159\n"
"v_add_f32 v20, v20, v160\n"
"v_add_f32 v21, v21, v161\n"
"v_add_f32 v20, v20, v162\n"
"v_add_f32 v21, v21, v163\n"
"v_cvt_pk_bf16_f32 v122, v160, v161\n"
"v_cvt_pk_bf16_f32 v123, v162, v163\n"
"v_add_f32 v22, v22, v164\n"
"s_sub_u32 s55, s55, 1\n"
"s_cbranch_scc0 L_body00_%=\n"
"s_cmp_eq_u32 s56, 0\n"
"s_cbranch_scc1 L_last1_%=\n"
"s_sub_u32 s56, s56, 1\n"
"s_branch L_body01_%=\n"
"L_last1_%=:\n"
"s_cmp_eq_u32 s69, 1\n"
"s_cbranch_scc1 L_lastS1_%=\n"
"s_cmp_eq_u32 s69, 0\n"
"s_cbranch_scc1 L_lastM1_%=\n"
"s_waitcnt vmcnt(0) lgkmcnt(0)\n"
"s_barrier\n"
"ds_read_b128 a[192:195], v4 offset:32768\n"
"ds_read_b128 a[196:199], v4 offset:33792\n"
"ds_read_b128 a[200:203], v4 offset:34816\n"
"ds_read_b128 a[204:207], v4 offset:35840\n"
"ds_read_b128 a[208:211], v4 offset:36864\n"
"ds_read_b128 a[212:215], v4 offset:37888\n"
"ds_read_b128 a[216:219], v4 offset:38912\n"
"ds_read_b128 a[220:223], v4 offset:39936\n"
"ds_read_b128 a[224:227], v4 offset:40960\n"
"ds_read_b128 a[228:231], v4 offset:41984\n"
"ds_read_b128 a[232:235], v4 offset:43008\n"
"ds_read_b128 a[236:239], v4 offset:44032\n"
"ds_read_b128 a[240:243], v4 offset:45056\n"
"ds_read_b128 a[244:247], v4 offset:46080\n"
"s_lshl_b32 s96, s53, 5\n"
"s_add_u32 s96, s96, 126881792\n"
"v_lshlrev_b32 v237, 2, v0\n"
"v_add_u32 v237, s96, v237\n"
"s_lshl_b32 s97, s68, 4\n"
"s_add_u32 s97, s97, 126882816\n"
"s_cmp_eq_u32 s69, 0\n"
"s_cselect_b32 s97, s97, s96\n"
"v_mov_b32 v14, s97\n"
"v_cmp_gt_u32 vcc, 8, v0\n"
"s_nop 1\n"
"v_cndmask_b32 v237, v14, v237, vcc\n"
"v_cmp_ne_u32 vcc, s35, v236\n"
"s_nop 4\n"
"s_and_b32 vcc_lo, vcc_lo, 511\n"
"s_cbranch_scc0 L_vpd_ll1_%=\n"
"s_mov_b32 s99, 0\n"
"L_vpl_ll1_%=:\n"
"buffer_load_dword v236, v237, s[36:39], 0 offen sc1\n"
"s_waitcnt vmcnt(0)\n"
"v_cmp_ne_u32 vcc, s35, v236\n"
"s_nop 4\n"
"s_and_b32 vcc_lo, vcc_lo, 511\n"
"s_cbranch_scc0 L_vpd_ll1_%=\n"
"s_sleep 1\n"
"s_add_u32 s99, s99, 1\n"
"s_cmp_lt_u32 s99, 0x200000\n"
"s_cbranch_scc1 L_vpl_ll1_%=\n"
"L_vpd_ll1_%=:\n"
"v_and_b32 v10, 15, v0\n"
"s_lshl_b32 s96, s53, 7\n"
"s_lshl_b32 s97, s49, 5\n"
"s_add_u32 s96, s96, s97\n"
"s_lshl_b32 s96, s96, 2\n"
"s_add_u32 s96, s96, 125829120\n"
"v_lshlrev_b32 v10, 2, v10\n"
"buffer_load_dword v28, v10, s[36:39], s96 offen sc1\n"
"buffer_load_dword v29, v10, s[36:39], s96 offen offset:64 sc1\n"
"v_exp_f32 v165, v93\n"
"s_nop 0\n"
"v_add_f32 v23, v23, v165\n"
"v_cvt_pk_bf16_f32 v126, v164, v165\n"
"v_exp_f32 v166, v94\n"
"s_nop 0\n"
"v_add_f32 v22, v22, v166\n"
"v_exp_f32 v167, v95\n"
"s_nop 0\n"
"v_add_f32 v23, v23, v167\n"
"v_add_f32 v20, v20, v21\n"
"v_add_f32 v22, v22, v23\n"
"v_cvt_pk_bf16_f32 v127, v166, v167\n"
"v_max_f32 v10, v20, v22\n"
"v_cmp_lt_f32 vcc, s66, v10\n"
"s_nop 4\n"
"s_cbranch_vccnz L_tr_l1_%=\n"
"v_add_f32 v18, v18, v20\n"
"v_add_f32 v19, v19, v22\n"
"L_rt_l1_%=:\n"
"s_nop 1\n"
"s_waitcnt lgkmcnt(6)\n"
"v_mfma_f32_16x16x32_bf16 a[0:3], a[192:195], v[112:115], a[0:3]\n"
"ds_read_b128 a[248:251], v4 offset:47104\n"
"s_and_b32 s54, %1, 7\n"
"s_mov_b32 s50, %2\n"
"s_lshr_b32 s96, %1, 3\n"
"s_and_b32 s52, s96, 1\n"
"s_mov_b32 s51, s53\n"
"s_mov_b64 s[40:41], %3\n"
"v_mfma_f32_16x16x32_bf16 a[4:7], a[192:195], v[116:119], a[4:7]\n"
"s_and_b32 s41, s41, 0xffff\n"
"s_mov_b32 s42, 3670016\n"
"s_mov_b32 s43, 0x20000\n"
"s_mov_b64 s[44:45], %4\n"
"s_and_b32 s45, s45, 0xffff\n"
"v_mfma_f32_16x16x32_bf16 a[8:11], a[196:199], v[112:115], a[8:11]\n"
"ds_read_b128 a[252:255], v4 offset:48128\n"
"s_mov_b32 s46, 401408\n"
"s_mov_b32 s47, 0x20000\n"
"s_mul_i32 s96, s50, 7\n"
"s_add_u32 s96, s96, 0\n"
"s_lshl_b32 s96, s96, 2\n"
"v_mfma_f32_16x16x32_bf16 a[12:15], a[196:199], v[116:119], a[12:15]\n"
"v_lshrrev_b32 v164, 4, v0\n"
"v_add_u32 v164, s96, v164\n"
"v_mul_u32_u24 v165, 147, v164\n"
"v_lshrrev_b32 v165, 10, v165\n"
"v_mul_u32_u24 v166, 7, v165\n"
"v_mfma_f32_16x16x32_bf16 a[16:19], a[200:203], v[112:115], a[16:19]\n"
"ds_read_b128 a[192:195], v4 offset:49152\n"
"v_sub_u32 v164, v164, v166\n"
"v_lshrrev_b32 v166, 2, v0\n"
"v_and_b32 v166, 3, v166\n"
"v_lshl_add_u32 v166, v165, 2, v166\n"
"v_mul_u32_u24 v166, 896, v166\n"
"v_mfma_f32_16x16x32_bf16 a[20:23], a[200:203], v[116:119], a[20:23]\n"
"v_lshl_add_u32 v166, v164, 6, v166\n"
"v_and_b32 v165, 1, v165\n"
"v_lshlrev_b32 v165, 1, v165\n"
"v_and_b32 v164, 3, v0\n"
"v_xor_b32 v165, v165, v164\n"
"v_mfma_f32_16x16x32_bf16 a[24:27], a[204:207], v[112:115], a[24:27]\n"
"ds_read_b128 a[196:199], v4 offset:50176\n"
"v_lshl_add_u32 v152, v165, 4, v166\n"
"s_mul_i32 s96, s50, 7\n"
"s_add_u32 s96, s96, 1\n"
"s_lshl_b32 s96, s96, 2\n"
"v_lshrrev_b32 v164, 4, v0\n"
"v_mfma_f32_16x16x32_bf16 a[28:31], a[204:207], v[116:119], a[28:31]\n"
"v_add_u32 v164, s96, v164\n"
"v_mul_u32_u24 v165, 147, v164\n"
"v_lshrrev_b32 v165, 10, v165\n"
"v_mul_u32_u24 v166, 7, v165\n"
"v_sub_u32 v164, v164, v166\n"
"v_lshrrev_b32 v166, 2, v0\n"
"v_mfma_f32_16x16x32_bf16 a[32:35], a[208:211], v[112:115], a[32:35]\n"
"ds_read_b128 a[200:203], v4 offset:51200\n"
"v_and_b32 v166, 3, v166\n"
"v_lshl_add_u32 v166, v165, 2, v166\n"
"v_mul_u32_u24 v166, 896, v166\n"
"v_lshl_add_u32 v166, v164, 6, v166\n"
"v_and_b32 v165, 1, v165\n"
"v_mfma_f32_16x16x32_bf16 a[36:39], a[208:211], v[116:119], a[36:39]\n"
"v_lshlrev_b32 v165, 1, v165\n"
"v_and_b32 v164, 3, v0\n"
"v_xor_b32 v165, v165, v164\n"
"v_lshl_add_u32 v153, v165, 4, v166\n"
"s_mul_i32 s96, s50, 7\n"
"v_mfma_f32_16x16x32_bf16 a[40:43], a[212:215], v[112:115], a[40:43]\n"
"ds_read_b128 a[204:207], v4 offset:52224\n"
"s_add_u32 s96, s96, 2\n"
"s_lshl_b32 s96, s96, 2\n"
"v_lshrrev_b32 v164, 4, v0\n"
"v_add_u32 v164, s96, v164\n"
"v_mul_u32_u24 v165, 147, v164\n"
"v_mfma_f32_16x16x32_bf16 a[44:47], a[212:215], v[116:119], a[44:47]\n"
"v_lshrrev_b32 v165, 10, v165\n"
"v_mul_u32_u24 v166, 7, v165\n"
"v_sub_u32 v164, v164, v166\n"
"v_lshrrev_b32 v166, 2, v0\n"
"v_and_b32 v166, 3, v166\n"
"v_mfma_f32_16x16x32_bf16 a[48:51], a[216:219], v[112:115], a[48:51]\n"
"ds_read_b128 a[208:211], v4 offset:53248\n"
"v_lshl_add_u32 v166, v165, 2, v166\n"
"v_mul_u32_u24 v166, 896, v166\n"
"v_lshl_add_u32 v166, v164, 6, v166\n"
"v_and_b32 v165, 1, v165\n"
"v_lshlrev_b32 v165, 1, v165\n"
"v_mfma_f32_16x16x32_bf16 a[52:55], a[216:219], v[116:119], a[52:55]\n"
"v_and_b32 v164, 3, v0\n"
"v_xor_b32 v165, v165, v164\n"
"v_lshl_add_u32 v154, v165, 4, v166\n"
"s_mul_i32 s96, s50, 7\n"
"s_add_u32 s96, s96, 3\n"
"v_mfma_f32_16x16x32_bf16 a[56:59], a[220:223], v[112:115], a[56:59]\n"
"ds_read_b128 a[212:215], v4 offset:54272\n"
"s_lshl_b32 s96, s96, 2\n"
"v_lshrrev_b32 v164, 4, v0\n"
"v_add_u32 v164, s96, v164\n"
"v_mul_u32_u24 v165, 147, v164\n"
"v_lshrrev_b32 v165, 10, v165\n"
"v_mul_u32_u24 v166, 7, v165\n"
"v_mfma_f32_16x16x32_bf16 a[60:63], a[220:223], v[116:119], a[60:63]\n"
"s_waitcnt lgkmcnt(6)\n"
"v_sub_u32 v164, v164, v166\n"
"v_lshrrev_b32 v166, 2, v0\n"
"v_and_b32 v166, 3, v166\n"
"v_lshl_add_u32 v166, v165, 2, v166\n"
"v_mul_u32_u24 v166, 896, v166\n"
"v_mfma_f32_16x16x32_bf16 a[64:67], a[224:227], v[112:115], a[64:67]\n"
"ds_read_b128 a[216:219], v4 offset:55296\n"
"v_lshl_add_u32 v166, v164, 6, v166\n"
"v_and_b32 v165, 1, v165\n"
"v_lshlrev_b32 v165, 1, v165\n"
"v_and_b32 v164, 3, v0\n"
"v_xor_b32 v165, v165, v164\n"
"v_mfma_f32_16x16x32_bf16 a[68:71], a[224:227], v[116:119], a[68:71]\n"
"v_lshl_add_u32 v155, v165, 4, v166\n"
"s_mul_i32 s96, s50, 7\n"
"s_add_u32 s96, s96, 4\n"
"s_lshl_b32 s96, s96, 2\n"
"v_lshrrev_b32 v164, 4, v0\n"
"v_mfma_f32_16x16x32_bf16 a[72:75], a[228:231], v[112:115], a[72:75]\n"
"ds_read_b128 a[220:223], v4 offset:56320\n"
"v_add_u32 v164, s96, v164\n"
"v_mul_u32_u24 v165, 147, v164\n"
"v_lshrrev_b32 v165, 10, v165\n"
"v_mul_u32_u24 v166, 7, v165\n"
"v_sub_u32 v164, v164, v166\n"
"v_mfma_f32_16x16x32_bf16 a[76:79], a[228:231], v[116:119], a[76:79]\n"
"v_lshrrev_b32 v166, 2, v0\n"
"v_and_b32 v166, 3, v166\n"
"v_lshl_add_u32 v166, v165, 2, v166\n"
"v_mul_u32_u24 v166, 896, v166\n"
"v_lshl_add_u32 v166, v164, 6, v166\n"
"v_mfma_f32_16x16x32_bf16 a[80:83], a[232:235], v[112:115], a[80:83]\n"
"ds_read_b128 a[224:227], v4 offset:57344\n"
"v_and_b32 v165, 1, v165\n"
"v_lshlrev_b32 v165, 1, v165\n"
"v_and_b32 v164, 3, v0\n"
"v_xor_b32 v165, v165, v164\n"
"v_lshl_add_u32 v156, v165, 4, v166\n"
"v_mfma_f32_16x16x32_bf16 a[84:87], a[232:235], v[116:119], a[84:87]\n"
"s_mul_i32 s96, s50, 7\n"
"s_add_u32 s96, s96, 5\n"
"s_lshl_b32 s96, s96, 2\n"
"v_lshrrev_b32 v164, 4, v0\n"
"v_add_u32 v164, s96, v164\n"
"v_mul_u32_u24 v165, 147, v164\n"
"v_mfma_f32_16x16x32_bf16 a[88:91], a[236:239], v[112:115], a[88:91]\n"
"ds_read_b128 a[228:231], v4 offset:58368\n"
"v_lshrrev_b32 v165, 10, v165\n"
"v_mul_u32_u24 v166, 7, v165\n"
"v_sub_u32 v164, v164, v166\n"
"v_lshrrev_b32 v166, 2, v0\n"
"v_and_b32 v166, 3, v166\n"
"v_mfma_f32_16x16x32_bf16 a[92:95], a[236:239], v[116:119], a[92:95]\n"
"v_lshl_add_u32 v166, v165, 2, v166\n"
"v_mul_u32_u24 v166, 896, v166\n"
"v_lshl_add_u32 v166, v164, 6, v166\n"
"v_and_b32 v165, 1, v165\n"
"v_lshlrev_b32 v165, 1, v165\n"
"v_mfma_f32_16x16x32_bf16 a[96:99], a[240:243], v[112:115], a[96:99]\n"
"ds_read_b128 a[232:235], v4 offset:59392\n"
"v_and_b32 v164, 3, v0\n"
"v_xor_b32 v165, v165, v164\n"
"v_lshl_add_u32 v157, v165, 4, v166\n"
"s_mul_i32 s96, s50, 7\n"
"s_add_u32 s96, s96, 6\n"
"v_mfma_f32_16x16x32_bf16 a[100:103], a[240:243], v[116:119], a[100:103]\n"
"s_lshl_b32 s96, s96, 2\n"
"v_lshrrev_b32 v164, 4, v0\n"
"v_add_u32 v164, s96, v164\n"
"v_mul_u32_u24 v165, 147, v164\n"
"v_lshrrev_b32 v165, 10, v165\n"
"v_mfma_f32_16x16x32_bf16 a[104:107], a[244:247], v[112:115], a[104:107]\n"
"ds_read_b128 a[236:239], v4 offset:60416\n"
"v_mul_u32_u24 v166, 7, v165\n"
"v_sub_u32 v164, v164, v166\n"
"v_lshrrev_b32 v166, 2, v0\n"
"v_and_b32 v166, 3, v166\n"
"v_lshl_add_u32 v166, v165, 2, v166\n"
"v_mfma_f32_16x16x32_bf16 a[108:111], a[244:247], v[116:119], a[108:111]\n"
"v_mul_u32_u24 v166, 896, v166\n"
"v_lshl_add_u32 v166, v164, 6, v166\n"
"v_and_b32 v165, 1, v165\n"
"v_lshlrev_b32 v165, 1, v165\n"
"v_and_b32 v164, 3, v0\n"
"v_mfma_f32_16x16x32_bf16 a[112:115], a[248:251], v[112:115], a[112:115]\n"
"ds_read_b128 a[240:243], v4 offset:61440\n"
"v_xor_b32 v165, v165, v164\n"
"v_lshl_add_u32 v158, v165, 4, v166\n"
"s_mul_i32 s56, s50, 7168\n"
"s_lshl_b32 s96, s54, 1\n"
"s_add_u32 s96, s96, s52\n"
"s_mul_i32 s55, s96, 229376\n"
"v_mfma_f32_16x16x32_bf16 a[116:119], a[248:251], v[116:119], a[116:119]\n"
"s_mul_i32 s96, s52, 448\n"
"s_add_u32 s55, s55, s96\n"
"s_lshl_b32 s96, s51, 3\n"
"s_lshl_b32 s97, s50, 1\n"
"s_add_u32 s96, s96, s97\n"
"v_mfma_f32_16x16x32_bf16 a[120:123], a[252:255], v[112:115], a[120:123]\n"
"ds_read_b128 a[244:247], v4 offset:62464\n"
"s_mul_i32 s57, s96, 142\n"
"s_lshl_b32 s57, s57, 10\n"
"s_add_u32 s57, s57, 58720256\n"
"s_lshl_b32 s97, s54, 11\n"
"s_add_u32 s97, s97, 131072\n"
"v_mfma_f32_16x16x32_bf16 a[124:127], a[252:255], v[116:119], a[124:127]\n"
"s_waitcnt lgkmcnt(6)\n"
"s_add_u32 s66, s97, s57\n"
"s_cmp_eq_u32 s54, 7\n"
"s_cselect_b32 s66, 0xf0000000, s66\n"
"s_add_u32 s67, s66, 145408\n"
"s_lshl_b32 s76, s96, 6\n"
"v_mfma_f32_16x16x32_bf16 a[0:3], a[192:195], v[120:123], a[0:3]\n"
"ds_read_b128 a[248:251], v4 offset:63488\n"
"s_add_u32 s76, s76, 125829120\n"
"v_and_b32 v164, 15, v0\n"
"v_lshlrev_b32 v167, 2, v164\n"
"s_lshl_b32 s96, s54, 1\n"
"s_xor_b32 s97, s52, 1\n"
"v_mfma_f32_16x16x32_bf16 a[4:7], a[192:195], v[124:127], a[4:7]\n"
"s_add_u32 s96, s96, s97\n"
"s_lshl_b32 s96, s96, 13\n"
"s_add_u32 s57, s57, s96\n"
"s_add_u32 s58, s57, 145408\n"
"v_lshrrev_b32 v164, 4, v0\n"
"v_mfma_f32_16x16x32_bf16 a[8:11], a[196:199], v[120:123], a[8:11]\n"
"ds_read_b128 a[252:255], v4 offset:64512\n"
"v_mul_u32_u24 v165, 1792, v164\n"
"v_and_b32 v166, 1, v164\n"
"v_lshl_add_u32 v165, v166, 5, v165\n"
"v_lshrrev_b32 v166, 2, v0\n"
"v_and_b32 v166, 3, v166\n"
"v_mfma_f32_16x16x32_bf16 a[12:15], a[196:199], v[124:127], a[12:15]\n"
"v_lshl_add_u32 v165, v166, 6, v165\n"
"v_and_b32 v166, 3, v0\n"
"v_lshl_add_u32 v160, v166, 3, v165\n"
"v_xor_b32 v161, 32, v160\n"
"s_mov_b32 s59, 0\n"
"v_mfma_f32_16x16x32_bf16 a[16:19], a[200:203], v[120:123], a[16:19]\n"
"v_mfma_f32_16x16x32_bf16 a[20:23], a[200:203], v[124:127], a[20:23]\n"
"s_mov_b32 m0, s56\n"
"s_nop 0\n"
"buffer_load_dwordx4 v152, s[40:43], s55 offen lds\n"
"v_mfma_f32_16x16x32_bf16 a[24:27], a[204:207], v[120:123], a[24:27]\n"
"s_add_u32 m0, m0, 0x400\n"
"s_nop 0\n"
"buffer_load_dwordx4 v153, s[40:43], s55 offen lds\n"
"v_mfma_f32_16x16x32_bf16 a[28:31], a[204:207], v[124:127], a[28:31]\n"
"v_mfma_f32_16x16x32_bf16 a[32:35], a[208:211], v[120:123], a[32:35]\n"
"s_add_u32 m0, m0, 0x400\n"
"s_nop 0\n"
"buffer_load_dwordx4 v154, s[40:43], s55 offen lds\n"
"v_mfma_f32_16x16x32_bf16 a[36:39], a[208:211], v[124:127], a[36:39]\n"
"v_mfma_f32_16x16x32_bf16 a[40:43], a[212:215], v[120:123], a[40:43]\n"
"s_add_u32 m0, m0, 0x400\n"
"s_nop 0\n"
"buffer_load_dwordx4 v155, s[40:43], s55 offen lds\n"
"v_mfma_f32_16x16x32_bf16 a[44:47], a[212:215], v[124:127], a[44:47]\n"
"v_mfma_f32_16x16x32_bf16 a[48:51], a[216:219], v[120:123], a[48:51]\n"
"s_add_u32 m0, m0, 0x400\n"
"s_nop 0\n"
"buffer_load_dwordx4 v156, s[40:43], s55 offen lds\n"
"v_mfma_f32_16x16x32_bf16 a[52:55], a[216:219], v[124:127], a[52:55]\n"
"v_mfma_f32_16x16x32_bf16 a[56:59], a[220:223], v[120:123], a[56:59]\n"
"s_add_u32 m0, m0, 0x400\n"
"s_nop 0\n"
"buffer_load_dwordx4 v157, s[40:43], s55 offen lds\n"
"v_mfma_f32_16x16x32_bf16 a[60:63], a[220:223], v[124:127], a[60:63]\n"
"s_waitcnt lgkmcnt(0)\n"
"v_mfma_f32_16x16x32_bf16 a[64:67], a[224:227], v[120:123], a[64:67]\n"
"s_add_u32 m0, m0, 0x400\n"
"s_nop 0\n"
"buffer_load_dwordx4 v158, s[40:43], s55 offen lds\n"
"s_add_u32 s55, s55, 57344\n"
"s_add_u32 s56, s56, 28672\n"
"s_sub_u32 s96, s56, 143360\n"
"s_cmp_ge_u32 s56, 143360\n"
"s_cselect_b32 s56, s96, s56\n"
"v_mfma_f32_16x16x32_bf16 a[68:71], a[224:227], v[124:127], a[68:71]\n"
"v_mfma_f32_16x16x32_bf16 a[72:75], a[228:231], v[120:123], a[72:75]\n"
"s_mov_b32 m0, s56\n"
"s_nop 0\n"
"buffer_load_dwordx4 v152, s[40:43], s55 offen lds\n"
"v_mfma_f32_16x16x32_bf16 a[76:79], a[228:231], v[124:127], a[76:79]\n"
"s_add_u32 m0, m0, 0x400\n"
"s_nop 0\n"
"buffer_load_dwordx4 v153, s[40:43], s55 offen lds\n"
"v_mfma_f32_16x16x32_bf16 a[80:83], a[232:235], v[120:123], a[80:83]\n"
"v_mfma_f32_16x16x32_bf16 a[84:87], a[232:235], v[124:127], a[84:87]\n"
"s_add_u32 m0, m0, 0x400\n"
"s_nop 0\n"
"buffer_load_dwordx4 v154, s[40:43], s55 offen lds\n"
"v_mfma_f32_16x16x32_bf16 a[88:91], a[236:239], v[120:123], a[88:91]\n"
"v_mfma_f32_16x16x32_bf16 a[92:95], a[236:239], v[124:127], a[92:95]\n"
"s_add_u32 m0, m0, 0x400\n"
"s_nop 0\n"
"buffer_load_dwordx4 v155, s[40:43], s55 offen lds\n"
"v_mfma_f32_16x16x32_bf16 a[96:99], a[240:243], v[120:123], a[96:99]\n"
"v_mfma_f32_16x16x32_bf16 a[100:103], a[240:243], v[124:127], a[100:103]\n"
"s_add_u32 m0, m0, 0x400\n"
"s_nop 0\n"
"buffer_load_dwordx4 v156, s[40:43], s55 offen lds\n"
"v_mfma_f32_16x16x32_bf16 a[104:107], a[244:247], v[120:123], a[104:107]\n"
"v_mfma_f32_16x16x32_bf16 a[108:111], a[244:247], v[124:127], a[108:111]\n"
"s_add_u32 m0, m0, 0x400\n"
"s_nop 0\n"
"buffer_load_dwordx4 v157, s[40:43], s55 offen lds\n"
"v_mfma_f32_16x16x32_bf16 a[112:115], a[248:251], v[120:123], a[112:115]\n"
"v_mfma_f32_16x16x32_bf16 a[116:119], a[248:251], v[124:127], a[116:119]\n"
"s_add_u32 m0, m0, 0x400\n"
"s_nop 0\n"
"buffer_load_dwordx4 v158, s[40:43], s55 offen lds\n"
"s_add_u32 s55, s55, 57344\n"
"s_add_u32 s56, s56, 28672\n"
"s_sub_u32 s96, s56, 143360\n"
"s_cmp_ge_u32 s56, 143360\n"
"s_cselect_b32 s56, s96, s56\n"
"v_mfma_f32_16x16x32_bf16 a[120:123], a[252:255], v[120:123], a[120:123]\n"
"v_mfma_f32_16x16x32_bf16 a[124:127], a[252:255], v[124:127], a[124:127]\n"
"s_nop 15\n"
"s_branch L_finF_%=\n"
"L_lastM1_%=:\n"
"s_waitcnt vmcnt(0) lgkmcnt(0)\n"
"s_barrier\n"
"ds_read_b128 a[192:195], v4 offset:32768\n"
"ds_read_b128 a[196:199], v4 offset:33792\n"
"ds_read_b128 a[200:203], v4 offset:34816\n"
"ds_read_b128 a[204:207], v4 offset:35840\n"
"ds_read_b128 a[208:211], v4 offset:36864\n"
"ds_read_b128 a[212:215], v4 offset:37888\n"
"ds_read_b128 a[216:219], v4 offset:38912\n"
"ds_read_b128 a[220:223], v4 offset:39936\n"
"ds_read_b128 a[224:227], v4 offset:40960\n"
"ds_read_b128 a[228:231], v4 offset:41984\n"
"ds_read_b128 a[232:235], v4 offset:43008\n"
"ds_read_b128 a[236:239], v4 offset:44032\n"
"ds_read_b128 a[240:243], v4 offset:45056\n"
"ds_read_b128 a[244:247], v4 offset:46080\n"
"s_lshl_b32 s96, s53, 5\n"
"s_add_u32 s96, s96, 126881792\n"
"v_lshlrev_b32 v237, 2, v0\n"
"v_add_u32 v237, s96, v237\n"
"s_lshl_b32 s97, s68, 4\n"
"s_add_u32 s97, s97, 126882816\n"
"s_cmp_eq_u32 s69, 0\n"
"s_cselect_b32 s97, s97, s96\n"
"v_mov_b32 v14, s97\n"
"v_cmp_gt_u32 vcc, 8, v0\n"
"s_nop 1\n"
"v_cndmask_b32 v237, v14, v237, vcc\n"
"v_cmp_ne_u32 vcc, s35, v236\n"
"s_nop 4\n"
"s_and_b32 vcc_lo, vcc_lo, 511\n"
"s_cbranch_scc0 L_vpd_llm1_%=\n"
"s_mov_b32 s99, 0\n"
"L_vpl_llm1_%=:\n"
"buffer_load_dword v236, v237, s[36:39], 0 offen sc1\n"
"s_waitcnt vmcnt(0)\n"
"v_cmp_ne_u32 vcc, s35, v236\n"
"s_nop 4\n"
"s_and_b32 vcc_lo, vcc_lo, 511\n"
"s_cbranch_scc0 L_vpd_llm1_%=\n"
"s_sleep 1\n"
"s_add_u32 s99, s99, 1\n"
"s_cmp_lt_u32 s99, 0x200000\n"
"s_cbranch_scc1 L_vpl_llm1_%=\n"
"L_vpd_llm1_%=:\n"
"v_and_b32 v10, 15, v0\n"
"s_lshl_b32 s96, s53, 7\n"
"s_lshl_b32 s97, s49, 5\n"
"s_add_u32 s96, s96, s97\n"
"s_lshl_b32 s96, s96, 2\n"
"s_add_u32 s96, s96, 125829120\n"
"v_lshlrev_b32 v10, 2, v10\n"
"buffer_load_dword v28, v10, s[36:39], s96 offen sc1\n"
"buffer_load_dword v29, v10, s[36:39], s96 offen offset:64 sc1\n"
"s_lshl_b32 s78, s68, 16\n"
"s_lshl_b32 s96, s49, 14\n"
"s_add_u32 s78, s78, s96\n"
"s_add_u32 s78, s78, 79691776\n"
"s_lshl_b32 s98, s68, 12\n"
"s_lshl_b32 s96, s49, 10\n"
"s_add_u32 s98, s98, s96\n"
"s_add_u32 s98, s98, 97517568\n"
"s_add_u32 s99, s78, 8192\n"
"v_exp_f32 v165, v93\n"
"s_nop 0\n"
"v_add_f32 v23, v23, v165\n"
"v_cvt_pk_bf16_f32 v126, v164, v165\n"
"v_exp_f32 v166, v94\n"
"s_nop 0\n"
"v_add_f32 v22, v22, v166\n"
"v_exp_f32 v167, v95\n"
"s_nop 0\n"
"v_add_f32 v23, v23, v167\n"
"v_add_f32 v20, v20, v21\n"
"v_add_f32 v22, v22, v23\n"
"v_cvt_pk_bf16_f32 v127, v166, v167\n"
"v_max_f32 v10, v20, v22\n"
"v_cmp_lt_f32 vcc, s66, v10\n"
"s_nop 4\n"
"s_cbranch_vccnz L_tr_lm1_%=\n"
"v_add_f32 v18, v18, v20\n"
"v_add_f32 v19, v19, v22\n"
"L_rt_lm1_%=:\n"
"s_nop 1\n"
"s_waitcnt lgkmcnt(6)\n"
"v_mfma_f32_16x16x32_bf16 a[0:3], a[192:195], v[112:115], a[0:3]\n"
"ds_read_b128 a[248:251], v4 offset:47104\n"
"s_and_b32 s54, %1, 7\n"
"s_mov_b32 s50, %2\n"
"s_lshr_b32 s96, %1, 3\n"
"s_and_b32 s52, s96, 1\n"
"s_mov_b32 s51, s53\n"
"s_mov_b64 s[40:41], %3\n"
"v_mfma_f32_16x16x32_bf16 a[4:7], a[192:195], v[116:119], a[4:7]\n"
"buffer_load_dwordx4 v[232:235], v1, s[36:39], s98 offen sc1\n"
"s_and_b32 s41, s41, 0xffff\n"
"s_mov_b32 s42, 3670016\n"
"s_mov_b32 s43, 0x20000\n"
"s_mov_b64 s[44:45], %4\n"
"s_and_b32 s45, s45, 0xffff\n"
"v_mfma_f32_16x16x32_bf16 a[8:11], a[196:199], v[112:115], a[8:11]\n"
"ds_read_b128 a[252:255], v4 offset:48128\n"
"buffer_load_dwordx4 v[168:171], v1, s[36:39], s78 offen offset:0 sc1\n"
"s_mov_b32 s46, 401408\n"
"s_mov_b32 s47, 0x20000\n"
"s_mul_i32 s96, s50, 7\n"
"s_add_u32 s96, s96, 0\n"
"s_lshl_b32 s96, s96, 2\n"
"v_mfma_f32_16x16x32_bf16 a[12:15], a[196:199], v[116:119], a[12:15]\n"
"buffer_load_dwordx4 v[172:175], v1, s[36:39], s78 offen offset:1024 sc1\n"
"v_lshrrev_b32 v164, 4, v0\n"
"v_add_u32 v164, s96, v164\n"
"v_mul_u32_u24 v165, 147, v164\n"
"v_lshrrev_b32 v165, 10, v165\n"
"v_mul_u32_u24 v166, 7, v165\n"
"v_mfma_f32_16x16x32_bf16 a[16:19], a[200:203], v[112:115], a[16:19]\n"
"ds_read_b128 a[192:195], v4 offset:49152\n"
"v_sub_u32 v164, v164, v166\n"
"v_lshrrev_b32 v166, 2, v0\n"
"v_and_b32 v166, 3, v166\n"
"v_lshl_add_u32 v166, v165, 2, v166\n"
"v_mul_u32_u24 v166, 896, v166\n"
"v_mfma_f32_16x16x32_bf16 a[20:23], a[200:203], v[116:119], a[20:23]\n"
"buffer_load_dwordx4 v[176:179], v1, s[36:39], s78 offen offset:2048 sc1\n"
"v_lshl_add_u32 v166, v164, 6, v166\n"
"v_and_b32 v165, 1, v165\n"
"v_lshlrev_b32 v165, 1, v165\n"
"v_and_b32 v164, 3, v0\n"
"v_xor_b32 v165, v165, v164\n"
"v_mfma_f32_16x16x32_bf16 a[24:27], a[204:207], v[112:115], a[24:27]\n"
"ds_read_b128 a[196:199], v4 offset:50176\n"
"buffer_load_dwordx4 v[180:183], v1, s[36:39], s78 offen offset:3072 sc1\n"
"v_lshl_add_u32 v152, v165, 4, v166\n"
"s_mul_i32 s96, s50, 7\n"
"s_add_u32 s96, s96, 1\n"
"s_lshl_b32 s96, s96, 2\n"
"v_lshrrev_b32 v164, 4, v0\n"
"v_mfma_f32_16x16x32_bf16 a[28:31], a[204:207], v[116:119], a[28:31]\n"
"v_add_u32 v164, s96, v164\n"
"v_mul_u32_u24 v165, 147, v164\n"
"v_lshrrev_b32 v165, 10, v165\n"
"v_mul_u32_u24 v166, 7, v165\n"
"v_sub_u32 v164, v164, v166\n"
"v_lshrrev_b32 v166, 2, v0\n"
"v_mfma_f32_16x16x32_bf16 a[32:35], a[208:211], v[112:115], a[32:35]\n"
"ds_read_b128 a[200:203], v4 offset:51200\n"
"buffer_load_dwordx4 v[184:187], v2, s[36:39], s78 offen offset:0 sc1\n"
"v_and_b32 v166, 3, v166\n"
"v_lshl_add_u32 v166, v165, 2, v166\n"
"v_mul_u32_u24 v166, 896, v166\n"
"v_lshl_add_u32 v166, v164, 6, v166\n"
"v_and_b32 v165, 1, v165\n"
"v_mfma_f32_16x16x32_bf16 a[36:39], a[208:211], v[116:119], a[36:39]\n"
"buffer_load_dwordx4 v[188:191], v2, s[36:39], s78 offen offset:1024 sc1\n"
"v_lshlrev_b32 v165, 1, v165\n"
"v_and_b32 v164, 3, v0\n"
"v_xor_b32 v165, v165, v164\n"
"v_lshl_add_u32 v153, v165, 4, v166\n"
"s_mul_i32 s96, s50, 7\n"
"v_mfma_f32_16x16x32_bf16 a[40:43], a[212:215], v[112:115], a[40:43]\n"
"ds_read_b128 a[204:207], v4 offset:52224\n"
"buffer_load_dwordx4 v[192:195], v2, s[36:39], s78 offen offset:2048 sc1\n"
"s_add_u32 s96, s96, 2\n"
"s_lshl_b32 s96, s96, 2\n"
"v_lshrrev_b32 v164, 4, v0\n"
"v_add_u32 v164, s96, v164\n"
"v_mul_u32_u24 v165, 147, v164\n"
"v_mfma_f32_16x16x32_bf16 a[44:47], a[212:215], v[116:119], a[44:47]\n"
"v_lshrrev_b32 v165, 10, v165\n"
"v_mul_u32_u24 v166, 7, v165\n"
"v_sub_u32 v164, v164, v166\n"
"v_lshrrev_b32 v166, 2, v0\n"
"v_and_b32 v166, 3, v166\n"
"v_mfma_f32_16x16x32_bf16 a[48:51], a[216:219], v[112:115], a[48:51]\n"
"ds_read_b128 a[208:211], v4 offset:53248\n"
"buffer_load_dwordx4 v[196:199], v2, s[36:39], s78 offen offset:3072 sc1\n"
"v_lshl_add_u32 v166, v165, 2, v166\n"
"v_mul_u32_u24 v166, 896, v166\n"
"v_lshl_add_u32 v166, v164, 6, v166\n"
"v_and_b32 v165, 1, v165\n"
"v_lshlrev_b32 v165, 1, v165\n"
"v_mfma_f32_16x16x32_bf16 a[52:55], a[216:219], v[116:119], a[52:55]\n"
"buffer_load_dwordx4 v[200:203], v1, s[36:39], s99 offen offset:0 sc1\n"
"v_and_b32 v164, 3, v0\n"
"v_xor_b32 v165, v165, v164\n"
"v_lshl_add_u32 v154, v165, 4, v166\n"
"s_mul_i32 s96, s50, 7\n"
"s_add_u32 s96, s96, 3\n"
"v_mfma_f32_16x16x32_bf16 a[56:59], a[220:223], v[112:115], a[56:59]\n"
"ds_read_b128 a[212:215], v4 offset:54272\n"
"s_lshl_b32 s96, s96, 2\n"
"v_lshrrev_b32 v164, 4, v0\n"
"v_add_u32 v164, s96, v164\n"
"v_mul_u32_u24 v165, 147, v164\n"
"v_lshrrev_b32 v165, 10, v165\n"
"v_mul_u32_u24 v166, 7, v165\n"
"v_mfma_f32_16x16x32_bf16 a[60:63], a[220:223], v[116:119], a[60:63]\n"
"s_waitcnt lgkmcnt(6)\n"
"buffer_load_dwordx4 v[204:207], v1, s[36:39], s99 offen offset:1024 sc1\n"
"v_sub_u32 v164, v164, v166\n"
"v_lshrrev_b32 v166, 2, v0\n"
"v_and_b32 v166, 3, v166\n"
"v_lshl_add_u32 v166, v165, 2, v166\n"
"v_mul_u32_u24 v166, 896, v166\n"
"v_mfma_f32_16x16x32_bf16 a[64:67], a[224:227], v[112:115], a[64:67]\n"
"ds_read_b128 a[216:219], v4 offset:55296\n"
"buffer_load_dwordx4 v[208:211], v1, s[36:39], s99 offen offset:2048 sc1\n"
"v_lshl_add_u32 v166, v164, 6, v166\n"
"v_and_b32 v165, 1, v165\n"
"v_lshlrev_b32 v165, 1, v165\n"
"v_and_b32 v164, 3, v0\n"
"v_xor_b32 v165, v165, v164\n"
"v_mfma_f32_16x16x32_bf16 a[68:71], a[224:227], v[116:119], a[68:71]\n"
"buffer_load_dwordx4 v[212:215], v1, s[36:39], s99 offen offset:3072 sc1\n"
"v_lshl_add_u32 v155, v165, 4, v166\n"
"s_mul_i32 s96, s50, 7\n"
"s_add_u32 s96, s96, 4\n"
"s_lshl_b32 s96, s96, 2\n"
"v_lshrrev_b32 v164, 4, v0\n"
"v_mfma_f32_16x16x32_bf16 a[72:75], a[228:231], v[112:115], a[72:75]\n"
"ds_read_b128 a[220:223], v4 offset:56320\n"
"v_add_u32 v164, s96, v164\n"
"v_mul_u32_u24 v165, 147, v164\n"
"v_lshrrev_b32 v165, 10, v165\n"
"v_mul_u32_u24 v166, 7, v165\n"
"v_sub_u32 v164, v164, v166\n"
"v_mfma_f32_16x16x32_bf16 a[76:79], a[228:231], v[116:119], a[76:79]\n"
"buffer_load_dwordx4 v[216:219], v2, s[36:39], s99 offen offset:0 sc1\n"
"v_lshrrev_b32 v166, 2, v0\n"
"v_and_b32 v166, 3, v166\n"
"v_lshl_add_u32 v166, v165, 2, v166\n"
"v_mul_u32_u24 v166, 896, v166\n"
"v_lshl_add_u32 v166, v164, 6, v166\n"
"v_mfma_f32_16x16x32_bf16 a[80:83], a[232:235], v[112:115], a[80:83]\n"
"ds_read_b128 a[224:227], v4 offset:57344\n"
"buffer_load_dwordx4 v[220:223], v2, s[36:39], s99 offen offset:1024 sc1\n"
"v_and_b32 v165, 1, v165\n"
"v_lshlrev_b32 v165, 1, v165\n"
"v_and_b32 v164, 3, v0\n"
"v_xor_b32 v165, v165, v164\n"
"v_lshl_add_u32 v156, v165, 4, v166\n"
"v_mfma_f32_16x16x32_bf16 a[84:87], a[232:235], v[116:119], a[84:87]\n"
"s_mul_i32 s96, s50, 7\n"
"s_add_u32 s96, s96, 5\n"
"s_lshl_b32 s96, s96, 2\n"
"v_lshrrev_b32 v164, 4, v0\n"
"v_add_u32 v164, s96, v164\n"
"v_mul_u32_u24 v165, 147, v164\n"
"v_mfma_f32_16x16x32_bf16 a[88:91], a[236:239], v[112:115], a[88:91]\n"
"ds_read_b128 a[228:231], v4 offset:58368\n"
"buffer_load_dwordx4 v[224:227], v2, s[36:39], s99 offen offset:2048 sc1\n"
"v_lshrrev_b32 v165, 10, v165\n"
"v_mul_u32_u24 v166, 7, v165\n"
"v_sub_u32 v164, v164, v166\n"
"v_lshrrev_b32 v166, 2, v0\n"
"v_and_b32 v166, 3, v166\n"
"v_mfma_f32_16x16x32_bf16 a[92:95], a[236:239], v[116:119], a[92:95]\n"
"buffer_load_dwordx4 v[228:231], v2, s[36:39], s99 offen offset:3072 sc1\n"
"v_lshl_add_u32 v166, v165, 2, v166\n"
"v_mul_u32_u24 v166, 896, v166\n"
"v_lshl_add_u32 v166, v164, 6, v166\n"
"v_and_b32 v165, 1, v165\n"
"v_lshlrev_b32 v165, 1, v165\n"
"v_mfma_f32_16x16x32_bf16 a[96:99], a[240:243], v[112:115], a[96:99]\n"
"ds_read_b128 a[232:235], v4 offset:59392\n"
"v_and_b32 v164, 3, v0\n"
"v_xor_b32 v165, v165, v164\n"
"v_lshl_add_u32 v157, v165, 4, v166\n"
"s_mul_i32 s96, s50, 7\n"
"s_add_u32 s96, s96, 6\n"
"v_mfma_f32_16x16x32_bf16 a[100:103], a[240:243], v[116:119], a[100:103]\n"
"s_lshl_b32 s96, s96, 2\n"
"v_lshrrev_b32 v164, 4, v0\n"
"v_add_u32 v164, s96, v164\n"
"v_mul_u32_u24 v165, 147, v164\n"
"v_lshrrev_b32 v165, 10, v165\n"
"v_mfma_f32_16x16x32_bf16 a[104:107], a[244:247], v[112:115], a[104:107]\n"
"ds_read_b128 a[236:239], v4 offset:60416\n"
"v_mul_u32_u24 v166, 7, v165\n"
"v_sub_u32 v164, v164, v166\n"
"v_lshrrev_b32 v166, 2, v0\n"
"v_and_b32 v166, 3, v166\n"
"v_lshl_add_u32 v166, v165, 2, v166\n"
"v_mfma_f32_16x16x32_bf16 a[108:111], a[244:247], v[116:119], a[108:111]\n"
"v_mul_u32_u24 v166, 896, v166\n"
"v_lshl_add_u32 v166, v164, 6, v166\n"
"v_and_b32 v165, 1, v165\n"
"v_lshlrev_b32 v165, 1, v165\n"
"v_and_b32 v164, 3, v0\n"
"v_mfma_f32_16x16x32_bf16 a[112:115], a[248:251], v[112:115], a[112:115]\n"
"ds_read_b128 a[240:243], v4 offset:61440\n"
"v_xor_b32 v165, v165, v164\n"
"v_lshl_add_u32 v158, v165, 4, v166\n"
"s_mul_i32 s56, s50, 7168\n"
"s_lshl_b32 s96, s54, 1\n"
"s_add_u32 s96, s96, s52\n"
"s_mul_i32 s55, s96, 229376\n"
"v_mfma_f32_16x16x32_bf16 a[116:119], a[248:251], v[116:119], a[116:119]\n"
"s_mul_i32 s96, s52, 448\n"
"s_add_u32 s55, s55, s96\n"
"s_lshl_b32 s96, s51, 3\n"
"s_lshl_b32 s97, s50, 1\n"
"s_add_u32 s96, s96, s97\n"
"v_mfma_f32_16x16x32_bf16 a[120:123], a[252:255], v[112:115], a[120:123]\n"
"ds_read_b128 a[244:247], v4 offset:62464\n"
"s_mul_i32 s57, s96, 142\n"
"s_lshl_b32 s57, s57, 10\n"
"s_add_u32 s57, s57, 58720256\n"
"s_lshl_b32 s97, s54, 11\n"
"s_add_u32 s97, s97, 131072\n"
"v_mfma_f32_16x16x32_bf16 a[124:127], a[252:255], v[116:119], a[124:127]\n"
"s_waitcnt lgkmcnt(6)\n"
"s_add_u32 s66, s97, s57\n"
"s_cmp_eq_u32 s54, 7\n"
"s_cselect_b32 s66, 0xf0000000, s66\n"
"s_add_u32 s67, s66, 145408\n"
"s_lshl_b32 s76, s96, 6\n"
"v_mfma_f32_16x16x32_bf16 a[0:3], a[192:195], v[120:123], a[0:3]\n"
"ds_read_b128 a[248:251], v4 offset:63488\n"
"s_add_u32 s76, s76, 125829120\n"
"v_and_b32 v164, 15, v0\n"
"v_lshlrev_b32 v167, 2, v164\n"
"s_lshl_b32 s96, s54, 1\n"
"s_xor_b32 s97, s52, 1\n"
"v_mfma_f32_16x16x32_bf16 a[4:7], a[192:195], v[124:127], a[4:7]\n"
"s_add_u32 s96, s96, s97\n"
"s_lshl_b32 s96, s96, 13\n"
"s_add_u32 s57, s57, s96\n"
"s_add_u32 s58, s57, 145408\n"
"v_lshrrev_b32 v164, 4, v0\n"
"v_mfma_f32_16x16x32_bf16 a[8:11], a[196:199], v[120:123], a[8:11]\n"
"ds_read_b128 a[252:255], v4 offset:64512\n"
"v_mul_u32_u24 v165, 1792, v164\n"
"v_and_b32 v166, 1, v164\n"
"v_lshl_add_u32 v165, v166, 5, v165\n"
"v_lshrrev_b32 v166, 2, v0\n"
"v_and_b32 v166, 3, v166\n"
"v_mfma_f32_16x16x32_bf16 a[12:15], a[196:199], v[124:127], a[12:15]\n"
"v_lshl_add_u32 v165, v166, 6, v165\n"
"v_and_b32 v166, 3, v0\n"
"v_lshl_add_u32 v160, v166, 3, v165\n"
"v_xor_b32 v161, 32, v160\n"
"s_mov_b32 s59, 0\n"
"v_mfma_f32_16x16x32_bf16 a[16:19], a[200:203], v[120:123], a[16:19]\n"
"v_mfma_f32_16x16x32_bf16 a[20:23], a[200:203], v[124:127], a[20:23]\n"
"s_mov_b32 m0, s56\n"
"s_nop 0\n"
"buffer_load_dwordx4 v152, s[40:43], s55 offen lds\n"
"v_mfma_f32_16x16x32_bf16 a[24:27], a[204:207], v[120:123], a[24:27]\n"
"s_add_u32 m0, m0, 0x400\n"
"s_nop 0\n"
"buffer_load_dwordx4 v153, s[40:43], s55 offen lds\n"
"v_mfma_f32_16x16x32_bf16 a[28:31], a[204:207], v[124:127], a[28:31]\n"
"v_mfma_f32_16x16x32_bf16 a[32:35], a[208:211], v[120:123], a[32:35]\n"
"s_add_u32 m0, m0, 0x400\n"
"s_nop 0\n"
"buffer_load_dwordx4 v154, s[40:43], s55 offen lds\n"
"v_mfma_f32_16x16x32_bf16 a[36:39], a[208:211], v[124:127], a[36:39]\n"
"v_mfma_f32_16x16x32_bf16 a[40:43], a[212:215], v[120:123], a[40:43]\n"
"s_add_u32 m0, m0, 0x400\n"
"s_nop 0\n"
"buffer_load_dwordx4 v155, s[40:43], s55 offen lds\n"
"v_mfma_f32_16x16x32_bf16 a[44:47], a[212:215], v[124:127], a[44:47]\n"
"v_mfma_f32_16x16x32_bf16 a[48:51], a[216:219], v[120:123], a[48:51]\n"
"s_add_u32 m0, m0, 0x400\n"
"s_nop 0\n"
"buffer_load_dwordx4 v156, s[40:43], s55 offen lds\n"
"v_mfma_f32_16x16x32_bf16 a[52:55], a[216:219], v[124:127], a[52:55]\n"
"v_mfma_f32_16x16x32_bf16 a[56:59], a[220:223], v[120:123], a[56:59]\n"
"s_add_u32 m0, m0, 0x400\n"
"s_nop 0\n"
"buffer_load_dwordx4 v157, s[40:43], s55 offen lds\n"
"v_mfma_f32_16x16x32_bf16 a[60:63], a[220:223], v[124:127], a[60:63]\n"
"s_waitcnt lgkmcnt(0)\n"
"v_mfma_f32_16x16x32_bf16 a[64:67], a[224:227], v[120:123], a[64:67]\n"
"s_add_u32 m0, m0, 0x400\n"
"s_nop 0\n"
"buffer_load_dwordx4 v158, s[40:43], s55 offen lds\n"
"s_add_u32 s55, s55, 57344\n"
"s_add_u32 s56, s56, 28672\n"
"s_sub_u32 s96, s56, 143360\n"
"s_cmp_ge_u32 s56, 143360\n"
"s_cselect_b32 s56, s96, s56\n"
"v_mfma_f32_16x16x32_bf16 a[68:71], a[224:227], v[124:127], a[68:71]\n"
"v_mfma_f32_16x16x32_bf16 a[72:75], a[228:231], v[120:123], a[72:75]\n"
"s_mov_b32 m0, s56\n"
"s_nop 0\n"
"buffer_load_dwordx4 v152, s[40:43], s55 offen lds\n"
"v_mfma_f32_16x16x32_bf16 a[76:79], a[228:231], v[124:127], a[76:79]\n"
"s_add_u32 m0, m0, 0x400\n"
"s_nop 0\n"
"buffer_load_dwordx4 v153, s[40:43], s55 offen lds\n"
"v_mfma_f32_16x16x32_bf16 a[80:83], a[232:235], v[120:123], a[80:83]\n"
"v_mfma_f32_16x16x32_bf16 a[84:87], a[232:235], v[124:127], a[84:87]\n"
"s_add_u32 m0, m0, 0x400\n"
"s_nop 0\n"
"buffer_load_dwordx4 v154, s[40:43], s55 offen lds\n"
"v_mfma_f32_16x16x32_bf16 a[88:91], a[236:239], v[120:123], a[88:91]\n"
"v_mfma_f32_16x16x32_bf16 a[92:95], a[236:239], v[124:127], a[92:95]\n"
"s_add_u32 m0, m0, 0x400\n"
"s_nop 0\n"
"buffer_load_dwordx4 v155, s[40:43], s55 offen lds\n"
"v_mfma_f32_16x16x32_bf16 a[96:99], a[240:243], v[120:123], a[96:99]\n"
"v_mfma_f32_16x16x32_bf16 a[100:103], a[240:243], v[124:127], a[100:103]\n"
"s_add_u32 m0, m0, 0x400\n"
"s_nop 0\n"
"buffer_load_dwordx4 v156, s[40:43], s55 offen lds\n"
"v_mfma_f32_16x16x32_bf16 a[104:107], a[244:247], v[120:123], a[104:107]\n"
"v_mfma_f32_16x16x32_bf16 a[108:111], a[244:247], v[124:127], a[108:111]\n"
"s_add_u32 m0, m0, 0x400\n"
"s_nop 0\n"
"buffer_load_dwordx4 v157, s[40:43], s55 offen lds\n"
"v_mfma_f32_16x16x32_bf16 a[112:115], a[248:251], v[120:123], a[112:115]\n"
"v_mfma_f32_16x16x32_bf16 a[116:119], a[248:251], v[124:127], a[116:119]\n"
"s_add_u32 m0, m0, 0x400\n"
"s_nop 0\n"
"buffer_load_dwordx4 v158, s[40:43], s55 offen lds\n"
"s_add_u32 s55, s55, 57344\n"
"s_add_u32 s56, s56, 28672\n"
"s_sub_u32 s96, s56, 143360\n"
"s_cmp_ge_u32 s56, 143360\n"
"s_cselect_b32 s56, s96, s56\n"
"v_mfma_f32_16x16x32_bf16 a[120:123], a[252:255], v[120:123], a[120:123]\n"
"v_mfma_f32_16x16x32_bf16 a[124:127], a[252:255], v[124:127], a[124:127]\n"
"s_nop 15\n"
"s_branch L_finM_%=\n"
"L_lastS1_%=:\n"
"s_waitcnt vmcnt(8) lgkmcnt(0)\n"
"s_barrier\n"
"ds_read_b128 a[192:195], v4 offset:32768\n"
"ds_read_b128 a[196:199], v4 offset:33792\n"
"ds_read_b128 a[200:203], v4 offset:34816\n"
"ds_read_b128 a[204:207], v4 offset:35840\n"
"ds_read_b128 a[208:211], v4 offset:36864\n"
"ds_read_b128 a[212:215], v4 offset:37888\n"
"ds_read_b128 a[216:219], v4 offset:38912\n"
"ds_read_b128 a[220:223], v4 offset:39936\n"
"ds_read_b128 a[224:227], v4 offset:40960\n"
"ds_read_b128 a[228:231], v4 offset:41984\n"
"ds_read_b128 a[232:235], v4 offset:43008\n"
"ds_read_b128 a[236:239], v4 offset:44032\n"
"ds_read_b128 a[240:243], v4 offset:45056\n"
"ds_read_b128 a[244:247], v4 offset:46080\n"
"s_mov_b32 s53, s51\n"
"s_lshl_b32 s79, s48, 20\n"
"s_lshl_b32 s96, s53, 16\n"
"s_add_u32 s79, s79, s96\n"
"s_lshl_b32 s96, s49, 14\n"
"s_add_u32 s79, s79, s96\n"
"v_and_b32 v10, 15, v0\n"
"v_lshrrev_b32 v11, 4, v0\n"
"v_lshlrev_b32 v11, 2, v11\n"
"v_sub_u32 v10, v10, v11\n"
"s_lshl_b32 s96, s53, 7\n"
"s_lshl_b32 s97, s49, 5\n"
"s_add_u32 s96, s96, s97\n"
"v_add_u32 v7, s96, v10\n"
"v_add_u32 v8, 16, v7\n"
"s_add_u32 s99, s79, 8192\n"
"v_exp_f32 v165, v93\n"
"s_nop 0\n"
"v_add_f32 v23, v23, v165\n"
"v_cvt_pk_bf16_f32 v126, v164, v165\n"
"v_exp_f32 v166, v94\n"
"s_nop 0\n"
"v_add_f32 v22, v22, v166\n"
"v_exp_f32 v167, v95\n"
"s_nop 0\n"
"v_add_f32 v23, v23, v167\n"
"v_add_f32 v20, v20, v21\n"
"v_add_f32 v22, v22, v23\n"
"v_cvt_pk_bf16_f32 v127, v166, v167\n"
"v_max_f32 v10, v20, v22\n"
"v_cmp_lt_f32 vcc, s66, v10\n"
"s_nop 4\n"
"s_cbranch_vccnz L_tr_ls1_%=\n"
"v_add_f32 v18, v18, v20\n"
"v_add_f32 v19, v19, v22\n"
"L_rt_ls1_%=:\n"
"s_nop 1\n"
"s_waitcnt lgkmcnt(6)\n"
"v_mfma_f32_16x16x32_bf16 a[0:3], a[192:195], v[112:115], a[0:3]\n"
"ds_read_b128 a[248:251], v4 offset:47104\n"
"v_mfma_f32_16x16x32_bf16 a[4:7], a[192:195], v[116:119], a[4:7]\n"
"s_add_u32 m0, s65, 65536\n"
"s_nop 0\n"
"buffer_load_dwordx4 v1, s[44:47], s60 offen offset:0 sc1 lds\n"
"v_mfma_f32_16x16x32_bf16 a[8:11], a[196:199], v[112:115], a[8:11]\n"
"ds_read_b128 a[252:255], v4 offset:48128\n"
"v_mfma_f32_16x16x32_bf16 a[12:15], a[196:199], v[116:119], a[12:15]\n"
"buffer_load_dwordx4 v1, s[44:47], s60 offen offset:1024 sc1 lds\n"
"v_mfma_f32_16x16x32_bf16 a[16:19], a[200:203], v[112:115], a[16:19]\n"
"ds_read_b128 a[192:195], v4 offset:49152\n"
"v_mfma_f32_16x16x32_bf16 a[20:23], a[200:203], v[116:119], a[20:23]\n"
"buffer_load_dwordx4 v1, s[44:47], s60 offen offset:2048 sc1 lds\n"
"v_mfma_f32_16x16x32_bf16 a[24:27], a[204:207], v[112:115], a[24:27]\n"
"ds_read_b128 a[196:199], v4 offset:50176\n"
"v_mfma_f32_16x16x32_bf16 a[28:31], a[204:207], v[116:119], a[28:31]\n"
"v_mfma_f32_16x16x32_bf16 a[32:35], a[208:211], v[112:115], a[32:35]\n"
"ds_read_b128 a[200:203], v4 offset:51200\n"
"buffer_load_dwordx4 v1, s[44:47], s60 offen offset:3072 sc1 lds\n"
"v_mfma_f32_16x16x32_bf16 a[36:39], a[208:211], v[116:119], a[36:39]\n"
"v_mfma_f32_16x16x32_bf16 a[40:43], a[212:215], v[112:115], a[40:43]\n"
"ds_read_b128 a[204:207], v4 offset:52224\n"
"s_add_u32 m0, s65, 69632\n"
"s_nop 0\n"
"buffer_load_dwordx4 v2, s[44:47], s60 offen offset:0 sc1 lds\n"
"v_mfma_f32_16x16x32_bf16 a[44:47], a[212:215], v[116:119], a[44:47]\n"
"v_mfma_f32_16x16x32_bf16 a[48:51], a[216:219], v[112:115], a[48:51]\n"
"ds_read_b128 a[208:211], v4 offset:53248\n"
"buffer_load_dwordx4 v2, s[44:47], s60 offen offset:1024 sc1 lds\n"
"v_mfma_f32_16x16x32_bf16 a[52:55], a[216:219], v[116:119], a[52:55]\n"
"v_mfma_f32_16x16x32_bf16 a[56:59], a[220:223], v[112:115], a[56:59]\n"
"ds_read_b128 a[212:215], v4 offset:54272\n"
"v_mfma_f32_16x16x32_bf16 a[60:63], a[220:223], v[116:119], a[60:63]\n"
"s_waitcnt lgkmcnt(6)\n"
"buffer_load_dwordx4 v2, s[44:47], s60 offen offset:2048 sc1 lds\n"
"v_mfma_f32_16x16x32_bf16 a[64:67], a[224:227], v[112:115], a[64:67]\n"
"ds_read_b128 a[216:219], v4 offset:55296\n"
"v_mfma_f32_16x16x32_bf16 a[68:71], a[224:227], v[116:119], a[68:71]\n"
"buffer_load_dwordx4 v2, s[44:47], s60 offen offset:3072 sc1 lds\n"
"v_mfma_f32_16x16x32_bf16 a[72:75], a[228:231], v[112:115], a[72:75]\n"
"ds_read_b128 a[220:223], v4 offset:56320\n"
"v_mfma_f32_16x16x32_bf16 a[76:79], a[228:231], v[116:119], a[76:79]\n"
"buffer_load_dwordx4 a[128:131], v1, s[36:39], s79 offen offset:0 sc1\n"
"v_mfma_f32_16x16x32_bf16 a[80:83], a[232:235], v[112:115], a[80:83]\n"
"ds_read_b128 a[224:227], v4 offset:57344\n"
"v_mfma_f32_16x16x32_bf16 a[84:87], a[232:235], v[116:119], a[84:87]\n"
"v_mfma_f32_16x16x32_bf16 a[88:91], a[236:239], v[112:115], a[88:91]\n"
"ds_read_b128 a[228:231], v4 offset:58368\n"
"buffer_load_dwordx4 a[132:135], v1, s[36:39], s79 offen offset:1024 sc1\n"
"v_mfma_f32_16x16x32_bf16 a[92:95], a[236:239], v[116:119], a[92:95]\n"
"v_mfma_f32_16x16x32_bf16 a[96:99], a[240:243], v[112:115], a[96:99]\n"
"ds_read_b128 a[232:235], v4 offset:59392\n"
"buffer_load_dwordx4 a[136:139], v1, s[36:39], s79 offen offset:2048 sc1\n"
"v_mfma_f32_16x16x32_bf16 a[100:103], a[240:243], v[116:119], a[100:103]\n"
"v_mfma_f32_16x16x32_bf16 a[104:107], a[244:247], v[112:115], a[104:107]\n"
"ds_read_b128 a[236:239], v4 offset:60416\n"
"buffer_load_dwordx4 a[140:143], v1, s[36:39], s79 offen offset:3072 sc1\n"
"v_mfma_f32_16x16x32_bf16 a[108:111], a[244:247], v[116:119], a[108:111]\n"
"v_mfma_f32_16x16x32_bf16 a[112:115], a[248:251], v[112:115], a[112:115]\n"
"ds_read_b128 a[240:243], v4 offset:61440\n"
"v_mfma_f32_16x16x32_bf16 a[116:119], a[248:251], v[116:119], a[116:119]\n"
"buffer_load_dwordx4 a[144:147], v2, s[36:39], s79 offen offset:0 sc1\n"
"v_mfma_f32_16x16x32_bf16 a[120:123], a[252:255], v[112:115], a[120:123]\n"
"ds_read_b128 a[244:247], v4 offset:62464\n"
"v_mfma_f32_16x16x32_bf16 a[124:127], a[252:255], v[116:119], a[124:127]\n"
"s_waitcnt lgkmcnt(6)\n"
"buffer_load_dwordx4 a[148:151], v2, s[36:39], s79 offen offset:1024 sc1\n"
"v_mfma_f32_16x16x32_bf16 a[0:3], a[192:195], v[120:123], a[0:3]\n"
"ds_read_b128 a[248:251], v4 offset:63488\n"
"v_mfma_f32_16x16x32_bf16 a[4:7], a[192:195], v[124:127], a[4:7]\n"
"buffer_load_dwordx4 a[152:155], v2, s[36:39], s79 offen offset:2048 sc1\n"
"v_mfma_f32_16x16x32_bf16 a[8:11], a[196:199], v[120:123], a[8:11]\n"
"ds_read_b128 a[252:255], v4 offset:64512\n"
"v_mfma_f32_16x16x32_bf16 a[12:15], a[196:199], v[124:127], a[12:15]\n"
"v_mfma_f32_16x16x32_bf16 a[16:19], a[200:203], v[120:123], a[16:19]\n"
"ds_read_b128 a[192:195], v3 offset:0\n"
"buffer_load_dwordx4 a[156:159], v2, s[36:39], s79 offen offset:3072 sc1\n"
"v_mfma_f32_16x16x32_bf16 a[20:23], a[200:203], v[124:127], a[20:23]\n"
"v_mfma_f32_16x16x32_bf16 a[24:27], a[204:207], v[120:123], a[24:27]\n"
"ds_read_b128 a[196:199], v3 offset:1024\n"
"buffer_load_dwordx4 a[160:163], v1, s[36:39], s99 offen offset:0 sc1\n"
"v_mfma_f32_16x16x32_bf16 a[28:31], a[204:207], v[124:127], a[28:31]\n"
"v_mfma_f32_16x16x32_bf16 a[32:35], a[208:211], v[120:123], a[32:35]\n"
"ds_read_b128 a[200:203], v3 offset:2048\n"
"buffer_load_dwordx4 a[164:167], v1, s[36:39], s99 offen offset:1024 sc1\n"
"v_mfma_f32_16x16x32_bf16 a[36:39], a[208:211], v[124:127], a[36:39]\n"
"v_mfma_f32_16x16x32_bf16 a[40:43], a[212:215], v[120:123], a[40:43]\n"
"ds_read_b128 a[204:207], v3 offset:3072\n"
"v_mfma_f32_16x16x32_bf16 a[44:47], a[212:215], v[124:127], a[44:47]\n"
"buffer_load_dwordx4 a[168:171], v1, s[36:39], s99 offen offset:2048 sc1\n"
"v_mfma_f32_16x16x32_bf16 a[48:51], a[216:219], v[120:123], a[48:51]\n"
"ds_read_b128 a[208:211], v3 offset:4096\n"
"v_mfma_f32_16x16x32_bf16 a[52:55], a[216:219], v[124:127], a[52:55]\n"
"buffer_load_dwordx4 a[172:175], v1, s[36:39], s99 offen offset:3072 sc1\n"
"v_mfma_f32_16x16x32_bf16 a[56:59], a[220:223], v[120:123], a[56:59]\n"
"ds_read_b128 a[212:215], v3 offset:5120\n"
"v_mfma_f32_16x16x32_bf16 a[60:63], a[220:223], v[124:127], a[60:63]\n"
"s_waitcnt lgkmcnt(6)\n"
"buffer_load_dwordx4 a[176:179], v2, s[36:39], s99 offen offset:0 sc1\n"
"v_mfma_f32_16x16x32_bf16 a[64:67], a[224:227], v[120:123], a[64:67]\n"
"ds_read_b128 a[216:219], v3 offset:6144\n"
"v_mfma_f32_16x16x32_bf16 a[68:71], a[224:227], v[124:127], a[68:71]\n"
"v_mfma_f32_16x16x32_bf16 a[72:75], a[228:231], v[120:123], a[72:75]\n"
"ds_read_b128 a[220:223], v3 offset:7168\n"
"buffer_load_dwordx4 a[180:183], v2, s[36:39], s99 offen offset:1024 sc1\n"
"v_mfma_f32_16x16x32_bf16 a[76:79], a[228:231], v[124:127], a[76:79]\n"
"v_mfma_f32_16x16x32_bf16 a[80:83], a[232:235], v[120:123], a[80:83]\n"
"ds_read_b128 a[224:227], v3 offset:8192\n"
"buffer_load_dwordx4 a[184:187], v2, s[36:39], s99 offen offset:2048 sc1\n"
"v_mfma_f32_16x16x32_bf16 a[84:87], a[232:235], v[124:127], a[84:87]\n"
"v_mfma_f32_16x16x32_bf16 a[88:91], a[236:239], v[120:123], a[88:91]\n"
"ds_read_b128 a[228:231], v3 offset:9216\n"
"buffer_load_dwordx4 a[188:191], v2, s[36:39], s99 offen offset:3072 sc1\n"
"v_mfma_f32_16x16x32_bf16 a[92:95], a[236:239], v[124:127], a[92:95]\n"
"v_mfma_f32_16x16x32_bf16 a[96:99], a[240:243], v[120:123], a[96:99]\n"
"ds_read_b128 a[232:235], v3 offset:10240\n"
"v_mfma_f32_16x16x32_bf16 a[100:103], a[240:243], v[124:127], a[100:103]\n"
"v_mfma_f32_16x16x32_bf16 a[104:107], a[244:247], v[120:123], a[104:107]\n"
"ds_read_b128 a[236:239], v3 offset:11264\n"
"v_mfma_f32_16x16x32_bf16 a[108:111], a[244:247], v[124:127], a[108:111]\n"
"v_mfma_f32_16x16x32_bf16 a[112:115], a[248:251], v[120:123], a[112:115]\n"
"ds_read_b128 a[240:243], v3 offset:12288\n"
"v_mfma_f32_16x16x32_bf16 a[116:119], a[248:251], v[124:127], a[116:119]\n"
"v_mfma_f32_16x16x32_bf16 a[120:123], a[252:255], v[120:123], a[120:123]\n"
"ds_read_b128 a[244:247], v3 offset:13312\n"
"v_mfma_f32_16x16x32_bf16 a[124:127], a[252:255], v[124:127], a[124:127]\n"
"s_nop 15\n"
"s_lshl_b32 s78, s68, 16\n"
"s_lshl_b32 s96, s49, 14\n"
"s_add_u32 s78, s78, s96\n"
"s_add_u32 s78, s78, 79691776\n"
"s_lshl_b32 s98, s68, 12\n"
"s_lshl_b32 s96, s49, 10\n"
"s_add_u32 s98, s98, s96\n"
"s_add_u32 s98, s98, 97517568\n"
"buffer_store_dwordx4 v[16:19], v1, s[36:39], s98 offen sc1\n"
"v_accvgpr_read_b32 v136, a0\n"
"v_accvgpr_read_b32 v137, a1\n"
"v_accvgpr_read_b32 v138, a2\n"
"v_accvgpr_read_b32 v139, a3\n"
"v_accvgpr_read_b32 v140, a8\n"
"v_accvgpr_read_b32 v141, a9\n"
"v_accvgpr_read_b32 v142, a10\n"
"v_accvgpr_read_b32 v143, a11\n"
"v_cvt_pk_bf16_f32 v168, v136, v137\n"
"v_cvt_pk_bf16_f32 v169, v138, v139\n"
"v_cvt_pk_bf16_f32 v170, v140, v141\n"
"v_cvt_pk_bf16_f32 v171, v142, v143\n"
"buffer_store_dwordx4 v[168:171], v1, s[36:39], s78 offen offset:0 sc1\n"
"v_accvgpr_read_b32 v144, a16\n"
"v_accvgpr_read_b32 v145, a17\n"
"v_accvgpr_read_b32 v146, a18\n"
"v_accvgpr_read_b32 v147, a19\n"
"v_accvgpr_read_b32 v148, a24\n"
"v_accvgpr_read_b32 v149, a25\n"
"v_accvgpr_read_b32 v150, a26\n"
"v_accvgpr_read_b32 v151, a27\n"
"v_cvt_pk_bf16_f32 v172, v144, v145\n"
"v_cvt_pk_bf16_f32 v173, v146, v147\n"
"v_cvt_pk_bf16_f32 v174, v148, v149\n"
"v_cvt_pk_bf16_f32 v175, v150, v151\n"
"buffer_store_dwordx4 v[172:175], v1, s[36:39], s78 offen offset:1024 sc1\n"
"v_accvgpr_read_b32 v152, a32\n"
"v_accvgpr_read_b32 v153, a33\n"
"v_accvgpr_read_b32 v154, a34\n"
"v_accvgpr_read_b32 v155, a35\n"
"v_accvgpr_read_b32 v156, a40\n"
"v_accvgpr_read_b32 v157, a41\n"
"v_accvgpr_read_b32 v158, a42\n"
"v_accvgpr_read_b32 v159, a43\n"
"v_cvt_pk_bf16_f32 v176, v152, v153\n"
"v_cvt_pk_bf16_f32 v177, v154, v155\n"
"v_cvt_pk_bf16_f32 v178, v156, v157\n"
"v_cvt_pk_bf16_f32 v179, v158, v159\n"
"buffer_store_dwordx4 v[176:179], v1, s[36:39], s78 offen offset:2048 sc1\n"
"v_accvgpr_read_b32 v160, a48\n"
"v_accvgpr_read_b32 v161, a49\n"
"v_accvgpr_read_b32 v162, a50\n"
"v_accvgpr_read_b32 v163, a51\n"
"v_accvgpr_read_b32 v164, a56\n"
"v_accvgpr_read_b32 v165, a57\n"
"v_accvgpr_read_b32 v166, a58\n"
"v_accvgpr_read_b32 v167, a59\n"
"v_cvt_pk_bf16_f32 v180, v160, v161\n"
"v_cvt_pk_bf16_f32 v181, v162, v163\n"
"v_cvt_pk_bf16_f32 v182, v164, v165\n"
"v_cvt_pk_bf16_f32 v183, v166, v167\n"
"buffer_store_dwordx4 v[180:183], v1, s[36:39], s78 offen offset:3072 sc1\n"
"v_accvgpr_read_b32 v136, a64\n"
"v_accvgpr_read_b32 v137, a65\n"
"v_accvgpr_read_b32 v138, a66\n"
"v_accvgpr_read_b32 v139, a67\n"
"v_accvgpr_read_b32 v140, a72\n"
"v_accvgpr_read_b32 v141, a73\n"
"v_accvgpr_read_b32 v142, a74\n"
"v_accvgpr_read_b32 v143, a75\n"
"v_cvt_pk_bf16_f32 v168, v136, v137\n"
"v_cvt_pk_bf16_f32 v169, v138, v139\n"
"v_cvt_pk_bf16_f32 v170, v140, v141\n"
"v_cvt_pk_bf16_f32 v171, v142, v143\n"
"buffer_store_dwordx4 v[168:171], v2, s[36:39], s78 offen offset:0 sc1\n"
"v_accvgpr_read_b32 v144, a80\n"
"v_accvgpr_read_b32 v145, a81\n"
"v_accvgpr_read_b32 v146, a82\n"
"v_accvgpr_read_b32 v147, a83\n"
"v_accvgpr_read_b32 v148, a88\n"
"v_accvgpr_read_b32 v149, a89\n"
"v_accvgpr_read_b32 v150, a90\n"
"v_accvgpr_read_b32 v151, a91\n"
"v_cvt_pk_bf16_f32 v172, v144, v145\n"
"v_cvt_pk_bf16_f32 v173, v146, v147\n"
"v_cvt_pk_bf16_f32 v174, v148, v149\n"
"v_cvt_pk_bf16_f32 v175, v150, v151\n"
"buffer_store_dwordx4 v[172:175], v2, s[36:39], s78 offen offset:1024 sc1\n"
"v_accvgpr_read_b32 v152, a96\n"
"v_accvgpr_read_b32 v153, a97\n"
"v_accvgpr_read_b32 v154, a98\n"
"v_accvgpr_read_b32 v155, a99\n"
"v_accvgpr_read_b32 v156, a104\n"
"v_accvgpr_read_b32 v157, a105\n"
"v_accvgpr_read_b32 v158, a106\n"
"v_accvgpr_read_b32 v159, a107\n"
"v_cvt_pk_bf16_f32 v176, v152, v153\n"
"v_cvt_pk_bf16_f32 v177, v154, v155\n"
"v_cvt_pk_bf16_f32 v178, v156, v157\n"
"v_cvt_pk_bf16_f32 v179, v158, v159\n"
"buffer_store_dwordx4 v[176:179], v2, s[36:39], s78 offen offset:2048 sc1\n"
"v_accvgpr_read_b32 v160, a112\n"
"v_accvgpr_read_b32 v161, a113\n"
"v_accvgpr_read_b32 v162, a114\n"
"v_accvgpr_read_b32 v163, a115\n"
"v_accvgpr_read_b32 v164, a120\n"
"v_accvgpr_read_b32 v165, a121\n"
"v_accvgpr_read_b32 v166, a122\n"
"v_accvgpr_read_b32 v167, a123\n"
"v_cvt_pk_bf16_f32 v180, v160, v161\n"
"v_cvt_pk_bf16_f32 v181, v162, v163\n"
"v_cvt_pk_bf16_f32 v182, v164, v165\n"
"v_cvt_pk_bf16_f32 v183, v166, v167\n"
"buffer_store_dwordx4 v[180:183], v2, s[36:39], s78 offen offset:3072 sc1\n"
"v_accvgpr_read_b32 v136, a4\n"
"v_accvgpr_read_b32 v137, a5\n"
"v_accvgpr_read_b32 v138, a6\n"
"v_accvgpr_read_b32 v139, a7\n"
"v_accvgpr_read_b32 v140, a12\n"
"v_accvgpr_read_b32 v141, a13\n"
"v_accvgpr_read_b32 v142, a14\n"
"v_accvgpr_read_b32 v143, a15\n"
"v_cvt_pk_bf16_f32 v168, v136, v137\n"
"v_cvt_pk_bf16_f32 v169, v138, v139\n"
"v_cvt_pk_bf16_f32 v170, v140, v141\n"
"v_cvt_pk_bf16_f32 v171, v142, v143\n"
"s_add_u32 s78, s78, 8192\n"
"buffer_store_dwordx4 v[168:171], v1, s[36:39], s78 offen offset:0 sc1\n"
"v_accvgpr_read_b32 v144, a20\n"
"v_accvgpr_read_b32 v145, a21\n"
"v_accvgpr_read_b32 v146, a22\n"
"v_accvgpr_read_b32 v147, a23\n"
"v_accvgpr_read_b32 v148, a28\n"
"v_accvgpr_read_b32 v149, a29\n"
"v_accvgpr_read_b32 v150, a30\n"
"v_accvgpr_read_b32 v151, a31\n"
"v_cvt_pk_bf16_f32 v172, v144, v145\n"
"v_cvt_pk_bf16_f32 v173, v146, v147\n"
"v_cvt_pk_bf16_f32 v174, v148, v149\n"
"v_cvt_pk_bf16_f32 v175, v150, v151\n"
"buffer_store_dwordx4 v[172:175], v1, s[36:39], s78 offen offset:1024 sc1\n"
"v_accvgpr_read_b32 v152, a36\n"
"v_accvgpr_read_b32 v153, a37\n"
"v_accvgpr_read_b32 v154, a38\n"
"v_accvgpr_read_b32 v155, a39\n"
"v_accvgpr_read_b32 v156, a44\n"
"v_accvgpr_read_b32 v157, a45\n"
"v_accvgpr_read_b32 v158, a46\n"
"v_accvgpr_read_b32 v159, a47\n"
"v_cvt_pk_bf16_f32 v176, v152, v153\n"
"v_cvt_pk_bf16_f32 v177, v154, v155\n"
"v_cvt_pk_bf16_f32 v178, v156, v157\n"
"v_cvt_pk_bf16_f32 v179, v158, v159\n"
"buffer_store_dwordx4 v[176:179], v1, s[36:39], s78 offen offset:2048 sc1\n"
"v_accvgpr_read_b32 v160, a52\n"
"v_accvgpr_read_b32 v161, a53\n"
"v_accvgpr_read_b32 v162, a54\n"
"v_accvgpr_read_b32 v163, a55\n"
"v_accvgpr_read_b32 v164, a60\n"
"v_accvgpr_read_b32 v165, a61\n"
"v_accvgpr_read_b32 v166, a62\n"
"v_accvgpr_read_b32 v167, a63\n"
"v_cvt_pk_bf16_f32 v180, v160, v161\n"
"v_cvt_pk_bf16_f32 v181, v162, v163\n"
"v_cvt_pk_bf16_f32 v182, v164, v165\n"
"v_cvt_pk_bf16_f32 v183, v166, v167\n"
"buffer_store_dwordx4 v[180:183], v1, s[36:39], s78 offen offset:3072 sc1\n"
"v_accvgpr_read_b32 v136, a68\n"
"v_accvgpr_read_b32 v137, a69\n"
"v_accvgpr_read_b32 v138, a70\n"
"v_accvgpr_read_b32 v139, a71\n"
"v_accvgpr_read_b32 v140, a76\n"
"v_accvgpr_read_b32 v141, a77\n"
"v_accvgpr_read_b32 v142, a78\n"
"v_accvgpr_read_b32 v143, a79\n"
"v_cvt_pk_bf16_f32 v168, v136, v137\n"
"v_cvt_pk_bf16_f32 v169, v138, v139\n"
"v_cvt_pk_bf16_f32 v170, v140, v141\n"
"v_cvt_pk_bf16_f32 v171, v142, v143\n"
"buffer_store_dwordx4 v[168:171], v2, s[36:39], s78 offen offset:0 sc1\n"
"v_accvgpr_read_b32 v144, a84\n"
"v_accvgpr_read_b32 v145, a85\n"
"v_accvgpr_read_b32 v146, a86\n"
"v_accvgpr_read_b32 v147, a87\n"
"v_accvgpr_read_b32 v148, a92\n"
"v_accvgpr_read_b32 v149, a93\n"
"v_accvgpr_read_b32 v150, a94\n"
"v_accvgpr_read_b32 v151, a95\n"
"v_cvt_pk_bf16_f32 v172, v144, v145\n"
"v_cvt_pk_bf16_f32 v173, v146, v147\n"
"v_cvt_pk_bf16_f32 v174, v148, v149\n"
"v_cvt_pk_bf16_f32 v175, v150, v151\n"
"buffer_store_dwordx4 v[172:175], v2, s[36:39], s78 offen offset:1024 sc1\n"
"v_accvgpr_read_b32 v152, a100\n"
"v_accvgpr_read_b32 v153, a101\n"
"v_accvgpr_read_b32 v154, a102\n"
"v_accvgpr_read_b32 v155, a103\n"
"v_accvgpr_read_b32 v156, a108\n"
"v_accvgpr_read_b32 v157, a109\n"
"v_accvgpr_read_b32 v158, a110\n"
"v_accvgpr_read_b32 v159, a111\n"
"v_cvt_pk_bf16_f32 v176, v152, v153\n"
"v_cvt_pk_bf16_f32 v177, v154, v155\n"
"v_cvt_pk_bf16_f32 v178, v156, v157\n"
"v_cvt_pk_bf16_f32 v179, v158, v159\n"
"buffer_store_dwordx4 v[176:179], v2, s[36:39], s78 offen offset:2048 sc1\n"
"v_accvgpr_read_b32 v160, a116\n"
"v_accvgpr_read_b32 v161, a117\n"
"v_accvgpr_read_b32 v162, a118\n"
"v_accvgpr_read_b32 v163, a119\n"
"v_accvgpr_read_b32 v164, a124\n"
"v_accvgpr_read_b32 v165, a125\n"
"v_accvgpr_read_b32 v166, a126\n"
"v_accvgpr_read_b32 v167, a127\n"
"v_cvt_pk_bf16_f32 v180, v160, v161\n"
"v_cvt_pk_bf16_f32 v181, v162, v163\n"
"v_cvt_pk_bf16_f32 v182, v164, v165\n"
"v_cvt_pk_bf16_f32 v183, v166, v167\n"
"buffer_store_dwordx4 v[180:183], v2, s[36:39], s78 offen offset:3072 sc1\n"
"s_mov_b32 s69, 2\n"
"s_mov_b32 s82, 0\n"
"s_lshl_b32 s83, s51, 1\n"
"s_add_u32 s83, s83, 2\n"
"s_lshl_b32 s96, s53, 1\n"
"s_add_u32 s97, s82, 1\n"
"s_min_u32 s98, s83, s96\n"
"s_max_u32 s98, s98, s97\n"
"s_sub_u32 s55, s98, s97\n"
"s_sub_u32 s56, s83, s98\n"
"s_mov_b32 s54, s98\n"
"s_waitcnt vmcnt(17)\n"
"s_waitcnt lgkmcnt(6)\n"
"v_mfma_f32_16x16x32_bf16 v[32:35], a[192:195], a[128:131], 0\n"
"ds_read_b128 a[248:251], v3 offset:14336\n"
"v_accvgpr_write_b32 a0, 0\n"
"v_accvgpr_write_b32 a1, 0\n"
"v_mfma_f32_16x16x32_bf16 v[36:39], a[192:195], a[160:163], 0\n"
"v_accvgpr_write_b32 a2, 0\n"
"v_accvgpr_write_b32 a3, 0\n"
"v_mfma_f32_16x16x32_bf16 v[32:35], a[196:199], a[132:135], v[32:35]\n"
"ds_read_b128 a[252:255], v3 offset:15360\n"
"v_accvgpr_write_b32 a4, 0\n"
"v_accvgpr_write_b32 a5, 0\n"
"v_mfma_f32_16x16x32_bf16 v[36:39], a[196:199], a[164:167], v[36:39]\n"
"v_accvgpr_write_b32 a6, 0\n"
"v_accvgpr_write_b32 a7, 0\n"
"v_mfma_f32_16x16x32_bf16 v[32:35], a[200:203], a[136:139], v[32:35]\n"
"ds_read_b128 a[192:195], v3 offset:16384\n"
"v_accvgpr_write_b32 a8, 0\n"
"v_accvgpr_write_b32 a9, 0\n"
"v_mfma_f32_16x16x32_bf16 v[36:39], a[200:203], a[168:171], v[36:39]\n"
"v_accvgpr_write_b32 a10, 0\n"
"v_accvgpr_write_b32 a11, 0\n"
"v_mfma_f32_16x16x32_bf16 v[32:35], a[204:207], a[140:143], v[32:35]\n"
"ds_read_b128 a[196:199], v3 offset:17408\n"
"v_accvgpr_write_b32 a12, 0\n"
"v_accvgpr_write_b32 a13, 0\n"
"v_mfma_f32_16x16x32_bf16 v[36:39], a[204:207], a[172:175], v[36:39]\n"
"v_accvgpr_write_b32 a14, 0\n"
"v_accvgpr_write_b32 a15, 0\n"
"v_mfma_f32_16x16x32_bf16 v[32:35], a[208:211], a[144:147], v[32:35]\n"
"ds_read_b128 a[200:203], v3 offset:18432\n"
"v_accvgpr_write_b32 a16, 0\n"
"v_accvgpr_write_b32 a17, 0\n"
"v_mfma_f32_16x16x32_bf16 v[36:39], a[208:211], a[176:179], v[36:39]\n"
"v_accvgpr_write_b32 a18, 0\n"
"v_accvgpr_write_b32 a19, 0\n"
"v_mfma_f32_16x16x32_bf16 v[32:35], a[212:215], a[148:151], v[32:35]\n"
"ds_read_b128 a[204:207], v3 offset:19456\n"
"v_accvgpr_write_b32 a20, 0\n"
"v_accvgpr_write_b32 a21, 0\n"
"v_mfma_f32_16x16x32_bf16 v[36:39], a[212:215], a[180:183], v[36:39]\n"
"v_accvgpr_write_b32 a22, 0\n"
"v_accvgpr_write_b32 a23, 0\n"
"v_mfma_f32_16x16x32_bf16 v[32:35], a[216:219], a[152:155], v[32:35]\n"
"ds_read_b128 a[208:211], v3 offset:20480\n"
"v_accvgpr_write_b32 a24, 0\n"
"v_accvgpr_write_b32 a25, 0\n"
"v_mfma_f32_16x16x32_bf16 v[36:39], a[216:219], a[184:187], v[36:39]\n"
"v_accvgpr_write_b32 a26, 0\n"
"v_accvgpr_write_b32 a27, 0\n"
"v_mfma_f32_16x16x32_bf16 v[32:35], a[220:223], a[156:159], v[32:35]\n"
"ds_read_b128 a[212:215], v3 offset:21504\n"
"v_accvgpr_write_b32 a28, 0\n"
"v_accvgpr_write_b32 a29, 0\n"
"v_mfma_f32_16x16x32_bf16 v[36:39], a[220:223], a[188:191], v[36:39]\n"
"s_waitcnt lgkmcnt(6)\n"
"v_accvgpr_write_b32 a30, 0\n"
"v_accvgpr_write_b32 a31, 0\n"
"v_mfma_f32_16x16x32_bf16 v[40:43], a[224:227], a[128:131], 0\n"
"ds_read_b128 a[216:219], v3 offset:22528\n"
"v_accvgpr_write_b32 a32, 0\n"
"v_accvgpr_write_b32 a33, 0\n"
"v_mfma_f32_16x16x32_bf16 v[44:47], a[224:227], a[160:163], 0\n"
"v_accvgpr_write_b32 a34, 0\n"
"v_accvgpr_write_b32 a35, 0\n"
"v_mfma_f32_16x16x32_bf16 v[40:43], a[228:231], a[132:135], v[40:43]\n"
"ds_read_b128 a[220:223], v3 offset:23552\n"
"v_accvgpr_write_b32 a36, 0\n"
"v_accvgpr_write_b32 a37, 0\n"
"v_mfma_f32_16x16x32_bf16 v[44:47], a[228:231], a[164:167], v[44:47]\n"
"v_accvgpr_write_b32 a38, 0\n"
"v_accvgpr_write_b32 a39, 0\n"
"v_mfma_f32_16x16x32_bf16 v[40:43], a[232:235], a[136:139], v[40:43]\n"
"ds_read_b128 a[224:227], v3 offset:24576\n"
"v_accvgpr_write_b32 a40, 0\n"
"v_accvgpr_write_b32 a41, 0\n"
"v_mfma_f32_16x16x32_bf16 v[44:47], a[232:235], a[168:171], v[44:47]\n"
"v_accvgpr_write_b32 a42, 0\n"
"v_accvgpr_write_b32 a43, 0\n"
"v_mfma_f32_16x16x32_bf16 v[40:43], a[236:239], a[140:143], v[40:43]\n"
"ds_read_b128 a[228:231], v3 offset:25600\n"
"v_accvgpr_write_b32 a44, 0\n"
"v_accvgpr_write_b32 a45, 0\n"
"v_mfma_f32_16x16x32_bf16 v[44:47], a[236:239], a[172:175], v[44:47]\n"
"v_accvgpr_write_b32 a46, 0\n"
"v_accvgpr_write_b32 a47, 0\n"
"v_mfma_f32_16x16x32_bf16 v[40:43], a[240:243], a[144:147], v[40:43]\n"
"ds_read_b128 a[232:235], v3 offset:26624\n"
"v_accvgpr_write_b32 a48, 0\n"
"v_accvgpr_write_b32 a49, 0\n"
"v_mfma_f32_16x16x32_bf16 v[44:47], a[240:243], a[176:179], v[44:47]\n"
"v_accvgpr_write_b32 a50, 0\n"
"v_accvgpr_write_b32 a51, 0\n"
"v_mfma_f32_16x16x32_bf16 v[40:43], a[244:247], a[148:151], v[40:43]\n"
"ds_read_b128 a[236:239], v3 offset:27648\n"
"v_accvgpr_write_b32 a52, 0\n"
"v_accvgpr_write_b32 a53, 0\n"
"v_mfma_f32_16x16x32_bf16 v[44:47], a[244:247], a[180:183], v[44:47]\n"
"v_accvgpr_write_b32 a54, 0\n"
"v_accvgpr_write_b32 a55, 0\n"
"v_mfma_f32_16x16x32_bf16 v[40:43], a[248:251], a[152:155], v[40:43]\n"
"ds_read_b128 a[240:243], v3 offset:28672\n"
"v_accvgpr_write_b32 a56, 0\n"
"v_accvgpr_write_b32 a57, 0\n"
"v_mfma_f32_16x16x32_bf16 v[44:47], a[248:251], a[184:187], v[44:47]\n"
"v_accvgpr_write_b32 a58, 0\n"
"v_accvgpr_write_b32 a59, 0\n"
"v_mfma_f32_16x16x32_bf16 v[40:43], a[252:255], a[156:159], v[40:43]\n"
"ds_read_b128 a[244:247], v3 offset:29696\n"
"v_accvgpr_write_b32 a60, 0\n"
"v_accvgpr_write_b32 a61, 0\n"
"v_mfma_f32_16x16x32_bf16 v[44:47], a[252:255], a[188:191], v[44:47]\n"
"s_waitcnt lgkmcnt(6)\n"
"v_accvgpr_write_b32 a62, 0\n"
"v_accvgpr_write_b32 a63, 0\n"
"v_mfma_f32_16x16x32_bf16 v[48:51], a[192:195], a[128:131], 0\n"
"ds_read_b128 a[248:251], v3 offset:30720\n"
"v_accvgpr_write_b32 a64, 0\n"
"v_accvgpr_write_b32 a65, 0\n"
"v_mfma_f32_16x16x32_bf16 v[52:55], a[192:195], a[160:163], 0\n"
"v_accvgpr_write_b32 a66, 0\n"
"v_accvgpr_write_b32 a67, 0\n"
"v_mfma_f32_16x16x32_bf16 v[48:51], a[196:199], a[132:135], v[48:51]\n"
"ds_read_b128 a[252:255], v3 offset:31744\n"
"v_accvgpr_write_b32 a68, 0\n"
"v_accvgpr_write_b32 a69, 0\n"
"v_mfma_f32_16x16x32_bf16 v[52:55], a[196:199], a[164:167], v[52:55]\n"
"v_accvgpr_write_b32 a70, 0\n"
"v_accvgpr_write_b32 a71, 0\n"
"v_mfma_f32_16x16x32_bf16 v[48:51], a[200:203], a[136:139], v[48:51]\n"
"v_accvgpr_write_b32 a72, 0\n"
"v_accvgpr_write_b32 a73, 0\n"
"v_mfma_f32_16x16x32_bf16 v[52:55], a[200:203], a[168:171], v[52:55]\n"
"v_accvgpr_write_b32 a74, 0\n"
"v_accvgpr_write_b32 a75, 0\n"
"v_mfma_f32_16x16x32_bf16 v[48:51], a[204:207], a[140:143], v[48:51]\n"
"v_accvgpr_write_b32 a76, 0\n"
"v_accvgpr_write_b32 a77, 0\n"
"v_mfma_f32_16x16x32_bf16 v[52:55], a[204:207], a[172:175], v[52:55]\n"
"v_accvgpr_write_b32 a78, 0\n"
"v_accvgpr_write_b32 a79, 0\n"
"v_mfma_f32_16x16x32_bf16 v[48:51], a[208:211], a[144:147], v[48:51]\n"
"v_accvgpr_write_b32 a80, 0\n"
"v_accvgpr_write_b32 a81, 0\n"
"v_mfma_f32_16x16x32_bf16 v[52:55], a[208:211], a[176:179], v[52:55]\n"
"v_accvgpr_write_b32 a82, 0\n"
"v_accvgpr_write_b32 a83, 0\n"
"v_mfma_f32_16x16x32_bf16 v[48:51], a[212:215], a[148:151], v[48:51]\n"
"v_accvgpr_write_b32 a84, 0\n"
"v_accvgpr_write_b32 a85, 0\n"
"v_mfma_f32_16x16x32_bf16 v[52:55], a[212:215], a[180:183], v[52:55]\n"
"v_accvgpr_write_b32 a86, 0\n"
"v_accvgpr_write_b32 a87, 0\n"
"v_mfma_f32_16x16x32_bf16 v[48:51], a[216:219], a[152:155], v[48:51]\n"
"v_accvgpr_write_b32 a88, 0\n"
"v_accvgpr_write_b32 a89, 0\n"
"v_mfma_f32_16x16x32_bf16 v[52:55], a[216:219], a[184:187], v[52:55]\n"
"v_accvgpr_write_b32 a90, 0\n"
"v_accvgpr_write_b32 a91, 0\n"
"v_mfma_f32_16x16x32_bf16 v[48:51], a[220:223], a[156:159], v[48:51]\n"
"v_accvgpr_write_b32 a92, 0\n"
"v_accvgpr_write_b32 a93, 0\n"
"v_mfma_f32_16x16x32_bf16 v[52:55], a[220:223], a[188:191], v[52:55]\n"
"s_waitcnt lgkmcnt(0)\n"
"v_accvgpr_write_b32 a94, 0\n"
"v_accvgpr_write_b32 a95, 0\n"
"v_mfma_f32_16x16x32_bf16 v[56:59], a[224:227], a[128:131], 0\n"
"v_accvgpr_write_b32 a96, 0\n"
"v_accvgpr_write_b32 a97, 0\n"
"v_mfma_f32_16x16x32_bf16 v[60:63], a[224:227], a[160:163], 0\n"
"v_accvgpr_write_b32 a98, 0\n"
"v_accvgpr_write_b32 a99, 0\n"
"v_mfma_f32_16x16x32_bf16 v[56:59], a[228:231], a[132:135], v[56:59]\n"
"v_accvgpr_write_b32 a100, 0\n"
"v_accvgpr_write_b32 a101, 0\n"
"v_mfma_f32_16x16x32_bf16 v[60:63], a[228:231], a[164:167], v[60:63]\n"
"v_accvgpr_write_b32 a102, 0\n"
"v_accvgpr_write_b32 a103, 0\n"
"v_mfma_f32_16x16x32_bf16 v[56:59], a[232:235], a[136:139], v[56:59]\n"
"v_accvgpr_write_b32 a104, 0\n"
"v_accvgpr_write_b32 a105, 0\n"
"v_mfma_f32_16x16x32_bf16 v[60:63], a[232:235], a[168:171], v[60:63]\n"
"v_accvgpr_write_b32 a106, 0\n"
"v_accvgpr_write_b32 a107, 0\n"
"v_mfma_f32_16x16x32_bf16 v[56:59], a[236:239], a[140:143], v[56:59]\n"
"v_accvgpr_write_b32 a108, 0\n"
"v_accvgpr_write_b32 a109, 0\n"
"v_mfma_f32_16x16x32_bf16 v[60:63], a[236:239], a[172:175], v[60:63]\n"
"v_accvgpr_write_b32 a110, 0\n"
"v_accvgpr_write_b32 a111, 0\n"
"v_mfma_f32_16x16x32_bf16 v[56:59], a[240:243], a[144:147], v[56:59]\n"
"v_accvgpr_write_b32 a112, 0\n"
"v_accvgpr_write_b32 a113, 0\n"
"v_mfma_f32_16x16x32_bf16 v[60:63], a[240:243], a[176:179], v[60:63]\n"
"v_accvgpr_write_b32 a114, 0\n"
"v_accvgpr_write_b32 a115, 0\n"
"v_mfma_f32_16x16x32_bf16 v[56:59], a[244:247], a[148:151], v[56:59]\n"
"v_accvgpr_write_b32 a116, 0\n"
"v_accvgpr_write_b32 a117, 0\n"
"v_mfma_f32_16x16x32_bf16 v[60:63], a[244:247], a[180:183], v[60:63]\n"
"v_accvgpr_write_b32 a118, 0\n"
"v_accvgpr_write_b32 a119, 0\n"
"v_mfma_f32_16x16x32_bf16 v[56:59], a[248:251], a[152:155], v[56:59]\n"
"v_accvgpr_write_b32 a120, 0\n"
"v_accvgpr_write_b32 a121, 0\n"
"v_mfma_f32_16x16x32_bf16 v[60:63], a[248:251], a[184:187], v[60:63]\n"
"v_accvgpr_write_b32 a122, 0\n"
"v_accvgpr_write_b32 a123, 0\n"
"v_mfma_f32_16x16x32_bf16 v[56:59], a[252:255], a[156:159], v[56:59]\n"
"v_accvgpr_write_b32 a124, 0\n"
"v_accvgpr_write_b32 a125, 0\n"
"v_mfma_f32_16x16x32_bf16 v[60:63], a[252:255], a[188:191], v[60:63]\n"
"v_accvgpr_write_b32 a126, 0\n"
"v_accvgpr_write_b32 a127, 0\n"
"s_barrier\n"
"s_add_u32 s60, s61, 0x8000\n"
"s_sub_u32 s62, s62, 1\n"
"s_cbranch_scc1 L_ev_fs1_%=\n"
"L_evr_fs1_%=:\n"
"s_add_u32 m0, s65, 0\n"
"s_nop 0\n"
"buffer_load_dwordx4 v1, s[40:43], s60 offen offset:0 sc1 lds\n"
"buffer_load_dwordx4 v1, s[40:43], s60 offen offset:1024 sc1 lds\n"
"buffer_load_dwordx4 v1, s[40:43], s60 offen offset:2048 sc1 lds\n"
"buffer_load_dwordx4 v1, s[40:43], s60 offen offset:3072 sc1 lds\n"
"s_add_u32 m0, s65, 4096\n"
"s_nop 0\n"
"buffer_load_dwordx4 v2, s[40:43], s60 offen offset:0 sc1 lds\n"
"buffer_load_dwordx4 v2, s[40:43], s60 offen offset:1024 sc1 lds\n"
"buffer_load_dwordx4 v2, s[40:43], s60 offen offset:2048 sc1 lds\n"
"buffer_load_dwordx4 v2, s[40:43], s60 offen offset:3072 sc1 lds\n"
"ds_read_b128 a[192:195], v3 offset:32768\n"
"ds_read_b128 a[196:199], v3 offset:33792\n"
"ds_read_b128 a[200:203], v3 offset:34816\n"
"ds_read_b128 a[204:207], v3 offset:35840\n"
"ds_read_b128 a[208:211], v3 offset:36864\n"
"ds_read_b128 a[212:215], v3 offset:37888\n"
"ds_read_b128 a[216:219], v3 offset:38912\n"
"ds_read_b128 a[220:223], v3 offset:39936\n"
"ds_read_b128 a[224:227], v3 offset:40960\n"
"ds_read_b128 a[228:231], v3 offset:41984\n"
"ds_read_b128 a[232:235], v3 offset:43008\n"
"ds_read_b128 a[236:239], v3 offset:44032\n"
"ds_read_b128 a[240:243], v3 offset:45056\n"
"ds_read_b128 a[244:247], v3 offset:46080\n"
"v_mov_b32 v16, 0\n"
"v_mov_b32 v18, 0\n"
"v_mov_b32 v17, 0\n"
"v_mov_b32 v19, 0\n"
"s_lshl_b32 s96, s53, 1\n"
"s_cmp_lt_u32 s82, s96\n"
"s_cbranch_scc1 L_fnm_fs1_%=\n"
"s_lshl_b32 s96, s82, 6\n"
"v_subrev_u32 v5, s96, v7\n"
"v_subrev_u32 v6, s96, v8\n"
"s_nop 7\n"
"v_cmp_gt_i32_e64 s[70:71], 0, v5\n"
"v_cmp_gt_i32_e64 s[72:73], 1, v5\n"
"v_cmp_gt_i32_e64 s[74:75], 2, v5\n"
"v_cndmask_b32_e64 v32, v32, v9, s[70:71]\n"
"v_cmp_gt_i32_e64 s[76:77], 3, v5\n"
"v_cndmask_b32_e64 v33, v33, v9, s[72:73]\n"
"v_cmp_gt_i32_e64 s[70:71], 0, v6\n"
"v_cndmask_b32_e64 v34, v34, v9, s[74:75]\n"
"v_cmp_gt_i32_e64 s[72:73], 1, v6\n"
"v_cndmask_b32_e64 v35, v35, v9, s[76:77]\n"
"v_cmp_gt_i32_e64 s[74:75], 2, v6\n"
"v_cndmask_b32_e64 v36, v36, v9, s[70:71]\n"
"v_cmp_gt_i32_e64 s[76:77], 3, v6\n"
"v_cndmask_b32_e64 v37, v37, v9, s[72:73]\n"
"v_cmp_gt_i32_e64 s[70:71], 16, v5\n"
"v_cndmask_b32_e64 v38, v38, v9, s[74:75]\n"
"v_cmp_gt_i32_e64 s[72:73], 17, v5\n"
"v_cndmask_b32_e64 v39, v39, v9, s[76:77]\n"
"v_cmp_gt_i32_e64 s[74:75], 18, v5\n"
"v_cndmask_b32_e64 v40, v40, v9, s[70:71]\n"
"v_cmp_gt_i32_e64 s[76:77], 19, v5\n"
"v_cndmask_b32_e64 v41, v41, v9, s[72:73]\n"
"v_cmp_gt_i32_e64 s[70:71], 16, v6\n"
"v_cndmask_b32_e64 v42, v42, v9, s[74:75]\n"
"v_cmp_gt_i32_e64 s[72:73], 17, v6\n"
"v_cndmask_b32_e64 v43, v43, v9, s[76:77]\n"
"v_cmp_gt_i32_e64 s[74:75], 18, v6\n"
"v_cndmask_b32_e64 v44, v44, v9, s[70:71]\n"
"v_cmp_gt_i32_e64 s[76:77], 19, v6\n"
"v_cndmask_b32_e64 v45, v45, v9, s[72:73]\n"
"v_cmp_gt_i32_e64 s[70:71], 32, v5\n"
"v_cndmask_b32_e64 v46, v46, v9, s[74:75]\n"
"v_cmp_gt_i32_e64 s[72:73], 33, v5\n"
"v_cndmask_b32_e64 v47, v47, v9, s[76:77]\n"
"v_cmp_gt_i32_e64 s[74:75], 34, v5\n"
"v_cndmask_b32_e64 v48, v48, v9, s[70:71]\n"
"v_cmp_gt_i32_e64 s[76:77], 35, v5\n"
"v_cndmask_b32_e64 v49, v49, v9, s[72:73]\n"
"v_cmp_gt_i32_e64 s[70:71], 32, v6\n"
"v_cndmask_b32_e64 v50, v50, v9, s[74:75]\n"
"v_cmp_gt_i32_e64 s[72:73], 33, v6\n"
"v_cndmask_b32_e64 v51, v51, v9, s[76:77]\n"
"v_cmp_gt_i32_e64 s[74:75], 34, v6\n"
"v_cndmask_b32_e64 v52, v52, v9, s[70:71]\n"
"v_cmp_gt_i32_e64 s[76:77], 35, v6\n"
"v_cndmask_b32_e64 v53, v53, v9, s[72:73]\n"
"v_cmp_gt_i32_e64 s[70:71], 48, v5\n"
"v_cndmask_b32_e64 v54, v54, v9, s[74:75]\n"
"v_cmp_gt_i32_e64 s[72:73], 49, v5\n"
"v_cndmask_b32_e64 v55, v55, v9, s[76:77]\n"
"v_cmp_gt_i32_e64 s[74:75], 50, v5\n"
"v_cndmask_b32_e64 v56, v56, v9, s[70:71]\n"
"v_cmp_gt_i32_e64 s[76:77], 51, v5\n"
"v_cndmask_b32_e64 v57, v57, v9, s[72:73]\n"
"v_cmp_gt_i32_e64 s[70:71], 48, v6\n"
"v_cndmask_b32_e64 v58, v58, v9, s[74:75]\n"
"v_cmp_gt_i32_e64 s[72:73], 49, v6\n"
"v_cndmask_b32_e64 v59, v59, v9, s[76:77]\n"
"v_cmp_gt_i32_e64 s[74:75], 50, v6\n"
"v_cndmask_b32_e64 v60, v60, v9, s[70:71]\n"
"v_cmp_gt_i32_e64 s[76:77], 51, v6\n"
"v_cndmask_b32_e64 v61, v61, v9, s[72:73]\n"
"s_nop 0\n"
"v_cndmask_b32_e64 v62, v62, v9, s[74:75]\n"
"s_nop 0\n"
"v_cndmask_b32_e64 v63, v63, v9, s[76:77]\n"
"L_fnm_fs1_%=:\n"
"s_nop 7\n"
"v_max3_f32 v24, v32, v33, v34\n"
"v_max3_f32 v24, v24, v35, v40\n"
"v_max3_f32 v24, v24, v41, v42\n"
"v_max3_f32 v24, v24, v43, v48\n"
"v_max3_f32 v24, v24, v49, v50\n"
"v_max3_f32 v24, v24, v51, v56\n"
"v_max3_f32 v24, v24, v57, v58\n"
"v_max_f32 v24, v24, v59\n"
"v_max3_f32 v25, v36, v37, v38\n"
"v_max3_f32 v25, v25, v39, v44\n"
"v_max3_f32 v25, v25, v45, v46\n"
"v_max3_f32 v25, v25, v47, v52\n"
"v_max3_f32 v25, v25, v53, v54\n"
"v_max3_f32 v25, v25, v55, v60\n"
"v_max3_f32 v25, v25, v61, v62\n"
"v_max_f32 v25, v25, v63\n"
"v_mov_b32 v11, v24\n"
"s_nop 1\n"
"v_permlane16_swap_b32 v24, v11\n"
"s_nop 1\n"
"v_max_f32 v24, v24, v11\n"
"v_mov_b32 v11, v24\n"
"s_nop 1\n"
"v_permlane32_swap_b32 v24, v11\n"
"s_nop 1\n"
"v_max_f32 v24, v24, v11\n"
"v_max_f32 v24, s67, v24\n"
"v_mov_b32 v11, v25\n"
"s_nop 1\n"
"v_permlane16_swap_b32 v25, v11\n"
"s_nop 1\n"
"v_max_f32 v25, v25, v11\n"
"v_mov_b32 v11, v25\n"
"s_nop 1\n"
"v_permlane32_swap_b32 v25, v11\n"
"s_nop 1\n"
"v_max_f32 v25, v25, v11\n"
"v_max_f32 v25, s67, v25\n"
"v_add_f32 v16, v16, v24\n"
"v_sub_f32 v128, 0, v16\n"
"v_sub_f32 v129, 0, v16\n"
"v_sub_f32 v130, 0, v16\n"
"v_sub_f32 v131, 0, v16\n"
"v_add_f32 v17, v17, v25\n"
"v_sub_f32 v132, 0, v17\n"
"v_sub_f32 v133, 0, v17\n"
"v_sub_f32 v134, 0, v17\n"
"v_sub_f32 v135, 0, v17\n"
"v_sub_f32 v32, v32, v24\n"
"v_sub_f32 v33, v33, v24\n"
"v_sub_f32 v34, v34, v24\n"
"v_sub_f32 v35, v35, v24\n"
"v_sub_f32 v36, v36, v25\n"
"v_sub_f32 v37, v37, v25\n"
"v_sub_f32 v38, v38, v25\n"
"v_sub_f32 v39, v39, v25\n"
"v_sub_f32 v40, v40, v24\n"
"v_sub_f32 v41, v41, v24\n"
"v_sub_f32 v42, v42, v24\n"
"v_sub_f32 v43, v43, v24\n"
"v_sub_f32 v44, v44, v25\n"
"v_sub_f32 v45, v45, v25\n"
"v_sub_f32 v46, v46, v25\n"
"v_sub_f32 v47, v47, v25\n"
"v_sub_f32 v48, v48, v24\n"
"v_sub_f32 v49, v49, v24\n"
"v_sub_f32 v50, v50, v24\n"
"v_sub_f32 v51, v51, v24\n"
"v_sub_f32 v52, v52, v25\n"
"v_sub_f32 v53, v53, v25\n"
"v_sub_f32 v54, v54, v25\n"
"v_sub_f32 v55, v55, v25\n"
"v_sub_f32 v56, v56, v24\n"
"v_sub_f32 v57, v57, v24\n"
"v_sub_f32 v58, v58, v24\n"
"v_sub_f32 v59, v59, v24\n"
"v_sub_f32 v60, v60, v25\n"
"v_sub_f32 v61, v61, v25\n"
"v_sub_f32 v62, v62, v25\n"
"v_sub_f32 v63, v63, v25\n"
"v_exp_f32 v136, v32\n"
"v_exp_f32 v137, v33\n"
"v_exp_f32 v138, v34\n"
"v_exp_f32 v139, v35\n"
"v_exp_f32 v140, v36\n"
"v_exp_f32 v141, v37\n"
"v_exp_f32 v142, v38\n"
"v_exp_f32 v143, v39\n"
"v_exp_f32 v144, v40\n"
"v_exp_f32 v145, v41\n"
"v_exp_f32 v146, v42\n"
"v_exp_f32 v147, v43\n"
"v_exp_f32 v148, v44\n"
"v_exp_f32 v149, v45\n"
"v_exp_f32 v150, v46\n"
"v_exp_f32 v151, v47\n"
"v_exp_f32 v152, v48\n"
"v_exp_f32 v153, v49\n"
"v_exp_f32 v154, v50\n"
"v_exp_f32 v155, v51\n"
"v_exp_f32 v156, v52\n"
"v_exp_f32 v157, v53\n"
"v_exp_f32 v158, v54\n"
"v_exp_f32 v159, v55\n"
"v_exp_f32 v160, v56\n"
"v_exp_f32 v161, v57\n"
"v_exp_f32 v162, v58\n"
"v_exp_f32 v163, v59\n"
"v_exp_f32 v164, v60\n"
"s_nop 0\n"
"v_add_f32 v20, v136, v138\n"
"v_add_f32 v21, v137, v139\n"
"v_cvt_pk_bf16_f32 v96, v136, v137\n"
"v_cvt_pk_bf16_f32 v97, v138, v139\n"
"v_add_f32 v22, v140, v142\n"
"v_add_f32 v23, v141, v143\n"
"v_cvt_pk_bf16_f32 v100, v140, v141\n"
"v_cvt_pk_bf16_f32 v101, v142, v143\n"
"v_add_f32 v20, v20, v144\n"
"v_add_f32 v21, v21, v145\n"
"v_add_f32 v20, v20, v146\n"
"v_add_f32 v21, v21, v147\n"
"v_cvt_pk_bf16_f32 v98, v144, v145\n"
"v_cvt_pk_bf16_f32 v99, v146, v147\n"
"v_add_f32 v22, v22, v148\n"
"v_add_f32 v23, v23, v149\n"
"v_add_f32 v22, v22, v150\n"
"v_add_f32 v23, v23, v151\n"
"v_cvt_pk_bf16_f32 v102, v148, v149\n"
"v_cvt_pk_bf16_f32 v103, v150, v151\n"
"v_add_f32 v20, v20, v152\n"
"v_add_f32 v21, v21, v153\n"
"v_add_f32 v20, v20, v154\n"
"v_add_f32 v21, v21, v155\n"
"v_cvt_pk_bf16_f32 v104, v152, v153\n"
"v_cvt_pk_bf16_f32 v105, v154, v155\n"
"v_add_f32 v22, v22, v156\n"
"v_add_f32 v23, v23, v157\n"
"v_add_f32 v22, v22, v158\n"
"v_add_f32 v23, v23, v159\n"
"v_cvt_pk_bf16_f32 v108, v156, v157\n"
"v_cvt_pk_bf16_f32 v109, v158, v159\n"
"v_add_f32 v20, v20, v160\n"
"v_add_f32 v21, v21, v161\n"
"v_add_f32 v20, v20, v162\n"
"v_add_f32 v21, v21, v163\n"
"v_cvt_pk_bf16_f32 v106, v160, v161\n"
"v_cvt_pk_bf16_f32 v107, v162, v163\n"
"v_add_f32 v22, v22, v164\n"
"s_sub_u32 s55, s55, 1\n"
"s_cbranch_scc0 L_body10_%=\n"
"s_cmp_eq_u32 s56, 0\n"
"s_cbranch_scc1 L_last0_%=\n"
"s_sub_u32 s56, s56, 1\n"
"s_branch L_body11_%=\n"
"L_finF_%=:\n"
"s_waitcnt vmcnt(14)\n"
"v_mov_b32 v10, v18\n"
"s_nop 1\n"
"v_permlane16_swap_b32 v18, v10\n"
"s_nop 1\n"
"v_add_f32 v18, v18, v10\n"
"v_mov_b32 v10, v18\n"
"s_nop 1\n"
"v_permlane32_swap_b32 v18, v10\n"
"s_nop 1\n"
"v_add_f32 v18, v18, v10\n"
"v_mov_b32 v10, v19\n"
"s_nop 1\n"
"v_permlane16_swap_b32 v19, v10\n"
"s_nop 1\n"
"v_add_f32 v19, v19, v10\n"
"v_mov_b32 v10, v19\n"
"s_nop 1\n"
"v_permlane32_swap_b32 v19, v10\n"
"s_nop 1\n"
"v_add_f32 v19, v19, v10\n"
"v_rcp_f32 v24, v18\n"
"v_rcp_f32 v25, v19\n"
"s_nop 0\n"
"v_mul_f32 v24, v24, v28\n"
"v_mul_f32 v25, v25, v29\n"
"s_lshl_b32 s96, s53, 3\n"
"s_lshl_b32 s97, s49, 1\n"
"s_add_u32 s96, s96, s97\n"
"s_mul_i32 s96, s96, 142\n"
"s_lshl_b32 s97, s48, 3\n"
"s_add_u32 s96, s96, s97\n"
"s_lshl_b32 s78, s96, 10\n"
"s_add_u32 s78, s78, 58720256\n"
"s_add_u32 s79, s78, 145408\n"
"v_accvgpr_read_b32 v32, a0\n"
"v_accvgpr_read_b32 v33, a1\n"
"v_accvgpr_read_b32 v34, a2\n"
"v_accvgpr_read_b32 v35, a3\n"
"v_accvgpr_read_b32 v36, a8\n"
"v_accvgpr_read_b32 v37, a9\n"
"v_accvgpr_read_b32 v38, a10\n"
"v_accvgpr_read_b32 v39, a11\n"
"v_mul_f32 v32, v32, v24\n"
"v_mul_f32 v33, v33, v24\n"
"v_mul_f32 v34, v34, v24\n"
"v_mul_f32 v35, v35, v24\n"
"v_mul_f32 v36, v36, v24\n"
"v_mul_f32 v37, v37, v24\n"
"v_mul_f32 v38, v38, v24\n"
"v_mul_f32 v39, v39, v24\n"
"v_cvt_pk_bf16_f32 v168, v32, v33\n"
"v_cvt_pk_bf16_f32 v169, v34, v35\n"
"v_cvt_pk_bf16_f32 v170, v36, v37\n"
"v_cvt_pk_bf16_f32 v171, v38, v39\n"
"buffer_store_dwordx4 v[168:171], v1, s[36:39], s78 offen offset:0 sc1\n"
"v_accvgpr_read_b32 v40, a4\n"
"v_accvgpr_read_b32 v41, a5\n"
"v_accvgpr_read_b32 v42, a6\n"
"v_accvgpr_read_b32 v43, a7\n"
"v_accvgpr_read_b32 v44, a12\n"
"v_accvgpr_read_b32 v45, a13\n"
"v_accvgpr_read_b32 v46, a14\n"
"v_accvgpr_read_b32 v47, a15\n"
"v_mul_f32 v40, v40, v25\n"
"v_mul_f32 v41, v41, v25\n"
"v_mul_f32 v42, v42, v25\n"
"v_mul_f32 v43, v43, v25\n"
"v_mul_f32 v44, v44, v25\n"
"v_mul_f32 v45, v45, v25\n"
"v_mul_f32 v46, v46, v25\n"
"v_mul_f32 v47, v47, v25\n"
"v_cvt_pk_bf16_f32 v200, v40, v41\n"
"v_cvt_pk_bf16_f32 v201, v42, v43\n"
"v_cvt_pk_bf16_f32 v202, v44, v45\n"
"v_cvt_pk_bf16_f32 v203, v46, v47\n"
"buffer_store_dwordx4 v[200:203], v1, s[36:39], s79 offen offset:0 sc1\n"
"s_lshl_b32 s97, s51, 4\n"
"s_lshl_b32 s98, s54, 1\n"
"s_add_u32 s97, s97, s98\n"
"s_add_u32 s61, s97, s52\n"
"s_mov_b64 s[62:63], %0\n"
"s_lshl_b32 s96, s61, 4\n"
"s_add_u32 s96, s96, 126885888\n"
"s_add_u32 s62, s62, s96\n"
"s_addc_u32 s63, s63, 0\n"
"s_lshl_b32 s96, s50, 2\n"
"s_add_u32 s62, s62, s96\n"
"s_addc_u32 s63, s63, 0\n"
"s_mov_b64 s[72:73], %5\n"
"s_and_b32 s73, s73, 0xffff\n"
"s_mov_b32 s74, 1835008\n"
"s_mov_b32 s75, 0x20000\n"
"s_waitcnt vmcnt(9)\n"
"s_barrier\n"
"v_add_u32 v162, s59, v160\n"
"v_add_u32 v163, s59, v161\n"
"s_add_u32 s59, s59, 28672\n"
"s_sub_u32 s96, s59, 143360\n"
"s_cmp_ge_u32 s59, 143360\n"
"s_cselect_b32 s59, s96, s59\n"
"ds_read_b64_tr_b16 v[56:57], v162 offset:0\n"
"ds_read_b64_tr_b16 v[58:59], v162 offset:7168\n"
"ds_read_b64_tr_b16 v[60:61], v163 offset:0\n"
"ds_read_b64_tr_b16 v[62:63], v163 offset:7168\n"
"ds_read_b64_tr_b16 v[64:65], v162 offset:256\n"
"ds_read_b64_tr_b16 v[66:67], v162 offset:7424\n"
"ds_read_b64_tr_b16 v[68:69], v163 offset:256\n"
"ds_read_b64_tr_b16 v[70:71], v163 offset:7424\n"
"ds_read_b64_tr_b16 v[72:73], v162 offset:512\n"
"ds_read_b64_tr_b16 v[74:75], v162 offset:7680\n"
"ds_read_b64_tr_b16 v[76:77], v163 offset:512\n"
"ds_read_b64_tr_b16 v[78:79], v163 offset:7680\n"
"ds_read_b64_tr_b16 v[80:81], v162 offset:768\n"
"ds_read_b64_tr_b16 v[82:83], v162 offset:7936\n"
"s_waitcnt lgkmcnt(10)\n"
"v_mfma_f32_16x16x32_bf16 a[128:131], v[56:59], v[168:171], 0\n"
"ds_read_b64_tr_b16 v[84:85], v163 offset:768\n"
"v_accvgpr_read_b32 v32, a16\n"
"v_accvgpr_read_b32 v33, a17\n"
"v_accvgpr_read_b32 v34, a18\n"
"v_accvgpr_read_b32 v35, a19\n"
"v_accvgpr_read_b32 v36, a24\n"
"v_mfma_f32_16x16x32_bf16 a[132:135], v[56:59], v[200:203], 0\n"
"ds_read_b64_tr_b16 v[86:87], v163 offset:7936\n"
"buffer_load_dwordx4 v[232:235], v1, s[36:39], s66 offen offset:0 sc1\n"
"v_accvgpr_read_b32 v37, a25\n"
"v_accvgpr_read_b32 v38, a26\n"
"v_accvgpr_read_b32 v39, a27\n"
"v_mul_f32 v32, v32, v24\n"
"v_mul_f32 v33, v33, v24\n"
"v_mfma_f32_16x16x32_bf16 a[136:139], v[60:63], v[168:171], 0\n"
"ds_read_b64_tr_b16 v[88:89], v162 offset:1024\n"
"v_mul_f32 v34, v34, v24\n"
"v_mul_f32 v35, v35, v24\n"
"v_mul_f32 v36, v36, v24\n"
"v_mul_f32 v37, v37, v24\n"
"v_mfma_f32_16x16x32_bf16 a[140:143], v[60:63], v[200:203], 0\n"
"s_waitcnt lgkmcnt(9)\n"
"ds_read_b64_tr_b16 v[90:91], v162 offset:8192\n"
"buffer_load_dwordx4 v[236:239], v1, s[36:39], s67 offen offset:0 sc1\n"
"v_mul_f32 v38, v38, v24\n"
"v_mul_f32 v39, v39, v24\n"
"v_cvt_pk_bf16_f32 v172, v32, v33\n"
"v_cvt_pk_bf16_f32 v173, v34, v35\n"
"v_cvt_pk_bf16_f32 v174, v36, v37\n"
"v_mfma_f32_16x16x32_bf16 a[144:147], v[64:67], v[168:171], 0\n"
"ds_read_b64_tr_b16 v[92:93], v163 offset:1024\n"
"v_cvt_pk_bf16_f32 v175, v38, v39\n"
"buffer_store_dwordx4 v[172:175], v1, s[36:39], s78 offen offset:1024 sc1\n"
"v_accvgpr_read_b32 v40, a20\n"
"v_accvgpr_read_b32 v41, a21\n"
"v_accvgpr_read_b32 v42, a22\n"
"v_mfma_f32_16x16x32_bf16 a[148:151], v[64:67], v[200:203], 0\n"
"ds_read_b64_tr_b16 v[94:95], v163 offset:8192\n"
"buffer_load_dwordx4 v[240:243], v1, s[36:39], s66 offen offset:1024 sc1\n"
"v_accvgpr_read_b32 v43, a23\n"
"v_accvgpr_read_b32 v44, a28\n"
"v_accvgpr_read_b32 v45, a29\n"
"v_accvgpr_read_b32 v46, a30\n"
"v_mfma_f32_16x16x32_bf16 a[152:155], v[68:71], v[168:171], 0\n"
"ds_read_b64_tr_b16 v[56:57], v162 offset:1280\n"
"v_accvgpr_read_b32 v47, a31\n"
"v_mul_f32 v40, v40, v25\n"
"v_mul_f32 v41, v41, v25\n"
"v_mul_f32 v42, v42, v25\n"
"v_mul_f32 v43, v43, v25\n"
"v_mfma_f32_16x16x32_bf16 a[156:159], v[68:71], v[200:203], 0\n"
"s_waitcnt lgkmcnt(9)\n"
"ds_read_b64_tr_b16 v[58:59], v162 offset:8448\n"
"buffer_load_dwordx4 v[244:247], v1, s[36:39], s67 offen offset:1024 sc1\n"
"v_mul_f32 v44, v44, v25\n"
"v_mul_f32 v45, v45, v25\n"
"v_mul_f32 v46, v46, v25\n"
"v_mul_f32 v47, v47, v25\n"
"v_cvt_pk_bf16_f32 v204, v40, v41\n"
"v_mfma_f32_16x16x32_bf16 a[160:163], v[72:75], v[168:171], 0\n"
"ds_read_b64_tr_b16 v[60:61], v163 offset:1280\n"
"v_cvt_pk_bf16_f32 v205, v42, v43\n"
"v_cvt_pk_bf16_f32 v206, v44, v45\n"
"v_cvt_pk_bf16_f32 v207, v46, v47\n"
"buffer_store_dwordx4 v[204:207], v1, s[36:39], s79 offen offset:1024 sc1\n"
"v_mfma_f32_16x16x32_bf16 a[164:167], v[72:75], v[200:203], 0\n"
"ds_read_b64_tr_b16 v[62:63], v163 offset:8448\n"
"buffer_load_dword v248, v167, s[36:39], s76 offen sc1\n"
"v_accvgpr_read_b32 v32, a32\n"
"v_accvgpr_read_b32 v33, a33\n"
"v_accvgpr_read_b32 v34, a34\n"
"v_accvgpr_read_b32 v35, a35\n"
"v_accvgpr_read_b32 v36, a40\n"
"v_mfma_f32_16x16x32_bf16 a[168:171], v[76:79], v[168:171], 0\n"
"ds_read_b64_tr_b16 v[64:65], v162 offset:1536\n"
"v_accvgpr_read_b32 v37, a41\n"
"v_accvgpr_read_b32 v38, a42\n"
"v_accvgpr_read_b32 v39, a43\n"
"v_mul_f32 v32, v32, v24\n"
"v_mul_f32 v33, v33, v24\n"
"v_mfma_f32_16x16x32_bf16 a[172:175], v[76:79], v[200:203], 0\n"
"s_waitcnt lgkmcnt(9)\n"
"ds_read_b64_tr_b16 v[66:67], v162 offset:8704\n"
"buffer_load_dword v249, v167, s[36:39], s76 offen offset:64 sc1\n"
"v_mul_f32 v34, v34, v24\n"
"v_mul_f32 v35, v35, v24\n"
"v_mul_f32 v36, v36, v24\n"
"v_mul_f32 v37, v37, v24\n"
"v_mfma_f32_16x16x32_bf16 a[176:179], v[80:83], v[168:171], 0\n"
"ds_read_b64_tr_b16 v[68:69], v163 offset:1536\n"
"v_mul_f32 v38, v38, v24\n"
"v_mul_f32 v39, v39, v24\n"
"v_cvt_pk_bf16_f32 v176, v32, v33\n"
"v_cvt_pk_bf16_f32 v177, v34, v35\n"
"v_cvt_pk_bf16_f32 v178, v36, v37\n"
"v_mfma_f32_16x16x32_bf16 a[180:183], v[80:83], v[200:203], 0\n"
"ds_read_b64_tr_b16 v[70:71], v163 offset:8704\n"
"s_mov_b32 m0, s56\n"
"v_cvt_pk_bf16_f32 v179, v38, v39\n"
"buffer_store_dwordx4 v[176:179], v1, s[36:39], s78 offen offset:2048 sc1\n"
"v_accvgpr_read_b32 v40, a36\n"
"v_accvgpr_read_b32 v41, a37\n"
"v_accvgpr_read_b32 v42, a38\n"
"v_mfma_f32_16x16x32_bf16 a[184:187], v[84:87], v[168:171], 0\n"
"ds_read_b64_tr_b16 v[72:73], v162 offset:14336\n"
"buffer_load_dwordx4 v152, s[40:43], s55 offen lds\n"
"v_accvgpr_read_b32 v43, a39\n"
"v_accvgpr_read_b32 v44, a44\n"
"v_accvgpr_read_b32 v45, a45\n"
"v_accvgpr_read_b32 v46, a46\n"
"v_mfma_f32_16x16x32_bf16 a[188:191], v[84:87], v[200:203], 0\n"
"s_waitcnt lgkmcnt(9)\n"
"ds_read_b64_tr_b16 v[74:75], v162 offset:21504\n"
"s_add_u32 m0, m0, 0x400\n"
"v_accvgpr_read_b32 v47, a47\n"
"v_mul_f32 v40, v40, v25\n"
"v_mul_f32 v41, v41, v25\n"
"v_mul_f32 v42, v42, v25\n"
"v_mul_f32 v43, v43, v25\n"
"v_mfma_f32_16x16x32_bf16 a[192:195], v[88:91], v[168:171], 0\n"
"ds_read_b64_tr_b16 v[76:77], v163 offset:14336\n"
"buffer_load_dwordx4 v153, s[40:43], s55 offen lds\n"
"v_mul_f32 v44, v44, v25\n"
"v_mul_f32 v45, v45, v25\n"
"v_mul_f32 v46, v46, v25\n"
"v_mul_f32 v47, v47, v25\n"
"v_cvt_pk_bf16_f32 v208, v40, v41\n"
"v_mfma_f32_16x16x32_bf16 a[196:199], v[88:91], v[200:203], 0\n"
"ds_read_b64_tr_b16 v[78:79], v163 offset:21504\n"
"s_add_u32 m0, m0, 0x400\n"
"v_cvt_pk_bf16_f32 v209, v42, v43\n"
"v_cvt_pk_bf16_f32 v210, v44, v45\n"
"v_cvt_pk_bf16_f32 v211, v46, v47\n"
"buffer_store_dwordx4 v[208:211], v1, s[36:39], s79 offen offset:2048 sc1\n"
"v_mfma_f32_16x16x32_bf16 a[200:203], v[92:95], v[168:171], 0\n"
"ds_read_b64_tr_b16 v[80:81], v162 offset:14592\n"
"buffer_load_dwordx4 v154, s[40:43], s55 offen lds\n"
"v_accvgpr_read_b32 v32, a48\n"
"v_accvgpr_read_b32 v33, a49\n"
"v_accvgpr_read_b32 v34, a50\n"
"v_accvgpr_read_b32 v35, a51\n"
"v_accvgpr_read_b32 v36, a56\n"
"v_mfma_f32_16x16x32_bf16 a[204:207], v[92:95], v[200:203], 0\n"
"s_waitcnt lgkmcnt(9)\n"
"ds_read_b64_tr_b16 v[82:83], v162 offset:21760\n"
"s_add_u32 m0, m0, 0x400\n"
"v_accvgpr_read_b32 v37, a57\n"
"v_accvgpr_read_b32 v38, a58\n"
"v_accvgpr_read_b32 v39, a59\n"
"v_mul_f32 v32, v32, v24\n"
"v_mul_f32 v33, v33, v24\n"
"v_mfma_f32_16x16x32_bf16 a[208:211], v[56:59], v[168:171], 0\n"
"ds_read_b64_tr_b16 v[84:85], v163 offset:14592\n"
"buffer_load_dwordx4 v155, s[40:43], s55 offen lds\n"
"v_mul_f32 v34, v34, v24\n"
"v_mul_f32 v35, v35, v24\n"
"v_mul_f32 v36, v36, v24\n"
"v_mul_f32 v37, v37, v24\n"
"v_mfma_f32_16x16x32_bf16 a[212:215], v[56:59], v[200:203], 0\n"
"ds_read_b64_tr_b16 v[86:87], v163 offset:21760\n"
"s_add_u32 m0, m0, 0x400\n"
"v_mul_f32 v38, v38, v24\n"
"v_mul_f32 v39, v39, v24\n"
"v_cvt_pk_bf16_f32 v180, v32, v33\n"
"v_cvt_pk_bf16_f32 v181, v34, v35\n"
"v_cvt_pk_bf16_f32 v182, v36, v37\n"
"v_mfma_f32_16x16x32_bf16 a[216:219], v[60:63], v[168:171], 0\n"
"ds_read_b64_tr_b16 v[88:89], v162 offset:14848\n"
"buffer_load_dwordx4 v156, s[40:43], s55 offen lds\n"
"v_cvt_pk_bf16_f32 v183, v38, v39\n"
"buffer_store_dwordx4 v[180:183], v1, s[36:39], s78 offen offset:3072 sc1\n"
"v_accvgpr_read_b32 v40, a52\n"
"v_accvgpr_read_b32 v41, a53\n"
"v_accvgpr_read_b32 v42, a54\n"
"v_mfma_f32_16x16x32_bf16 a[220:223], v[60:63], v[200:203], 0\n"
"s_waitcnt lgkmcnt(9)\n"
"ds_read_b64_tr_b16 v[90:91], v162 offset:22016\n"
"s_add_u32 m0, m0, 0x400\n"
"v_accvgpr_read_b32 v43, a55\n"
"v_accvgpr_read_b32 v44, a60\n"
"v_accvgpr_read_b32 v45, a61\n"
"v_accvgpr_read_b32 v46, a62\n"
"v_mfma_f32_16x16x32_bf16 a[224:227], v[64:67], v[168:171], 0\n"
"ds_read_b64_tr_b16 v[92:93], v163 offset:14848\n"
"buffer_load_dwordx4 v157, s[40:43], s55 offen lds\n"
"v_accvgpr_read_b32 v47, a63\n"
"v_mul_f32 v40, v40, v25\n"
"v_mul_f32 v41, v41, v25\n"
"v_mul_f32 v42, v42, v25\n"
"v_mul_f32 v43, v43, v25\n"
"v_mfma_f32_16x16x32_bf16 a[228:231], v[64:67], v[200:203], 0\n"
"ds_read_b64_tr_b16 v[94:95], v163 offset:22016\n"
"s_add_u32 m0, m0, 0x400\n"
"v_mul_f32 v44, v44, v25\n"
"v_mul_f32 v45, v45, v25\n"
"v_mul_f32 v46, v46, v25\n"
"v_mul_f32 v47, v47, v25\n"
"v_cvt_pk_bf16_f32 v212, v40, v41\n"
"v_mfma_f32_16x16x32_bf16 a[232:235], v[68:71], v[168:171], 0\n"
"ds_read_b64_tr_b16 v[56:57], v162 offset:15104\n"
"buffer_load_dwordx4 v158, s[40:43], s55 offen lds\n"
"s_add_u32 s55, s55, 57344\n"
"s_add_u32 s56, s56, 28672\n"
"s_sub_u32 s96, s56, 143360\n"
"s_cmp_ge_u32 s56, 143360\n"
"s_cselect_b32 s56, s96, s56\n"
"v_cvt_pk_bf16_f32 v213, v42, v43\n"
"v_cvt_pk_bf16_f32 v214, v44, v45\n"
"v_cvt_pk_bf16_f32 v215, v46, v47\n"
"buffer_store_dwordx4 v[212:215], v1, s[36:39], s79 offen offset:3072 sc1\n"
"v_mfma_f32_16x16x32_bf16 a[236:239], v[68:71], v[200:203], 0\n"
"s_waitcnt lgkmcnt(9)\n"
"ds_read_b64_tr_b16 v[58:59], v162 offset:22272\n"
"s_mov_b32 m0, s56\n"
"v_mfma_f32_16x16x32_bf16 a[128:131], v[72:75], v[172:175], a[128:131]\n"
"ds_read_b64_tr_b16 v[60:61], v163 offset:15104\n"
"buffer_load_dwordx4 v152, s[40:43], s55 offen lds\n"
"v_accvgpr_read_b32 v32, a64\n"
"v_accvgpr_read_b32 v33, a65\n"
"v_accvgpr_read_b32 v34, a66\n"
"v_accvgpr_read_b32 v35, a67\n"
"v_accvgpr_read_b32 v36, a72\n"
"v_mfma_f32_16x16x32_bf16 a[132:135], v[72:75], v[204:207], a[132:135]\n"
"ds_read_b64_tr_b16 v[62:63], v163 offset:22272\n"
"s_add_u32 m0, m0, 0x400\n"
"v_accvgpr_read_b32 v37, a73\n"
"v_accvgpr_read_b32 v38, a74\n"
"v_accvgpr_read_b32 v39, a75\n"
"v_mul_f32 v32, v32, v24\n"
"v_mul_f32 v33, v33, v24\n"
"v_mfma_f32_16x16x32_bf16 a[136:139], v[76:79], v[172:175], a[136:139]\n"
"ds_read_b64_tr_b16 v[64:65], v162 offset:15360\n"
"buffer_load_dwordx4 v153, s[40:43], s55 offen lds\n"
"v_mul_f32 v34, v34, v24\n"
"v_mul_f32 v35, v35, v24\n"
"v_mul_f32 v36, v36, v24\n"
"v_mul_f32 v37, v37, v24\n"
"v_mfma_f32_16x16x32_bf16 a[140:143], v[76:79], v[204:207], a[140:143]\n"
"s_waitcnt lgkmcnt(9)\n"
"ds_read_b64_tr_b16 v[66:67], v162 offset:22528\n"
"s_add_u32 m0, m0, 0x400\n"
"v_mul_f32 v38, v38, v24\n"
"v_mul_f32 v39, v39, v24\n"
"v_cvt_pk_bf16_f32 v184, v32, v33\n"
"v_cvt_pk_bf16_f32 v185, v34, v35\n"
"v_cvt_pk_bf16_f32 v186, v36, v37\n"
"v_mfma_f32_16x16x32_bf16 a[144:147], v[80:83], v[172:175], a[144:147]\n"
"ds_read_b64_tr_b16 v[68:69], v163 offset:15360\n"
"buffer_load_dwordx4 v154, s[40:43], s55 offen lds\n"
"v_cvt_pk_bf16_f32 v187, v38, v39\n"
"buffer_store_dwordx4 v[184:187], v2, s[36:39], s78 offen offset:0 sc1\n"
"v_accvgpr_read_b32 v40, a68\n"
"v_accvgpr_read_b32 v41, a69\n"
"v_accvgpr_read_b32 v42, a70\n"
"v_mfma_f32_16x16x32_bf16 a[148:151], v[80:83], v[204:207], a[148:151]\n"
"ds_read_b64_tr_b16 v[70:71], v163 offset:22528\n"
"s_add_u32 m0, m0, 0x400\n"
"v_accvgpr_read_b32 v43, a71\n"
"v_accvgpr_read_b32 v44, a76\n"
"v_accvgpr_read_b32 v45, a77\n"
"v_accvgpr_read_b32 v46, a78\n"
"v_mfma_f32_16x16x32_bf16 a[152:155], v[84:87], v[172:175], a[152:155]\n"
"ds_read_b64_tr_b16 v[72:73], v162 offset:15616\n"
"buffer_load_dwordx4 v155, s[40:43], s55 offen lds\n"
"v_accvgpr_read_b32 v47, a79\n"
"v_mul_f32 v40, v40, v25\n"
"v_mul_f32 v41, v41, v25\n"
"v_mul_f32 v42, v42, v25\n"
"v_mul_f32 v43, v43, v25\n"
"v_mfma_f32_16x16x32_bf16 a[156:159], v[84:87], v[204:207], a[156:159]\n"
"s_waitcnt lgkmcnt(9)\n"
"ds_read_b64_tr_b16 v[74:75], v162 offset:22784\n"
"s_add_u32 m0, m0, 0x400\n"
"v_mul_f32 v44, v44, v25\n"
"v_mul_f32 v45, v45, v25\n"
"v_mul_f32 v46, v46, v25\n"
"v_mul_f32 v47, v47, v25\n"
"v_cvt_pk_bf16_f32 v216, v40, v41\n"
"v_mfma_f32_16x16x32_bf16 a[160:163], v[88:91], v[172:175], a[160:163]\n"
"ds_read_b64_tr_b16 v[76:77], v163 offset:15616\n"
"buffer_load_dwordx4 v156, s[40:43], s55 offen lds\n"
"v_cvt_pk_bf16_f32 v217, v42, v43\n"
"v_cvt_pk_bf16_f32 v218, v44, v45\n"
"v_cvt_pk_bf16_f32 v219, v46, v47\n"
"buffer_store_dwordx4 v[216:219], v2, s[36:39], s79 offen offset:0 sc1\n"
"v_mfma_f32_16x16x32_bf16 a[164:167], v[88:91], v[204:207], a[164:167]\n"
"ds_read_b64_tr_b16 v[78:79], v163 offset:22784\n"
"s_add_u32 m0, m0, 0x400\n"
"v_accvgpr_read_b32 v32, a80\n"
"v_accvgpr_read_b32 v33, a81\n"
"v_accvgpr_read_b32 v34, a82\n"
"v_accvgpr_read_b32 v35, a83\n"
"v_accvgpr_read_b32 v36, a88\n"
"v_mfma_f32_16x16x32_bf16 a[168:171], v[92:95], v[172:175], a[168:171]\n"
"ds_read_b64_tr_b16 v[80:81], v162 offset:15872\n"
"buffer_load_dwordx4 v157, s[40:43], s55 offen lds\n"
"v_accvgpr_read_b32 v37, a89\n"
"v_accvgpr_read_b32 v38, a90\n"
"v_accvgpr_read_b32 v39, a91\n"
"v_mul_f32 v32, v32, v24\n"
"v_mul_f32 v33, v33, v24\n"
"v_mfma_f32_16x16x32_bf16 a[172:175], v[92:95], v[204:207], a[172:175]\n"
"s_waitcnt lgkmcnt(9)\n"
"ds_read_b64_tr_b16 v[82:83], v162 offset:23040\n"
"s_add_u32 m0, m0, 0x400\n"
"v_mul_f32 v34, v34, v24\n"
"v_mul_f32 v35, v35, v24\n"
"v_mul_f32 v36, v36, v24\n"
"v_mul_f32 v37, v37, v24\n"
"v_mfma_f32_16x16x32_bf16 a[176:179], v[56:59], v[172:175], a[176:179]\n"
"ds_read_b64_tr_b16 v[84:85], v163 offset:15872\n"
"buffer_load_dwordx4 v158, s[40:43], s55 offen lds\n"
"s_add_u32 s55, s55, 57344\n"
"s_add_u32 s56, s56, 28672\n"
"s_sub_u32 s96, s56, 143360\n"
"s_cmp_ge_u32 s56, 143360\n"
"s_cselect_b32 s56, s96, s56\n"
"s_mul_i32 s55, s54, 57344\n"
"s_mul_i32 s96, s52, 448\n"
"s_add_u32 s55, s55, s96\n"
"s_cmp_eq_u32 s54, 7\n"
"s_cselect_b32 s55, 0xf0000000, s55\n"
"v_mul_f32 v38, v38, v24\n"
"v_mul_f32 v39, v39, v24\n"
"v_cvt_pk_bf16_f32 v188, v32, v33\n"
"v_cvt_pk_bf16_f32 v189, v34, v35\n"
"v_cvt_pk_bf16_f32 v190, v36, v37\n"
"v_mfma_f32_16x16x32_bf16 a[180:183], v[56:59], v[204:207], a[180:183]\n"
"ds_read_b64_tr_b16 v[86:87], v163 offset:23040\n"
"s_waitcnt vmcnt(30)\n"
"s_barrier\n"
"v_add_u32 v162, s59, v160\n"
"v_add_u32 v163, s59, v161\n"
"s_add_u32 s59, s59, 28672\n"
"s_sub_u32 s96, s59, 143360\n"
"s_cmp_ge_u32 s59, 143360\n"
"s_cselect_b32 s59, s96, s59\n"
"v_cvt_pk_bf16_f32 v191, v38, v39\n"
"buffer_store_dwordx4 v[188:191], v2, s[36:39], s78 offen offset:1024 sc1\n"
"v_accvgpr_read_b32 v40, a84\n"
"v_accvgpr_read_b32 v41, a85\n"
"v_accvgpr_read_b32 v42, a86\n"
"v_mfma_f32_16x16x32_bf16 a[184:187], v[60:63], v[172:175], a[184:187]\n"
"ds_read_b64_tr_b16 v[88:89], v162 offset:0\n"
"v_accvgpr_read_b32 v43, a87\n"
"v_accvgpr_read_b32 v44, a92\n"
"v_accvgpr_read_b32 v45, a93\n"
"v_accvgpr_read_b32 v46, a94\n"
"v_mfma_f32_16x16x32_bf16 a[188:191], v[60:63], v[204:207], a[188:191]\n"
"s_waitcnt lgkmcnt(9)\n"
"ds_read_b64_tr_b16 v[90:91], v162 offset:7168\n"
"s_mov_b32 m0, s56\n"
"v_accvgpr_read_b32 v47, a95\n"
"v_mul_f32 v40, v40, v25\n"
"v_mul_f32 v41, v41, v25\n"
"v_mul_f32 v42, v42, v25\n"
"v_mul_f32 v43, v43, v25\n"
"v_mfma_f32_16x16x32_bf16 a[192:195], v[64:67], v[172:175], a[192:195]\n"
"ds_read_b64_tr_b16 v[92:93], v163 offset:0\n"
"buffer_load_dwordx4 v152, s[44:47], s55 offen lds\n"
"v_mul_f32 v44, v44, v25\n"
"v_mul_f32 v45, v45, v25\n"
"v_mul_f32 v46, v46, v25\n"
"v_mul_f32 v47, v47, v25\n"
"v_cvt_pk_bf16_f32 v220, v40, v41\n"
"v_mfma_f32_16x16x32_bf16 a[196:199], v[64:67], v[204:207], a[196:199]\n"
"ds_read_b64_tr_b16 v[94:95], v163 offset:7168\n"
"v_cvt_pk_bf16_f32 v221, v42, v43\n"
"v_cvt_pk_bf16_f32 v222, v44, v45\n"
"v_cvt_pk_bf16_f32 v223, v46, v47\n"
"buffer_store_dwordx4 v[220:223], v2, s[36:39], s79 offen offset:1024 sc1\n"
"v_mfma_f32_16x16x32_bf16 a[200:203], v[68:71], v[172:175], a[200:203]\n"
"ds_read_b64_tr_b16 v[56:57], v162 offset:256\n"
"v_accvgpr_read_b32 v32, a96\n"
"v_accvgpr_read_b32 v33, a97\n"
"v_accvgpr_read_b32 v34, a98\n"
"v_accvgpr_read_b32 v35, a99\n"
"v_accvgpr_read_b32 v36, a104\n"
"v_mfma_f32_16x16x32_bf16 a[204:207], v[68:71], v[204:207], a[204:207]\n"
"s_waitcnt lgkmcnt(9)\n"
"ds_read_b64_tr_b16 v[58:59], v162 offset:7424\n"
"s_add_u32 m0, m0, 0x400\n"
"v_accvgpr_read_b32 v37, a105\n"
"v_accvgpr_read_b32 v38, a106\n"
"v_accvgpr_read_b32 v39, a107\n"
"v_mul_f32 v32, v32, v24\n"
"v_mul_f32 v33, v33, v24\n"
"v_mfma_f32_16x16x32_bf16 a[208:211], v[72:75], v[172:175], a[208:211]\n"
"ds_read_b64_tr_b16 v[60:61], v163 offset:256\n"
"buffer_load_dwordx4 v153, s[44:47], s55 offen lds\n"
"v_mul_f32 v34, v34, v24\n"
"v_mul_f32 v35, v35, v24\n"
"v_mul_f32 v36, v36, v24\n"
"v_mul_f32 v37, v37, v24\n"
"v_mfma_f32_16x16x32_bf16 a[212:215], v[72:75], v[204:207], a[212:215]\n"
"ds_read_b64_tr_b16 v[62:63], v163 offset:7424\n"
"v_mul_f32 v38, v38, v24\n"
"v_mul_f32 v39, v39, v24\n"
"v_cvt_pk_bf16_f32 v192, v32, v33\n"
"v_cvt_pk_bf16_f32 v193, v34, v35\n"
"v_cvt_pk_bf16_f32 v194, v36, v37\n"
"v_mfma_f32_16x16x32_bf16 a[216:219], v[76:79], v[172:175], a[216:219]\n"
"ds_read_b64_tr_b16 v[64:65], v162 offset:512\n"
"v_cvt_pk_bf16_f32 v195, v38, v39\n"
"buffer_store_dwordx4 v[192:195], v2, s[36:39], s78 offen offset:2048 sc1\n"
"v_accvgpr_read_b32 v40, a100\n"
"v_accvgpr_read_b32 v41, a101\n"
"v_accvgpr_read_b32 v42, a102\n"
"v_mfma_f32_16x16x32_bf16 a[220:223], v[76:79], v[204:207], a[220:223]\n"
"s_waitcnt lgkmcnt(9)\n"
"ds_read_b64_tr_b16 v[66:67], v162 offset:7680\n"
"s_add_u32 m0, m0, 0x400\n"
"v_accvgpr_read_b32 v43, a103\n"
"v_accvgpr_read_b32 v44, a108\n"
"v_accvgpr_read_b32 v45, a109\n"
"v_accvgpr_read_b32 v46, a110\n"
"v_mfma_f32_16x16x32_bf16 a[224:227], v[80:83], v[172:175], a[224:227]\n"
"ds_read_b64_tr_b16 v[68:69], v163 offset:512\n"
"buffer_load_dwordx4 v154, s[44:47], s55 offen lds\n"
"v_accvgpr_read_b32 v47, a111\n"
"v_mul_f32 v40, v40, v25\n"
"v_mul_f32 v41, v41, v25\n"
"v_mul_f32 v42, v42, v25\n"
"v_mul_f32 v43, v43, v25\n"
"v_mfma_f32_16x16x32_bf16 a[228:231], v[80:83], v[204:207], a[228:231]\n"
"ds_read_b64_tr_b16 v[70:71], v163 offset:7680\n"
"v_mul_f32 v44, v44, v25\n"
"v_mul_f32 v45, v45, v25\n"
"v_mul_f32 v46, v46, v25\n"
"v_mul_f32 v47, v47, v25\n"
"v_cvt_pk_bf16_f32 v224, v40, v41\n"
"v_mfma_f32_16x16x32_bf16 a[232:235], v[84:87], v[172:175], a[232:235]\n"
"ds_read_b64_tr_b16 v[72:73], v162 offset:768\n"
"v_cvt_pk_bf16_f32 v225, v42, v43\n"
"v_cvt_pk_bf16_f32 v226, v44, v45\n"
"v_cvt_pk_bf16_f32 v227, v46, v47\n"
"buffer_store_dwordx4 v[224:227], v2, s[36:39], s79 offen offset:2048 sc1\n"
"v_mfma_f32_16x16x32_bf16 a[236:239], v[84:87], v[204:207], a[236:239]\n"
"s_waitcnt lgkmcnt(9)\n"
"ds_read_b64_tr_b16 v[74:75], v162 offset:7936\n"
"s_add_u32 m0, m0, 0x400\n"
"v_mfma_f32_16x16x32_bf16 a[128:131], v[88:91], v[176:179], a[128:131]\n"
"ds_read_b64_tr_b16 v[76:77], v163 offset:768\n"
"buffer_load_dwordx4 v155, s[44:47], s55 offen lds\n"
"v_accvgpr_read_b32 v32, a112\n"
"v_accvgpr_read_b32 v33, a113\n"
"v_mfma_f32_16x16x32_bf16 a[132:135], v[88:91], v[208:211], a[132:135]\n"
"ds_read_b64_tr_b16 v[78:79], v163 offset:7936\n"
"v_accvgpr_read_b32 v34, a114\n"
"v_accvgpr_read_b32 v35, a115\n"
"v_mfma_f32_16x16x32_bf16 a[136:139], v[92:95], v[176:179], a[136:139]\n"
"ds_read_b64_tr_b16 v[80:81], v162 offset:1024\n"
"v_accvgpr_read_b32 v36, a120\n"
"v_mfma_f32_16x16x32_bf16 a[140:143], v[92:95], v[208:211], a[140:143]\n"
"s_waitcnt lgkmcnt(9)\n"
"ds_read_b64_tr_b16 v[82:83], v162 offset:8192\n"
"s_add_u32 m0, m0, 0x400\n"
"v_accvgpr_read_b32 v37, a121\n"
"v_accvgpr_read_b32 v38, a122\n"
"v_mfma_f32_16x16x32_bf16 a[144:147], v[56:59], v[176:179], a[144:147]\n"
"ds_read_b64_tr_b16 v[84:85], v163 offset:1024\n"
"buffer_load_dwordx4 v156, s[44:47], s55 offen lds\n"
"v_accvgpr_read_b32 v39, a123\n"
"v_mfma_f32_16x16x32_bf16 a[148:151], v[56:59], v[208:211], a[148:151]\n"
"ds_read_b64_tr_b16 v[86:87], v163 offset:8192\n"
"v_mul_f32 v32, v32, v24\n"
"v_mul_f32 v33, v33, v24\n"
"v_mfma_f32_16x16x32_bf16 a[152:155], v[60:63], v[176:179], a[152:155]\n"
"ds_read_b64_tr_b16 v[88:89], v162 offset:1280\n"
"v_mul_f32 v34, v34, v24\n"
"v_mfma_f32_16x16x32_bf16 a[156:159], v[60:63], v[208:211], a[156:159]\n"
"s_waitcnt lgkmcnt(9)\n"
"ds_read_b64_tr_b16 v[90:91], v162 offset:8448\n"
"s_add_u32 m0, m0, 0x400\n"
"v_mul_f32 v35, v35, v24\n"
"v_mul_f32 v36, v36, v24\n"
"v_mfma_f32_16x16x32_bf16 a[160:163], v[64:67], v[176:179], a[160:163]\n"
"ds_read_b64_tr_b16 v[92:93], v163 offset:1280\n"
"buffer_load_dwordx4 v157, s[44:47], s55 offen lds\n"
"v_mul_f32 v37, v37, v24\n"
"v_mfma_f32_16x16x32_bf16 a[164:167], v[64:67], v[208:211], a[164:167]\n"
"ds_read_b64_tr_b16 v[94:95], v163 offset:8448\n"
"v_mul_f32 v38, v38, v24\n"
"v_mul_f32 v39, v39, v24\n"
"v_mfma_f32_16x16x32_bf16 a[168:171], v[68:71], v[176:179], a[168:171]\n"
"ds_read_b64_tr_b16 v[56:57], v162 offset:1536\n"
"v_cvt_pk_bf16_f32 v196, v32, v33\n"
"v_cvt_pk_bf16_f32 v197, v34, v35\n"
"v_mfma_f32_16x16x32_bf16 a[172:175], v[68:71], v[208:211], a[172:175]\n"
"s_waitcnt lgkmcnt(9)\n"
"ds_read_b64_tr_b16 v[58:59], v162 offset:8704\n"
"s_add_u32 m0, m0, 0x400\n"
"v_cvt_pk_bf16_f32 v198, v36, v37\n"
"v_mfma_f32_16x16x32_bf16 a[176:179], v[72:75], v[176:179], a[176:179]\n"
"ds_read_b64_tr_b16 v[60:61], v163 offset:1536\n"
"buffer_load_dwordx4 v158, s[44:47], s55 offen lds\n"
"v_cvt_pk_bf16_f32 v199, v38, v39\n"
"buffer_store_dwordx4 v[196:199], v2, s[36:39], s78 offen offset:3072 sc1\n"
"v_mfma_f32_16x16x32_bf16 a[180:183], v[72:75], v[208:211], a[180:183]\n"
"ds_read_b64_tr_b16 v[62:63], v163 offset:8704\n"
"v_accvgpr_read_b32 v40, a116\n"
"v_mfma_f32_16x16x32_bf16 a[184:187], v[76:79], v[176:179], a[184:187]\n"
"ds_read_b64_tr_b16 v[64:65], v162 offset:14336\n"
"v_accvgpr_read_b32 v41, a117\n"
"v_accvgpr_read_b32 v42, a118\n"
"v_mfma_f32_16x16x32_bf16 a[188:191], v[76:79], v[208:211], a[188:191]\n"
"s_waitcnt lgkmcnt(9)\n"
"ds_read_b64_tr_b16 v[66:67], v162 offset:21504\n"
"s_add_u32 s55, s55, 57344\n"
"s_add_u32 s56, s56, 28672\n"
"s_sub_u32 s96, s56, 143360\n"
"s_cmp_ge_u32 s56, 143360\n"
"s_cselect_b32 s56, s96, s56\n"
"s_lshl_b32 s96, s54, 1\n"
"s_xor_b32 s55, s52, 1\n"
"s_add_u32 s96, s96, s55\n"
"s_mul_i32 s55, s96, 229376\n"
"s_mul_i32 s96, s52, 448\n"
"s_add_u32 s55, s55, s96\n"
"v_accvgpr_read_b32 v43, a119\n"
"v_mfma_f32_16x16x32_bf16 a[192:195], v[80:83], v[176:179], a[192:195]\n"
"ds_read_b64_tr_b16 v[68:69], v163 offset:14336\n"
"v_accvgpr_read_b32 v44, a124\n"
"v_accvgpr_read_b32 v45, a125\n"
"v_mfma_f32_16x16x32_bf16 a[196:199], v[80:83], v[208:211], a[196:199]\n"
"ds_read_b64_tr_b16 v[70:71], v163 offset:21504\n"
"v_accvgpr_read_b32 v46, a126\n"
"v_mfma_f32_16x16x32_bf16 a[200:203], v[84:87], v[176:179], a[200:203]\n"
"ds_read_b64_tr_b16 v[72:73], v162 offset:14592\n"
"v_accvgpr_read_b32 v47, a127\n"
"v_mul_f32 v40, v40, v25\n"
"v_mfma_f32_16x16x32_bf16 a[204:207], v[84:87], v[208:211], a[204:207]\n"
"s_waitcnt lgkmcnt(9)\n"
"ds_read_b64_tr_b16 v[74:75], v162 offset:21760\n"
"v_mul_f32 v41, v41, v25\n"
"v_mul_f32 v42, v42, v25\n"
"v_mfma_f32_16x16x32_bf16 a[208:211], v[88:91], v[176:179], a[208:211]\n"
"ds_read_b64_tr_b16 v[76:77], v163 offset:14592\n"
"v_mul_f32 v43, v43, v25\n"
"v_mfma_f32_16x16x32_bf16 a[212:215], v[88:91], v[208:211], a[212:215]\n"
"ds_read_b64_tr_b16 v[78:79], v163 offset:21760\n"
"v_mul_f32 v44, v44, v25\n"
"v_mul_f32 v45, v45, v25\n"
"v_mfma_f32_16x16x32_bf16 a[216:219], v[92:95], v[176:179], a[216:219]\n"
"ds_read_b64_tr_b16 v[80:81], v162 offset:14848\n"
"v_mul_f32 v46, v46, v25\n"
"v_mfma_f32_16x16x32_bf16 a[220:223], v[92:95], v[208:211], a[220:223]\n"
"s_waitcnt lgkmcnt(9)\n"
"ds_read_b64_tr_b16 v[82:83], v162 offset:22016\n"
"v_mul_f32 v47, v47, v25\n"
"v_cvt_pk_bf16_f32 v228, v40, v41\n"
"v_mfma_f32_16x16x32_bf16 a[224:227], v[56:59], v[176:179], a[224:227]\n"
"ds_read_b64_tr_b16 v[84:85], v163 offset:14848\n"
"v_cvt_pk_bf16_f32 v229, v42, v43\n"
"v_mfma_f32_16x16x32_bf16 a[228:231], v[56:59], v[208:211], a[228:231]\n"
"ds_read_b64_tr_b16 v[86:87], v163 offset:22016\n"
"v_cvt_pk_bf16_f32 v230, v44, v45\n"
"v_cvt_pk_bf16_f32 v231, v46, v47\n"
"v_mfma_f32_16x16x32_bf16 a[232:235], v[60:63], v[176:179], a[232:235]\n"
"ds_read_b64_tr_b16 v[88:89], v162 offset:15104\n"
"buffer_store_dwordx4 v[228:231], v2, s[36:39], s79 offen offset:3072 sc1\n"
"v_mfma_f32_16x16x32_bf16 a[236:239], v[60:63], v[208:211], a[236:239]\n"
"s_waitcnt lgkmcnt(9)\n"
"ds_read_b64_tr_b16 v[90:91], v162 offset:22272\n"
"v_mfma_f32_16x16x32_bf16 a[128:131], v[64:67], v[180:183], a[128:131]\n"
"ds_read_b64_tr_b16 v[92:93], v163 offset:15104\n"
"v_mfma_f32_16x16x32_bf16 a[132:135], v[64:67], v[212:215], a[132:135]\n"
"ds_read_b64_tr_b16 v[94:95], v163 offset:22272\n"
"v_mfma_f32_16x16x32_bf16 a[136:139], v[68:71], v[180:183], a[136:139]\n"
"ds_read_b64_tr_b16 v[56:57], v162 offset:15360\n"
"v_mfma_f32_16x16x32_bf16 a[140:143], v[68:71], v[212:215], a[140:143]\n"
"s_waitcnt lgkmcnt(9)\n"
"ds_read_b64_tr_b16 v[58:59], v162 offset:22528\n"
"v_mfma_f32_16x16x32_bf16 a[144:147], v[72:75], v[180:183], a[144:147]\n"
"ds_read_b64_tr_b16 v[60:61], v163 offset:15360\n"
"s_waitcnt vmcnt(33)\n"
"v_sub_f32 v248, 1.0, v248\n"
"v_mfma_f32_16x16x32_bf16 a[148:151], v[72:75], v[212:215], a[148:151]\n"
"ds_read_b64_tr_b16 v[62:63], v163 offset:22528\n"
"v_sub_f32 v249, 1.0, v249\n"
"v_mfma_f32_16x16x32_bf16 a[152:155], v[76:79], v[180:183], a[152:155]\n"
"ds_read_b64_tr_b16 v[64:65], v162 offset:15616\n"
"v_lshlrev_b32 v48, 16, v232\n"
"v_mfma_f32_16x16x32_bf16 a[156:159], v[76:79], v[212:215], a[156:159]\n"
"s_waitcnt lgkmcnt(9)\n"
"ds_read_b64_tr_b16 v[66:67], v162 offset:22784\n"
"v_and_b32 v49, 0xffff0000, v232\n"
"v_mfma_f32_16x16x32_bf16 a[160:163], v[80:83], v[180:183], a[160:163]\n"
"ds_read_b64_tr_b16 v[68:69], v163 offset:15616\n"
"v_mul_f32 v48, v48, v248\n"
"v_mfma_f32_16x16x32_bf16 a[164:167], v[80:83], v[212:215], a[164:167]\n"
"ds_read_b64_tr_b16 v[70:71], v163 offset:22784\n"
"v_mfma_f32_16x16x32_bf16 a[168:171], v[84:87], v[180:183], a[168:171]\n"
"ds_read_b64_tr_b16 v[72:73], v162 offset:15872\n"
"v_mul_f32 v49, v49, v248\n"
"v_mfma_f32_16x16x32_bf16 a[172:175], v[84:87], v[212:215], a[172:175]\n"
"s_waitcnt lgkmcnt(9)\n"
"ds_read_b64_tr_b16 v[74:75], v162 offset:23040\n"
"v_cvt_pk_bf16_f32 v232, v48, v49\n"
"v_mfma_f32_16x16x32_bf16 a[176:179], v[88:91], v[180:183], a[176:179]\n"
"ds_read_b64_tr_b16 v[76:77], v163 offset:15872\n"
"v_lshlrev_b32 v48, 16, v233\n"
"v_mfma_f32_16x16x32_bf16 a[180:183], v[88:91], v[212:215], a[180:183]\n"
"ds_read_b64_tr_b16 v[78:79], v163 offset:23040\n"
"s_waitcnt vmcnt(23)\n"
"s_barrier\n"
"v_add_u32 v162, s59, v160\n"
"v_add_u32 v163, s59, v161\n"
"s_add_u32 s59, s59, 28672\n"
"s_sub_u32 s96, s59, 143360\n"
"s_cmp_ge_u32 s59, 143360\n"
"s_cselect_b32 s59, s96, s59\n"
"v_and_b32 v49, 0xffff0000, v233\n"
"v_mfma_f32_16x16x32_bf16 a[184:187], v[92:95], v[180:183], a[184:187]\n"
"ds_read_b64_tr_b16 v[80:81], v162 offset:0\n"
"v_mul_f32 v48, v48, v248\n"
"v_mfma_f32_16x16x32_bf16 a[188:191], v[92:95], v[212:215], a[188:191]\n"
"s_waitcnt lgkmcnt(9)\n"
"ds_read_b64_tr_b16 v[82:83], v162 offset:7168\n"
"s_mov_b32 m0, s56\n"
"v_mfma_f32_16x16x32_bf16 a[192:195], v[56:59], v[180:183], a[192:195]\n"
"ds_read_b64_tr_b16 v[84:85], v163 offset:0\n"
"buffer_load_dwordx4 v152, s[40:43], s55 offen lds\n"
"v_mul_f32 v49, v49, v248\n"
"v_mfma_f32_16x16x32_bf16 a[196:199], v[56:59], v[212:215], a[196:199]\n"
"ds_read_b64_tr_b16 v[86:87], v163 offset:7168\n"
"v_cvt_pk_bf16_f32 v233, v48, v49\n"
"v_mfma_f32_16x16x32_bf16 a[200:203], v[60:63], v[180:183], a[200:203]\n"
"ds_read_b64_tr_b16 v[88:89], v162 offset:256\n"
"v_lshlrev_b32 v48, 16, v234\n"
"v_mfma_f32_16x16x32_bf16 a[204:207], v[60:63], v[212:215], a[204:207]\n"
"s_waitcnt lgkmcnt(9)\n"
"ds_read_b64_tr_b16 v[90:91], v162 offset:7424\n"
"s_add_u32 m0, m0, 0x400\n"
"v_and_b32 v49, 0xffff0000, v234\n"
"v_mfma_f32_16x16x32_bf16 a[208:211], v[64:67], v[180:183], a[208:211]\n"
"ds_read_b64_tr_b16 v[92:93], v163 offset:256\n"
"buffer_load_dwordx4 v153, s[40:43], s55 offen lds\n"
"v_mfma_f32_16x16x32_bf16 a[212:215], v[64:67], v[212:215], a[212:215]\n"
"ds_read_b64_tr_b16 v[94:95], v163 offset:7424\n"
"v_mul_f32 v48, v48, v248\n"
"v_mfma_f32_16x16x32_bf16 a[216:219], v[68:71], v[180:183], a[216:219]\n"
"ds_read_b64_tr_b16 v[56:57], v162 offset:512\n"
"v_mul_f32 v49, v49, v248\n"
"v_mfma_f32_16x16x32_bf16 a[220:223], v[68:71], v[212:215], a[220:223]\n"
"s_waitcnt lgkmcnt(9)\n"
"ds_read_b64_tr_b16 v[58:59], v162 offset:7680\n"
"s_add_u32 m0, m0, 0x400\n"
"v_cvt_pk_bf16_f32 v234, v48, v49\n"
"v_mfma_f32_16x16x32_bf16 a[224:227], v[72:75], v[180:183], a[224:227]\n"
"ds_read_b64_tr_b16 v[60:61], v163 offset:512\n"
"buffer_load_dwordx4 v154, s[40:43], s55 offen lds\n"
"v_lshlrev_b32 v48, 16, v235\n"
"v_mfma_f32_16x16x32_bf16 a[228:231], v[72:75], v[212:215], a[228:231]\n"
"ds_read_b64_tr_b16 v[62:63], v163 offset:7680\n"
"v_and_b32 v49, 0xffff0000, v235\n"
"v_mfma_f32_16x16x32_bf16 a[232:235], v[76:79], v[180:183], a[232:235]\n"
"ds_read_b64_tr_b16 v[64:65], v162 offset:768\n"
"v_mfma_f32_16x16x32_bf16 a[236:239], v[76:79], v[212:215], a[236:239]\n"
"s_waitcnt lgkmcnt(9)\n"
"ds_read_b64_tr_b16 v[66:67], v162 offset:7936\n"
"s_add_u32 m0, m0, 0x400\n"
"v_mul_f32 v48, v48, v248\n"
"v_mfma_f32_16x16x32_bf16 a[128:131], v[80:83], v[184:187], a[128:131]\n"
"ds_read_b64_tr_b16 v[68:69], v163 offset:768\n"
"buffer_load_dwordx4 v155, s[40:43], s55 offen lds\n"
"v_mul_f32 v49, v49, v248\n"
"v_mfma_f32_16x16x32_bf16 a[132:135], v[80:83], v[216:219], a[132:135]\n"
"ds_read_b64_tr_b16 v[70:71], v163 offset:7936\n"
"v_cvt_pk_bf16_f32 v235, v48, v49\n"
"v_mfma_f32_16x16x32_bf16 a[136:139], v[84:87], v[184:187], a[136:139]\n"
"ds_read_b64_tr_b16 v[72:73], v162 offset:1024\n"
"v_lshlrev_b32 v48, 16, v236\n"
"v_mfma_f32_16x16x32_bf16 a[140:143], v[84:87], v[216:219], a[140:143]\n"
"s_waitcnt lgkmcnt(9)\n"
"ds_read_b64_tr_b16 v[74:75], v162 offset:8192\n"
"s_add_u32 m0, m0, 0x400\n"
"v_mfma_f32_16x16x32_bf16 a[144:147], v[88:91], v[184:187], a[144:147]\n"
"ds_read_b64_tr_b16 v[76:77], v163 offset:1024\n"
"buffer_load_dwordx4 v156, s[40:43], s55 offen lds\n"
"v_and_b32 v49, 0xffff0000, v236\n"
"v_mfma_f32_16x16x32_bf16 a[148:151], v[88:91], v[216:219], a[148:151]\n"
"ds_read_b64_tr_b16 v[78:79], v163 offset:8192\n"
"v_mul_f32 v48, v48, v249\n"
"v_mfma_f32_16x16x32_bf16 a[152:155], v[92:95], v[184:187], a[152:155]\n"
"ds_read_b64_tr_b16 v[80:81], v162 offset:1280\n"
"v_mul_f32 v49, v49, v249\n"
"v_mfma_f32_16x16x32_bf16 a[156:159], v[92:95], v[216:219], a[156:159]\n"
"s_waitcnt lgkmcnt(9)\n"
"ds_read_b64_tr_b16 v[82:83], v162 offset:8448\n"
"s_add_u32 m0, m0, 0x400\n"
"v_cvt_pk_bf16_f32 v236, v48, v49\n"
"v_mfma_f32_16x16x32_bf16 a[160:163], v[56:59], v[184:187], a[160:163]\n"
"ds_read_b64_tr_b16 v[84:85], v163 offset:1280\n"
"buffer_load_dwordx4 v157, s[40:43], s55 offen lds\n"
"v_lshlrev_b32 v48, 16, v237\n"
"v_mfma_f32_16x16x32_bf16 a[164:167], v[56:59], v[216:219], a[164:167]\n"
"ds_read_b64_tr_b16 v[86:87], v163 offset:8448\n"
"v_mfma_f32_16x16x32_bf16 a[168:171], v[60:63], v[184:187], a[168:171]\n"
"ds_read_b64_tr_b16 v[88:89], v162 offset:1536\n"
"v_and_b32 v49, 0xffff0000, v237\n"
"v_mfma_f32_16x16x32_bf16 a[172:175], v[60:63], v[216:219], a[172:175]\n"
"s_waitcnt lgkmcnt(9)\n"
"ds_read_b64_tr_b16 v[90:91], v162 offset:8704\n"
"s_add_u32 m0, m0, 0x400\n"
"v_mul_f32 v48, v48, v249\n"
"v_mfma_f32_16x16x32_bf16 a[176:179], v[64:67], v[184:187], a[176:179]\n"
"ds_read_b64_tr_b16 v[92:93], v163 offset:1536\n"
"buffer_load_dwordx4 v158, s[40:43], s55 offen lds\n"
"v_mul_f32 v49, v49, v249\n"
"v_mfma_f32_16x16x32_bf16 a[180:183], v[64:67], v[216:219], a[180:183]\n"
"ds_read_b64_tr_b16 v[94:95], v163 offset:8704\n"
"v_cvt_pk_bf16_f32 v237, v48, v49\n"
"v_mfma_f32_16x16x32_bf16 a[184:187], v[68:71], v[184:187], a[184:187]\n"
"ds_read_b64_tr_b16 v[56:57], v162 offset:14336\n"
"v_mfma_f32_16x16x32_bf16 a[188:191], v[68:71], v[216:219], a[188:191]\n"
"s_waitcnt lgkmcnt(9)\n"
"ds_read_b64_tr_b16 v[58:59], v162 offset:21504\n"
"s_add_u32 s55, s55, 57344\n"
"s_add_u32 s56, s56, 28672\n"
"s_sub_u32 s96, s56, 143360\n"
"s_cmp_ge_u32 s56, 143360\n"
"s_cselect_b32 s56, s96, s56\n"
"v_lshlrev_b32 v48, 16, v238\n"
"v_mfma_f32_16x16x32_bf16 a[192:195], v[72:75], v[184:187], a[192:195]\n"
"ds_read_b64_tr_b16 v[60:61], v163 offset:14336\n"
"v_and_b32 v49, 0xffff0000, v238\n"
"v_mfma_f32_16x16x32_bf16 a[196:199], v[72:75], v[216:219], a[196:199]\n"
"ds_read_b64_tr_b16 v[62:63], v163 offset:21504\n"
"v_mul_f32 v48, v48, v249\n"
"v_mfma_f32_16x16x32_bf16 a[200:203], v[76:79], v[184:187], a[200:203]\n"
"ds_read_b64_tr_b16 v[64:65], v162 offset:14592\n"
"v_mul_f32 v49, v49, v249\n"
"v_mfma_f32_16x16x32_bf16 a[204:207], v[76:79], v[216:219], a[204:207]\n"
"s_waitcnt lgkmcnt(9)\n"
"ds_read_b64_tr_b16 v[66:67], v162 offset:21760\n"
"v_cvt_pk_bf16_f32 v238, v48, v49\n"
"v_mfma_f32_16x16x32_bf16 a[208:211], v[80:83], v[184:187], a[208:211]\n"
"ds_read_b64_tr_b16 v[68:69], v163 offset:14592\n"
"v_mfma_f32_16x16x32_bf16 a[212:215], v[80:83], v[216:219], a[212:215]\n"
"ds_read_b64_tr_b16 v[70:71], v163 offset:21760\n"
"v_lshlrev_b32 v48, 16, v239\n"
"v_mfma_f32_16x16x32_bf16 a[216:219], v[84:87], v[184:187], a[216:219]\n"
"ds_read_b64_tr_b16 v[72:73], v162 offset:14848\n"
"v_and_b32 v49, 0xffff0000, v239\n"
"v_mfma_f32_16x16x32_bf16 a[220:223], v[84:87], v[216:219], a[220:223]\n"
"s_waitcnt lgkmcnt(9)\n"
"ds_read_b64_tr_b16 v[74:75], v162 offset:22016\n"
"v_mul_f32 v48, v48, v249\n"
"v_mfma_f32_16x16x32_bf16 a[224:227], v[88:91], v[184:187], a[224:227]\n"
"ds_read_b64_tr_b16 v[76:77], v163 offset:14848\n"
"v_mul_f32 v49, v49, v249\n"
"v_mfma_f32_16x16x32_bf16 a[228:231], v[88:91], v[216:219], a[228:231]\n"
"ds_read_b64_tr_b16 v[78:79], v163 offset:22016\n"
"v_mfma_f32_16x16x32_bf16 a[232:235], v[92:95], v[184:187], a[232:235]\n"
"ds_read_b64_tr_b16 v[80:81], v162 offset:15104\n"
"v_cvt_pk_bf16_f32 v239, v48, v49\n"
"v_mfma_f32_16x16x32_bf16 a[236:239], v[92:95], v[216:219], a[236:239]\n"
"s_waitcnt lgkmcnt(9)\n"
"ds_read_b64_tr_b16 v[82:83], v162 offset:22272\n"
"v_lshlrev_b32 v48, 16, v240\n"
"v_mfma_f32_16x16x32_bf16 a[128:131], v[56:59], v[188:191], a[128:131]\n"
"ds_read_b64_tr_b16 v[84:85], v163 offset:15104\n"
"s_waitcnt vmcnt(7)\n"
"v_mov_b32 v11, 0\n"
"v_mov_b32 v10, s35\n"
"s_mov_b64 exec, 1\n"
"global_store_dword v11, v10, s[62:63] sc1\n"
"s_mov_b64 exec, -1\n"
"L_sg_c1f_%=:\n"
"s_xor_b32 s61, s61, 1\n"
"s_mov_b64 s[62:63], %0\n"
"s_lshl_b32 s96, s61, 4\n"
"s_add_u32 s96, s96, 126885888\n"
"s_add_u32 s62, s62, s96\n"
"s_addc_u32 s63, s63, 0\n"
"s_lshl_b32 s96, s50, 2\n"
"s_add_u32 s62, s62, s96\n"
"s_addc_u32 s63, s63, 0\n"
"v_and_b32 v49, 0xffff0000, v240\n"
"v_mfma_f32_16x16x32_bf16 a[132:135], v[56:59], v[220:223], a[132:135]\n"
"ds_read_b64_tr_b16 v[86:87], v163 offset:22272\n"
"v_mul_f32 v48, v48, v248\n"
"v_mfma_f32_16x16x32_bf16 a[136:139], v[60:63], v[188:191], a[136:139]\n"
"ds_read_b64_tr_b16 v[88:89], v162 offset:15360\n"
"v_mul_f32 v49, v49, v248\n"
"v_mfma_f32_16x16x32_bf16 a[140:143], v[60:63], v[220:223], a[140:143]\n"
"s_waitcnt lgkmcnt(9)\n"
"ds_read_b64_tr_b16 v[90:91], v162 offset:22528\n"
"v_mfma_f32_16x16x32_bf16 a[144:147], v[64:67], v[188:191], a[144:147]\n"
"ds_read_b64_tr_b16 v[92:93], v163 offset:15360\n"
"v_cvt_pk_bf16_f32 v240, v48, v49\n"
"v_mfma_f32_16x16x32_bf16 a[148:151], v[64:67], v[220:223], a[148:151]\n"
"ds_read_b64_tr_b16 v[94:95], v163 offset:22528\n"
"v_lshlrev_b32 v48, 16, v241\n"
"v_mfma_f32_16x16x32_bf16 a[152:155], v[68:71], v[188:191], a[152:155]\n"
"ds_read_b64_tr_b16 v[56:57], v162 offset:15616\n"
"v_and_b32 v49, 0xffff0000, v241\n"
"v_mfma_f32_16x16x32_bf16 a[156:159], v[68:71], v[220:223], a[156:159]\n"
"s_waitcnt lgkmcnt(9)\n"
"ds_read_b64_tr_b16 v[58:59], v162 offset:22784\n"
"v_mul_f32 v48, v48, v248\n"
"v_mfma_f32_16x16x32_bf16 a[160:163], v[72:75], v[188:191], a[160:163]\n"
"ds_read_b64_tr_b16 v[60:61], v163 offset:15616\n"
"v_mul_f32 v49, v49, v248\n"
"v_mfma_f32_16x16x32_bf16 a[164:167], v[72:75], v[220:223], a[164:167]\n"
"ds_read_b64_tr_b16 v[62:63], v163 offset:22784\n"
"v_mfma_f32_16x16x32_bf16 a[168:171], v[76:79], v[188:191], a[168:171]\n"
"ds_read_b64_tr_b16 v[64:65], v162 offset:15872\n"
"v_cvt_pk_bf16_f32 v241, v48, v49\n"
"v_mfma_f32_16x16x32_bf16 a[172:175], v[76:79], v[220:223], a[172:175]\n"
"s_waitcnt lgkmcnt(9)\n"
"ds_read_b64_tr_b16 v[66:67], v162 offset:23040\n"
"v_lshlrev_b32 v48, 16, v242\n"
"v_mfma_f32_16x16x32_bf16 a[176:179], v[80:83], v[188:191], a[176:179]\n"
"ds_read_b64_tr_b16 v[68:69], v163 offset:15872\n"
"v_and_b32 v49, 0xffff0000, v242\n"
"v_mfma_f32_16x16x32_bf16 a[180:183], v[80:83], v[220:223], a[180:183]\n"
"ds_read_b64_tr_b16 v[70:71], v163 offset:23040\n"
"s_waitcnt vmcnt(21)\n"
"s_barrier\n"
"v_add_u32 v162, s59, v160\n"
"v_add_u32 v163, s59, v161\n"
"s_add_u32 s59, s59, 28672\n"
"s_sub_u32 s96, s59, 143360\n"
"s_cmp_ge_u32 s59, 143360\n"
"s_cselect_b32 s59, s96, s59\n"
"v_mul_f32 v48, v48, v248\n"
"v_mfma_f32_16x16x32_bf16 a[184:187], v[84:87], v[188:191], a[184:187]\n"
"ds_read_b64_tr_b16 v[72:73], v162 offset:0\n"
"v_mfma_f32_16x16x32_bf16 a[188:191], v[84:87], v[220:223], a[188:191]\n"
"s_waitcnt lgkmcnt(9)\n"
"ds_read_b64_tr_b16 v[74:75], v162 offset:7168\n"
"s_mov_b32 m0, s56\n"
"v_mul_f32 v49, v49, v248\n"
"v_mfma_f32_16x16x32_bf16 a[192:195], v[88:91], v[188:191], a[192:195]\n"
"ds_read_b64_tr_b16 v[76:77], v163 offset:0\n"
"buffer_load_dwordx4 v152, s[40:43], s55 offen lds\n"
"v_cvt_pk_bf16_f32 v242, v48, v49\n"
"v_mfma_f32_16x16x32_bf16 a[196:199], v[88:91], v[220:223], a[196:199]\n"
"ds_read_b64_tr_b16 v[78:79], v163 offset:7168\n"
"v_lshlrev_b32 v48, 16, v243\n"
"v_mfma_f32_16x16x32_bf16 a[200:203], v[92:95], v[188:191], a[200:203]\n"
"ds_read_b64_tr_b16 v[80:81], v162 offset:256\n"
"v_and_b32 v49, 0xffff0000, v243\n"
"v_mfma_f32_16x16x32_bf16 a[204:207], v[92:95], v[220:223], a[204:207]\n"
"s_waitcnt lgkmcnt(9)\n"
"ds_read_b64_tr_b16 v[82:83], v162 offset:7424\n"
"s_add_u32 m0, m0, 0x400\n"
"v_mul_f32 v48, v48, v248\n"
"v_mfma_f32_16x16x32_bf16 a[208:211], v[56:59], v[188:191], a[208:211]\n"
"ds_read_b64_tr_b16 v[84:85], v163 offset:256\n"
"buffer_load_dwordx4 v153, s[40:43], s55 offen lds\n"
"v_mfma_f32_16x16x32_bf16 a[212:215], v[56:59], v[220:223], a[212:215]\n"
"ds_read_b64_tr_b16 v[86:87], v163 offset:7424\n"
"v_mul_f32 v49, v49, v248\n"
"v_mfma_f32_16x16x32_bf16 a[216:219], v[60:63], v[188:191], a[216:219]\n"
"ds_read_b64_tr_b16 v[88:89], v162 offset:512\n"
"v_cvt_pk_bf16_f32 v243, v48, v49\n"
"v_mfma_f32_16x16x32_bf16 a[220:223], v[60:63], v[220:223], a[220:223]\n"
"s_waitcnt lgkmcnt(9)\n"
"ds_read_b64_tr_b16 v[90:91], v162 offset:7680\n"
"s_add_u32 m0, m0, 0x400\n"
"v_lshlrev_b32 v48, 16, v244\n"
"v_mfma_f32_16x16x32_bf16 a[224:227], v[64:67], v[188:191], a[224:227]\n"
"ds_read_b64_tr_b16 v[92:93], v163 offset:512\n"
"buffer_load_dwordx4 v154, s[40:43], s55 offen lds\n"
"v_and_b32 v49, 0xffff0000, v244\n"
"v_mfma_f32_16x16x32_bf16 a[228:231], v[64:67], v[220:223], a[228:231]\n"
"ds_read_b64_tr_b16 v[94:95], v163 offset:7680\n"
"v_mfma_f32_16x16x32_bf16 a[232:235], v[68:71], v[188:191], a[232:235]\n"
"ds_read_b64_tr_b16 v[56:57], v162 offset:768\n"
"v_mul_f32 v48, v48, v249\n"
"v_mfma_f32_16x16x32_bf16 a[236:239], v[68:71], v[220:223], a[236:239]\n"
"s_waitcnt lgkmcnt(9)\n"
"ds_read_b64_tr_b16 v[58:59], v162 offset:7936\n"
"s_add_u32 m0, m0, 0x400\n"
"v_mul_f32 v49, v49, v249\n"
"v_mfma_f32_16x16x32_bf16 a[128:131], v[72:75], v[192:195], a[128:131]\n"
"ds_read_b64_tr_b16 v[60:61], v163 offset:768\n"
"buffer_load_dwordx4 v155, s[40:43], s55 offen lds\n"
"v_cvt_pk_bf16_f32 v244, v48, v49\n"
"v_mfma_f32_16x16x32_bf16 a[132:135], v[72:75], v[224:227], a[132:135]\n"
"ds_read_b64_tr_b16 v[62:63], v163 offset:7936\n"
"v_lshlrev_b32 v48, 16, v245\n"
"v_mfma_f32_16x16x32_bf16 a[136:139], v[76:79], v[192:195], a[136:139]\n"
"ds_read_b64_tr_b16 v[64:65], v162 offset:1024\n"
"v_and_b32 v49, 0xffff0000, v245\n"
"v_mfma_f32_16x16x32_bf16 a[140:143], v[76:79], v[224:227], a[140:143]\n"
"s_waitcnt lgkmcnt(9)\n"
"ds_read_b64_tr_b16 v[66:67], v162 offset:8192\n"
"s_add_u32 m0, m0, 0x400\n"
"v_mfma_f32_16x16x32_bf16 a[144:147], v[80:83], v[192:195], a[144:147]\n"
"ds_read_b64_tr_b16 v[68:69], v163 offset:1024\n"
"buffer_load_dwordx4 v156, s[40:43], s55 offen lds\n"
"v_mul_f32 v48, v48, v249\n"
"v_mfma_f32_16x16x32_bf16 a[148:151], v[80:83], v[224:227], a[148:151]\n"
"ds_read_b64_tr_b16 v[70:71], v163 offset:8192\n"
"v_mul_f32 v49, v49, v249\n"
"v_mfma_f32_16x16x32_bf16 a[152:155], v[84:87], v[192:195], a[152:155]\n"
"ds_read_b64_tr_b16 v[72:73], v162 offset:1280\n"
"v_cvt_pk_bf16_f32 v245, v48, v49\n"
"v_mfma_f32_16x16x32_bf16 a[156:159], v[84:87], v[224:227], a[156:159]\n"
"s_waitcnt lgkmcnt(9)\n"
"ds_read_b64_tr_b16 v[74:75], v162 offset:8448\n"
"s_add_u32 m0, m0, 0x400\n"
"v_lshlrev_b32 v48, 16, v246\n"
"v_mfma_f32_16x16x32_bf16 a[160:163], v[88:91], v[192:195], a[160:163]\n"
"ds_read_b64_tr_b16 v[76:77], v163 offset:1280\n"
"buffer_load_dwordx4 v157, s[40:43], s55 offen lds\n"
"v_mfma_f32_16x16x32_bf16 a[164:167], v[88:91], v[224:227], a[164:167]\n"
"ds_read_b64_tr_b16 v[78:79], v163 offset:8448\n"
"v_and_b32 v49, 0xffff0000, v246\n"
"v_mfma_f32_16x16x32_bf16 a[168:171], v[92:95], v[192:195], a[168:171]\n"
"ds_read_b64_tr_b16 v[80:81], v162 offset:1536\n"
"v_mul_f32 v48, v48, v249\n"
"v_mfma_f32_16x16x32_bf16 a[172:175], v[92:95], v[224:227], a[172:175]\n"
"s_waitcnt lgkmcnt(9)\n"
"ds_read_b64_tr_b16 v[82:83], v162 offset:8704\n"
"s_add_u32 m0, m0, 0x400\n"
"v_mul_f32 v49, v49, v249\n"
"v_mfma_f32_16x16x32_bf16 a[176:179], v[56:59], v[192:195], a[176:179]\n"
"ds_read_b64_tr_b16 v[84:85], v163 offset:1536\n"
"buffer_load_dwordx4 v158, s[40:43], s55 offen lds\n"
"v_cvt_pk_bf16_f32 v246, v48, v49\n"
"v_mfma_f32_16x16x32_bf16 a[180:183], v[56:59], v[224:227], a[180:183]\n"
"ds_read_b64_tr_b16 v[86:87], v163 offset:8704\n"
"v_lshlrev_b32 v48, 16, v247\n"
"v_mfma_f32_16x16x32_bf16 a[184:187], v[60:63], v[192:195], a[184:187]\n"
"ds_read_b64_tr_b16 v[88:89], v162 offset:14336\n"
"v_mfma_f32_16x16x32_bf16 a[188:191], v[60:63], v[224:227], a[188:191]\n"
"s_waitcnt lgkmcnt(9)\n"
"ds_read_b64_tr_b16 v[90:91], v162 offset:21504\n"
"s_add_u32 s55, s55, 57344\n"
"s_add_u32 s56, s56, 28672\n"
"s_sub_u32 s96, s56, 143360\n"
"s_cmp_ge_u32 s56, 143360\n"
"s_cselect_b32 s56, s96, s56\n"
"v_and_b32 v49, 0xffff0000, v247\n"
"v_mfma_f32_16x16x32_bf16 a[192:195], v[64:67], v[192:195], a[192:195]\n"
"ds_read_b64_tr_b16 v[92:93], v163 offset:14336\n"
"v_mul_f32 v48, v48, v249\n"
"v_mfma_f32_16x16x32_bf16 a[196:199], v[64:67], v[224:227], a[196:199]\n"
"ds_read_b64_tr_b16 v[94:95], v163 offset:21504\n"
"v_mul_f32 v49, v49, v249\n"
"v_mfma_f32_16x16x32_bf16 a[200:203], v[68:71], v[192:195], a[200:203]\n"
"ds_read_b64_tr_b16 v[56:57], v162 offset:14592\n"
"v_cvt_pk_bf16_f32 v247, v48, v49\n"
"v_mfma_f32_16x16x32_bf16 a[204:207], v[68:71], v[224:227], a[204:207]\n"
"s_waitcnt lgkmcnt(9)\n"
"ds_read_b64_tr_b16 v[58:59], v162 offset:21760\n"
"v_mfma_f32_16x16x32_bf16 a[208:211], v[72:75], v[192:195], a[208:211]\n"
"ds_read_b64_tr_b16 v[60:61], v163 offset:14592\n"
"v_mfma_f32_16x16x32_bf16 a[212:215], v[72:75], v[224:227], a[212:215]\n"
"ds_read_b64_tr_b16 v[62:63], v163 offset:21760\n"
"v_mfma_f32_16x16x32_bf16 a[216:219], v[76:79], v[192:195], a[216:219]\n"
"ds_read_b64_tr_b16 v[64:65], v162 offset:14848\n"
"v_mfma_f32_16x16x32_bf16 a[220:223], v[76:79], v[224:227], a[220:223]\n"
"s_waitcnt lgkmcnt(9)\n"
"ds_read_b64_tr_b16 v[66:67], v162 offset:22016\n"
"v_mfma_f32_16x16x32_bf16 a[224:227], v[80:83], v[192:195], a[224:227]\n"
"ds_read_b64_tr_b16 v[68:69], v163 offset:14848\n"
"v_mfma_f32_16x16x32_bf16 a[228:231], v[80:83], v[224:227], a[228:231]\n"
"ds_read_b64_tr_b16 v[70:71], v163 offset:22016\n"
"v_mfma_f32_16x16x32_bf16 a[232:235], v[84:87], v[192:195], a[232:235]\n"
"ds_read_b64_tr_b16 v[72:73], v162 offset:15104\n"
"v_mfma_f32_16x16x32_bf16 a[236:239], v[84:87], v[224:227], a[236:239]\n"
"s_waitcnt lgkmcnt(9)\n"
"ds_read_b64_tr_b16 v[74:75], v162 offset:22272\n"
"v_mfma_f32_16x16x32_bf16 a[128:131], v[88:91], v[196:199], a[128:131]\n"
"ds_read_b64_tr_b16 v[76:77], v163 offset:15104\n"
"v_mfma_f32_16x16x32_bf16 a[132:135], v[88:91], v[228:231], a[132:135]\n"
"ds_read_b64_tr_b16 v[78:79], v163 offset:22272\n"
"v_mfma_f32_16x16x32_bf16 a[136:139], v[92:95], v[196:199], a[136:139]\n"
"ds_read_b64_tr_b16 v[80:81], v162 offset:15360\n"
"v_mfma_f32_16x16x32_bf16 a[140:143], v[92:95], v[228:231], a[140:143]\n"
"s_waitcnt lgkmcnt(9)\n"
"ds_read_b64_tr_b16 v[82:83], v162 offset:22528\n"
"v_mfma_f32_16x16x32_bf16 a[144:147], v[56:59], v[196:199], a[144:147]\n"
"ds_read_b64_tr_b16 v[84:85], v163 offset:15360\n"
"v_mov_b32 v11, 0\n"
"v_mfma_f32_16x16x32_bf16 a[148:151], v[56:59], v[228:231], a[148:151]\n"
"ds_read_b64_tr_b16 v[86:87], v163 offset:22528\n"
"global_load_dword v251, v11, s[62:63] sc1\n"
"v_mfma_f32_16x16x32_bf16 a[152:155], v[60:63], v[196:199], a[152:155]\n"
"ds_read_b64_tr_b16 v[88:89], v162 offset:15616\n"
"v_mfma_f32_16x16x32_bf16 a[156:159], v[60:63], v[228:231], a[156:159]\n"
"s_waitcnt lgkmcnt(9)\n"
"ds_read_b64_tr_b16 v[90:91], v162 offset:22784\n"
"v_mfma_f32_16x16x32_bf16 a[160:163], v[64:67], v[196:199], a[160:163]\n"
"ds_read_b64_tr_b16 v[92:93], v163 offset:15616\n"
"v_mfma_f32_16x16x32_bf16 a[164:167], v[64:67], v[228:231], a[164:167]\n"
"ds_read_b64_tr_b16 v[94:95], v163 offset:22784\n"
"v_mfma_f32_16x16x32_bf16 a[168:171], v[68:71], v[196:199], a[168:171]\n"
"ds_read_b64_tr_b16 v[56:57], v162 offset:15872\n"
"v_mfma_f32_16x16x32_bf16 a[172:175], v[68:71], v[228:231], a[172:175]\n"
"s_waitcnt lgkmcnt(9)\n"
"ds_read_b64_tr_b16 v[58:59], v162 offset:23040\n"
"v_mfma_f32_16x16x32_bf16 a[176:179], v[72:75], v[196:199], a[176:179]\n"
"ds_read_b64_tr_b16 v[60:61], v163 offset:15872\n"
"v_mfma_f32_16x16x32_bf16 a[180:183], v[72:75], v[228:231], a[180:183]\n"
"ds_read_b64_tr_b16 v[62:63], v163 offset:23040\n"
"s_waitcnt vmcnt(18)\n"
"s_barrier\n"
"v_add_u32 v162, s59, v160\n"
"v_add_u32 v163, s59, v161\n"
"s_add_u32 s59, s59, 28672\n"
"s_sub_u32 s96, s59, 143360\n"
"s_cmp_ge_u32 s59, 143360\n"
"s_cselect_b32 s59, s96, s59\n"
"v_mfma_f32_16x16x32_bf16 a[184:187], v[76:79], v[196:199], a[184:187]\n"
"ds_read_b64_tr_b16 v[64:65], v162 offset:0\n"
"v_mfma_f32_16x16x32_bf16 a[188:191], v[76:79], v[228:231], a[188:191]\n"
"s_waitcnt lgkmcnt(9)\n"
"ds_read_b64_tr_b16 v[66:67], v162 offset:7168\n"
"s_mov_b32 m0, s56\n"
"v_mfma_f32_16x16x32_bf16 a[192:195], v[80:83], v[196:199], a[192:195]\n"
"ds_read_b64_tr_b16 v[68:69], v163 offset:0\n"
"buffer_load_dwordx4 v152, s[40:43], s55 offen lds\n"
"v_mfma_f32_16x16x32_bf16 a[196:199], v[80:83], v[228:231], a[196:199]\n"
"ds_read_b64_tr_b16 v[70:71], v163 offset:7168\n"
"v_mfma_f32_16x16x32_bf16 a[200:203], v[84:87], v[196:199], a[200:203]\n"
"ds_read_b64_tr_b16 v[72:73], v162 offset:256\n"
"v_mfma_f32_16x16x32_bf16 a[204:207], v[84:87], v[228:231], a[204:207]\n"
"s_waitcnt lgkmcnt(9)\n"
"ds_read_b64_tr_b16 v[74:75], v162 offset:7424\n"
"s_add_u32 m0, m0, 0x400\n"
"v_mfma_f32_16x16x32_bf16 a[208:211], v[88:91], v[196:199], a[208:211]\n"
"ds_read_b64_tr_b16 v[76:77], v163 offset:256\n"
"buffer_load_dwordx4 v153, s[40:43], s55 offen lds\n"
"v_mfma_f32_16x16x32_bf16 a[212:215], v[88:91], v[228:231], a[212:215]\n"
"ds_read_b64_tr_b16 v[78:79], v163 offset:7424\n"
"v_mfma_f32_16x16x32_bf16 a[216:219], v[92:95], v[196:199], a[216:219]\n"
"ds_read_b64_tr_b16 v[80:81], v162 offset:512\n"
"v_mfma_f32_16x16x32_bf16 a[220:223], v[92:95], v[228:231], a[220:223]\n"
"s_waitcnt lgkmcnt(9)\n"
"ds_read_b64_tr_b16 v[82:83], v162 offset:7680\n"
"s_add_u32 m0, m0, 0x400\n"
"v_mfma_f32_16x16x32_bf16 a[224:227], v[56:59], v[196:199], a[224:227]\n"
"ds_read_b64_tr_b16 v[84:85], v163 offset:512\n"
"buffer_load_dwordx4 v154, s[40:43], s55 offen lds\n"
"v_mfma_f32_16x16x32_bf16 a[228:231], v[56:59], v[228:231], a[228:231]\n"
"ds_read_b64_tr_b16 v[86:87], v163 offset:7680\n"
"v_mfma_f32_16x16x32_bf16 a[232:235], v[60:63], v[196:199], a[232:235]\n"
"ds_read_b64_tr_b16 v[88:89], v162 offset:768\n"
"v_mfma_f32_16x16x32_bf16 a[236:239], v[60:63], v[228:231], a[236:239]\n"
"s_waitcnt lgkmcnt(9)\n"
"ds_read_b64_tr_b16 v[90:91], v162 offset:7936\n"
"s_add_u32 m0, m0, 0x400\n"
"v_mfma_f32_16x16x32_bf16 a[128:131], v[64:67], v[232:235], a[128:131]\n"
"ds_read_b64_tr_b16 v[92:93], v163 offset:768\n"
"buffer_load_dwordx4 v155, s[40:43], s55 offen lds\n"
"v_mfma_f32_16x16x32_bf16 a[132:135], v[64:67], v[236:239], a[132:135]\n"
"ds_read_b64_tr_b16 v[94:95], v163 offset:7936\n"
"v_mfma_f32_16x16x32_bf16 a[136:139], v[68:71], v[232:235], a[136:139]\n"
"ds_read_b64_tr_b16 v[56:57], v162 offset:1024\n"
"v_mfma_f32_16x16x32_bf16 a[140:143], v[68:71], v[236:239], a[140:143]\n"
"s_waitcnt lgkmcnt(9)\n"
"ds_read_b64_tr_b16 v[58:59], v162 offset:8192\n"
"s_add_u32 m0, m0, 0x400\n"
"v_mfma_f32_16x16x32_bf16 a[144:147], v[72:75], v[232:235], a[144:147]\n"
"ds_read_b64_tr_b16 v[60:61], v163 offset:1024\n"
"buffer_load_dwordx4 v156, s[40:43], s55 offen lds\n"
"v_mfma_f32_16x16x32_bf16 a[148:151], v[72:75], v[236:239], a[148:151]\n"
"s_waitcnt vmcnt(5)\n"
"v_readfirstlane_b32 s97, v251\n"
"s_nop 3\n"
"s_cmp_eq_u32 s97, s35\n"
"s_cbranch_scc1 L_pd_c1f_%=\n"
"v_lshlrev_b32 v11, 2, v0\n"
"v_min_u32 v11, 0, v11\n"
"s_mov_b32 s99, 0\n"
"L_pl_c1f_%=:\n"
"global_load_dword v10, v11, s[62:63] sc1\n"
"s_waitcnt vmcnt(0)\n"
"v_cmp_ne_u32 vcc, s35, v10\n"
"s_nop 4\n"
"s_cmp_eq_u64 vcc, 0\n"
"s_cbranch_scc1 L_pd_c1f_%=\n"
"s_sleep 1\n"
"s_add_u32 s99, s99, 1\n"
"s_cmp_lt_u32 s99, 0x200000\n"
"s_cbranch_scc1 L_pl_c1f_%=\n"
"L_pd_c1f_%=:\n"
"ds_read_b64_tr_b16 v[62:63], v163 offset:8192\n"
"v_mfma_f32_16x16x32_bf16 a[152:155], v[76:79], v[232:235], a[152:155]\n"
"ds_read_b64_tr_b16 v[64:65], v162 offset:1280\n"
"buffer_load_dwordx4 a[0:3], v1, s[36:39], s57 offen offset:0 sc1\n"
"v_mfma_f32_16x16x32_bf16 a[156:159], v[76:79], v[236:239], a[156:159]\n"
"s_waitcnt lgkmcnt(9)\n"
"ds_read_b64_tr_b16 v[66:67], v162 offset:8448\n"
"s_add_u32 m0, m0, 0x400\n"
"buffer_load_dwordx4 a[4:7], v1, s[36:39], s58 offen offset:0 sc1\n"
"v_mfma_f32_16x16x32_bf16 a[160:163], v[80:83], v[232:235], a[160:163]\n"
"ds_read_b64_tr_b16 v[68:69], v163 offset:1280\n"
"buffer_load_dwordx4 v157, s[40:43], s55 offen lds\n"
"buffer_load_dwordx4 a[8:11], v1, s[36:39], s57 offen offset:1024 sc1\n"
"v_mfma_f32_16x16x32_bf16 a[164:167], v[80:83], v[236:239], a[164:167]\n"
"ds_read_b64_tr_b16 v[70:71], v163 offset:8448\n"
"buffer_load_dwordx4 a[12:15], v1, s[36:39], s58 offen offset:1024 sc1\n"
"v_mfma_f32_16x16x32_bf16 a[168:171], v[84:87], v[232:235], a[168:171]\n"
"ds_read_b64_tr_b16 v[72:73], v162 offset:1536\n"
"buffer_load_dwordx4 a[16:19], v1, s[36:39], s57 offen offset:2048 sc1\n"
"v_mfma_f32_16x16x32_bf16 a[172:175], v[84:87], v[236:239], a[172:175]\n"
"s_waitcnt lgkmcnt(9)\n"
"ds_read_b64_tr_b16 v[74:75], v162 offset:8704\n"
"s_add_u32 m0, m0, 0x400\n"
"buffer_load_dwordx4 a[20:23], v1, s[36:39], s58 offen offset:2048 sc1\n"
"v_mfma_f32_16x16x32_bf16 a[176:179], v[88:91], v[232:235], a[176:179]\n"
"ds_read_b64_tr_b16 v[76:77], v163 offset:1536\n"
"buffer_load_dwordx4 v158, s[40:43], s55 offen lds\n"
"buffer_load_dwordx4 a[24:27], v1, s[36:39], s57 offen offset:3072 sc1\n"
"v_mfma_f32_16x16x32_bf16 a[180:183], v[88:91], v[236:239], a[180:183]\n"
"ds_read_b64_tr_b16 v[78:79], v163 offset:8704\n"
"buffer_load_dwordx4 a[28:31], v1, s[36:39], s58 offen offset:3072 sc1\n"
"s_add_u32 s57, s57, 4096\n"
"s_add_u32 s58, s58, 4096\n"
"v_mfma_f32_16x16x32_bf16 a[184:187], v[92:95], v[232:235], a[184:187]\n"
"ds_read_b64_tr_b16 v[80:81], v162 offset:14336\n"
"buffer_load_dwordx4 a[32:35], v1, s[36:39], s57 offen offset:0 sc1\n"
"v_mfma_f32_16x16x32_bf16 a[188:191], v[92:95], v[236:239], a[188:191]\n"
"s_waitcnt lgkmcnt(9)\n"
"ds_read_b64_tr_b16 v[82:83], v162 offset:21504\n"
"s_add_u32 s55, s55, 57344\n"
"s_add_u32 s56, s56, 28672\n"
"s_sub_u32 s96, s56, 143360\n"
"s_cmp_ge_u32 s56, 143360\n"
"s_cselect_b32 s56, s96, s56\n"
"buffer_load_dwordx4 a[36:39], v1, s[36:39], s58 offen offset:0 sc1\n"
"v_mfma_f32_16x16x32_bf16 a[192:195], v[56:59], v[232:235], a[192:195]\n"
"ds_read_b64_tr_b16 v[84:85], v163 offset:14336\n"
"buffer_load_dwordx4 a[40:43], v1, s[36:39], s57 offen offset:1024 sc1\n"
"v_mfma_f32_16x16x32_bf16 a[196:199], v[56:59], v[236:239], a[196:199]\n"
"ds_read_b64_tr_b16 v[86:87], v163 offset:21504\n"
"buffer_load_dwordx4 a[44:47], v1, s[36:39], s58 offen offset:1024 sc1\n"
"v_mfma_f32_16x16x32_bf16 a[200:203], v[60:63], v[232:235], a[200:203]\n"
"ds_read_b64_tr_b16 v[88:89], v162 offset:14592\n"
"buffer_load_dwordx4 a[48:51], v1, s[36:39], s57 offen offset:2048 sc1\n"
"v_mfma_f32_16x16x32_bf16 a[204:207], v[60:63], v[236:239], a[204:207]\n"
"s_waitcnt lgkmcnt(9)\n"
"ds_read_b64_tr_b16 v[90:91], v162 offset:21760\n"
"buffer_load_dwordx4 a[52:55], v1, s[36:39], s58 offen offset:2048 sc1\n"
"v_mfma_f32_16x16x32_bf16 a[208:211], v[64:67], v[232:235], a[208:211]\n"
"ds_read_b64_tr_b16 v[92:93], v163 offset:14592\n"
"buffer_load_dwordx4 a[56:59], v1, s[36:39], s57 offen offset:3072 sc1\n"
"v_mfma_f32_16x16x32_bf16 a[212:215], v[64:67], v[236:239], a[212:215]\n"
"ds_read_b64_tr_b16 v[94:95], v163 offset:21760\n"
"buffer_load_dwordx4 a[60:63], v1, s[36:39], s58 offen offset:3072 sc1\n"
"v_mfma_f32_16x16x32_bf16 a[216:219], v[68:71], v[232:235], a[216:219]\n"
"ds_read_b64_tr_b16 v[56:57], v162 offset:14848\n"
"v_mfma_f32_16x16x32_bf16 a[220:223], v[68:71], v[236:239], a[220:223]\n"
"s_waitcnt lgkmcnt(9)\n"
"ds_read_b64_tr_b16 v[58:59], v162 offset:22016\n"
"v_mfma_f32_16x16x32_bf16 a[224:227], v[72:75], v[232:235], a[224:227]\n"
"ds_read_b64_tr_b16 v[60:61], v163 offset:14848\n"
"v_mfma_f32_16x16x32_bf16 a[228:231], v[72:75], v[236:239], a[228:231]\n"
"ds_read_b64_tr_b16 v[62:63], v163 offset:22016\n"
"v_mfma_f32_16x16x32_bf16 a[232:235], v[76:79], v[232:235], a[232:235]\n"
"ds_read_b64_tr_b16 v[64:65], v162 offset:15104\n"
"v_mfma_f32_16x16x32_bf16 a[236:239], v[76:79], v[236:239], a[236:239]\n"
"s_waitcnt lgkmcnt(9)\n"
"ds_read_b64_tr_b16 v[66:67], v162 offset:22272\n"
"v_mfma_f32_16x16x32_bf16 a[128:131], v[80:83], v[240:243], a[128:131]\n"
"ds_read_b64_tr_b16 v[68:69], v163 offset:15104\n"
"v_mfma_f32_16x16x32_bf16 a[132:135], v[80:83], v[244:247], a[132:135]\n"
"ds_read_b64_tr_b16 v[70:71], v163 offset:22272\n"
"v_mfma_f32_16x16x32_bf16 a[136:139], v[84:87], v[240:243], a[136:139]\n"
"ds_read_b64_tr_b16 v[72:73], v162 offset:15360\n"
"v_mfma_f32_16x16x32_bf16 a[140:143], v[84:87], v[244:247], a[140:143]\n"
"s_waitcnt lgkmcnt(9)\n"
"ds_read_b64_tr_b16 v[74:75], v162 offset:22528\n"
"v_mfma_f32_16x16x32_bf16 a[144:147], v[88:91], v[240:243], a[144:147]\n"
"ds_read_b64_tr_b16 v[76:77], v163 offset:15360\n"
"v_mfma_f32_16x16x32_bf16 a[148:151], v[88:91], v[244:247], a[148:151]\n"
"ds_read_b64_tr_b16 v[78:79], v163 offset:22528\n"
"v_mfma_f32_16x16x32_bf16 a[152:155], v[92:95], v[240:243], a[152:155]\n"
"ds_read_b64_tr_b16 v[80:81], v162 offset:15616\n"
"v_mfma_f32_16x16x32_bf16 a[156:159], v[92:95], v[244:247], a[156:159]\n"
"s_waitcnt lgkmcnt(9)\n"
"ds_read_b64_tr_b16 v[82:83], v162 offset:22784\n"
"v_mfma_f32_16x16x32_bf16 a[160:163], v[56:59], v[240:243], a[160:163]\n"
"ds_read_b64_tr_b16 v[84:85], v163 offset:15616\n"
"v_mfma_f32_16x16x32_bf16 a[164:167], v[56:59], v[244:247], a[164:167]\n"
"ds_read_b64_tr_b16 v[86:87], v163 offset:22784\n"
"v_mfma_f32_16x16x32_bf16 a[168:171], v[60:63], v[240:243], a[168:171]\n"
"ds_read_b64_tr_b16 v[88:89], v162 offset:15872\n"
"v_mfma_f32_16x16x32_bf16 a[172:175], v[60:63], v[244:247], a[172:175]\n"
"s_waitcnt lgkmcnt(9)\n"
"ds_read_b64_tr_b16 v[90:91], v162 offset:23040\n"
"v_mfma_f32_16x16x32_bf16 a[176:179], v[64:67], v[240:243], a[176:179]\n"
"ds_read_b64_tr_b16 v[92:93], v163 offset:15872\n"
"v_mfma_f32_16x16x32_bf16 a[180:183], v[64:67], v[244:247], a[180:183]\n"
"ds_read_b64_tr_b16 v[94:95], v163 offset:23040\n"
"s_waitcnt vmcnt(32)\n"
"s_barrier\n"
"v_add_u32 v162, s59, v160\n"
"v_add_u32 v163, s59, v161\n"
"s_add_u32 s59, s59, 28672\n"
"s_sub_u32 s96, s59, 143360\n"
"s_cmp_ge_u32 s59, 143360\n"
"s_cselect_b32 s59, s96, s59\n"
"v_mfma_f32_16x16x32_bf16 a[184:187], v[68:71], v[240:243], a[184:187]\n"
"ds_read_b64_tr_b16 v[56:57], v162 offset:0\n"
"v_mfma_f32_16x16x32_bf16 a[188:191], v[68:71], v[244:247], a[188:191]\n"
"s_waitcnt lgkmcnt(9)\n"
"ds_read_b64_tr_b16 v[58:59], v162 offset:7168\n"
"s_mov_b32 m0, s56\n"
"v_mfma_f32_16x16x32_bf16 a[192:195], v[72:75], v[240:243], a[192:195]\n"
"ds_read_b64_tr_b16 v[60:61], v163 offset:0\n"
"buffer_load_dwordx4 v152, s[40:43], s55 offen lds\n"
"v_mfma_f32_16x16x32_bf16 a[196:199], v[72:75], v[244:247], a[196:199]\n"
"ds_read_b64_tr_b16 v[62:63], v163 offset:7168\n"
"v_mfma_f32_16x16x32_bf16 a[200:203], v[76:79], v[240:243], a[200:203]\n"
"ds_read_b64_tr_b16 v[64:65], v162 offset:256\n"
"v_mfma_f32_16x16x32_bf16 a[204:207], v[76:79], v[244:247], a[204:207]\n"
"s_waitcnt lgkmcnt(9)\n"
"ds_read_b64_tr_b16 v[66:67], v162 offset:7424\n"
"s_add_u32 m0, m0, 0x400\n"
"v_mfma_f32_16x16x32_bf16 a[208:211], v[80:83], v[240:243], a[208:211]\n"
"ds_read_b64_tr_b16 v[68:69], v163 offset:256\n"
"buffer_load_dwordx4 v153, s[40:43], s55 offen lds\n"
"v_mfma_f32_16x16x32_bf16 a[212:215], v[80:83], v[244:247], a[212:215]\n"
"ds_read_b64_tr_b16 v[70:71], v163 offset:7424\n"
"v_mfma_f32_16x16x32_bf16 a[216:219], v[84:87], v[240:243], a[216:219]\n"
"ds_read_b64_tr_b16 v[72:73], v162 offset:512\n"
"v_mfma_f32_16x16x32_bf16 a[220:223], v[84:87], v[244:247], a[220:223]\n"
"s_waitcnt lgkmcnt(9)\n"
"ds_read_b64_tr_b16 v[74:75], v162 offset:7680\n"
"s_add_u32 m0, m0, 0x400\n"
"v_mfma_f32_16x16x32_bf16 a[224:227], v[88:91], v[240:243], a[224:227]\n"
"ds_read_b64_tr_b16 v[76:77], v163 offset:512\n"
"buffer_load_dwordx4 v154, s[40:43], s55 offen lds\n"
"v_mfma_f32_16x16x32_bf16 a[228:231], v[88:91], v[244:247], a[228:231]\n"
"ds_read_b64_tr_b16 v[78:79], v163 offset:7680\n"
"v_mfma_f32_16x16x32_bf16 a[232:235], v[92:95], v[240:243], a[232:235]\n"
"ds_read_b64_tr_b16 v[80:81], v162 offset:768\n"
"v_mfma_f32_16x16x32_bf16 a[236:239], v[92:95], v[244:247], a[236:239]\n"
"s_waitcnt vmcnt(19)\n"
"s_waitcnt lgkmcnt(9)\n"
"ds_read_b64_tr_b16 v[82:83], v162 offset:7936\n"
"s_add_u32 m0, m0, 0x400\n"
"v_mfma_f32_16x16x32_bf16 a[128:131], v[56:59], a[0:3], a[128:131]\n"
"ds_read_b64_tr_b16 v[84:85], v163 offset:768\n"
"buffer_load_dwordx4 v155, s[40:43], s55 offen lds\n"
"v_mfma_f32_16x16x32_bf16 a[132:135], v[56:59], a[4:7], a[132:135]\n"
"ds_read_b64_tr_b16 v[86:87], v163 offset:7936\n"
"v_mfma_f32_16x16x32_bf16 a[136:139], v[60:63], a[0:3], a[136:139]\n"
"ds_read_b64_tr_b16 v[88:89], v162 offset:1024\n"
"v_mfma_f32_16x16x32_bf16 a[140:143], v[60:63], a[4:7], a[140:143]\n"
"s_waitcnt lgkmcnt(9)\n"
"ds_read_b64_tr_b16 v[90:91], v162 offset:8192\n"
"s_add_u32 m0, m0, 0x400\n"
"v_mfma_f32_16x16x32_bf16 a[144:147], v[64:67], a[0:3], a[144:147]\n"
"ds_read_b64_tr_b16 v[92:93], v163 offset:1024\n"
"buffer_load_dwordx4 v156, s[40:43], s55 offen lds\n"
"v_mfma_f32_16x16x32_bf16 a[148:151], v[64:67], a[4:7], a[148:151]\n"
"ds_read_b64_tr_b16 v[94:95], v163 offset:8192\n"
"v_mfma_f32_16x16x32_bf16 a[152:155], v[68:71], a[0:3], a[152:155]\n"
"ds_read_b64_tr_b16 v[56:57], v162 offset:1280\n"
"v_mfma_f32_16x16x32_bf16 a[156:159], v[68:71], a[4:7], a[156:159]\n"
"s_waitcnt lgkmcnt(9)\n"
"ds_read_b64_tr_b16 v[58:59], v162 offset:8448\n"
"s_add_u32 m0, m0, 0x400\n"
"v_mfma_f32_16x16x32_bf16 a[160:163], v[72:75], a[0:3], a[160:163]\n"
"ds_read_b64_tr_b16 v[60:61], v163 offset:1280\n"
"buffer_load_dwordx4 v157, s[40:43], s55 offen lds\n"
"v_mfma_f32_16x16x32_bf16 a[164:167], v[72:75], a[4:7], a[164:167]\n"
"ds_read_b64_tr_b16 v[62:63], v163 offset:8448\n"
"v_mfma_f32_16x16x32_bf16 a[168:171], v[76:79], a[0:3], a[168:171]\n"
"ds_read_b64_tr_b16 v[64:65], v162 offset:1536\n"
"v_mfma_f32_16x16x32_bf16 a[172:175], v[76:79], a[4:7], a[172:175]\n"
"s_waitcnt lgkmcnt(9)\n"
"ds_read_b64_tr_b16 v[66:67], v162 offset:8704\n"
"s_add_u32 m0, m0, 0x400\n"
"v_mfma_f32_16x16x32_bf16 a[176:179], v[80:83], a[0:3], a[176:179]\n"
"ds_read_b64_tr_b16 v[68:69], v163 offset:1536\n"
"buffer_load_dwordx4 v158, s[40:43], s55 offen lds\n"
"v_mfma_f32_16x16x32_bf16 a[180:183], v[80:83], a[4:7], a[180:183]\n"
"ds_read_b64_tr_b16 v[70:71], v163 offset:8704\n"
"v_mfma_f32_16x16x32_bf16 a[184:187], v[84:87], a[0:3], a[184:187]\n"
"ds_read_b64_tr_b16 v[72:73], v162 offset:14336\n"
"v_mfma_f32_16x16x32_bf16 a[188:191], v[84:87], a[4:7], a[188:191]\n"
"s_waitcnt lgkmcnt(9)\n"
"ds_read_b64_tr_b16 v[74:75], v162 offset:21504\n"
"s_add_u32 s55, s55, 57344\n"
"s_add_u32 s56, s56, 28672\n"
"s_sub_u32 s96, s56, 143360\n"
"s_cmp_ge_u32 s56, 143360\n"
"s_cselect_b32 s56, s96, s56\n"
"v_mfma_f32_16x16x32_bf16 a[192:195], v[88:91], a[0:3], a[192:195]\n"
"ds_read_b64_tr_b16 v[76:77], v163 offset:14336\n"
"v_mfma_f32_16x16x32_bf16 a[196:199], v[88:91], a[4:7], a[196:199]\n"
"ds_read_b64_tr_b16 v[78:79], v163 offset:21504\n"
"v_mfma_f32_16x16x32_bf16 a[200:203], v[92:95], a[0:3], a[200:203]\n"
"ds_read_b64_tr_b16 v[80:81], v162 offset:14592\n"
"v_mfma_f32_16x16x32_bf16 a[204:207], v[92:95], a[4:7], a[204:207]\n"
"s_waitcnt lgkmcnt(9)\n"
"ds_read_b64_tr_b16 v[82:83], v162 offset:21760\n"
"v_mfma_f32_16x16x32_bf16 a[208:211], v[56:59], a[0:3], a[208:211]\n"
"ds_read_b64_tr_b16 v[84:85], v163 offset:14592\n"
"v_mfma_f32_16x16x32_bf16 a[212:215], v[56:59], a[4:7], a[212:215]\n"
"ds_read_b64_tr_b16 v[86:87], v163 offset:21760\n"
"v_mfma_f32_16x16x32_bf16 a[216:219], v[60:63], a[0:3], a[216:219]\n"
"ds_read_b64_tr_b16 v[88:89], v162 offset:14848\n"
"v_mfma_f32_16x16x32_bf16 a[220:223], v[60:63], a[4:7], a[220:223]\n"
"s_waitcnt lgkmcnt(9)\n"
"ds_read_b64_tr_b16 v[90:91], v162 offset:22016\n"
"v_mfma_f32_16x16x32_bf16 a[224:227], v[64:67], a[0:3], a[224:227]\n"
"ds_read_b64_tr_b16 v[92:93], v163 offset:14848\n"
"v_mfma_f32_16x16x32_bf16 a[228:231], v[64:67], a[4:7], a[228:231]\n"
"ds_read_b64_tr_b16 v[94:95], v163 offset:22016\n"
"v_mfma_f32_16x16x32_bf16 a[232:235], v[68:71], a[0:3], a[232:235]\n"
"ds_read_b64_tr_b16 v[56:57], v162 offset:15104\n"
"v_mfma_f32_16x16x32_bf16 a[236:239], v[68:71], a[4:7], a[236:239]\n"
"s_waitcnt vmcnt(20)\n"
"s_waitcnt lgkmcnt(9)\n"
"ds_read_b64_tr_b16 v[58:59], v162 offset:22272\n"
"v_mfma_f32_16x16x32_bf16 a[128:131], v[72:75], a[8:11], a[128:131]\n"
"ds_read_b64_tr_b16 v[60:61], v163 offset:15104\n"
"v_mfma_f32_16x16x32_bf16 a[132:135], v[72:75], a[12:15], a[132:135]\n"
"ds_read_b64_tr_b16 v[62:63], v163 offset:22272\n"
"v_mfma_f32_16x16x32_bf16 a[136:139], v[76:79], a[8:11], a[136:139]\n"
"ds_read_b64_tr_b16 v[64:65], v162 offset:15360\n"
"v_mfma_f32_16x16x32_bf16 a[140:143], v[76:79], a[12:15], a[140:143]\n"
"s_waitcnt lgkmcnt(9)\n"
"ds_read_b64_tr_b16 v[66:67], v162 offset:22528\n"
"v_mfma_f32_16x16x32_bf16 a[144:147], v[80:83], a[8:11], a[144:147]\n"
"ds_read_b64_tr_b16 v[68:69], v163 offset:15360\n"
"v_mfma_f32_16x16x32_bf16 a[148:151], v[80:83], a[12:15], a[148:151]\n"
"ds_read_b64_tr_b16 v[70:71], v163 offset:22528\n"
"v_mfma_f32_16x16x32_bf16 a[152:155], v[84:87], a[8:11], a[152:155]\n"
"ds_read_b64_tr_b16 v[72:73], v162 offset:15616\n"
"v_mfma_f32_16x16x32_bf16 a[156:159], v[84:87], a[12:15], a[156:159]\n"
"s_waitcnt lgkmcnt(9)\n"
"ds_read_b64_tr_b16 v[74:75], v162 offset:22784\n"
"v_mfma_f32_16x16x32_bf16 a[160:163], v[88:91], a[8:11], a[160:163]\n"
"ds_read_b64_tr_b16 v[76:77], v163 offset:15616\n"
"v_mfma_f32_16x16x32_bf16 a[164:167], v[88:91], a[12:15], a[164:167]\n"
"ds_read_b64_tr_b16 v[78:79], v163 offset:22784\n"
"v_mfma_f32_16x16x32_bf16 a[168:171], v[92:95], a[8:11], a[168:171]\n"
"ds_read_b64_tr_b16 v[80:81], v162 offset:15872\n"
"v_mfma_f32_16x16x32_bf16 a[172:175], v[92:95], a[12:15], a[172:175]\n"
"s_waitcnt lgkmcnt(9)\n"
"ds_read_b64_tr_b16 v[82:83], v162 offset:23040\n"
"v_mfma_f32_16x16x32_bf16 a[176:179], v[56:59], a[8:11], a[176:179]\n"
"ds_read_b64_tr_b16 v[84:85], v163 offset:15872\n"
"v_mfma_f32_16x16x32_bf16 a[180:183], v[56:59], a[12:15], a[180:183]\n"
"ds_read_b64_tr_b16 v[86:87], v163 offset:23040\n"
"s_waitcnt vmcnt(31)\n"
"s_barrier\n"
"v_add_u32 v162, s59, v160\n"
"v_add_u32 v163, s59, v161\n"
"s_add_u32 s59, s59, 28672\n"
"s_sub_u32 s96, s59, 143360\n"
"s_cmp_ge_u32 s59, 143360\n"
"s_cselect_b32 s59, s96, s59\n"
"v_mfma_f32_16x16x32_bf16 a[184:187], v[60:63], a[8:11], a[184:187]\n"
"ds_read_b64_tr_b16 v[88:89], v162 offset:0\n"
"v_mfma_f32_16x16x32_bf16 a[188:191], v[60:63], a[12:15], a[188:191]\n"
"s_waitcnt lgkmcnt(9)\n"
"ds_read_b64_tr_b16 v[90:91], v162 offset:7168\n"
"v_mfma_f32_16x16x32_bf16 a[192:195], v[64:67], a[8:11], a[192:195]\n"
"ds_read_b64_tr_b16 v[92:93], v163 offset:0\n"
"v_mfma_f32_16x16x32_bf16 a[196:199], v[64:67], a[12:15], a[196:199]\n"
"ds_read_b64_tr_b16 v[94:95], v163 offset:7168\n"
"v_mfma_f32_16x16x32_bf16 a[200:203], v[68:71], a[8:11], a[200:203]\n"
"ds_read_b64_tr_b16 v[56:57], v162 offset:256\n"
"v_mfma_f32_16x16x32_bf16 a[204:207], v[68:71], a[12:15], a[204:207]\n"
"s_waitcnt lgkmcnt(9)\n"
"ds_read_b64_tr_b16 v[58:59], v162 offset:7424\n"
"v_mfma_f32_16x16x32_bf16 a[208:211], v[72:75], a[8:11], a[208:211]\n"
"ds_read_b64_tr_b16 v[60:61], v163 offset:256\n"
"v_mfma_f32_16x16x32_bf16 a[212:215], v[72:75], a[12:15], a[212:215]\n"
"ds_read_b64_tr_b16 v[62:63], v163 offset:7424\n"
"v_mfma_f32_16x16x32_bf16 a[216:219], v[76:79], a[8:11], a[216:219]\n"
"ds_read_b64_tr_b16 v[64:65], v162 offset:512\n"
"v_mfma_f32_16x16x32_bf16 a[220:223], v[76:79], a[12:15], a[220:223]\n"
"s_waitcnt lgkmcnt(9)\n"
"ds_read_b64_tr_b16 v[66:67], v162 offset:7680\n"
"v_mfma_f32_16x16x32_bf16 a[224:227], v[80:83], a[8:11], a[224:227]\n"
"ds_read_b64_tr_b16 v[68:69], v163 offset:512\n"
"v_mfma_f32_16x16x32_bf16 a[228:231], v[80:83], a[12:15], a[228:231]\n"
"ds_read_b64_tr_b16 v[70:71], v163 offset:7680\n"
"v_mfma_f32_16x16x32_bf16 a[232:235], v[84:87], a[8:11], a[232:235]\n"
"ds_read_b64_tr_b16 v[72:73], v162 offset:768\n"
"v_mfma_f32_16x16x32_bf16 a[236:239], v[84:87], a[12:15], a[236:239]\n"
"s_waitcnt vmcnt(18)\n"
"s_waitcnt lgkmcnt(9)\n"
"ds_read_b64_tr_b16 v[74:75], v162 offset:7936\n"
"v_mfma_f32_16x16x32_bf16 a[128:131], v[88:91], a[16:19], a[128:131]\n"
"ds_read_b64_tr_b16 v[76:77], v163 offset:768\n"
"v_mfma_f32_16x16x32_bf16 a[132:135], v[88:91], a[20:23], a[132:135]\n"
"ds_read_b64_tr_b16 v[78:79], v163 offset:7936\n"
"v_mfma_f32_16x16x32_bf16 a[136:139], v[92:95], a[16:19], a[136:139]\n"
"ds_read_b64_tr_b16 v[80:81], v162 offset:1024\n"
"v_mfma_f32_16x16x32_bf16 a[140:143], v[92:95], a[20:23], a[140:143]\n"
"s_waitcnt lgkmcnt(9)\n"
"ds_read_b64_tr_b16 v[82:83], v162 offset:8192\n"
"v_mfma_f32_16x16x32_bf16 a[144:147], v[56:59], a[16:19], a[144:147]\n"
"ds_read_b64_tr_b16 v[84:85], v163 offset:1024\n"
"v_mfma_f32_16x16x32_bf16 a[148:151], v[56:59], a[20:23], a[148:151]\n"
"ds_read_b64_tr_b16 v[86:87], v163 offset:8192\n"
"v_mfma_f32_16x16x32_bf16 a[152:155], v[60:63], a[16:19], a[152:155]\n"
"ds_read_b64_tr_b16 v[88:89], v162 offset:1280\n"
"v_mfma_f32_16x16x32_bf16 a[156:159], v[60:63], a[20:23], a[156:159]\n"
"s_waitcnt lgkmcnt(9)\n"
"ds_read_b64_tr_b16 v[90:91], v162 offset:8448\n"
"v_mfma_f32_16x16x32_bf16 a[160:163], v[64:67], a[16:19], a[160:163]\n"
"ds_read_b64_tr_b16 v[92:93], v163 offset:1280\n"
"v_mfma_f32_16x16x32_bf16 a[164:167], v[64:67], a[20:23], a[164:167]\n"
"ds_read_b64_tr_b16 v[94:95], v163 offset:8448\n"
"v_mfma_f32_16x16x32_bf16 a[168:171], v[68:71], a[16:19], a[168:171]\n"
"ds_read_b64_tr_b16 v[56:57], v162 offset:1536\n"
"v_mfma_f32_16x16x32_bf16 a[172:175], v[68:71], a[20:23], a[172:175]\n"
"s_waitcnt lgkmcnt(9)\n"
"ds_read_b64_tr_b16 v[58:59], v162 offset:8704\n"
"v_mfma_f32_16x16x32_bf16 a[176:179], v[72:75], a[16:19], a[176:179]\n"
"ds_read_b64_tr_b16 v[60:61], v163 offset:1536\n"
"v_mfma_f32_16x16x32_bf16 a[180:183], v[72:75], a[20:23], a[180:183]\n"
"ds_read_b64_tr_b16 v[62:63], v163 offset:8704\n"
"v_mfma_f32_16x16x32_bf16 a[184:187], v[76:79], a[16:19], a[184:187]\n"
"ds_read_b64_tr_b16 v[64:65], v162 offset:14336\n"
"v_mfma_f32_16x16x32_bf16 a[188:191], v[76:79], a[20:23], a[188:191]\n"
"s_waitcnt lgkmcnt(9)\n"
"ds_read_b64_tr_b16 v[66:67], v162 offset:21504\n"
"v_mfma_f32_16x16x32_bf16 a[192:195], v[80:83], a[16:19], a[192:195]\n"
"ds_read_b64_tr_b16 v[68:69], v163 offset:14336\n"
"v_mfma_f32_16x16x32_bf16 a[196:199], v[80:83], a[20:23], a[196:199]\n"
"ds_read_b64_tr_b16 v[70:71], v163 offset:21504\n"
"v_mfma_f32_16x16x32_bf16 a[200:203], v[84:87], a[16:19], a[200:203]\n"
"ds_read_b64_tr_b16 v[72:73], v162 offset:14592\n"
"v_mfma_f32_16x16x32_bf16 a[204:207], v[84:87], a[20:23], a[204:207]\n"
"s_waitcnt lgkmcnt(9)\n"
"ds_read_b64_tr_b16 v[74:75], v162 offset:21760\n"
"v_mfma_f32_16x16x32_bf16 a[208:211], v[88:91], a[16:19], a[208:211]\n"
"ds_read_b64_tr_b16 v[76:77], v163 offset:14592\n"
"v_mfma_f32_16x16x32_bf16 a[212:215], v[88:91], a[20:23], a[212:215]\n"
"ds_read_b64_tr_b16 v[78:79], v163 offset:21760\n"
"v_mfma_f32_16x16x32_bf16 a[216:219], v[92:95], a[16:19], a[216:219]\n"
"ds_read_b64_tr_b16 v[80:81], v162 offset:14848\n"
"v_mfma_f32_16x16x32_bf16 a[220:223], v[92:95], a[20:23], a[220:223]\n"
"s_waitcnt lgkmcnt(9)\n"
"ds_read_b64_tr_b16 v[82:83], v162 offset:22016\n"
"v_mfma_f32_16x16x32_bf16 a[224:227], v[56:59], a[16:19], a[224:227]\n"
"ds_read_b64_tr_b16 v[84:85], v163 offset:14848\n"
"v_mfma_f32_16x16x32_bf16 a[228:231], v[56:59], a[20:23], a[228:231]\n"
"ds_read_b64_tr_b16 v[86:87], v163 offset:22016\n"
"v_mfma_f32_16x16x32_bf16 a[232:235], v[60:63], a[16:19], a[232:235]\n"
"ds_read_b64_tr_b16 v[88:89], v162 offset:15104\n"
"v_mfma_f32_16x16x32_bf16 a[236:239], v[60:63], a[20:23], a[236:239]\n"
"s_waitcnt vmcnt(15)\n"
"s_waitcnt lgkmcnt(9)\n"
"ds_read_b64_tr_b16 v[90:91], v162 offset:22272\n"
"v_mfma_f32_16x16x32_bf16 a[128:131], v[64:67], a[24:27], a[128:131]\n"
"ds_read_b64_tr_b16 v[92:93], v163 offset:15104\n"
"v_mfma_f32_16x16x32_bf16 a[132:135], v[64:67], a[28:31], a[132:135]\n"
"ds_read_b64_tr_b16 v[94:95], v163 offset:22272\n"
"v_mfma_f32_16x16x32_bf16 a[136:139], v[68:71], a[24:27], a[136:139]\n"
"ds_read_b64_tr_b16 v[56:57], v162 offset:15360\n"
"v_mfma_f32_16x16x32_bf16 a[140:143], v[68:71], a[28:31], a[140:143]\n"
"s_waitcnt lgkmcnt(9)\n"
"ds_read_b64_tr_b16 v[58:59], v162 offset:22528\n"
"v_mfma_f32_16x16x32_bf16 a[144:147], v[72:75], a[24:27], a[144:147]\n"
"ds_read_b64_tr_b16 v[60:61], v163 offset:15360\n"
"v_mfma_f32_16x16x32_bf16 a[148:151], v[72:75], a[28:31], a[148:151]\n"
"ds_read_b64_tr_b16 v[62:63], v163 offset:22528\n"
"v_mfma_f32_16x16x32_bf16 a[152:155], v[76:79], a[24:27], a[152:155]\n"
"ds_read_b64_tr_b16 v[64:65], v162 offset:15616\n"
"v_mfma_f32_16x16x32_bf16 a[156:159], v[76:79], a[28:31], a[156:159]\n"
"s_waitcnt lgkmcnt(9)\n"
"ds_read_b64_tr_b16 v[66:67], v162 offset:22784\n"
"v_mfma_f32_16x16x32_bf16 a[160:163], v[80:83], a[24:27], a[160:163]\n"
"ds_read_b64_tr_b16 v[68:69], v163 offset:15616\n"
"v_mfma_f32_16x16x32_bf16 a[164:167], v[80:83], a[28:31], a[164:167]\n"
"ds_read_b64_tr_b16 v[70:71], v163 offset:22784\n"
"v_mfma_f32_16x16x32_bf16 a[168:171], v[84:87], a[24:27], a[168:171]\n"
"ds_read_b64_tr_b16 v[72:73], v162 offset:15872\n"
"v_mfma_f32_16x16x32_bf16 a[172:175], v[84:87], a[28:31], a[172:175]\n"
"s_waitcnt lgkmcnt(9)\n"
"ds_read_b64_tr_b16 v[74:75], v162 offset:23040\n"
"v_mfma_f32_16x16x32_bf16 a[176:179], v[88:91], a[24:27], a[176:179]\n"
"ds_read_b64_tr_b16 v[76:77], v163 offset:15872\n"
"v_mfma_f32_16x16x32_bf16 a[180:183], v[88:91], a[28:31], a[180:183]\n"
"ds_read_b64_tr_b16 v[78:79], v163 offset:23040\n"
"s_waitcnt vmcnt(17)\n"
"s_barrier\n"
"v_add_u32 v162, s59, v160\n"
"v_add_u32 v163, s59, v161\n"
"s_add_u32 s59, s59, 28672\n"
"s_sub_u32 s96, s59, 143360\n"
"s_cmp_ge_u32 s59, 143360\n"
"s_cselect_b32 s59, s96, s59\n"
"v_mfma_f32_16x16x32_bf16 a[184:187], v[92:95], a[24:27], a[184:187]\n"
"ds_read_b64_tr_b16 v[80:81], v162 offset:0\n"
"v_mfma_f32_16x16x32_bf16 a[188:191], v[92:95], a[28:31], a[188:191]\n"
"s_waitcnt lgkmcnt(9)\n"
"ds_read_b64_tr_b16 v[82:83], v162 offset:7168\n"
"v_mfma_f32_16x16x32_bf16 a[192:195], v[56:59], a[24:27], a[192:195]\n"
"ds_read_b64_tr_b16 v[84:85], v163 offset:0\n"
"v_mfma_f32_16x16x32_bf16 a[196:199], v[56:59], a[28:31], a[196:199]\n"
"ds_read_b64_tr_b16 v[86:87], v163 offset:7168\n"
"v_mfma_f32_16x16x32_bf16 a[200:203], v[60:63], a[24:27], a[200:203]\n"
"ds_read_b64_tr_b16 v[88:89], v162 offset:256\n"
"v_mfma_f32_16x16x32_bf16 a[204:207], v[60:63], a[28:31], a[204:207]\n"
"s_waitcnt lgkmcnt(9)\n"
"ds_read_b64_tr_b16 v[90:91], v162 offset:7424\n"
"v_mfma_f32_16x16x32_bf16 a[208:211], v[64:67], a[24:27], a[208:211]\n"
"ds_read_b64_tr_b16 v[92:93], v163 offset:256\n"
"v_mfma_f32_16x16x32_bf16 a[212:215], v[64:67], a[28:31], a[212:215]\n"
"ds_read_b64_tr_b16 v[94:95], v163 offset:7424\n"
"v_mfma_f32_16x16x32_bf16 a[216:219], v[68:71], a[24:27], a[216:219]\n"
"ds_read_b64_tr_b16 v[56:57], v162 offset:512\n"
"v_mfma_f32_16x16x32_bf16 a[220:223], v[68:71], a[28:31], a[220:223]\n"
"s_waitcnt lgkmcnt(9)\n"
"ds_read_b64_tr_b16 v[58:59], v162 offset:7680\n"
"v_mfma_f32_16x16x32_bf16 a[224:227], v[72:75], a[24:27], a[224:227]\n"
"ds_read_b64_tr_b16 v[60:61], v163 offset:512\n"
"v_mfma_f32_16x16x32_bf16 a[228:231], v[72:75], a[28:31], a[228:231]\n"
"ds_read_b64_tr_b16 v[62:63], v163 offset:7680\n"
"v_mfma_f32_16x16x32_bf16 a[232:235], v[76:79], a[24:27], a[232:235]\n"
"ds_read_b64_tr_b16 v[64:65], v162 offset:768\n"
"v_mfma_f32_16x16x32_bf16 a[236:239], v[76:79], a[28:31], a[236:239]\n"
"s_waitcnt vmcnt(13)\n"
"s_waitcnt lgkmcnt(9)\n"
"ds_read_b64_tr_b16 v[66:67], v162 offset:7936\n"
"v_mfma_f32_16x16x32_bf16 a[128:131], v[80:83], a[32:35], a[128:131]\n"
"ds_read_b64_tr_b16 v[68:69], v163 offset:768\n"
"v_mfma_f32_16x16x32_bf16 a[132:135], v[80:83], a[36:39], a[132:135]\n"
"ds_read_b64_tr_b16 v[70:71], v163 offset:7936\n"
"v_mfma_f32_16x16x32_bf16 a[136:139], v[84:87], a[32:35], a[136:139]\n"
"ds_read_b64_tr_b16 v[72:73], v162 offset:1024\n"
"v_mfma_f32_16x16x32_bf16 a[140:143], v[84:87], a[36:39], a[140:143]\n"
"s_waitcnt lgkmcnt(9)\n"
"ds_read_b64_tr_b16 v[74:75], v162 offset:8192\n"
"v_mfma_f32_16x16x32_bf16 a[144:147], v[88:91], a[32:35], a[144:147]\n"
"ds_read_b64_tr_b16 v[76:77], v163 offset:1024\n"
"v_mfma_f32_16x16x32_bf16 a[148:151], v[88:91], a[36:39], a[148:151]\n"
"ds_read_b64_tr_b16 v[78:79], v163 offset:8192\n"
"v_mfma_f32_16x16x32_bf16 a[152:155], v[92:95], a[32:35], a[152:155]\n"
"ds_read_b64_tr_b16 v[80:81], v162 offset:1280\n"
"v_mfma_f32_16x16x32_bf16 a[156:159], v[92:95], a[36:39], a[156:159]\n"
"s_waitcnt lgkmcnt(9)\n"
"ds_read_b64_tr_b16 v[82:83], v162 offset:8448\n"
"v_mfma_f32_16x16x32_bf16 a[160:163], v[56:59], a[32:35], a[160:163]\n"
"ds_read_b64_tr_b16 v[84:85], v163 offset:1280\n"
"v_mfma_f32_16x16x32_bf16 a[164:167], v[56:59], a[36:39], a[164:167]\n"
"ds_read_b64_tr_b16 v[86:87], v163 offset:8448\n"
"v_mfma_f32_16x16x32_bf16 a[168:171], v[60:63], a[32:35], a[168:171]\n"
"ds_read_b64_tr_b16 v[88:89], v162 offset:1536\n"
"v_mfma_f32_16x16x32_bf16 a[172:175], v[60:63], a[36:39], a[172:175]\n"
"s_waitcnt lgkmcnt(9)\n"
"ds_read_b64_tr_b16 v[90:91], v162 offset:8704\n"
"v_mfma_f32_16x16x32_bf16 a[176:179], v[64:67], a[32:35], a[176:179]\n"
"ds_read_b64_tr_b16 v[92:93], v163 offset:1536\n"
"v_mfma_f32_16x16x32_bf16 a[180:183], v[64:67], a[36:39], a[180:183]\n"
"ds_read_b64_tr_b16 v[94:95], v163 offset:8704\n"
"v_mfma_f32_16x16x32_bf16 a[184:187], v[68:71], a[32:35], a[184:187]\n"
"ds_read_b64_tr_b16 v[56:57], v162 offset:14336\n"
"v_mfma_f32_16x16x32_bf16 a[188:191], v[68:71], a[36:39], a[188:191]\n"
"s_waitcnt lgkmcnt(9)\n"
"ds_read_b64_tr_b16 v[58:59], v162 offset:21504\n"
"v_mfma_f32_16x16x32_bf16 a[192:195], v[72:75], a[32:35], a[192:195]\n"
"ds_read_b64_tr_b16 v[60:61], v163 offset:14336\n"
"v_mfma_f32_16x16x32_bf16 a[196:199], v[72:75], a[36:39], a[196:199]\n"
"ds_read_b64_tr_b16 v[62:63], v163 offset:21504\n"
"v_mfma_f32_16x16x32_bf16 a[200:203], v[76:79], a[32:35], a[200:203]\n"
"ds_read_b64_tr_b16 v[64:65], v162 offset:14592\n"
"v_mfma_f32_16x16x32_bf16 a[204:207], v[76:79], a[36:39], a[204:207]\n"
"s_waitcnt lgkmcnt(9)\n"
"ds_read_b64_tr_b16 v[66:67], v162 offset:21760\n"
"v_mfma_f32_16x16x32_bf16 a[208:211], v[80:83], a[32:35], a[208:211]\n"
"ds_read_b64_tr_b16 v[68:69], v163 offset:14592\n"
"v_mfma_f32_16x16x32_bf16 a[212:215], v[80:83], a[36:39], a[212:215]\n"
"ds_read_b64_tr_b16 v[70:71], v163 offset:21760\n"
"v_mfma_f32_16x16x32_bf16 a[216:219], v[84:87], a[32:35], a[216:219]\n"
"ds_read_b64_tr_b16 v[72:73], v162 offset:14848\n"
"v_mfma_f32_16x16x32_bf16 a[220:223], v[84:87], a[36:39], a[220:223]\n"
"s_waitcnt lgkmcnt(9)\n"
"ds_read_b64_tr_b16 v[74:75], v162 offset:22016\n"
"v_mfma_f32_16x16x32_bf16 a[224:227], v[88:91], a[32:35], a[224:227]\n"
"ds_read_b64_tr_b16 v[76:77], v163 offset:14848\n"
"v_mfma_f32_16x16x32_bf16 a[228:231], v[88:91], a[36:39], a[228:231]\n"
"ds_read_b64_tr_b16 v[78:79], v163 offset:22016\n"
"v_mfma_f32_16x16x32_bf16 a[232:235], v[92:95], a[32:35], a[232:235]\n"
"ds_read_b64_tr_b16 v[80:81], v162 offset:15104\n"
"v_mfma_f32_16x16x32_bf16 a[236:239], v[92:95], a[36:39], a[236:239]\n"
"s_waitcnt vmcnt(11)\n"
"s_waitcnt lgkmcnt(9)\n"
"ds_read_b64_tr_b16 v[82:83], v162 offset:22272\n"
"v_mfma_f32_16x16x32_bf16 a[128:131], v[56:59], a[40:43], a[128:131]\n"
"ds_read_b64_tr_b16 v[84:85], v163 offset:15104\n"
"v_mfma_f32_16x16x32_bf16 a[132:135], v[56:59], a[44:47], a[132:135]\n"
"ds_read_b64_tr_b16 v[86:87], v163 offset:22272\n"
"v_mfma_f32_16x16x32_bf16 a[136:139], v[60:63], a[40:43], a[136:139]\n"
"ds_read_b64_tr_b16 v[88:89], v162 offset:15360\n"
"v_mfma_f32_16x16x32_bf16 a[140:143], v[60:63], a[44:47], a[140:143]\n"
"s_waitcnt lgkmcnt(9)\n"
"ds_read_b64_tr_b16 v[90:91], v162 offset:22528\n"
"v_mfma_f32_16x16x32_bf16 a[144:147], v[64:67], a[40:43], a[144:147]\n"
"ds_read_b64_tr_b16 v[92:93], v163 offset:15360\n"
"v_mfma_f32_16x16x32_bf16 a[148:151], v[64:67], a[44:47], a[148:151]\n"
"ds_read_b64_tr_b16 v[94:95], v163 offset:22528\n"
"v_mfma_f32_16x16x32_bf16 a[152:155], v[68:71], a[40:43], a[152:155]\n"
"ds_read_b64_tr_b16 v[56:57], v162 offset:15616\n"
"v_mfma_f32_16x16x32_bf16 a[156:159], v[68:71], a[44:47], a[156:159]\n"
"s_waitcnt lgkmcnt(9)\n"
"ds_read_b64_tr_b16 v[58:59], v162 offset:22784\n"
"v_mfma_f32_16x16x32_bf16 a[160:163], v[72:75], a[40:43], a[160:163]\n"
"ds_read_b64_tr_b16 v[60:61], v163 offset:15616\n"
"v_mfma_f32_16x16x32_bf16 a[164:167], v[72:75], a[44:47], a[164:167]\n"
"ds_read_b64_tr_b16 v[62:63], v163 offset:22784\n"
"v_mfma_f32_16x16x32_bf16 a[168:171], v[76:79], a[40:43], a[168:171]\n"
"ds_read_b64_tr_b16 v[64:65], v162 offset:15872\n"
"v_mfma_f32_16x16x32_bf16 a[172:175], v[76:79], a[44:47], a[172:175]\n"
"s_waitcnt lgkmcnt(9)\n"
"ds_read_b64_tr_b16 v[66:67], v162 offset:23040\n"
"v_mfma_f32_16x16x32_bf16 a[176:179], v[80:83], a[40:43], a[176:179]\n"
"ds_read_b64_tr_b16 v[68:69], v163 offset:15872\n"
"v_mfma_f32_16x16x32_bf16 a[180:183], v[80:83], a[44:47], a[180:183]\n"
"ds_read_b64_tr_b16 v[70:71], v163 offset:23040\n"
"s_waitcnt vmcnt(0)\n"
"s_barrier\n"
"v_add_u32 v162, s59, v160\n"
"v_add_u32 v163, s59, v161\n"
"s_add_u32 s59, s59, 28672\n"
"s_sub_u32 s96, s59, 143360\n"
"s_cmp_ge_u32 s59, 143360\n"
"s_cselect_b32 s59, s96, s59\n"
"v_mfma_f32_16x16x32_bf16 a[184:187], v[84:87], a[40:43], a[184:187]\n"
"ds_read_b64_tr_b16 v[72:73], v162 offset:0\n"
"v_mfma_f32_16x16x32_bf16 a[188:191], v[84:87], a[44:47], a[188:191]\n"
"s_waitcnt lgkmcnt(9)\n"
"ds_read_b64_tr_b16 v[74:75], v162 offset:7168\n"
"v_mfma_f32_16x16x32_bf16 a[192:195], v[88:91], a[40:43], a[192:195]\n"
"ds_read_b64_tr_b16 v[76:77], v163 offset:0\n"
"v_mfma_f32_16x16x32_bf16 a[196:199], v[88:91], a[44:47], a[196:199]\n"
"ds_read_b64_tr_b16 v[78:79], v163 offset:7168\n"
"v_mfma_f32_16x16x32_bf16 a[200:203], v[92:95], a[40:43], a[200:203]\n"
"ds_read_b64_tr_b16 v[80:81], v162 offset:256\n"
"v_mfma_f32_16x16x32_bf16 a[204:207], v[92:95], a[44:47], a[204:207]\n"
"s_waitcnt lgkmcnt(9)\n"
"ds_read_b64_tr_b16 v[82:83], v162 offset:7424\n"
"v_mfma_f32_16x16x32_bf16 a[208:211], v[56:59], a[40:43], a[208:211]\n"
"ds_read_b64_tr_b16 v[84:85], v163 offset:256\n"
"v_mfma_f32_16x16x32_bf16 a[212:215], v[56:59], a[44:47], a[212:215]\n"
"ds_read_b64_tr_b16 v[86:87], v163 offset:7424\n"
"v_mfma_f32_16x16x32_bf16 a[216:219], v[60:63], a[40:43], a[216:219]\n"
"ds_read_b64_tr_b16 v[88:89], v162 offset:512\n"
"v_mfma_f32_16x16x32_bf16 a[220:223], v[60:63], a[44:47], a[220:223]\n"
"s_waitcnt lgkmcnt(9)\n"
"ds_read_b64_tr_b16 v[90:91], v162 offset:7680\n"
"v_mfma_f32_16x16x32_bf16 a[224:227], v[64:67], a[40:43], a[224:227]\n"
"ds_read_b64_tr_b16 v[92:93], v163 offset:512\n"
"v_mfma_f32_16x16x32_bf16 a[228:231], v[64:67], a[44:47], a[228:231]\n"
"ds_read_b64_tr_b16 v[94:95], v163 offset:7680\n"
"v_mfma_f32_16x16x32_bf16 a[232:235], v[68:71], a[40:43], a[232:235]\n"
"ds_read_b64_tr_b16 v[56:57], v162 offset:768\n"
"v_mfma_f32_16x16x32_bf16 a[236:239], v[68:71], a[44:47], a[236:239]\n"
"s_waitcnt vmcnt(9)\n"
"s_waitcnt lgkmcnt(9)\n"
"ds_read_b64_tr_b16 v[58:59], v162 offset:7936\n"
"v_mfma_f32_16x16x32_bf16 a[128:131], v[72:75], a[48:51], a[128:131]\n"
"ds_read_b64_tr_b16 v[60:61], v163 offset:768\n"
"v_mfma_f32_16x16x32_bf16 a[132:135], v[72:75], a[52:55], a[132:135]\n"
"ds_read_b64_tr_b16 v[62:63], v163 offset:7936\n"
"v_mfma_f32_16x16x32_bf16 a[136:139], v[76:79], a[48:51], a[136:139]\n"
"ds_read_b64_tr_b16 v[64:65], v162 offset:1024\n"
"v_mfma_f32_16x16x32_bf16 a[140:143], v[76:79], a[52:55], a[140:143]\n"
"s_waitcnt lgkmcnt(9)\n"
"ds_read_b64_tr_b16 v[66:67], v162 offset:8192\n"
"v_mfma_f32_16x16x32_bf16 a[144:147], v[80:83], a[48:51], a[144:147]\n"
"ds_read_b64_tr_b16 v[68:69], v163 offset:1024\n"
"v_mfma_f32_16x16x32_bf16 a[148:151], v[80:83], a[52:55], a[148:151]\n"
"ds_read_b64_tr_b16 v[70:71], v163 offset:8192\n"
"v_mfma_f32_16x16x32_bf16 a[152:155], v[84:87], a[48:51], a[152:155]\n"
"ds_read_b64_tr_b16 v[72:73], v162 offset:1280\n"
"v_mfma_f32_16x16x32_bf16 a[156:159], v[84:87], a[52:55], a[156:159]\n"
"s_waitcnt lgkmcnt(9)\n"
"ds_read_b64_tr_b16 v[74:75], v162 offset:8448\n"
"v_mfma_f32_16x16x32_bf16 a[160:163], v[88:91], a[48:51], a[160:163]\n"
"ds_read_b64_tr_b16 v[76:77], v163 offset:1280\n"
"v_mfma_f32_16x16x32_bf16 a[164:167], v[88:91], a[52:55], a[164:167]\n"
"ds_read_b64_tr_b16 v[78:79], v163 offset:8448\n"
"v_mfma_f32_16x16x32_bf16 a[168:171], v[92:95], a[48:51], a[168:171]\n"
"ds_read_b64_tr_b16 v[80:81], v162 offset:1536\n"
"v_mfma_f32_16x16x32_bf16 a[172:175], v[92:95], a[52:55], a[172:175]\n"
"s_waitcnt lgkmcnt(9)\n"
"ds_read_b64_tr_b16 v[82:83], v162 offset:8704\n"
"v_mfma_f32_16x16x32_bf16 a[176:179], v[56:59], a[48:51], a[176:179]\n"
"ds_read_b64_tr_b16 v[84:85], v163 offset:1536\n"
"v_mfma_f32_16x16x32_bf16 a[180:183], v[56:59], a[52:55], a[180:183]\n"
"ds_read_b64_tr_b16 v[86:87], v163 offset:8704\n"
"v_mfma_f32_16x16x32_bf16 a[184:187], v[60:63], a[48:51], a[184:187]\n"
"ds_read_b64_tr_b16 v[88:89], v162 offset:14336\n"
"v_mfma_f32_16x16x32_bf16 a[188:191], v[60:63], a[52:55], a[188:191]\n"
"s_waitcnt lgkmcnt(9)\n"
"ds_read_b64_tr_b16 v[90:91], v162 offset:21504\n"
"v_mfma_f32_16x16x32_bf16 a[192:195], v[64:67], a[48:51], a[192:195]\n"
"ds_read_b64_tr_b16 v[92:93], v163 offset:14336\n"
"v_mfma_f32_16x16x32_bf16 a[196:199], v[64:67], a[52:55], a[196:199]\n"
"ds_read_b64_tr_b16 v[94:95], v163 offset:21504\n"
"v_mfma_f32_16x16x32_bf16 a[200:203], v[68:71], a[48:51], a[200:203]\n"
"ds_read_b64_tr_b16 v[56:57], v162 offset:14592\n"
"v_mfma_f32_16x16x32_bf16 a[204:207], v[68:71], a[52:55], a[204:207]\n"
"s_waitcnt lgkmcnt(9)\n"
"ds_read_b64_tr_b16 v[58:59], v162 offset:21760\n"
"v_mfma_f32_16x16x32_bf16 a[208:211], v[72:75], a[48:51], a[208:211]\n"
"ds_read_b64_tr_b16 v[60:61], v163 offset:14592\n"
"v_mfma_f32_16x16x32_bf16 a[212:215], v[72:75], a[52:55], a[212:215]\n"
"ds_read_b64_tr_b16 v[62:63], v163 offset:21760\n"
"v_mfma_f32_16x16x32_bf16 a[216:219], v[76:79], a[48:51], a[216:219]\n"
"ds_read_b64_tr_b16 v[64:65], v162 offset:14848\n"
"v_mfma_f32_16x16x32_bf16 a[220:223], v[76:79], a[52:55], a[220:223]\n"
"s_waitcnt lgkmcnt(9)\n"
"ds_read_b64_tr_b16 v[66:67], v162 offset:22016\n"
"v_mfma_f32_16x16x32_bf16 a[224:227], v[80:83], a[48:51], a[224:227]\n"
"ds_read_b64_tr_b16 v[68:69], v163 offset:14848\n"
"v_mfma_f32_16x16x32_bf16 a[228:231], v[80:83], a[52:55], a[228:231]\n"
"ds_read_b64_tr_b16 v[70:71], v163 offset:22016\n"
"v_mfma_f32_16x16x32_bf16 a[232:235], v[84:87], a[48:51], a[232:235]\n"
"ds_read_b64_tr_b16 v[72:73], v162 offset:15104\n"
"v_mfma_f32_16x16x32_bf16 a[236:239], v[84:87], a[52:55], a[236:239]\n"
"s_waitcnt vmcnt(7)\n"
"s_waitcnt lgkmcnt(9)\n"
"ds_read_b64_tr_b16 v[74:75], v162 offset:22272\n"
"v_mfma_f32_16x16x32_bf16 a[128:131], v[88:91], a[56:59], a[128:131]\n"
"ds_read_b64_tr_b16 v[76:77], v163 offset:15104\n"
"v_mfma_f32_16x16x32_bf16 a[132:135], v[88:91], a[60:63], a[132:135]\n"
"ds_read_b64_tr_b16 v[78:79], v163 offset:22272\n"
"v_mfma_f32_16x16x32_bf16 a[136:139], v[92:95], a[56:59], a[136:139]\n"
"ds_read_b64_tr_b16 v[80:81], v162 offset:15360\n"
"v_mfma_f32_16x16x32_bf16 a[140:143], v[92:95], a[60:63], a[140:143]\n"
"s_waitcnt lgkmcnt(9)\n"
"ds_read_b64_tr_b16 v[82:83], v162 offset:22528\n"
"v_mfma_f32_16x16x32_bf16 a[144:147], v[56:59], a[56:59], a[144:147]\n"
"ds_read_b64_tr_b16 v[84:85], v163 offset:15360\n"
"v_mfma_f32_16x16x32_bf16 a[148:151], v[56:59], a[60:63], a[148:151]\n"
"ds_read_b64_tr_b16 v[86:87], v163 offset:22528\n"
"v_mfma_f32_16x16x32_bf16 a[152:155], v[60:63], a[56:59], a[152:155]\n"
"ds_read_b64_tr_b16 v[88:89], v162 offset:15616\n"
"v_mfma_f32_16x16x32_bf16 a[156:159], v[60:63], a[60:63], a[156:159]\n"
"s_waitcnt lgkmcnt(9)\n"
"ds_read_b64_tr_b16 v[90:91], v162 offset:22784\n"
"v_mfma_f32_16x16x32_bf16 a[160:163], v[64:67], a[56:59], a[160:163]\n"
"ds_read_b64_tr_b16 v[92:93], v163 offset:15616\n"
"v_mfma_f32_16x16x32_bf16 a[164:167], v[64:67], a[60:63], a[164:167]\n"
"ds_read_b64_tr_b16 v[94:95], v163 offset:22784\n"
"v_mfma_f32_16x16x32_bf16 a[168:171], v[68:71], a[56:59], a[168:171]\n"
"ds_read_b64_tr_b16 v[56:57], v162 offset:15872\n"
"v_mfma_f32_16x16x32_bf16 a[172:175], v[68:71], a[60:63], a[172:175]\n"
"s_waitcnt lgkmcnt(9)\n"
"ds_read_b64_tr_b16 v[58:59], v162 offset:23040\n"
"v_mfma_f32_16x16x32_bf16 a[176:179], v[72:75], a[56:59], a[176:179]\n"
"ds_read_b64_tr_b16 v[60:61], v163 offset:15872\n"
"v_mfma_f32_16x16x32_bf16 a[180:183], v[72:75], a[60:63], a[180:183]\n"
"ds_read_b64_tr_b16 v[62:63], v163 offset:23040\n"
"v_mfma_f32_16x16x32_bf16 a[184:187], v[76:79], a[56:59], a[184:187]\n"
"v_mfma_f32_16x16x32_bf16 a[188:191], v[76:79], a[60:63], a[188:191]\n"
"s_waitcnt lgkmcnt(8)\n"
"v_mfma_f32_16x16x32_bf16 a[192:195], v[80:83], a[56:59], a[192:195]\n"
"v_mfma_f32_16x16x32_bf16 a[196:199], v[80:83], a[60:63], a[196:199]\n"
"v_mfma_f32_16x16x32_bf16 a[200:203], v[84:87], a[56:59], a[200:203]\n"
"v_mfma_f32_16x16x32_bf16 a[204:207], v[84:87], a[60:63], a[204:207]\n"
"s_waitcnt lgkmcnt(4)\n"
"v_mfma_f32_16x16x32_bf16 a[208:211], v[88:91], a[56:59], a[208:211]\n"
"v_mfma_f32_16x16x32_bf16 a[212:215], v[88:91], a[60:63], a[212:215]\n"
"v_mfma_f32_16x16x32_bf16 a[216:219], v[92:95], a[56:59], a[216:219]\n"
"v_mfma_f32_16x16x32_bf16 a[220:223], v[92:95], a[60:63], a[220:223]\n"
"s_waitcnt lgkmcnt(0)\n"
"v_mfma_f32_16x16x32_bf16 a[224:227], v[56:59], a[56:59], a[224:227]\n"
"v_mfma_f32_16x16x32_bf16 a[228:231], v[56:59], a[60:63], a[228:231]\n"
"v_mfma_f32_16x16x32_bf16 a[232:235], v[60:63], a[56:59], a[232:235]\n"
"v_mfma_f32_16x16x32_bf16 a[236:239], v[60:63], a[60:63], a[236:239]\n"
"s_branch L_end_%=\n"
"L_finM_%=:\n"
"s_waitcnt vmcnt(14)\n"
"v_max_f32 v12, v16, v232\n"
"v_sub_f32 v24, v16, v12\n"
"v_sub_f32 v26, v232, v12\n"
"v_exp_f32 v24, v24\n"
"v_exp_f32 v26, v26\n"
"s_nop 0\n"
"v_mul_f32 v18, v18, v24\n"
"v_fmac_f32 v18, v234, v26\n"
"v_max_f32 v13, v17, v233\n"
"v_sub_f32 v25, v17, v13\n"
"v_sub_f32 v27, v233, v13\n"
"v_exp_f32 v25, v25\n"
"v_exp_f32 v27, v27\n"
"s_nop 0\n"
"v_mul_f32 v19, v19, v25\n"
"v_fmac_f32 v19, v235, v27\n"
"v_mov_b32 v10, v18\n"
"s_nop 1\n"
"v_permlane16_swap_b32 v18, v10\n"
"s_nop 1\n"
"v_add_f32 v18, v18, v10\n"
"v_mov_b32 v10, v18\n"
"s_nop 1\n"
"v_permlane32_swap_b32 v18, v10\n"
"s_nop 1\n"
"v_add_f32 v18, v18, v10\n"
"v_mov_b32 v10, v19\n"
"s_nop 1\n"
"v_permlane16_swap_b32 v19, v10\n"
"s_nop 1\n"
"v_add_f32 v19, v19, v10\n"
"v_mov_b32 v10, v19\n"
"s_nop 1\n"
"v_permlane32_swap_b32 v19, v10\n"
"s_nop 1\n"
"v_add_f32 v19, v19, v10\n"
"v_rcp_f32 v12, v18\n"
"s_nop 0\n"
"v_mul_f32 v12, v12, v28\n"
"v_mul_f32 v24, v24, v12\n"
"v_mul_f32 v26, v26, v12\n"
"v_rcp_f32 v13, v19\n"
"s_nop 0\n"
"v_mul_f32 v13, v13, v29\n"
"v_mul_f32 v25, v25, v13\n"
"v_mul_f32 v27, v27, v13\n"
"s_lshl_b32 s96, s53, 3\n"
"s_lshl_b32 s97, s49, 1\n"
"s_add_u32 s96, s96, s97\n"
"s_mul_i32 s96, s96, 142\n"
"s_lshl_b32 s97, s48, 3\n"
"s_add_u32 s96, s96, s97\n"
"s_lshl_b32 s78, s96, 10\n"
"s_add_u32 s78, s78, 58720256\n"
"s_add_u32 s79, s78, 145408\n"
"v_accvgpr_read_b32 v32, a0\n"
"v_accvgpr_read_b32 v33, a1\n"
"v_accvgpr_read_b32 v34, a2\n"
"v_accvgpr_read_b32 v35, a3\n"
"v_accvgpr_read_b32 v36, a8\n"
"v_accvgpr_read_b32 v37, a9\n"
"v_accvgpr_read_b32 v38, a10\n"
"v_accvgpr_read_b32 v39, a11\n"
"v_mul_f32 v32, v32, v24\n"
"v_mul_f32 v33, v33, v24\n"
"v_mul_f32 v34, v34, v24\n"
"v_mul_f32 v35, v35, v24\n"
"v_mul_f32 v36, v36, v24\n"
"v_mul_f32 v37, v37, v24\n"
"v_mul_f32 v38, v38, v24\n"
"v_mul_f32 v39, v39, v24\n"
"v_lshlrev_b32 v10, 16, v168\n"
"v_and_b32 v11, 0xffff0000, v168\n"
"v_fmac_f32 v32, v10, v26\n"
"v_fmac_f32 v33, v11, v26\n"
"v_lshlrev_b32 v10, 16, v169\n"
"v_and_b32 v11, 0xffff0000, v169\n"
"v_fmac_f32 v34, v10, v26\n"
"v_fmac_f32 v35, v11, v26\n"
"v_lshlrev_b32 v10, 16, v170\n"
"v_and_b32 v11, 0xffff0000, v170\n"
"v_fmac_f32 v36, v10, v26\n"
"v_fmac_f32 v37, v11, v26\n"
"v_lshlrev_b32 v10, 16, v171\n"
"v_and_b32 v11, 0xffff0000, v171\n"
"v_fmac_f32 v38, v10, v26\n"
"v_fmac_f32 v39, v11, v26\n"
"v_cvt_pk_bf16_f32 v168, v32, v33\n"
"v_cvt_pk_bf16_f32 v169, v34, v35\n"
"v_cvt_pk_bf16_f32 v170, v36, v37\n"
"v_cvt_pk_bf16_f32 v171, v38, v39\n"
"buffer_store_dwordx4 v[168:171], v1, s[36:39], s78 offen offset:0 sc1\n"
"v_accvgpr_read_b32 v40, a4\n"
"v_accvgpr_read_b32 v41, a5\n"
"v_accvgpr_read_b32 v42, a6\n"
"v_accvgpr_read_b32 v43, a7\n"
"v_accvgpr_read_b32 v44, a12\n"
"v_accvgpr_read_b32 v45, a13\n"
"v_accvgpr_read_b32 v46, a14\n"
"v_accvgpr_read_b32 v47, a15\n"
"v_mul_f32 v40, v40, v25\n"
"v_mul_f32 v41, v41, v25\n"
"v_mul_f32 v42, v42, v25\n"
"v_mul_f32 v43, v43, v25\n"
"v_mul_f32 v44, v44, v25\n"
"v_mul_f32 v45, v45, v25\n"
"v_mul_f32 v46, v46, v25\n"
"v_mul_f32 v47, v47, v25\n"
"v_lshlrev_b32 v10, 16, v200\n"
"v_and_b32 v11, 0xffff0000, v200\n"
"v_fmac_f32 v40, v10, v27\n"
"v_fmac_f32 v41, v11, v27\n"
"v_lshlrev_b32 v10, 16, v201\n"
"v_and_b32 v11, 0xffff0000, v201\n"
"v_fmac_f32 v42, v10, v27\n"
"v_fmac_f32 v43, v11, v27\n"
"v_lshlrev_b32 v10, 16, v202\n"
"v_and_b32 v11, 0xffff0000, v202\n"
"v_fmac_f32 v44, v10, v27\n"
"v_fmac_f32 v45, v11, v27\n"
"v_lshlrev_b32 v10, 16, v203\n"
"v_and_b32 v11, 0xffff0000, v203\n"
"v_fmac_f32 v46, v10, v27\n"
"v_fmac_f32 v47, v11, v27\n"
"v_cvt_pk_bf16_f32 v200, v40, v41\n"
"v_cvt_pk_bf16_f32 v201, v42, v43\n"
"v_cvt_pk_bf16_f32 v202, v44, v45\n"
"v_cvt_pk_bf16_f32 v203, v46, v47\n"
"buffer_store_dwordx4 v[200:203], v1, s[36:39], s79 offen offset:0 sc1\n"
"s_lshl_b32 s97, s51, 4\n"
"s_lshl_b32 s98, s54, 1\n"
"s_add_u32 s97, s97, s98\n"
"s_add_u32 s61, s97, s52\n"
"s_mov_b64 s[62:63], %0\n"
"s_lshl_b32 s96, s61, 4\n"
"s_add_u32 s96, s96, 126885888\n"
"s_add_u32 s62, s62, s96\n"
"s_addc_u32 s63, s63, 0\n"
"s_lshl_b32 s96, s50, 2\n"
"s_add_u32 s62, s62, s96\n"
"s_addc_u32 s63, s63, 0\n"
"s_mov_b64 s[72:73], %5\n"
"s_and_b32 s73, s73, 0xffff\n"
"s_mov_b32 s74, 1835008\n"
"s_mov_b32 s75, 0x20000\n"
"s_waitcnt vmcnt(9)\n"
"s_barrier\n"
"v_add_u32 v162, s59, v160\n"
"v_add_u32 v163, s59, v161\n"
"s_add_u32 s59, s59, 28672\n"
"s_sub_u32 s96, s59, 143360\n"
"s_cmp_ge_u32 s59, 143360\n"
"s_cselect_b32 s59, s96, s59\n"
"ds_read_b64_tr_b16 v[56:57], v162 offset:0\n"
"ds_read_b64_tr_b16 v[58:59], v162 offset:7168\n"
"ds_read_b64_tr_b16 v[60:61], v163 offset:0\n"
"ds_read_b64_tr_b16 v[62:63], v163 offset:7168\n"
"ds_read_b64_tr_b16 v[64:65], v162 offset:256\n"
"ds_read_b64_tr_b16 v[66:67], v162 offset:7424\n"
"ds_read_b64_tr_b16 v[68:69], v163 offset:256\n"
"ds_read_b64_tr_b16 v[70:71], v163 offset:7424\n"
"ds_read_b64_tr_b16 v[72:73], v162 offset:512\n"
"ds_read_b64_tr_b16 v[74:75], v162 offset:7680\n"
"ds_read_b64_tr_b16 v[76:77], v163 offset:512\n"
"ds_read_b64_tr_b16 v[78:79], v163 offset:7680\n"
"ds_read_b64_tr_b16 v[80:81], v162 offset:768\n"
"ds_read_b64_tr_b16 v[82:83], v162 offset:7936\n"
"s_waitcnt lgkmcnt(10)\n"
"v_mfma_f32_16x16x32_bf16 a[128:131], v[56:59], v[168:171], 0\n"
"ds_read_b64_tr_b16 v[84:85], v163 offset:768\n"
"v_accvgpr_read_b32 v32, a16\n"
"v_accvgpr_read_b32 v33, a17\n"
"v_accvgpr_read_b32 v34, a18\n"
"v_accvgpr_read_b32 v35, a19\n"
"v_accvgpr_read_b32 v36, a24\n"
"v_accvgpr_read_b32 v37, a25\n"
"v_accvgpr_read_b32 v38, a26\n"
"v_accvgpr_read_b32 v39, a27\n"
"v_mul_f32 v32, v32, v24\n"
"v_mfma_f32_16x16x32_bf16 a[132:135], v[56:59], v[200:203], 0\n"
"ds_read_b64_tr_b16 v[86:87], v163 offset:7936\n"
"buffer_load_dwordx4 v[232:235], v1, s[36:39], s66 offen offset:0 sc1\n"
"v_mul_f32 v33, v33, v24\n"
"v_mul_f32 v34, v34, v24\n"
"v_mul_f32 v35, v35, v24\n"
"v_mul_f32 v36, v36, v24\n"
"v_mul_f32 v37, v37, v24\n"
"v_mul_f32 v38, v38, v24\n"
"v_mul_f32 v39, v39, v24\n"
"v_lshlrev_b32 v10, 16, v172\n"
"v_mfma_f32_16x16x32_bf16 a[136:139], v[60:63], v[168:171], 0\n"
"ds_read_b64_tr_b16 v[88:89], v162 offset:1024\n"
"v_and_b32 v11, 0xffff0000, v172\n"
"v_fmac_f32 v32, v10, v26\n"
"v_fmac_f32 v33, v11, v26\n"
"v_lshlrev_b32 v10, 16, v173\n"
"v_and_b32 v11, 0xffff0000, v173\n"
"v_fmac_f32 v34, v10, v26\n"
"v_fmac_f32 v35, v11, v26\n"
"v_lshlrev_b32 v10, 16, v174\n"
"v_mfma_f32_16x16x32_bf16 a[140:143], v[60:63], v[200:203], 0\n"
"s_waitcnt lgkmcnt(9)\n"
"ds_read_b64_tr_b16 v[90:91], v162 offset:8192\n"
"buffer_load_dwordx4 v[236:239], v1, s[36:39], s67 offen offset:0 sc1\n"
"v_and_b32 v11, 0xffff0000, v174\n"
"v_fmac_f32 v36, v10, v26\n"
"v_fmac_f32 v37, v11, v26\n"
"v_lshlrev_b32 v10, 16, v175\n"
"v_and_b32 v11, 0xffff0000, v175\n"
"v_fmac_f32 v38, v10, v26\n"
"v_fmac_f32 v39, v11, v26\n"
"v_cvt_pk_bf16_f32 v172, v32, v33\n"
"v_mfma_f32_16x16x32_bf16 a[144:147], v[64:67], v[168:171], 0\n"
"ds_read_b64_tr_b16 v[92:93], v163 offset:1024\n"
"v_cvt_pk_bf16_f32 v173, v34, v35\n"
"v_cvt_pk_bf16_f32 v174, v36, v37\n"
"v_cvt_pk_bf16_f32 v175, v38, v39\n"
"buffer_store_dwordx4 v[172:175], v1, s[36:39], s78 offen offset:1024 sc1\n"
"v_accvgpr_read_b32 v40, a20\n"
"v_accvgpr_read_b32 v41, a21\n"
"v_accvgpr_read_b32 v42, a22\n"
"v_accvgpr_read_b32 v43, a23\n"
"v_accvgpr_read_b32 v44, a28\n"
"v_mfma_f32_16x16x32_bf16 a[148:151], v[64:67], v[200:203], 0\n"
"ds_read_b64_tr_b16 v[94:95], v163 offset:8192\n"
"buffer_load_dwordx4 v[240:243], v1, s[36:39], s66 offen offset:1024 sc1\n"
"v_accvgpr_read_b32 v45, a29\n"
"v_accvgpr_read_b32 v46, a30\n"
"v_accvgpr_read_b32 v47, a31\n"
"v_mul_f32 v40, v40, v25\n"
"v_mul_f32 v41, v41, v25\n"
"v_mul_f32 v42, v42, v25\n"
"v_mul_f32 v43, v43, v25\n"
"v_mul_f32 v44, v44, v25\n"
"v_mfma_f32_16x16x32_bf16 a[152:155], v[68:71], v[168:171], 0\n"
"ds_read_b64_tr_b16 v[56:57], v162 offset:1280\n"
"v_mul_f32 v45, v45, v25\n"
"v_mul_f32 v46, v46, v25\n"
"v_mul_f32 v47, v47, v25\n"
"v_lshlrev_b32 v10, 16, v204\n"
"v_and_b32 v11, 0xffff0000, v204\n"
"v_fmac_f32 v40, v10, v27\n"
"v_fmac_f32 v41, v11, v27\n"
"v_lshlrev_b32 v10, 16, v205\n"
"v_mfma_f32_16x16x32_bf16 a[156:159], v[68:71], v[200:203], 0\n"
"s_waitcnt lgkmcnt(9)\n"
"ds_read_b64_tr_b16 v[58:59], v162 offset:8448\n"
"buffer_load_dwordx4 v[244:247], v1, s[36:39], s67 offen offset:1024 sc1\n"
"v_and_b32 v11, 0xffff0000, v205\n"
"v_fmac_f32 v42, v10, v27\n"
"v_fmac_f32 v43, v11, v27\n"
"v_lshlrev_b32 v10, 16, v206\n"
"v_and_b32 v11, 0xffff0000, v206\n"
"v_fmac_f32 v44, v10, v27\n"
"v_fmac_f32 v45, v11, v27\n"
"v_lshlrev_b32 v10, 16, v207\n"
"v_mfma_f32_16x16x32_bf16 a[160:163], v[72:75], v[168:171], 0\n"
"ds_read_b64_tr_b16 v[60:61], v163 offset:1280\n"
"v_and_b32 v11, 0xffff0000, v207\n"
"v_fmac_f32 v46, v10, v27\n"
"v_fmac_f32 v47, v11, v27\n"
"v_cvt_pk_bf16_f32 v204, v40, v41\n"
"v_cvt_pk_bf16_f32 v205, v42, v43\n"
"v_cvt_pk_bf16_f32 v206, v44, v45\n"
"v_cvt_pk_bf16_f32 v207, v46, v47\n"
"buffer_store_dwordx4 v[204:207], v1, s[36:39], s79 offen offset:1024 sc1\n"
"v_mfma_f32_16x16x32_bf16 a[164:167], v[72:75], v[200:203], 0\n"
"ds_read_b64_tr_b16 v[62:63], v163 offset:8448\n"
"buffer_load_dword v248, v167, s[36:39], s76 offen sc1\n"
"v_accvgpr_read_b32 v32, a32\n"
"v_accvgpr_read_b32 v33, a33\n"
"v_accvgpr_read_b32 v34, a34\n"
"v_accvgpr_read_b32 v35, a35\n"
"v_accvgpr_read_b32 v36, a40\n"
"v_accvgpr_read_b32 v37, a41\n"
"v_accvgpr_read_b32 v38, a42\n"
"v_accvgpr_read_b32 v39, a43\n"
"v_mul_f32 v32, v32, v24\n"
"v_mfma_f32_16x16x32_bf16 a[168:171], v[76:79], v[168:171], 0\n"
"ds_read_b64_tr_b16 v[64:65], v162 offset:1536\n"
"v_mul_f32 v33, v33, v24\n"
"v_mul_f32 v34, v34, v24\n"
"v_mul_f32 v35, v35, v24\n"
"v_mul_f32 v36, v36, v24\n"
"v_mul_f32 v37, v37, v24\n"
"v_mul_f32 v38, v38, v24\n"
"v_mul_f32 v39, v39, v24\n"
"v_lshlrev_b32 v10, 16, v176\n"
"v_mfma_f32_16x16x32_bf16 a[172:175], v[76:79], v[200:203], 0\n"
"s_waitcnt lgkmcnt(9)\n"
"ds_read_b64_tr_b16 v[66:67], v162 offset:8704\n"
"buffer_load_dword v249, v167, s[36:39], s76 offen offset:64 sc1\n"
"v_and_b32 v11, 0xffff0000, v176\n"
"v_fmac_f32 v32, v10, v26\n"
"v_fmac_f32 v33, v11, v26\n"
"v_lshlrev_b32 v10, 16, v177\n"
"v_and_b32 v11, 0xffff0000, v177\n"
"v_fmac_f32 v34, v10, v26\n"
"v_fmac_f32 v35, v11, v26\n"
"v_lshlrev_b32 v10, 16, v178\n"
"v_mfma_f32_16x16x32_bf16 a[176:179], v[80:83], v[168:171], 0\n"
"ds_read_b64_tr_b16 v[68:69], v163 offset:1536\n"
"v_and_b32 v11, 0xffff0000, v178\n"
"v_fmac_f32 v36, v10, v26\n"
"v_fmac_f32 v37, v11, v26\n"
"v_lshlrev_b32 v10, 16, v179\n"
"v_and_b32 v11, 0xffff0000, v179\n"
"v_fmac_f32 v38, v10, v26\n"
"v_fmac_f32 v39, v11, v26\n"
"v_cvt_pk_bf16_f32 v176, v32, v33\n"
"v_mfma_f32_16x16x32_bf16 a[180:183], v[80:83], v[200:203], 0\n"
"ds_read_b64_tr_b16 v[70:71], v163 offset:8704\n"
"s_mov_b32 m0, s56\n"
"v_cvt_pk_bf16_f32 v177, v34, v35\n"
"v_cvt_pk_bf16_f32 v178, v36, v37\n"
"v_cvt_pk_bf16_f32 v179, v38, v39\n"
"buffer_store_dwordx4 v[176:179], v1, s[36:39], s78 offen offset:2048 sc1\n"
"v_accvgpr_read_b32 v40, a36\n"
"v_accvgpr_read_b32 v41, a37\n"
"v_accvgpr_read_b32 v42, a38\n"
"v_accvgpr_read_b32 v43, a39\n"
"v_accvgpr_read_b32 v44, a44\n"
"v_mfma_f32_16x16x32_bf16 a[184:187], v[84:87], v[168:171], 0\n"
"ds_read_b64_tr_b16 v[72:73], v162 offset:14336\n"
"buffer_load_dwordx4 v152, s[40:43], s55 offen lds\n"
"v_accvgpr_read_b32 v45, a45\n"
"v_accvgpr_read_b32 v46, a46\n"
"v_accvgpr_read_b32 v47, a47\n"
"v_mul_f32 v40, v40, v25\n"
"v_mul_f32 v41, v41, v25\n"
"v_mul_f32 v42, v42, v25\n"
"v_mul_f32 v43, v43, v25\n"
"v_mul_f32 v44, v44, v25\n"
"v_mfma_f32_16x16x32_bf16 a[188:191], v[84:87], v[200:203], 0\n"
"s_waitcnt lgkmcnt(9)\n"
"ds_read_b64_tr_b16 v[74:75], v162 offset:21504\n"
"s_add_u32 m0, m0, 0x400\n"
"v_mul_f32 v45, v45, v25\n"
"v_mul_f32 v46, v46, v25\n"
"v_mul_f32 v47, v47, v25\n"
"v_lshlrev_b32 v10, 16, v208\n"
"v_and_b32 v11, 0xffff0000, v208\n"
"v_fmac_f32 v40, v10, v27\n"
"v_fmac_f32 v41, v11, v27\n"
"v_lshlrev_b32 v10, 16, v209\n"
"v_mfma_f32_16x16x32_bf16 a[192:195], v[88:91], v[168:171], 0\n"
"ds_read_b64_tr_b16 v[76:77], v163 offset:14336\n"
"buffer_load_dwordx4 v153, s[40:43], s55 offen lds\n"
"v_and_b32 v11, 0xffff0000, v209\n"
"v_fmac_f32 v42, v10, v27\n"
"v_fmac_f32 v43, v11, v27\n"
"v_lshlrev_b32 v10, 16, v210\n"
"v_and_b32 v11, 0xffff0000, v210\n"
"v_fmac_f32 v44, v10, v27\n"
"v_fmac_f32 v45, v11, v27\n"
"v_lshlrev_b32 v10, 16, v211\n"
"v_mfma_f32_16x16x32_bf16 a[196:199], v[88:91], v[200:203], 0\n"
"ds_read_b64_tr_b16 v[78:79], v163 offset:21504\n"
"s_add_u32 m0, m0, 0x400\n"
"v_and_b32 v11, 0xffff0000, v211\n"
"v_fmac_f32 v46, v10, v27\n"
"v_fmac_f32 v47, v11, v27\n"
"v_cvt_pk_bf16_f32 v208, v40, v41\n"
"v_cvt_pk_bf16_f32 v209, v42, v43\n"
"v_cvt_pk_bf16_f32 v210, v44, v45\n"
"v_cvt_pk_bf16_f32 v211, v46, v47\n"
"buffer_store_dwordx4 v[208:211], v1, s[36:39], s79 offen offset:2048 sc1\n"
"v_mfma_f32_16x16x32_bf16 a[200:203], v[92:95], v[168:171], 0\n"
"ds_read_b64_tr_b16 v[80:81], v162 offset:14592\n"
"buffer_load_dwordx4 v154, s[40:43], s55 offen lds\n"
"v_accvgpr_read_b32 v32, a48\n"
"v_accvgpr_read_b32 v33, a49\n"
"v_accvgpr_read_b32 v34, a50\n"
"v_accvgpr_read_b32 v35, a51\n"
"v_accvgpr_read_b32 v36, a56\n"
"v_accvgpr_read_b32 v37, a57\n"
"v_accvgpr_read_b32 v38, a58\n"
"v_accvgpr_read_b32 v39, a59\n"
"v_mul_f32 v32, v32, v24\n"
"v_mfma_f32_16x16x32_bf16 a[204:207], v[92:95], v[200:203], 0\n"
"s_waitcnt lgkmcnt(9)\n"
"ds_read_b64_tr_b16 v[82:83], v162 offset:21760\n"
"s_add_u32 m0, m0, 0x400\n"
"v_mul_f32 v33, v33, v24\n"
"v_mul_f32 v34, v34, v24\n"
"v_mul_f32 v35, v35, v24\n"
"v_mul_f32 v36, v36, v24\n"
"v_mul_f32 v37, v37, v24\n"
"v_mul_f32 v38, v38, v24\n"
"v_mul_f32 v39, v39, v24\n"
"v_lshlrev_b32 v10, 16, v180\n"
"v_mfma_f32_16x16x32_bf16 a[208:211], v[56:59], v[168:171], 0\n"
"ds_read_b64_tr_b16 v[84:85], v163 offset:14592\n"
"buffer_load_dwordx4 v155, s[40:43], s55 offen lds\n"
"v_and_b32 v11, 0xffff0000, v180\n"
"v_fmac_f32 v32, v10, v26\n"
"v_fmac_f32 v33, v11, v26\n"
"v_lshlrev_b32 v10, 16, v181\n"
"v_and_b32 v11, 0xffff0000, v181\n"
"v_fmac_f32 v34, v10, v26\n"
"v_fmac_f32 v35, v11, v26\n"
"v_lshlrev_b32 v10, 16, v182\n"
"v_mfma_f32_16x16x32_bf16 a[212:215], v[56:59], v[200:203], 0\n"
"ds_read_b64_tr_b16 v[86:87], v163 offset:21760\n"
"s_add_u32 m0, m0, 0x400\n"
"v_and_b32 v11, 0xffff0000, v182\n"
"v_fmac_f32 v36, v10, v26\n"
"v_fmac_f32 v37, v11, v26\n"
"v_lshlrev_b32 v10, 16, v183\n"
"v_and_b32 v11, 0xffff0000, v183\n"
"v_fmac_f32 v38, v10, v26\n"
"v_fmac_f32 v39, v11, v26\n"
"v_cvt_pk_bf16_f32 v180, v32, v33\n"
"v_mfma_f32_16x16x32_bf16 a[216:219], v[60:63], v[168:171], 0\n"
"ds_read_b64_tr_b16 v[88:89], v162 offset:14848\n"
"buffer_load_dwordx4 v156, s[40:43], s55 offen lds\n"
"v_cvt_pk_bf16_f32 v181, v34, v35\n"
"v_cvt_pk_bf16_f32 v182, v36, v37\n"
"v_cvt_pk_bf16_f32 v183, v38, v39\n"
"buffer_store_dwordx4 v[180:183], v1, s[36:39], s78 offen offset:3072 sc1\n"
"v_accvgpr_read_b32 v40, a52\n"
"v_accvgpr_read_b32 v41, a53\n"
"v_accvgpr_read_b32 v42, a54\n"
"v_accvgpr_read_b32 v43, a55\n"
"v_accvgpr_read_b32 v44, a60\n"
"v_mfma_f32_16x16x32_bf16 a[220:223], v[60:63], v[200:203], 0\n"
"s_waitcnt lgkmcnt(9)\n"
"ds_read_b64_tr_b16 v[90:91], v162 offset:22016\n"
"s_add_u32 m0, m0, 0x400\n"
"v_accvgpr_read_b32 v45, a61\n"
"v_accvgpr_read_b32 v46, a62\n"
"v_accvgpr_read_b32 v47, a63\n"
"v_mul_f32 v40, v40, v25\n"
"v_mul_f32 v41, v41, v25\n"
"v_mul_f32 v42, v42, v25\n"
"v_mul_f32 v43, v43, v25\n"
"v_mul_f32 v44, v44, v25\n"
"v_mfma_f32_16x16x32_bf16 a[224:227], v[64:67], v[168:171], 0\n"
"ds_read_b64_tr_b16 v[92:93], v163 offset:14848\n"
"buffer_load_dwordx4 v157, s[40:43], s55 offen lds\n"
"v_mul_f32 v45, v45, v25\n"
"v_mul_f32 v46, v46, v25\n"
"v_mul_f32 v47, v47, v25\n"
"v_lshlrev_b32 v10, 16, v212\n"
"v_and_b32 v11, 0xffff0000, v212\n"
"v_fmac_f32 v40, v10, v27\n"
"v_fmac_f32 v41, v11, v27\n"
"v_lshlrev_b32 v10, 16, v213\n"
"v_mfma_f32_16x16x32_bf16 a[228:231], v[64:67], v[200:203], 0\n"
"ds_read_b64_tr_b16 v[94:95], v163 offset:22016\n"
"s_add_u32 m0, m0, 0x400\n"
"v_and_b32 v11, 0xffff0000, v213\n"
"v_fmac_f32 v42, v10, v27\n"
"v_fmac_f32 v43, v11, v27\n"
"v_lshlrev_b32 v10, 16, v214\n"
"v_and_b32 v11, 0xffff0000, v214\n"
"v_fmac_f32 v44, v10, v27\n"
"v_fmac_f32 v45, v11, v27\n"
"v_lshlrev_b32 v10, 16, v215\n"
"v_mfma_f32_16x16x32_bf16 a[232:235], v[68:71], v[168:171], 0\n"
"ds_read_b64_tr_b16 v[56:57], v162 offset:15104\n"
"buffer_load_dwordx4 v158, s[40:43], s55 offen lds\n"
"s_add_u32 s55, s55, 57344\n"
"s_add_u32 s56, s56, 28672\n"
"s_sub_u32 s96, s56, 143360\n"
"s_cmp_ge_u32 s56, 143360\n"
"s_cselect_b32 s56, s96, s56\n"
"v_and_b32 v11, 0xffff0000, v215\n"
"v_fmac_f32 v46, v10, v27\n"
"v_fmac_f32 v47, v11, v27\n"
"v_cvt_pk_bf16_f32 v212, v40, v41\n"
"v_cvt_pk_bf16_f32 v213, v42, v43\n"
"v_cvt_pk_bf16_f32 v214, v44, v45\n"
"v_cvt_pk_bf16_f32 v215, v46, v47\n"
"buffer_store_dwordx4 v[212:215], v1, s[36:39], s79 offen offset:3072 sc1\n"
"v_mfma_f32_16x16x32_bf16 a[236:239], v[68:71], v[200:203], 0\n"
"s_waitcnt lgkmcnt(9)\n"
"ds_read_b64_tr_b16 v[58:59], v162 offset:22272\n"
"s_mov_b32 m0, s56\n"
"v_mfma_f32_16x16x32_bf16 a[128:131], v[72:75], v[172:175], a[128:131]\n"
"ds_read_b64_tr_b16 v[60:61], v163 offset:15104\n"
"buffer_load_dwordx4 v152, s[40:43], s55 offen lds\n"
"v_accvgpr_read_b32 v32, a64\n"
"v_accvgpr_read_b32 v33, a65\n"
"v_accvgpr_read_b32 v34, a66\n"
"v_accvgpr_read_b32 v35, a67\n"
"v_accvgpr_read_b32 v36, a72\n"
"v_accvgpr_read_b32 v37, a73\n"
"v_accvgpr_read_b32 v38, a74\n"
"v_accvgpr_read_b32 v39, a75\n"
"v_mul_f32 v32, v32, v24\n"
"v_mfma_f32_16x16x32_bf16 a[132:135], v[72:75], v[204:207], a[132:135]\n"
"ds_read_b64_tr_b16 v[62:63], v163 offset:22272\n"
"s_add_u32 m0, m0, 0x400\n"
"v_mul_f32 v33, v33, v24\n"
"v_mul_f32 v34, v34, v24\n"
"v_mul_f32 v35, v35, v24\n"
"v_mul_f32 v36, v36, v24\n"
"v_mul_f32 v37, v37, v24\n"
"v_mul_f32 v38, v38, v24\n"
"v_mul_f32 v39, v39, v24\n"
"v_lshlrev_b32 v10, 16, v184\n"
"v_mfma_f32_16x16x32_bf16 a[136:139], v[76:79], v[172:175], a[136:139]\n"
"ds_read_b64_tr_b16 v[64:65], v162 offset:15360\n"
"buffer_load_dwordx4 v153, s[40:43], s55 offen lds\n"
"v_and_b32 v11, 0xffff0000, v184\n"
"v_fmac_f32 v32, v10, v26\n"
"v_fmac_f32 v33, v11, v26\n"
"v_lshlrev_b32 v10, 16, v185\n"
"v_and_b32 v11, 0xffff0000, v185\n"
"v_fmac_f32 v34, v10, v26\n"
"v_fmac_f32 v35, v11, v26\n"
"v_lshlrev_b32 v10, 16, v186\n"
"v_mfma_f32_16x16x32_bf16 a[140:143], v[76:79], v[204:207], a[140:143]\n"
"s_waitcnt lgkmcnt(9)\n"
"ds_read_b64_tr_b16 v[66:67], v162 offset:22528\n"
"s_add_u32 m0, m0, 0x400\n"
"v_and_b32 v11, 0xffff0000, v186\n"
"v_fmac_f32 v36, v10, v26\n"
"v_fmac_f32 v37, v11, v26\n"
"v_lshlrev_b32 v10, 16, v187\n"
"v_and_b32 v11, 0xffff0000, v187\n"
"v_fmac_f32 v38, v10, v26\n"
"v_fmac_f32 v39, v11, v26\n"
"v_cvt_pk_bf16_f32 v184, v32, v33\n"
"v_mfma_f32_16x16x32_bf16 a[144:147], v[80:83], v[172:175], a[144:147]\n"
"ds_read_b64_tr_b16 v[68:69], v163 offset:15360\n"
"buffer_load_dwordx4 v154, s[40:43], s55 offen lds\n"
"v_cvt_pk_bf16_f32 v185, v34, v35\n"
"v_cvt_pk_bf16_f32 v186, v36, v37\n"
"v_cvt_pk_bf16_f32 v187, v38, v39\n"
"buffer_store_dwordx4 v[184:187], v2, s[36:39], s78 offen offset:0 sc1\n"
"v_accvgpr_read_b32 v40, a68\n"
"v_accvgpr_read_b32 v41, a69\n"
"v_accvgpr_read_b32 v42, a70\n"
"v_accvgpr_read_b32 v43, a71\n"
"v_accvgpr_read_b32 v44, a76\n"
"v_mfma_f32_16x16x32_bf16 a[148:151], v[80:83], v[204:207], a[148:151]\n"
"ds_read_b64_tr_b16 v[70:71], v163 offset:22528\n"
"s_add_u32 m0, m0, 0x400\n"
"v_accvgpr_read_b32 v45, a77\n"
"v_accvgpr_read_b32 v46, a78\n"
"v_accvgpr_read_b32 v47, a79\n"
"v_mul_f32 v40, v40, v25\n"
"v_mul_f32 v41, v41, v25\n"
"v_mul_f32 v42, v42, v25\n"
"v_mul_f32 v43, v43, v25\n"
"v_mul_f32 v44, v44, v25\n"
"v_mfma_f32_16x16x32_bf16 a[152:155], v[84:87], v[172:175], a[152:155]\n"
"ds_read_b64_tr_b16 v[72:73], v162 offset:15616\n"
"buffer_load_dwordx4 v155, s[40:43], s55 offen lds\n"
"v_mul_f32 v45, v45, v25\n"
"v_mul_f32 v46, v46, v25\n"
"v_mul_f32 v47, v47, v25\n"
"v_lshlrev_b32 v10, 16, v216\n"
"v_and_b32 v11, 0xffff0000, v216\n"
"v_fmac_f32 v40, v10, v27\n"
"v_fmac_f32 v41, v11, v27\n"
"v_lshlrev_b32 v10, 16, v217\n"
"v_mfma_f32_16x16x32_bf16 a[156:159], v[84:87], v[204:207], a[156:159]\n"
"s_waitcnt lgkmcnt(9)\n"
"ds_read_b64_tr_b16 v[74:75], v162 offset:22784\n"
"s_add_u32 m0, m0, 0x400\n"
"v_and_b32 v11, 0xffff0000, v217\n"
"v_fmac_f32 v42, v10, v27\n"
"v_fmac_f32 v43, v11, v27\n"
"v_lshlrev_b32 v10, 16, v218\n"
"v_and_b32 v11, 0xffff0000, v218\n"
"v_fmac_f32 v44, v10, v27\n"
"v_fmac_f32 v45, v11, v27\n"
"v_lshlrev_b32 v10, 16, v219\n"
"v_mfma_f32_16x16x32_bf16 a[160:163], v[88:91], v[172:175], a[160:163]\n"
"ds_read_b64_tr_b16 v[76:77], v163 offset:15616\n"
"buffer_load_dwordx4 v156, s[40:43], s55 offen lds\n"
"v_and_b32 v11, 0xffff0000, v219\n"
"v_fmac_f32 v46, v10, v27\n"
"v_fmac_f32 v47, v11, v27\n"
"v_cvt_pk_bf16_f32 v216, v40, v41\n"
"v_cvt_pk_bf16_f32 v217, v42, v43\n"
"v_cvt_pk_bf16_f32 v218, v44, v45\n"
"v_cvt_pk_bf16_f32 v219, v46, v47\n"
"buffer_store_dwordx4 v[216:219], v2, s[36:39], s79 offen offset:0 sc1\n"
"v_mfma_f32_16x16x32_bf16 a[164:167], v[88:91], v[204:207], a[164:167]\n"
"ds_read_b64_tr_b16 v[78:79], v163 offset:22784\n"
"s_add_u32 m0, m0, 0x400\n"
"v_accvgpr_read_b32 v32, a80\n"
"v_accvgpr_read_b32 v33, a81\n"
"v_accvgpr_read_b32 v34, a82\n"
"v_accvgpr_read_b32 v35, a83\n"
"v_accvgpr_read_b32 v36, a88\n"
"v_accvgpr_read_b32 v37, a89\n"
"v_accvgpr_read_b32 v38, a90\n"
"v_accvgpr_read_b32 v39, a91\n"
"v_mul_f32 v32, v32, v24\n"
"v_mfma_f32_16x16x32_bf16 a[168:171], v[92:95], v[172:175], a[168:171]\n"
"ds_read_b64_tr_b16 v[80:81], v162 offset:15872\n"
"buffer_load_dwordx4 v157, s[40:43], s55 offen lds\n"
"v_mul_f32 v33, v33, v24\n"
"v_mul_f32 v34, v34, v24\n"
"v_mul_f32 v35, v35, v24\n"
"v_mul_f32 v36, v36, v24\n"
"v_mul_f32 v37, v37, v24\n"
"v_mul_f32 v38, v38, v24\n"
"v_mul_f32 v39, v39, v24\n"
"v_lshlrev_b32 v10, 16, v188\n"
"v_mfma_f32_16x16x32_bf16 a[172:175], v[92:95], v[204:207], a[172:175]\n"
"s_waitcnt lgkmcnt(9)\n"
"ds_read_b64_tr_b16 v[82:83], v162 offset:23040\n"
"s_add_u32 m0, m0, 0x400\n"
"v_and_b32 v11, 0xffff0000, v188\n"
"v_fmac_f32 v32, v10, v26\n"
"v_fmac_f32 v33, v11, v26\n"
"v_lshlrev_b32 v10, 16, v189\n"
"v_and_b32 v11, 0xffff0000, v189\n"
"v_fmac_f32 v34, v10, v26\n"
"v_fmac_f32 v35, v11, v26\n"
"v_lshlrev_b32 v10, 16, v190\n"
"v_mfma_f32_16x16x32_bf16 a[176:179], v[56:59], v[172:175], a[176:179]\n"
"ds_read_b64_tr_b16 v[84:85], v163 offset:15872\n"
"buffer_load_dwordx4 v158, s[40:43], s55 offen lds\n"
"s_add_u32 s55, s55, 57344\n"
"s_add_u32 s56, s56, 28672\n"
"s_sub_u32 s96, s56, 143360\n"
"s_cmp_ge_u32 s56, 143360\n"
"s_cselect_b32 s56, s96, s56\n"
"s_mul_i32 s55, s54, 57344\n"
"s_mul_i32 s96, s52, 448\n"
"s_add_u32 s55, s55, s96\n"
"s_cmp_eq_u32 s54, 7\n"
"s_cselect_b32 s55, 0xf0000000, s55\n"
"v_and_b32 v11, 0xffff0000, v190\n"
"v_fmac_f32 v36, v10, v26\n"
"v_fmac_f32 v37, v11, v26\n"
"v_lshlrev_b32 v10, 16, v191\n"
"v_and_b32 v11, 0xffff0000, v191\n"
"v_fmac_f32 v38, v10, v26\n"
"v_fmac_f32 v39, v11, v26\n"
"v_cvt_pk_bf16_f32 v188, v32, v33\n"
"v_mfma_f32_16x16x32_bf16 a[180:183], v[56:59], v[204:207], a[180:183]\n"
"ds_read_b64_tr_b16 v[86:87], v163 offset:23040\n"
"s_waitcnt vmcnt(30)\n"
"s_barrier\n"
"v_add_u32 v162, s59, v160\n"
"v_add_u32 v163, s59, v161\n"
"s_add_u32 s59, s59, 28672\n"
"s_sub_u32 s96, s59, 143360\n"
"s_cmp_ge_u32 s59, 143360\n"
"s_cselect_b32 s59, s96, s59\n"
"v_cvt_pk_bf16_f32 v189, v34, v35\n"
"v_cvt_pk_bf16_f32 v190, v36, v37\n"
"v_cvt_pk_bf16_f32 v191, v38, v39\n"
"buffer_store_dwordx4 v[188:191], v2, s[36:39], s78 offen offset:1024 sc1\n"
"v_accvgpr_read_b32 v40, a84\n"
"v_accvgpr_read_b32 v41, a85\n"
"v_accvgpr_read_b32 v42, a86\n"
"v_accvgpr_read_b32 v43, a87\n"
"v_accvgpr_read_b32 v44, a92\n"
"v_mfma_f32_16x16x32_bf16 a[184:187], v[60:63], v[172:175], a[184:187]\n"
"ds_read_b64_tr_b16 v[88:89], v162 offset:0\n"
"v_accvgpr_read_b32 v45, a93\n"
"v_accvgpr_read_b32 v46, a94\n"
"v_accvgpr_read_b32 v47, a95\n"
"v_mul_f32 v40, v40, v25\n"
"v_mul_f32 v41, v41, v25\n"
"v_mul_f32 v42, v42, v25\n"
"v_mul_f32 v43, v43, v25\n"
"v_mul_f32 v44, v44, v25\n"
"v_mfma_f32_16x16x32_bf16 a[188:191], v[60:63], v[204:207], a[188:191]\n"
"s_waitcnt lgkmcnt(9)\n"
"ds_read_b64_tr_b16 v[90:91], v162 offset:7168\n"
"s_mov_b32 m0, s56\n"
"v_mul_f32 v45, v45, v25\n"
"v_mul_f32 v46, v46, v25\n"
"v_mul_f32 v47, v47, v25\n"
"v_lshlrev_b32 v10, 16, v220\n"
"v_and_b32 v11, 0xffff0000, v220\n"
"v_fmac_f32 v40, v10, v27\n"
"v_fmac_f32 v41, v11, v27\n"
"v_lshlrev_b32 v10, 16, v221\n"
"v_mfma_f32_16x16x32_bf16 a[192:195], v[64:67], v[172:175], a[192:195]\n"
"ds_read_b64_tr_b16 v[92:93], v163 offset:0\n"
"buffer_load_dwordx4 v152, s[44:47], s55 offen lds\n"
"v_and_b32 v11, 0xffff0000, v221\n"
"v_fmac_f32 v42, v10, v27\n"
"v_fmac_f32 v43, v11, v27\n"
"v_lshlrev_b32 v10, 16, v222\n"
"v_and_b32 v11, 0xffff0000, v222\n"
"v_fmac_f32 v44, v10, v27\n"
"v_fmac_f32 v45, v11, v27\n"
"v_lshlrev_b32 v10, 16, v223\n"
"v_mfma_f32_16x16x32_bf16 a[196:199], v[64:67], v[204:207], a[196:199]\n"
"ds_read_b64_tr_b16 v[94:95], v163 offset:7168\n"
"v_and_b32 v11, 0xffff0000, v223\n"
"v_fmac_f32 v46, v10, v27\n"
"v_fmac_f32 v47, v11, v27\n"
"v_cvt_pk_bf16_f32 v220, v40, v41\n"
"v_cvt_pk_bf16_f32 v221, v42, v43\n"
"v_cvt_pk_bf16_f32 v222, v44, v45\n"
"v_cvt_pk_bf16_f32 v223, v46, v47\n"
"buffer_store_dwordx4 v[220:223], v2, s[36:39], s79 offen offset:1024 sc1\n"
"v_mfma_f32_16x16x32_bf16 a[200:203], v[68:71], v[172:175], a[200:203]\n"
"ds_read_b64_tr_b16 v[56:57], v162 offset:256\n"
"v_accvgpr_read_b32 v32, a96\n"
"v_accvgpr_read_b32 v33, a97\n"
"v_accvgpr_read_b32 v34, a98\n"
"v_accvgpr_read_b32 v35, a99\n"
"v_accvgpr_read_b32 v36, a104\n"
"v_accvgpr_read_b32 v37, a105\n"
"v_accvgpr_read_b32 v38, a106\n"
"v_accvgpr_read_b32 v39, a107\n"
"v_mul_f32 v32, v32, v24\n"
"v_mfma_f32_16x16x32_bf16 a[204:207], v[68:71], v[204:207], a[204:207]\n"
"s_waitcnt lgkmcnt(9)\n"
"ds_read_b64_tr_b16 v[58:59], v162 offset:7424\n"
"s_add_u32 m0, m0, 0x400\n"
"v_mul_f32 v33, v33, v24\n"
"v_mul_f32 v34, v34, v24\n"
"v_mul_f32 v35, v35, v24\n"
"v_mul_f32 v36, v36, v24\n"
"v_mul_f32 v37, v37, v24\n"
"v_mul_f32 v38, v38, v24\n"
"v_mul_f32 v39, v39, v24\n"
"v_lshlrev_b32 v10, 16, v192\n"
"v_mfma_f32_16x16x32_bf16 a[208:211], v[72:75], v[172:175], a[208:211]\n"
"ds_read_b64_tr_b16 v[60:61], v163 offset:256\n"
"buffer_load_dwordx4 v153, s[44:47], s55 offen lds\n"
"v_and_b32 v11, 0xffff0000, v192\n"
"v_fmac_f32 v32, v10, v26\n"
"v_fmac_f32 v33, v11, v26\n"
"v_lshlrev_b32 v10, 16, v193\n"
"v_and_b32 v11, 0xffff0000, v193\n"
"v_fmac_f32 v34, v10, v26\n"
"v_fmac_f32 v35, v11, v26\n"
"v_lshlrev_b32 v10, 16, v194\n"
"v_mfma_f32_16x16x32_bf16 a[212:215], v[72:75], v[204:207], a[212:215]\n"
"ds_read_b64_tr_b16 v[62:63], v163 offset:7424\n"
"v_and_b32 v11, 0xffff0000, v194\n"
"v_fmac_f32 v36, v10, v26\n"
"v_fmac_f32 v37, v11, v26\n"
"v_lshlrev_b32 v10, 16, v195\n"
"v_and_b32 v11, 0xffff0000, v195\n"
"v_fmac_f32 v38, v10, v26\n"
"v_fmac_f32 v39, v11, v26\n"
"v_cvt_pk_bf16_f32 v192, v32, v33\n"
"v_mfma_f32_16x16x32_bf16 a[216:219], v[76:79], v[172:175], a[216:219]\n"
"ds_read_b64_tr_b16 v[64:65], v162 offset:512\n"
"v_cvt_pk_bf16_f32 v193, v34, v35\n"
"v_cvt_pk_bf16_f32 v194, v36, v37\n"
"v_cvt_pk_bf16_f32 v195, v38, v39\n"
"buffer_store_dwordx4 v[192:195], v2, s[36:39], s78 offen offset:2048 sc1\n"
"v_accvgpr_read_b32 v40, a100\n"
"v_accvgpr_read_b32 v41, a101\n"
"v_accvgpr_read_b32 v42, a102\n"
"v_accvgpr_read_b32 v43, a103\n"
"v_accvgpr_read_b32 v44, a108\n"
"v_mfma_f32_16x16x32_bf16 a[220:223], v[76:79], v[204:207], a[220:223]\n"
"s_waitcnt lgkmcnt(9)\n"
"ds_read_b64_tr_b16 v[66:67], v162 offset:7680\n"
"s_add_u32 m0, m0, 0x400\n"
"v_accvgpr_read_b32 v45, a109\n"
"v_accvgpr_read_b32 v46, a110\n"
"v_accvgpr_read_b32 v47, a111\n"
"v_mul_f32 v40, v40, v25\n"
"v_mul_f32 v41, v41, v25\n"
"v_mul_f32 v42, v42, v25\n"
"v_mul_f32 v43, v43, v25\n"
"v_mul_f32 v44, v44, v25\n"
"v_mfma_f32_16x16x32_bf16 a[224:227], v[80:83], v[172:175], a[224:227]\n"
"ds_read_b64_tr_b16 v[68:69], v163 offset:512\n"
"buffer_load_dwordx4 v154, s[44:47], s55 offen lds\n"
"v_mul_f32 v45, v45, v25\n"
"v_mul_f32 v46, v46, v25\n"
"v_mul_f32 v47, v47, v25\n"
"v_lshlrev_b32 v10, 16, v224\n"
"v_and_b32 v11, 0xffff0000, v224\n"
"v_fmac_f32 v40, v10, v27\n"
"v_fmac_f32 v41, v11, v27\n"
"v_lshlrev_b32 v10, 16, v225\n"
"v_mfma_f32_16x16x32_bf16 a[228:231], v[80:83], v[204:207], a[228:231]\n"
"ds_read_b64_tr_b16 v[70:71], v163 offset:7680\n"
"v_and_b32 v11, 0xffff0000, v225\n"
"v_fmac_f32 v42, v10, v27\n"
"v_fmac_f32 v43, v11, v27\n"
"v_lshlrev_b32 v10, 16, v226\n"
"v_and_b32 v11, 0xffff0000, v226\n"
"v_fmac_f32 v44, v10, v27\n"
"v_fmac_f32 v45, v11, v27\n"
"v_lshlrev_b32 v10, 16, v227\n"
"v_mfma_f32_16x16x32_bf16 a[232:235], v[84:87], v[172:175], a[232:235]\n"
"ds_read_b64_tr_b16 v[72:73], v162 offset:768\n"
"v_and_b32 v11, 0xffff0000, v227\n"
"v_fmac_f32 v46, v10, v27\n"
"v_fmac_f32 v47, v11, v27\n"
"v_cvt_pk_bf16_f32 v224, v40, v41\n"
"v_cvt_pk_bf16_f32 v225, v42, v43\n"
"v_cvt_pk_bf16_f32 v226, v44, v45\n"
"v_cvt_pk_bf16_f32 v227, v46, v47\n"
"buffer_store_dwordx4 v[224:227], v2, s[36:39], s79 offen offset:2048 sc1\n"
"v_mfma_f32_16x16x32_bf16 a[236:239], v[84:87], v[204:207], a[236:239]\n"
"s_waitcnt lgkmcnt(9)\n"
"ds_read_b64_tr_b16 v[74:75], v162 offset:7936\n"
"s_add_u32 m0, m0, 0x400\n"
"v_mfma_f32_16x16x32_bf16 a[128:131], v[88:91], v[176:179], a[128:131]\n"
"ds_read_b64_tr_b16 v[76:77], v163 offset:768\n"
"buffer_load_dwordx4 v155, s[44:47], s55 offen lds\n"
"v_accvgpr_read_b32 v32, a112\n"
"v_accvgpr_read_b32 v33, a113\n"
"v_accvgpr_read_b32 v34, a114\n"
"v_mfma_f32_16x16x32_bf16 a[132:135], v[88:91], v[208:211], a[132:135]\n"
"ds_read_b64_tr_b16 v[78:79], v163 offset:7936\n"
"v_accvgpr_read_b32 v35, a115\n"
"v_accvgpr_read_b32 v36, a120\n"
"v_accvgpr_read_b32 v37, a121\n"
"v_mfma_f32_16x16x32_bf16 a[136:139], v[92:95], v[176:179], a[136:139]\n"
"ds_read_b64_tr_b16 v[80:81], v162 offset:1024\n"
"v_accvgpr_read_b32 v38, a122\n"
"v_accvgpr_read_b32 v39, a123\n"
"v_mul_f32 v32, v32, v24\n"
"v_mfma_f32_16x16x32_bf16 a[140:143], v[92:95], v[208:211], a[140:143]\n"
"s_waitcnt lgkmcnt(9)\n"
"ds_read_b64_tr_b16 v[82:83], v162 offset:8192\n"
"s_add_u32 m0, m0, 0x400\n"
"v_mul_f32 v33, v33, v24\n"
"v_mul_f32 v34, v34, v24\n"
"v_mfma_f32_16x16x32_bf16 a[144:147], v[56:59], v[176:179], a[144:147]\n"
"ds_read_b64_tr_b16 v[84:85], v163 offset:1024\n"
"buffer_load_dwordx4 v156, s[44:47], s55 offen lds\n"
"v_mul_f32 v35, v35, v24\n"
"v_mul_f32 v36, v36, v24\n"
"v_mul_f32 v37, v37, v24\n"
"v_mfma_f32_16x16x32_bf16 a[148:151], v[56:59], v[208:211], a[148:151]\n"
"ds_read_b64_tr_b16 v[86:87], v163 offset:8192\n"
"v_mul_f32 v38, v38, v24\n"
"v_mul_f32 v39, v39, v24\n"
"v_lshlrev_b32 v10, 16, v196\n"
"v_mfma_f32_16x16x32_bf16 a[152:155], v[60:63], v[176:179], a[152:155]\n"
"ds_read_b64_tr_b16 v[88:89], v162 offset:1280\n"
"v_and_b32 v11, 0xffff0000, v196\n"
"v_fmac_f32 v32, v10, v26\n"
"v_fmac_f32 v33, v11, v26\n"
"v_mfma_f32_16x16x32_bf16 a[156:159], v[60:63], v[208:211], a[156:159]\n"
"s_waitcnt lgkmcnt(9)\n"
"ds_read_b64_tr_b16 v[90:91], v162 offset:8448\n"
"s_add_u32 m0, m0, 0x400\n"
"v_lshlrev_b32 v10, 16, v197\n"
"v_and_b32 v11, 0xffff0000, v197\n"
"v_mfma_f32_16x16x32_bf16 a[160:163], v[64:67], v[176:179], a[160:163]\n"
"ds_read_b64_tr_b16 v[92:93], v163 offset:1280\n"
"buffer_load_dwordx4 v157, s[44:47], s55 offen lds\n"
"v_fmac_f32 v34, v10, v26\n"
"v_fmac_f32 v35, v11, v26\n"
"v_lshlrev_b32 v10, 16, v198\n"
"v_mfma_f32_16x16x32_bf16 a[164:167], v[64:67], v[208:211], a[164:167]\n"
"ds_read_b64_tr_b16 v[94:95], v163 offset:8448\n"
"v_and_b32 v11, 0xffff0000, v198\n"
"v_fmac_f32 v36, v10, v26\n"
"v_fmac_f32 v37, v11, v26\n"
"v_mfma_f32_16x16x32_bf16 a[168:171], v[68:71], v[176:179], a[168:171]\n"
"ds_read_b64_tr_b16 v[56:57], v162 offset:1536\n"
"v_lshlrev_b32 v10, 16, v199\n"
"v_and_b32 v11, 0xffff0000, v199\n"
"v_fmac_f32 v38, v10, v26\n"
"v_mfma_f32_16x16x32_bf16 a[172:175], v[68:71], v[208:211], a[172:175]\n"
"s_waitcnt lgkmcnt(9)\n"
"ds_read_b64_tr_b16 v[58:59], v162 offset:8704\n"
"s_add_u32 m0, m0, 0x400\n"
"v_fmac_f32 v39, v11, v26\n"
"v_cvt_pk_bf16_f32 v196, v32, v33\n"
"v_mfma_f32_16x16x32_bf16 a[176:179], v[72:75], v[176:179], a[176:179]\n"
"ds_read_b64_tr_b16 v[60:61], v163 offset:1536\n"
"buffer_load_dwordx4 v158, s[44:47], s55 offen lds\n"
"v_cvt_pk_bf16_f32 v197, v34, v35\n"
"v_cvt_pk_bf16_f32 v198, v36, v37\n"
"v_cvt_pk_bf16_f32 v199, v38, v39\n"
"v_mfma_f32_16x16x32_bf16 a[180:183], v[72:75], v[208:211], a[180:183]\n"
"ds_read_b64_tr_b16 v[62:63], v163 offset:8704\n"
"buffer_store_dwordx4 v[196:199], v2, s[36:39], s78 offen offset:3072 sc1\n"
"v_accvgpr_read_b32 v40, a116\n"
"v_accvgpr_read_b32 v41, a117\n"
"v_mfma_f32_16x16x32_bf16 a[184:187], v[76:79], v[176:179], a[184:187]\n"
"ds_read_b64_tr_b16 v[64:65], v162 offset:14336\n"
"v_accvgpr_read_b32 v42, a118\n"
"v_accvgpr_read_b32 v43, a119\n"
"v_accvgpr_read_b32 v44, a124\n"
"v_mfma_f32_16x16x32_bf16 a[188:191], v[76:79], v[208:211], a[188:191]\n"
"s_waitcnt lgkmcnt(9)\n"
"ds_read_b64_tr_b16 v[66:67], v162 offset:21504\n"
"s_add_u32 s55, s55, 57344\n"
"s_add_u32 s56, s56, 28672\n"
"s_sub_u32 s96, s56, 143360\n"
"s_cmp_ge_u32 s56, 143360\n"
"s_cselect_b32 s56, s96, s56\n"
"s_lshl_b32 s96, s54, 1\n"
"s_xor_b32 s55, s52, 1\n"
"s_add_u32 s96, s96, s55\n"
"s_mul_i32 s55, s96, 229376\n"
"s_mul_i32 s96, s52, 448\n"
"s_add_u32 s55, s55, s96\n"
"v_accvgpr_read_b32 v45, a125\n"
"v_accvgpr_read_b32 v46, a126\n"
"v_mfma_f32_16x16x32_bf16 a[192:195], v[80:83], v[176:179], a[192:195]\n"
"ds_read_b64_tr_b16 v[68:69], v163 offset:14336\n"
"v_accvgpr_read_b32 v47, a127\n"
"v_mul_f32 v40, v40, v25\n"
"v_mul_f32 v41, v41, v25\n"
"v_mfma_f32_16x16x32_bf16 a[196:199], v[80:83], v[208:211], a[196:199]\n"
"ds_read_b64_tr_b16 v[70:71], v163 offset:21504\n"
"v_mul_f32 v42, v42, v25\n"
"v_mul_f32 v43, v43, v25\n"
"v_mul_f32 v44, v44, v25\n"
"v_mfma_f32_16x16x32_bf16 a[200:203], v[84:87], v[176:179], a[200:203]\n"
"ds_read_b64_tr_b16 v[72:73], v162 offset:14592\n"
"v_mul_f32 v45, v45, v25\n"
"v_mul_f32 v46, v46, v25\n"
"v_mul_f32 v47, v47, v25\n"
"v_mfma_f32_16x16x32_bf16 a[204:207], v[84:87], v[208:211], a[204:207]\n"
"s_waitcnt lgkmcnt(9)\n"
"ds_read_b64_tr_b16 v[74:75], v162 offset:21760\n"
"v_lshlrev_b32 v10, 16, v228\n"
"v_and_b32 v11, 0xffff0000, v228\n"
"v_mfma_f32_16x16x32_bf16 a[208:211], v[88:91], v[176:179], a[208:211]\n"
"ds_read_b64_tr_b16 v[76:77], v163 offset:14592\n"
"v_fmac_f32 v40, v10, v27\n"
"v_fmac_f32 v41, v11, v27\n"
"v_lshlrev_b32 v10, 16, v229\n"
"v_mfma_f32_16x16x32_bf16 a[212:215], v[88:91], v[208:211], a[212:215]\n"
"ds_read_b64_tr_b16 v[78:79], v163 offset:21760\n"
"v_and_b32 v11, 0xffff0000, v229\n"
"v_fmac_f32 v42, v10, v27\n"
"v_fmac_f32 v43, v11, v27\n"
"v_mfma_f32_16x16x32_bf16 a[216:219], v[92:95], v[176:179], a[216:219]\n"
"ds_read_b64_tr_b16 v[80:81], v162 offset:14848\n"
"v_lshlrev_b32 v10, 16, v230\n"
"v_and_b32 v11, 0xffff0000, v230\n"
"v_fmac_f32 v44, v10, v27\n"
"v_mfma_f32_16x16x32_bf16 a[220:223], v[92:95], v[208:211], a[220:223]\n"
"s_waitcnt lgkmcnt(9)\n"
"ds_read_b64_tr_b16 v[82:83], v162 offset:22016\n"
"v_fmac_f32 v45, v11, v27\n"
"v_lshlrev_b32 v10, 16, v231\n"
"v_mfma_f32_16x16x32_bf16 a[224:227], v[56:59], v[176:179], a[224:227]\n"
"ds_read_b64_tr_b16 v[84:85], v163 offset:14848\n"
"v_and_b32 v11, 0xffff0000, v231\n"
"v_fmac_f32 v46, v10, v27\n"
"v_fmac_f32 v47, v11, v27\n"
"v_mfma_f32_16x16x32_bf16 a[228:231], v[56:59], v[208:211], a[228:231]\n"
"ds_read_b64_tr_b16 v[86:87], v163 offset:22016\n"
"v_cvt_pk_bf16_f32 v228, v40, v41\n"
"v_cvt_pk_bf16_f32 v229, v42, v43\n"
"v_cvt_pk_bf16_f32 v230, v44, v45\n"
"v_mfma_f32_16x16x32_bf16 a[232:235], v[60:63], v[176:179], a[232:235]\n"
"ds_read_b64_tr_b16 v[88:89], v162 offset:15104\n"
"v_cvt_pk_bf16_f32 v231, v46, v47\n"
"buffer_store_dwordx4 v[228:231], v2, s[36:39], s79 offen offset:3072 sc1\n"
"v_mfma_f32_16x16x32_bf16 a[236:239], v[60:63], v[208:211], a[236:239]\n"
"s_waitcnt lgkmcnt(9)\n"
"ds_read_b64_tr_b16 v[90:91], v162 offset:22272\n"
"v_mfma_f32_16x16x32_bf16 a[128:131], v[64:67], v[180:183], a[128:131]\n"
"ds_read_b64_tr_b16 v[92:93], v163 offset:15104\n"
"v_mfma_f32_16x16x32_bf16 a[132:135], v[64:67], v[212:215], a[132:135]\n"
"ds_read_b64_tr_b16 v[94:95], v163 offset:22272\n"
"v_mfma_f32_16x16x32_bf16 a[136:139], v[68:71], v[180:183], a[136:139]\n"
"ds_read_b64_tr_b16 v[56:57], v162 offset:15360\n"
"v_mfma_f32_16x16x32_bf16 a[140:143], v[68:71], v[212:215], a[140:143]\n"
"s_waitcnt lgkmcnt(9)\n"
"ds_read_b64_tr_b16 v[58:59], v162 offset:22528\n"
"v_mfma_f32_16x16x32_bf16 a[144:147], v[72:75], v[180:183], a[144:147]\n"
"ds_read_b64_tr_b16 v[60:61], v163 offset:15360\n"
"s_waitcnt vmcnt(33)\n"
"v_sub_f32 v248, 1.0, v248\n"
"v_mfma_f32_16x16x32_bf16 a[148:151], v[72:75], v[212:215], a[148:151]\n"
"ds_read_b64_tr_b16 v[62:63], v163 offset:22528\n"
"v_sub_f32 v249, 1.0, v249\n"
"v_mfma_f32_16x16x32_bf16 a[152:155], v[76:79], v[180:183], a[152:155]\n"
"ds_read_b64_tr_b16 v[64:65], v162 offset:15616\n"
"v_lshlrev_b32 v48, 16, v232\n"
"v_mfma_f32_16x16x32_bf16 a[156:159], v[76:79], v[212:215], a[156:159]\n"
"s_waitcnt lgkmcnt(9)\n"
"ds_read_b64_tr_b16 v[66:67], v162 offset:22784\n"
"v_and_b32 v49, 0xffff0000, v232\n"
"v_mfma_f32_16x16x32_bf16 a[160:163], v[80:83], v[180:183], a[160:163]\n"
"ds_read_b64_tr_b16 v[68:69], v163 offset:15616\n"
"v_mul_f32 v48, v48, v248\n"
"v_mfma_f32_16x16x32_bf16 a[164:167], v[80:83], v[212:215], a[164:167]\n"
"ds_read_b64_tr_b16 v[70:71], v163 offset:22784\n"
"v_mfma_f32_16x16x32_bf16 a[168:171], v[84:87], v[180:183], a[168:171]\n"
"ds_read_b64_tr_b16 v[72:73], v162 offset:15872\n"
"v_mul_f32 v49, v49, v248\n"
"v_mfma_f32_16x16x32_bf16 a[172:175], v[84:87], v[212:215], a[172:175]\n"
"s_waitcnt lgkmcnt(9)\n"
"ds_read_b64_tr_b16 v[74:75], v162 offset:23040\n"
"v_cvt_pk_bf16_f32 v232, v48, v49\n"
"v_mfma_f32_16x16x32_bf16 a[176:179], v[88:91], v[180:183], a[176:179]\n"
"ds_read_b64_tr_b16 v[76:77], v163 offset:15872\n"
"v_lshlrev_b32 v48, 16, v233\n"
"v_mfma_f32_16x16x32_bf16 a[180:183], v[88:91], v[212:215], a[180:183]\n"
"ds_read_b64_tr_b16 v[78:79], v163 offset:23040\n"
"s_waitcnt vmcnt(23)\n"
"s_barrier\n"
"v_add_u32 v162, s59, v160\n"
"v_add_u32 v163, s59, v161\n"
"s_add_u32 s59, s59, 28672\n"
"s_sub_u32 s96, s59, 143360\n"
"s_cmp_ge_u32 s59, 143360\n"
"s_cselect_b32 s59, s96, s59\n"
"v_and_b32 v49, 0xffff0000, v233\n"
"v_mfma_f32_16x16x32_bf16 a[184:187], v[92:95], v[180:183], a[184:187]\n"
"ds_read_b64_tr_b16 v[80:81], v162 offset:0\n"
"v_mul_f32 v48, v48, v248\n"
"v_mfma_f32_16x16x32_bf16 a[188:191], v[92:95], v[212:215], a[188:191]\n"
"s_waitcnt lgkmcnt(9)\n"
"ds_read_b64_tr_b16 v[82:83], v162 offset:7168\n"
"s_mov_b32 m0, s56\n"
"v_mfma_f32_16x16x32_bf16 a[192:195], v[56:59], v[180:183], a[192:195]\n"
"ds_read_b64_tr_b16 v[84:85], v163 offset:0\n"
"buffer_load_dwordx4 v152, s[40:43], s55 offen lds\n"
"v_mul_f32 v49, v49, v248\n"
"v_mfma_f32_16x16x32_bf16 a[196:199], v[56:59], v[212:215], a[196:199]\n"
"ds_read_b64_tr_b16 v[86:87], v163 offset:7168\n"
"v_cvt_pk_bf16_f32 v233, v48, v49\n"
"v_mfma_f32_16x16x32_bf16 a[200:203], v[60:63], v[180:183], a[200:203]\n"
"ds_read_b64_tr_b16 v[88:89], v162 offset:256\n"
"v_lshlrev_b32 v48, 16, v234\n"
"v_mfma_f32_16x16x32_bf16 a[204:207], v[60:63], v[212:215], a[204:207]\n"
"s_waitcnt lgkmcnt(9)\n"
"ds_read_b64_tr_b16 v[90:91], v162 offset:7424\n"
"s_add_u32 m0, m0, 0x400\n"
"v_and_b32 v49, 0xffff0000, v234\n"
"v_mfma_f32_16x16x32_bf16 a[208:211], v[64:67], v[180:183], a[208:211]\n"
"ds_read_b64_tr_b16 v[92:93], v163 offset:256\n"
"buffer_load_dwordx4 v153, s[40:43], s55 offen lds\n"
"v_mfma_f32_16x16x32_bf16 a[212:215], v[64:67], v[212:215], a[212:215]\n"
"ds_read_b64_tr_b16 v[94:95], v163 offset:7424\n"
"v_mul_f32 v48, v48, v248\n"
"v_mfma_f32_16x16x32_bf16 a[216:219], v[68:71], v[180:183], a[216:219]\n"
"ds_read_b64_tr_b16 v[56:57], v162 offset:512\n"
"v_mul_f32 v49, v49, v248\n"
"v_mfma_f32_16x16x32_bf16 a[220:223], v[68:71], v[212:215], a[220:223]\n"
"s_waitcnt lgkmcnt(9)\n"
"ds_read_b64_tr_b16 v[58:59], v162 offset:7680\n"
"s_add_u32 m0, m0, 0x400\n"
"v_cvt_pk_bf16_f32 v234, v48, v49\n"
"v_mfma_f32_16x16x32_bf16 a[224:227], v[72:75], v[180:183], a[224:227]\n"
"ds_read_b64_tr_b16 v[60:61], v163 offset:512\n"
"buffer_load_dwordx4 v154, s[40:43], s55 offen lds\n"
"v_lshlrev_b32 v48, 16, v235\n"
"v_mfma_f32_16x16x32_bf16 a[228:231], v[72:75], v[212:215], a[228:231]\n"
"ds_read_b64_tr_b16 v[62:63], v163 offset:7680\n"
"v_and_b32 v49, 0xffff0000, v235\n"
"v_mfma_f32_16x16x32_bf16 a[232:235], v[76:79], v[180:183], a[232:235]\n"
"ds_read_b64_tr_b16 v[64:65], v162 offset:768\n"
"v_mfma_f32_16x16x32_bf16 a[236:239], v[76:79], v[212:215], a[236:239]\n"
"s_waitcnt lgkmcnt(9)\n"
"ds_read_b64_tr_b16 v[66:67], v162 offset:7936\n"
"s_add_u32 m0, m0, 0x400\n"
"v_mul_f32 v48, v48, v248\n"
"v_mfma_f32_16x16x32_bf16 a[128:131], v[80:83], v[184:187], a[128:131]\n"
"ds_read_b64_tr_b16 v[68:69], v163 offset:768\n"
"buffer_load_dwordx4 v155, s[40:43], s55 offen lds\n"
"v_mul_f32 v49, v49, v248\n"
"v_mfma_f32_16x16x32_bf16 a[132:135], v[80:83], v[216:219], a[132:135]\n"
"ds_read_b64_tr_b16 v[70:71], v163 offset:7936\n"
"v_cvt_pk_bf16_f32 v235, v48, v49\n"
"v_mfma_f32_16x16x32_bf16 a[136:139], v[84:87], v[184:187], a[136:139]\n"
"ds_read_b64_tr_b16 v[72:73], v162 offset:1024\n"
"v_lshlrev_b32 v48, 16, v236\n"
"v_mfma_f32_16x16x32_bf16 a[140:143], v[84:87], v[216:219], a[140:143]\n"
"s_waitcnt lgkmcnt(9)\n"
"ds_read_b64_tr_b16 v[74:75], v162 offset:8192\n"
"s_add_u32 m0, m0, 0x400\n"
"v_mfma_f32_16x16x32_bf16 a[144:147], v[88:91], v[184:187], a[144:147]\n"
"ds_read_b64_tr_b16 v[76:77], v163 offset:1024\n"
"buffer_load_dwordx4 v156, s[40:43], s55 offen lds\n"
"v_and_b32 v49, 0xffff0000, v236\n"
"v_mfma_f32_16x16x32_bf16 a[148:151], v[88:91], v[216:219], a[148:151]\n"
"ds_read_b64_tr_b16 v[78:79], v163 offset:8192\n"
"v_mul_f32 v48, v48, v249\n"
"v_mfma_f32_16x16x32_bf16 a[152:155], v[92:95], v[184:187], a[152:155]\n"
"ds_read_b64_tr_b16 v[80:81], v162 offset:1280\n"
"v_mul_f32 v49, v49, v249\n"
"v_mfma_f32_16x16x32_bf16 a[156:159], v[92:95], v[216:219], a[156:159]\n"
"s_waitcnt lgkmcnt(9)\n"
"ds_read_b64_tr_b16 v[82:83], v162 offset:8448\n"
"s_add_u32 m0, m0, 0x400\n"
"v_cvt_pk_bf16_f32 v236, v48, v49\n"
"v_mfma_f32_16x16x32_bf16 a[160:163], v[56:59], v[184:187], a[160:163]\n"
"ds_read_b64_tr_b16 v[84:85], v163 offset:1280\n"
"buffer_load_dwordx4 v157, s[40:43], s55 offen lds\n"
"v_lshlrev_b32 v48, 16, v237\n"
"v_mfma_f32_16x16x32_bf16 a[164:167], v[56:59], v[216:219], a[164:167]\n"
"ds_read_b64_tr_b16 v[86:87], v163 offset:8448\n"
"v_mfma_f32_16x16x32_bf16 a[168:171], v[60:63], v[184:187], a[168:171]\n"
"ds_read_b64_tr_b16 v[88:89], v162 offset:1536\n"
"v_and_b32 v49, 0xffff0000, v237\n"
"v_mfma_f32_16x16x32_bf16 a[172:175], v[60:63], v[216:219], a[172:175]\n"
"s_waitcnt lgkmcnt(9)\n"
"ds_read_b64_tr_b16 v[90:91], v162 offset:8704\n"
"s_add_u32 m0, m0, 0x400\n"
"v_mul_f32 v48, v48, v249\n"
"v_mfma_f32_16x16x32_bf16 a[176:179], v[64:67], v[184:187], a[176:179]\n"
"ds_read_b64_tr_b16 v[92:93], v163 offset:1536\n"
"buffer_load_dwordx4 v158, s[40:43], s55 offen lds\n"
"v_mul_f32 v49, v49, v249\n"
"v_mfma_f32_16x16x32_bf16 a[180:183], v[64:67], v[216:219], a[180:183]\n"
"ds_read_b64_tr_b16 v[94:95], v163 offset:8704\n"
"v_cvt_pk_bf16_f32 v237, v48, v49\n"
"v_mfma_f32_16x16x32_bf16 a[184:187], v[68:71], v[184:187], a[184:187]\n"
"ds_read_b64_tr_b16 v[56:57], v162 offset:14336\n"
"v_mfma_f32_16x16x32_bf16 a[188:191], v[68:71], v[216:219], a[188:191]\n"
"s_waitcnt lgkmcnt(9)\n"
"ds_read_b64_tr_b16 v[58:59], v162 offset:21504\n"
"s_add_u32 s55, s55, 57344\n"
"s_add_u32 s56, s56, 28672\n"
"s_sub_u32 s96, s56, 143360\n"
"s_cmp_ge_u32 s56, 143360\n"
"s_cselect_b32 s56, s96, s56\n"
"v_lshlrev_b32 v48, 16, v238\n"
"v_mfma_f32_16x16x32_bf16 a[192:195], v[72:75], v[184:187], a[192:195]\n"
"ds_read_b64_tr_b16 v[60:61], v163 offset:14336\n"
"v_and_b32 v49, 0xffff0000, v238\n"
"v_mfma_f32_16x16x32_bf16 a[196:199], v[72:75], v[216:219], a[196:199]\n"
"ds_read_b64_tr_b16 v[62:63], v163 offset:21504\n"
"v_mul_f32 v48, v48, v249\n"
"v_mfma_f32_16x16x32_bf16 a[200:203], v[76:79], v[184:187], a[200:203]\n"
"ds_read_b64_tr_b16 v[64:65], v162 offset:14592\n"
"v_mul_f32 v49, v49, v249\n"
"v_mfma_f32_16x16x32_bf16 a[204:207], v[76:79], v[216:219], a[204:207]\n"
"s_waitcnt lgkmcnt(9)\n"
"ds_read_b64_tr_b16 v[66:67], v162 offset:21760\n"
"v_cvt_pk_bf16_f32 v238, v48, v49\n"
"v_mfma_f32_16x16x32_bf16 a[208:211], v[80:83], v[184:187], a[208:211]\n"
"ds_read_b64_tr_b16 v[68:69], v163 offset:14592\n"
"v_mfma_f32_16x16x32_bf16 a[212:215], v[80:83], v[216:219], a[212:215]\n"
"ds_read_b64_tr_b16 v[70:71], v163 offset:21760\n"
"v_lshlrev_b32 v48, 16, v239\n"
"v_mfma_f32_16x16x32_bf16 a[216:219], v[84:87], v[184:187], a[216:219]\n"
"ds_read_b64_tr_b16 v[72:73], v162 offset:14848\n"
"v_and_b32 v49, 0xffff0000, v239\n"
"v_mfma_f32_16x16x32_bf16 a[220:223], v[84:87], v[216:219], a[220:223]\n"
"s_waitcnt lgkmcnt(9)\n"
"ds_read_b64_tr_b16 v[74:75], v162 offset:22016\n"
"v_mul_f32 v48, v48, v249\n"
"v_mfma_f32_16x16x32_bf16 a[224:227], v[88:91], v[184:187], a[224:227]\n"
"ds_read_b64_tr_b16 v[76:77], v163 offset:14848\n"
"v_mul_f32 v49, v49, v249\n"
"v_mfma_f32_16x16x32_bf16 a[228:231], v[88:91], v[216:219], a[228:231]\n"
"ds_read_b64_tr_b16 v[78:79], v163 offset:22016\n"
"v_mfma_f32_16x16x32_bf16 a[232:235], v[92:95], v[184:187], a[232:235]\n"
"ds_read_b64_tr_b16 v[80:81], v162 offset:15104\n"
"v_cvt_pk_bf16_f32 v239, v48, v49\n"
"v_mfma_f32_16x16x32_bf16 a[236:239], v[92:95], v[216:219], a[236:239]\n"
"s_waitcnt lgkmcnt(9)\n"
"ds_read_b64_tr_b16 v[82:83], v162 offset:22272\n"
"v_lshlrev_b32 v48, 16, v240\n"
"v_mfma_f32_16x16x32_bf16 a[128:131], v[56:59], v[188:191], a[128:131]\n"
"ds_read_b64_tr_b16 v[84:85], v163 offset:15104\n"
"s_waitcnt vmcnt(7)\n"
"v_mov_b32 v11, 0\n"
"v_mov_b32 v10, s35\n"
"s_mov_b64 exec, 1\n"
"global_store_dword v11, v10, s[62:63] sc1\n"
"s_mov_b64 exec, -1\n"
"L_sg_c1m_%=:\n"
"s_xor_b32 s61, s61, 1\n"
"s_mov_b64 s[62:63], %0\n"
"s_lshl_b32 s96, s61, 4\n"
"s_add_u32 s96, s96, 126885888\n"
"s_add_u32 s62, s62, s96\n"
"s_addc_u32 s63, s63, 0\n"
"s_lshl_b32 s96, s50, 2\n"
"s_add_u32 s62, s62, s96\n"
"s_addc_u32 s63, s63, 0\n"
"v_and_b32 v49, 0xffff0000, v240\n"
"v_mfma_f32_16x16x32_bf16 a[132:135], v[56:59], v[220:223], a[132:135]\n"
"ds_read_b64_tr_b16 v[86:87], v163 offset:22272\n"
"v_mul_f32 v48, v48, v248\n"
"v_mfma_f32_16x16x32_bf16 a[136:139], v[60:63], v[188:191], a[136:139]\n"
"ds_read_b64_tr_b16 v[88:89], v162 offset:15360\n"
"v_mul_f32 v49, v49, v248\n"
"v_mfma_f32_16x16x32_bf16 a[140:143], v[60:63], v[220:223], a[140:143]\n"
"s_waitcnt lgkmcnt(9)\n"
"ds_read_b64_tr_b16 v[90:91], v162 offset:22528\n"
"v_mfma_f32_16x16x32_bf16 a[144:147], v[64:67], v[188:191], a[144:147]\n"
"ds_read_b64_tr_b16 v[92:93], v163 offset:15360\n"
"v_cvt_pk_bf16_f32 v240, v48, v49\n"
"v_mfma_f32_16x16x32_bf16 a[148:151], v[64:67], v[220:223], a[148:151]\n"
"ds_read_b64_tr_b16 v[94:95], v163 offset:22528\n"
"v_lshlrev_b32 v48, 16, v241\n"
"v_mfma_f32_16x16x32_bf16 a[152:155], v[68:71], v[188:191], a[152:155]\n"
"ds_read_b64_tr_b16 v[56:57], v162 offset:15616\n"
"v_and_b32 v49, 0xffff0000, v241\n"
"v_mfma_f32_16x16x32_bf16 a[156:159], v[68:71], v[220:223], a[156:159]\n"
"s_waitcnt lgkmcnt(9)\n"
"ds_read_b64_tr_b16 v[58:59], v162 offset:22784\n"
"v_mul_f32 v48, v48, v248\n"
"v_mfma_f32_16x16x32_bf16 a[160:163], v[72:75], v[188:191], a[160:163]\n"
"ds_read_b64_tr_b16 v[60:61], v163 offset:15616\n"
"v_mul_f32 v49, v49, v248\n"
"v_mfma_f32_16x16x32_bf16 a[164:167], v[72:75], v[220:223], a[164:167]\n"
"ds_read_b64_tr_b16 v[62:63], v163 offset:22784\n"
"v_mfma_f32_16x16x32_bf16 a[168:171], v[76:79], v[188:191], a[168:171]\n"
"ds_read_b64_tr_b16 v[64:65], v162 offset:15872\n"
"v_cvt_pk_bf16_f32 v241, v48, v49\n"
"v_mfma_f32_16x16x32_bf16 a[172:175], v[76:79], v[220:223], a[172:175]\n"
"s_waitcnt lgkmcnt(9)\n"
"ds_read_b64_tr_b16 v[66:67], v162 offset:23040\n"
"v_lshlrev_b32 v48, 16, v242\n"
"v_mfma_f32_16x16x32_bf16 a[176:179], v[80:83], v[188:191], a[176:179]\n"
"ds_read_b64_tr_b16 v[68:69], v163 offset:15872\n"
"v_and_b32 v49, 0xffff0000, v242\n"
"v_mfma_f32_16x16x32_bf16 a[180:183], v[80:83], v[220:223], a[180:183]\n"
"ds_read_b64_tr_b16 v[70:71], v163 offset:23040\n"
"s_waitcnt vmcnt(21)\n"
"s_barrier\n"
"v_add_u32 v162, s59, v160\n"
"v_add_u32 v163, s59, v161\n"
"s_add_u32 s59, s59, 28672\n"
"s_sub_u32 s96, s59, 143360\n"
"s_cmp_ge_u32 s59, 143360\n"
"s_cselect_b32 s59, s96, s59\n"
"v_mul_f32 v48, v48, v248\n"
"v_mfma_f32_16x16x32_bf16 a[184:187], v[84:87], v[188:191], a[184:187]\n"
"ds_read_b64_tr_b16 v[72:73], v162 offset:0\n"
"v_mfma_f32_16x16x32_bf16 a[188:191], v[84:87], v[220:223], a[188:191]\n"
"s_waitcnt lgkmcnt(9)\n"
"ds_read_b64_tr_b16 v[74:75], v162 offset:7168\n"
"s_mov_b32 m0, s56\n"
"v_mul_f32 v49, v49, v248\n"
"v_mfma_f32_16x16x32_bf16 a[192:195], v[88:91], v[188:191], a[192:195]\n"
"ds_read_b64_tr_b16 v[76:77], v163 offset:0\n"
"buffer_load_dwordx4 v152, s[40:43], s55 offen lds\n"
"v_cvt_pk_bf16_f32 v242, v48, v49\n"
"v_mfma_f32_16x16x32_bf16 a[196:199], v[88:91], v[220:223], a[196:199]\n"
"ds_read_b64_tr_b16 v[78:79], v163 offset:7168\n"
"v_lshlrev_b32 v48, 16, v243\n"
"v_mfma_f32_16x16x32_bf16 a[200:203], v[92:95], v[188:191], a[200:203]\n"
"ds_read_b64_tr_b16 v[80:81], v162 offset:256\n"
"v_and_b32 v49, 0xffff0000, v243\n"
"v_mfma_f32_16x16x32_bf16 a[204:207], v[92:95], v[220:223], a[204:207]\n"
"s_waitcnt lgkmcnt(9)\n"
"ds_read_b64_tr_b16 v[82:83], v162 offset:7424\n"
"s_add_u32 m0, m0, 0x400\n"
"v_mul_f32 v48, v48, v248\n"
"v_mfma_f32_16x16x32_bf16 a[208:211], v[56:59], v[188:191], a[208:211]\n"
"ds_read_b64_tr_b16 v[84:85], v163 offset:256\n"
"buffer_load_dwordx4 v153, s[40:43], s55 offen lds\n"
"v_mfma_f32_16x16x32_bf16 a[212:215], v[56:59], v[220:223], a[212:215]\n"
"ds_read_b64_tr_b16 v[86:87], v163 offset:7424\n"
"v_mul_f32 v49, v49, v248\n"
"v_mfma_f32_16x16x32_bf16 a[216:219], v[60:63], v[188:191], a[216:219]\n"
"ds_read_b64_tr_b16 v[88:89], v162 offset:512\n"
"v_cvt_pk_bf16_f32 v243, v48, v49\n"
"v_mfma_f32_16x16x32_bf16 a[220:223], v[60:63], v[220:223], a[220:223]\n"
"s_waitcnt lgkmcnt(9)\n"
"ds_read_b64_tr_b16 v[90:91], v162 offset:7680\n"
"s_add_u32 m0, m0, 0x400\n"
"v_lshlrev_b32 v48, 16, v244\n"
"v_mfma_f32_16x16x32_bf16 a[224:227], v[64:67], v[188:191], a[224:227]\n"
"ds_read_b64_tr_b16 v[92:93], v163 offset:512\n"
"buffer_load_dwordx4 v154, s[40:43], s55 offen lds\n"
"v_and_b32 v49, 0xffff0000, v244\n"
"v_mfma_f32_16x16x32_bf16 a[228:231], v[64:67], v[220:223], a[228:231]\n"
"ds_read_b64_tr_b16 v[94:95], v163 offset:7680\n"
"v_mfma_f32_16x16x32_bf16 a[232:235], v[68:71], v[188:191], a[232:235]\n"
"ds_read_b64_tr_b16 v[56:57], v162 offset:768\n"
"v_mul_f32 v48, v48, v249\n"
"v_mfma_f32_16x16x32_bf16 a[236:239], v[68:71], v[220:223], a[236:239]\n"
"s_waitcnt lgkmcnt(9)\n"
"ds_read_b64_tr_b16 v[58:59], v162 offset:7936\n"
"s_add_u32 m0, m0, 0x400\n"
"v_mul_f32 v49, v49, v249\n"
"v_mfma_f32_16x16x32_bf16 a[128:131], v[72:75], v[192:195], a[128:131]\n"
"ds_read_b64_tr_b16 v[60:61], v163 offset:768\n"
"buffer_load_dwordx4 v155, s[40:43], s55 offen lds\n"
"v_cvt_pk_bf16_f32 v244, v48, v49\n"
"v_mfma_f32_16x16x32_bf16 a[132:135], v[72:75], v[224:227], a[132:135]\n"
"ds_read_b64_tr_b16 v[62:63], v163 offset:7936\n"
"v_lshlrev_b32 v48, 16, v245\n"
"v_mfma_f32_16x16x32_bf16 a[136:139], v[76:79], v[192:195], a[136:139]\n"
"ds_read_b64_tr_b16 v[64:65], v162 offset:1024\n"
"v_and_b32 v49, 0xffff0000, v245\n"
"v_mfma_f32_16x16x32_bf16 a[140:143], v[76:79], v[224:227], a[140:143]\n"
"s_waitcnt lgkmcnt(9)\n"
"ds_read_b64_tr_b16 v[66:67], v162 offset:8192\n"
"s_add_u32 m0, m0, 0x400\n"
"v_mfma_f32_16x16x32_bf16 a[144:147], v[80:83], v[192:195], a[144:147]\n"
"ds_read_b64_tr_b16 v[68:69], v163 offset:1024\n"
"buffer_load_dwordx4 v156, s[40:43], s55 offen lds\n"
"v_mul_f32 v48, v48, v249\n"
"v_mfma_f32_16x16x32_bf16 a[148:151], v[80:83], v[224:227], a[148:151]\n"
"ds_read_b64_tr_b16 v[70:71], v163 offset:8192\n"
"v_mul_f32 v49, v49, v249\n"
"v_mfma_f32_16x16x32_bf16 a[152:155], v[84:87], v[192:195], a[152:155]\n"
"ds_read_b64_tr_b16 v[72:73], v162 offset:1280\n"
"v_cvt_pk_bf16_f32 v245, v48, v49\n"
"v_mfma_f32_16x16x32_bf16 a[156:159], v[84:87], v[224:227], a[156:159]\n"
"s_waitcnt lgkmcnt(9)\n"
"ds_read_b64_tr_b16 v[74:75], v162 offset:8448\n"
"s_add_u32 m0, m0, 0x400\n"
"v_lshlrev_b32 v48, 16, v246\n"
"v_mfma_f32_16x16x32_bf16 a[160:163], v[88:91], v[192:195], a[160:163]\n"
"ds_read_b64_tr_b16 v[76:77], v163 offset:1280\n"
"buffer_load_dwordx4 v157, s[40:43], s55 offen lds\n"
"v_mfma_f32_16x16x32_bf16 a[164:167], v[88:91], v[224:227], a[164:167]\n"
"ds_read_b64_tr_b16 v[78:79], v163 offset:8448\n"
"v_and_b32 v49, 0xffff0000, v246\n"
"v_mfma_f32_16x16x32_bf16 a[168:171], v[92:95], v[192:195], a[168:171]\n"
"ds_read_b64_tr_b16 v[80:81], v162 offset:1536\n"
"v_mul_f32 v48, v48, v249\n"
"v_mfma_f32_16x16x32_bf16 a[172:175], v[92:95], v[224:227], a[172:175]\n"
"s_waitcnt lgkmcnt(9)\n"
"ds_read_b64_tr_b16 v[82:83], v162 offset:8704\n"
"s_add_u32 m0, m0, 0x400\n"
"v_mul_f32 v49, v49, v249\n"
"v_mfma_f32_16x16x32_bf16 a[176:179], v[56:59], v[192:195], a[176:179]\n"
"ds_read_b64_tr_b16 v[84:85], v163 offset:1536\n"
"buffer_load_dwordx4 v158, s[40:43], s55 offen lds\n"
"v_cvt_pk_bf16_f32 v246, v48, v49\n"
"v_mfma_f32_16x16x32_bf16 a[180:183], v[56:59], v[224:227], a[180:183]\n"
"ds_read_b64_tr_b16 v[86:87], v163 offset:8704\n"
"v_lshlrev_b32 v48, 16, v247\n"
"v_mfma_f32_16x16x32_bf16 a[184:187], v[60:63], v[192:195], a[184:187]\n"
"ds_read_b64_tr_b16 v[88:89], v162 offset:14336\n"
"v_mfma_f32_16x16x32_bf16 a[188:191], v[60:63], v[224:227], a[188:191]\n"
"s_waitcnt lgkmcnt(9)\n"
"ds_read_b64_tr_b16 v[90:91], v162 offset:21504\n"
"s_add_u32 s55, s55, 57344\n"
"s_add_u32 s56, s56, 28672\n"
"s_sub_u32 s96, s56, 143360\n"
"s_cmp_ge_u32 s56, 143360\n"
"s_cselect_b32 s56, s96, s56\n"
"v_and_b32 v49, 0xffff0000, v247\n"
"v_mfma_f32_16x16x32_bf16 a[192:195], v[64:67], v[192:195], a[192:195]\n"
"ds_read_b64_tr_b16 v[92:93], v163 offset:14336\n"
"v_mul_f32 v48, v48, v249\n"
"v_mfma_f32_16x16x32_bf16 a[196:199], v[64:67], v[224:227], a[196:199]\n"
"ds_read_b64_tr_b16 v[94:95], v163 offset:21504\n"
"v_mul_f32 v49, v49, v249\n"
"v_mfma_f32_16x16x32_bf16 a[200:203], v[68:71], v[192:195], a[200:203]\n"
"ds_read_b64_tr_b16 v[56:57], v162 offset:14592\n"
"v_cvt_pk_bf16_f32 v247, v48, v49\n"
"v_mfma_f32_16x16x32_bf16 a[204:207], v[68:71], v[224:227], a[204:207]\n"
"s_waitcnt lgkmcnt(9)\n"
"ds_read_b64_tr_b16 v[58:59], v162 offset:21760\n"
"v_mfma_f32_16x16x32_bf16 a[208:211], v[72:75], v[192:195], a[208:211]\n"
"ds_read_b64_tr_b16 v[60:61], v163 offset:14592\n"
"v_mfma_f32_16x16x32_bf16 a[212:215], v[72:75], v[224:227], a[212:215]\n"
"ds_read_b64_tr_b16 v[62:63], v163 offset:21760\n"
"v_mfma_f32_16x16x32_bf16 a[216:219], v[76:79], v[192:195], a[216:219]\n"
"ds_read_b64_tr_b16 v[64:65], v162 offset:14848\n"
"v_mfma_f32_16x16x32_bf16 a[220:223], v[76:79], v[224:227], a[220:223]\n"
"s_waitcnt lgkmcnt(9)\n"
"ds_read_b64_tr_b16 v[66:67], v162 offset:22016\n"
"v_mfma_f32_16x16x32_bf16 a[224:227], v[80:83], v[192:195], a[224:227]\n"
"ds_read_b64_tr_b16 v[68:69], v163 offset:14848\n"
"v_mfma_f32_16x16x32_bf16 a[228:231], v[80:83], v[224:227], a[228:231]\n"
"ds_read_b64_tr_b16 v[70:71], v163 offset:22016\n"
"v_mfma_f32_16x16x32_bf16 a[232:235], v[84:87], v[192:195], a[232:235]\n"
"ds_read_b64_tr_b16 v[72:73], v162 offset:15104\n"
"v_mfma_f32_16x16x32_bf16 a[236:239], v[84:87], v[224:227], a[236:239]\n"
"s_waitcnt lgkmcnt(9)\n"
"ds_read_b64_tr_b16 v[74:75], v162 offset:22272\n"
"v_mfma_f32_16x16x32_bf16 a[128:131], v[88:91], v[196:199], a[128:131]\n"
"ds_read_b64_tr_b16 v[76:77], v163 offset:15104\n"
"v_mfma_f32_16x16x32_bf16 a[132:135], v[88:91], v[228:231], a[132:135]\n"
"ds_read_b64_tr_b16 v[78:79], v163 offset:22272\n"
"v_mfma_f32_16x16x32_bf16 a[136:139], v[92:95], v[196:199], a[136:139]\n"
"ds_read_b64_tr_b16 v[80:81], v162 offset:15360\n"
"v_mfma_f32_16x16x32_bf16 a[140:143], v[92:95], v[228:231], a[140:143]\n"
"s_waitcnt lgkmcnt(9)\n"
"ds_read_b64_tr_b16 v[82:83], v162 offset:22528\n"
"v_mfma_f32_16x16x32_bf16 a[144:147], v[56:59], v[196:199], a[144:147]\n"
"ds_read_b64_tr_b16 v[84:85], v163 offset:15360\n"
"v_mov_b32 v11, 0\n"
"v_mfma_f32_16x16x32_bf16 a[148:151], v[56:59], v[228:231], a[148:151]\n"
"ds_read_b64_tr_b16 v[86:87], v163 offset:22528\n"
"global_load_dword v251, v11, s[62:63] sc1\n"
"v_mfma_f32_16x16x32_bf16 a[152:155], v[60:63], v[196:199], a[152:155]\n"
"ds_read_b64_tr_b16 v[88:89], v162 offset:15616\n"
"v_mfma_f32_16x16x32_bf16 a[156:159], v[60:63], v[228:231], a[156:159]\n"
"s_waitcnt lgkmcnt(9)\n"
"ds_read_b64_tr_b16 v[90:91], v162 offset:22784\n"
"v_mfma_f32_16x16x32_bf16 a[160:163], v[64:67], v[196:199], a[160:163]\n"
"ds_read_b64_tr_b16 v[92:93], v163 offset:15616\n"
"v_mfma_f32_16x16x32_bf16 a[164:167], v[64:67], v[228:231], a[164:167]\n"
"ds_read_b64_tr_b16 v[94:95], v163 offset:22784\n"
"v_mfma_f32_16x16x32_bf16 a[168:171], v[68:71], v[196:199], a[168:171]\n"
"ds_read_b64_tr_b16 v[56:57], v162 offset:15872\n"
"v_mfma_f32_16x16x32_bf16 a[172:175], v[68:71], v[228:231], a[172:175]\n"
"s_waitcnt lgkmcnt(9)\n"
"ds_read_b64_tr_b16 v[58:59], v162 offset:23040\n"
"v_mfma_f32_16x16x32_bf16 a[176:179], v[72:75], v[196:199], a[176:179]\n"
"ds_read_b64_tr_b16 v[60:61], v163 offset:15872\n"
"v_mfma_f32_16x16x32_bf16 a[180:183], v[72:75], v[228:231], a[180:183]\n"
"ds_read_b64_tr_b16 v[62:63], v163 offset:23040\n"
"s_waitcnt vmcnt(18)\n"
"s_barrier\n"
"v_add_u32 v162, s59, v160\n"
"v_add_u32 v163, s59, v161\n"
"s_add_u32 s59, s59, 28672\n"
"s_sub_u32 s96, s59, 143360\n"
"s_cmp_ge_u32 s59, 143360\n"
"s_cselect_b32 s59, s96, s59\n"
"v_mfma_f32_16x16x32_bf16 a[184:187], v[76:79], v[196:199], a[184:187]\n"
"ds_read_b64_tr_b16 v[64:65], v162 offset:0\n"
"v_mfma_f32_16x16x32_bf16 a[188:191], v[76:79], v[228:231], a[188:191]\n"
"s_waitcnt lgkmcnt(9)\n"
"ds_read_b64_tr_b16 v[66:67], v162 offset:7168\n"
"s_mov_b32 m0, s56\n"
"v_mfma_f32_16x16x32_bf16 a[192:195], v[80:83], v[196:199], a[192:195]\n"
"ds_read_b64_tr_b16 v[68:69], v163 offset:0\n"
"buffer_load_dwordx4 v152, s[40:43], s55 offen lds\n"
"v_mfma_f32_16x16x32_bf16 a[196:199], v[80:83], v[228:231], a[196:199]\n"
"ds_read_b64_tr_b16 v[70:71], v163 offset:7168\n"
"v_mfma_f32_16x16x32_bf16 a[200:203], v[84:87], v[196:199], a[200:203]\n"
"ds_read_b64_tr_b16 v[72:73], v162 offset:256\n"
"v_mfma_f32_16x16x32_bf16 a[204:207], v[84:87], v[228:231], a[204:207]\n"
"s_waitcnt lgkmcnt(9)\n"
"ds_read_b64_tr_b16 v[74:75], v162 offset:7424\n"
"s_add_u32 m0, m0, 0x400\n"
"v_mfma_f32_16x16x32_bf16 a[208:211], v[88:91], v[196:199], a[208:211]\n"
"ds_read_b64_tr_b16 v[76:77], v163 offset:256\n"
"buffer_load_dwordx4 v153, s[40:43], s55 offen lds\n"
"v_mfma_f32_16x16x32_bf16 a[212:215], v[88:91], v[228:231], a[212:215]\n"
"ds_read_b64_tr_b16 v[78:79], v163 offset:7424\n"
"v_mfma_f32_16x16x32_bf16 a[216:219], v[92:95], v[196:199], a[216:219]\n"
"ds_read_b64_tr_b16 v[80:81], v162 offset:512\n"
"v_mfma_f32_16x16x32_bf16 a[220:223], v[92:95], v[228:231], a[220:223]\n"
"s_waitcnt lgkmcnt(9)\n"
"ds_read_b64_tr_b16 v[82:83], v162 offset:7680\n"
"s_add_u32 m0, m0, 0x400\n"
"v_mfma_f32_16x16x32_bf16 a[224:227], v[56:59], v[196:199], a[224:227]\n"
"ds_read_b64_tr_b16 v[84:85], v163 offset:512\n"
"buffer_load_dwordx4 v154, s[40:43], s55 offen lds\n"
"v_mfma_f32_16x16x32_bf16 a[228:231], v[56:59], v[228:231], a[228:231]\n"
"ds_read_b64_tr_b16 v[86:87], v163 offset:7680\n"
"v_mfma_f32_16x16x32_bf16 a[232:235], v[60:63], v[196:199], a[232:235]\n"
"ds_read_b64_tr_b16 v[88:89], v162 offset:768\n"
"v_mfma_f32_16x16x32_bf16 a[236:239], v[60:63], v[228:231], a[236:239]\n"
"s_waitcnt lgkmcnt(9)\n"
"ds_read_b64_tr_b16 v[90:91], v162 offset:7936\n"
"s_add_u32 m0, m0, 0x400\n"
"v_mfma_f32_16x16x32_bf16 a[128:131], v[64:67], v[232:235], a[128:131]\n"
"ds_read_b64_tr_b16 v[92:93], v163 offset:768\n"
"buffer_load_dwordx4 v155, s[40:43], s55 offen lds\n"
"v_mfma_f32_16x16x32_bf16 a[132:135], v[64:67], v[236:239], a[132:135]\n"
"ds_read_b64_tr_b16 v[94:95], v163 offset:7936\n"
"v_mfma_f32_16x16x32_bf16 a[136:139], v[68:71], v[232:235], a[136:139]\n"
"ds_read_b64_tr_b16 v[56:57], v162 offset:1024\n"
"v_mfma_f32_16x16x32_bf16 a[140:143], v[68:71], v[236:239], a[140:143]\n"
"s_waitcnt lgkmcnt(9)\n"
"ds_read_b64_tr_b16 v[58:59], v162 offset:8192\n"
"s_add_u32 m0, m0, 0x400\n"
"v_mfma_f32_16x16x32_bf16 a[144:147], v[72:75], v[232:235], a[144:147]\n"
"ds_read_b64_tr_b16 v[60:61], v163 offset:1024\n"
"buffer_load_dwordx4 v156, s[40:43], s55 offen lds\n"
"v_mfma_f32_16x16x32_bf16 a[148:151], v[72:75], v[236:239], a[148:151]\n"
"s_waitcnt vmcnt(5)\n"
"v_readfirstlane_b32 s97, v251\n"
"s_nop 3\n"
"s_cmp_eq_u32 s97, s35\n"
"s_cbranch_scc1 L_pd_c1m_%=\n"
"v_lshlrev_b32 v11, 2, v0\n"
"v_min_u32 v11, 0, v11\n"
"s_mov_b32 s99, 0\n"
"L_pl_c1m_%=:\n"
"global_load_dword v10, v11, s[62:63] sc1\n"
"s_waitcnt vmcnt(0)\n"
"v_cmp_ne_u32 vcc, s35, v10\n"
"s_nop 4\n"
"s_cmp_eq_u64 vcc, 0\n"
"s_cbranch_scc1 L_pd_c1m_%=\n"
"s_sleep 1\n"
"s_add_u32 s99, s99, 1\n"
"s_cmp_lt_u32 s99, 0x200000\n"
"s_cbranch_scc1 L_pl_c1m_%=\n"
"L_pd_c1m_%=:\n"
"ds_read_b64_tr_b16 v[62:63], v163 offset:8192\n"
"v_mfma_f32_16x16x32_bf16 a[152:155], v[76:79], v[232:235], a[152:155]\n"
"ds_read_b64_tr_b16 v[64:65], v162 offset:1280\n"
"buffer_load_dwordx4 a[0:3], v1, s[36:39], s57 offen offset:0 sc1\n"
"v_mfma_f32_16x16x32_bf16 a[156:159], v[76:79], v[236:239], a[156:159]\n"
"s_waitcnt lgkmcnt(9)\n"
"ds_read_b64_tr_b16 v[66:67], v162 offset:8448\n"
"s_add_u32 m0, m0, 0x400\n"
"buffer_load_dwordx4 a[4:7], v1, s[36:39], s58 offen offset:0 sc1\n"
"v_mfma_f32_16x16x32_bf16 a[160:163], v[80:83], v[232:235], a[160:163]\n"
"ds_read_b64_tr_b16 v[68:69], v163 offset:1280\n"
"buffer_load_dwordx4 v157, s[40:43], s55 offen lds\n"
"buffer_load_dwordx4 a[8:11], v1, s[36:39], s57 offen offset:1024 sc1\n"
"v_mfma_f32_16x16x32_bf16 a[164:167], v[80:83], v[236:239], a[164:167]\n"
"ds_read_b64_tr_b16 v[70:71], v163 offset:8448\n"
"buffer_load_dwordx4 a[12:15], v1, s[36:39], s58 offen offset:1024 sc1\n"
"v_mfma_f32_16x16x32_bf16 a[168:171], v[84:87], v[232:235], a[168:171]\n"
"ds_read_b64_tr_b16 v[72:73], v162 offset:1536\n"
"buffer_load_dwordx4 a[16:19], v1, s[36:39], s57 offen offset:2048 sc1\n"
"v_mfma_f32_16x16x32_bf16 a[172:175], v[84:87], v[236:239], a[172:175]\n"
"s_waitcnt lgkmcnt(9)\n"
"ds_read_b64_tr_b16 v[74:75], v162 offset:8704\n"
"s_add_u32 m0, m0, 0x400\n"
"buffer_load_dwordx4 a[20:23], v1, s[36:39], s58 offen offset:2048 sc1\n"
"v_mfma_f32_16x16x32_bf16 a[176:179], v[88:91], v[232:235], a[176:179]\n"
"ds_read_b64_tr_b16 v[76:77], v163 offset:1536\n"
"buffer_load_dwordx4 v158, s[40:43], s55 offen lds\n"
"buffer_load_dwordx4 a[24:27], v1, s[36:39], s57 offen offset:3072 sc1\n"
"v_mfma_f32_16x16x32_bf16 a[180:183], v[88:91], v[236:239], a[180:183]\n"
"ds_read_b64_tr_b16 v[78:79], v163 offset:8704\n"
"buffer_load_dwordx4 a[28:31], v1, s[36:39], s58 offen offset:3072 sc1\n"
"s_add_u32 s57, s57, 4096\n"
"s_add_u32 s58, s58, 4096\n"
"v_mfma_f32_16x16x32_bf16 a[184:187], v[92:95], v[232:235], a[184:187]\n"
"ds_read_b64_tr_b16 v[80:81], v162 offset:14336\n"
"buffer_load_dwordx4 a[32:35], v1, s[36:39], s57 offen offset:0 sc1\n"
"v_mfma_f32_16x16x32_bf16 a[188:191], v[92:95], v[236:239], a[188:191]\n"
"s_waitcnt lgkmcnt(9)\n"
"ds_read_b64_tr_b16 v[82:83], v162 offset:21504\n"
"s_add_u32 s55, s55, 57344\n"
"s_add_u32 s56, s56, 28672\n"
"s_sub_u32 s96, s56, 143360\n"
"s_cmp_ge_u32 s56, 143360\n"
"s_cselect_b32 s56, s96, s56\n"
"buffer_load_dwordx4 a[36:39], v1, s[36:39], s58 offen offset:0 sc1\n"
"v_mfma_f32_16x16x32_bf16 a[192:195], v[56:59], v[232:235], a[192:195]\n"
"ds_read_b64_tr_b16 v[84:85], v163 offset:14336\n"
"buffer_load_dwordx4 a[40:43], v1, s[36:39], s57 offen offset:1024 sc1\n"
"v_mfma_f32_16x16x32_bf16 a[196:199], v[56:59], v[236:239], a[196:199]\n"
"ds_read_b64_tr_b16 v[86:87], v163 offset:21504\n"
"buffer_load_dwordx4 a[44:47], v1, s[36:39], s58 offen offset:1024 sc1\n"
"v_mfma_f32_16x16x32_bf16 a[200:203], v[60:63], v[232:235], a[200:203]\n"
"ds_read_b64_tr_b16 v[88:89], v162 offset:14592\n"
"buffer_load_dwordx4 a[48:51], v1, s[36:39], s57 offen offset:2048 sc1\n"
"v_mfma_f32_16x16x32_bf16 a[204:207], v[60:63], v[236:239], a[204:207]\n"
"s_waitcnt lgkmcnt(9)\n"
"ds_read_b64_tr_b16 v[90:91], v162 offset:21760\n"
"buffer_load_dwordx4 a[52:55], v1, s[36:39], s58 offen offset:2048 sc1\n"
"v_mfma_f32_16x16x32_bf16 a[208:211], v[64:67], v[232:235], a[208:211]\n"
"ds_read_b64_tr_b16 v[92:93], v163 offset:14592\n"
"buffer_load_dwordx4 a[56:59], v1, s[36:39], s57 offen offset:3072 sc1\n"
"v_mfma_f32_16x16x32_bf16 a[212:215], v[64:67], v[236:239], a[212:215]\n"
"ds_read_b64_tr_b16 v[94:95], v163 offset:21760\n"
"buffer_load_dwordx4 a[60:63], v1, s[36:39], s58 offen offset:3072 sc1\n"
"v_mfma_f32_16x16x32_bf16 a[216:219], v[68:71], v[232:235], a[216:219]\n"
"ds_read_b64_tr_b16 v[56:57], v162 offset:14848\n"
"v_mfma_f32_16x16x32_bf16 a[220:223], v[68:71], v[236:239], a[220:223]\n"
"s_waitcnt lgkmcnt(9)\n"
"ds_read_b64_tr_b16 v[58:59], v162 offset:22016\n"
"v_mfma_f32_16x16x32_bf16 a[224:227], v[72:75], v[232:235], a[224:227]\n"
"ds_read_b64_tr_b16 v[60:61], v163 offset:14848\n"
"v_mfma_f32_16x16x32_bf16 a[228:231], v[72:75], v[236:239], a[228:231]\n"
"ds_read_b64_tr_b16 v[62:63], v163 offset:22016\n"
"v_mfma_f32_16x16x32_bf16 a[232:235], v[76:79], v[232:235], a[232:235]\n"
"ds_read_b64_tr_b16 v[64:65], v162 offset:15104\n"
"v_mfma_f32_16x16x32_bf16 a[236:239], v[76:79], v[236:239], a[236:239]\n"
"s_waitcnt lgkmcnt(9)\n"
"ds_read_b64_tr_b16 v[66:67], v162 offset:22272\n"
"v_mfma_f32_16x16x32_bf16 a[128:131], v[80:83], v[240:243], a[128:131]\n"
"ds_read_b64_tr_b16 v[68:69], v163 offset:15104\n"
"v_mfma_f32_16x16x32_bf16 a[132:135], v[80:83], v[244:247], a[132:135]\n"
"ds_read_b64_tr_b16 v[70:71], v163 offset:22272\n"
"v_mfma_f32_16x16x32_bf16 a[136:139], v[84:87], v[240:243], a[136:139]\n"
"ds_read_b64_tr_b16 v[72:73], v162 offset:15360\n"
"v_mfma_f32_16x16x32_bf16 a[140:143], v[84:87], v[244:247], a[140:143]\n"
"s_waitcnt lgkmcnt(9)\n"
"ds_read_b64_tr_b16 v[74:75], v162 offset:22528\n"
"v_mfma_f32_16x16x32_bf16 a[144:147], v[88:91], v[240:243], a[144:147]\n"
"ds_read_b64_tr_b16 v[76:77], v163 offset:15360\n"
"v_mfma_f32_16x16x32_bf16 a[148:151], v[88:91], v[244:247], a[148:151]\n"
"ds_read_b64_tr_b16 v[78:79], v163 offset:22528\n"
"v_mfma_f32_16x16x32_bf16 a[152:155], v[92:95], v[240:243], a[152:155]\n"
"ds_read_b64_tr_b16 v[80:81], v162 offset:15616\n"
"v_mfma_f32_16x16x32_bf16 a[156:159], v[92:95], v[244:247], a[156:159]\n"
"s_waitcnt lgkmcnt(9)\n"
"ds_read_b64_tr_b16 v[82:83], v162 offset:22784\n"
"v_mfma_f32_16x16x32_bf16 a[160:163], v[56:59], v[240:243], a[160:163]\n"
"ds_read_b64_tr_b16 v[84:85], v163 offset:15616\n"
"v_mfma_f32_16x16x32_bf16 a[164:167], v[56:59], v[244:247], a[164:167]\n"
"ds_read_b64_tr_b16 v[86:87], v163 offset:22784\n"
"v_mfma_f32_16x16x32_bf16 a[168:171], v[60:63], v[240:243], a[168:171]\n"
"ds_read_b64_tr_b16 v[88:89], v162 offset:15872\n"
"v_mfma_f32_16x16x32_bf16 a[172:175], v[60:63], v[244:247], a[172:175]\n"
"s_waitcnt lgkmcnt(9)\n"
"ds_read_b64_tr_b16 v[90:91], v162 offset:23040\n"
"v_mfma_f32_16x16x32_bf16 a[176:179], v[64:67], v[240:243], a[176:179]\n"
"ds_read_b64_tr_b16 v[92:93], v163 offset:15872\n"
"v_mfma_f32_16x16x32_bf16 a[180:183], v[64:67], v[244:247], a[180:183]\n"
"ds_read_b64_tr_b16 v[94:95], v163 offset:23040\n"
"s_waitcnt vmcnt(32)\n"
"s_barrier\n"
"v_add_u32 v162, s59, v160\n"
"v_add_u32 v163, s59, v161\n"
"s_add_u32 s59, s59, 28672\n"
"s_sub_u32 s96, s59, 143360\n"
"s_cmp_ge_u32 s59, 143360\n"
"s_cselect_b32 s59, s96, s59\n"
"v_mfma_f32_16x16x32_bf16 a[184:187], v[68:71], v[240:243], a[184:187]\n"
"ds_read_b64_tr_b16 v[56:57], v162 offset:0\n"
"v_mfma_f32_16x16x32_bf16 a[188:191], v[68:71], v[244:247], a[188:191]\n"
"s_waitcnt lgkmcnt(9)\n"
"ds_read_b64_tr_b16 v[58:59], v162 offset:7168\n"
"s_mov_b32 m0, s56\n"
"v_mfma_f32_16x16x32_bf16 a[192:195], v[72:75], v[240:243], a[192:195]\n"
"ds_read_b64_tr_b16 v[60:61], v163 offset:0\n"
"buffer_load_dwordx4 v152, s[40:43], s55 offen lds\n"
"v_mfma_f32_16x16x32_bf16 a[196:199], v[72:75], v[244:247], a[196:199]\n"
"ds_read_b64_tr_b16 v[62:63], v163 offset:7168\n"
"v_mfma_f32_16x16x32_bf16 a[200:203], v[76:79], v[240:243], a[200:203]\n"
"ds_read_b64_tr_b16 v[64:65], v162 offset:256\n"
"v_mfma_f32_16x16x32_bf16 a[204:207], v[76:79], v[244:247], a[204:207]\n"
"s_waitcnt lgkmcnt(9)\n"
"ds_read_b64_tr_b16 v[66:67], v162 offset:7424\n"
"s_add_u32 m0, m0, 0x400\n"
"v_mfma_f32_16x16x32_bf16 a[208:211], v[80:83], v[240:243], a[208:211]\n"
"ds_read_b64_tr_b16 v[68:69], v163 offset:256\n"
"buffer_load_dwordx4 v153, s[40:43], s55 offen lds\n"
"v_mfma_f32_16x16x32_bf16 a[212:215], v[80:83], v[244:247], a[212:215]\n"
"ds_read_b64_tr_b16 v[70:71], v163 offset:7424\n"
"v_mfma_f32_16x16x32_bf16 a[216:219], v[84:87], v[240:243], a[216:219]\n"
"ds_read_b64_tr_b16 v[72:73], v162 offset:512\n"
"v_mfma_f32_16x16x32_bf16 a[220:223], v[84:87], v[244:247], a[220:223]\n"
"s_waitcnt lgkmcnt(9)\n"
"ds_read_b64_tr_b16 v[74:75], v162 offset:7680\n"
"s_add_u32 m0, m0, 0x400\n"
"v_mfma_f32_16x16x32_bf16 a[224:227], v[88:91], v[240:243], a[224:227]\n"
"ds_read_b64_tr_b16 v[76:77], v163 offset:512\n"
"buffer_load_dwordx4 v154, s[40:43], s55 offen lds\n"
"v_mfma_f32_16x16x32_bf16 a[228:231], v[88:91], v[244:247], a[228:231]\n"
"ds_read_b64_tr_b16 v[78:79], v163 offset:7680\n"
"v_mfma_f32_16x16x32_bf16 a[232:235], v[92:95], v[240:243], a[232:235]\n"
"ds_read_b64_tr_b16 v[80:81], v162 offset:768\n"
"v_mfma_f32_16x16x32_bf16 a[236:239], v[92:95], v[244:247], a[236:239]\n"
"s_waitcnt vmcnt(19)\n"
"s_waitcnt lgkmcnt(9)\n"
"ds_read_b64_tr_b16 v[82:83], v162 offset:7936\n"
"s_add_u32 m0, m0, 0x400\n"
"v_mfma_f32_16x16x32_bf16 a[128:131], v[56:59], a[0:3], a[128:131]\n"
"ds_read_b64_tr_b16 v[84:85], v163 offset:768\n"
"buffer_load_dwordx4 v155, s[40:43], s55 offen lds\n"
"v_mfma_f32_16x16x32_bf16 a[132:135], v[56:59], a[4:7], a[132:135]\n"
"ds_read_b64_tr_b16 v[86:87], v163 offset:7936\n"
"v_mfma_f32_16x16x32_bf16 a[136:139], v[60:63], a[0:3], a[136:139]\n"
"ds_read_b64_tr_b16 v[88:89], v162 offset:1024\n"
"v_mfma_f32_16x16x32_bf16 a[140:143], v[60:63], a[4:7], a[140:143]\n"
"s_waitcnt lgkmcnt(9)\n"
"ds_read_b64_tr_b16 v[90:91], v162 offset:8192\n"
"s_add_u32 m0, m0, 0x400\n"
"v_mfma_f32_16x16x32_bf16 a[144:147], v[64:67], a[0:3], a[144:147]\n"
"ds_read_b64_tr_b16 v[92:93], v163 offset:1024\n"
"buffer_load_dwordx4 v156, s[40:43], s55 offen lds\n"
"v_mfma_f32_16x16x32_bf16 a[148:151], v[64:67], a[4:7], a[148:151]\n"
"ds_read_b64_tr_b16 v[94:95], v163 offset:8192\n"
"v_mfma_f32_16x16x32_bf16 a[152:155], v[68:71], a[0:3], a[152:155]\n"
"ds_read_b64_tr_b16 v[56:57], v162 offset:1280\n"
"v_mfma_f32_16x16x32_bf16 a[156:159], v[68:71], a[4:7], a[156:159]\n"
"s_waitcnt lgkmcnt(9)\n"
"ds_read_b64_tr_b16 v[58:59], v162 offset:8448\n"
"s_add_u32 m0, m0, 0x400\n"
"v_mfma_f32_16x16x32_bf16 a[160:163], v[72:75], a[0:3], a[160:163]\n"
"ds_read_b64_tr_b16 v[60:61], v163 offset:1280\n"
"buffer_load_dwordx4 v157, s[40:43], s55 offen lds\n"
"v_mfma_f32_16x16x32_bf16 a[164:167], v[72:75], a[4:7], a[164:167]\n"
"ds_read_b64_tr_b16 v[62:63], v163 offset:8448\n"
"v_mfma_f32_16x16x32_bf16 a[168:171], v[76:79], a[0:3], a[168:171]\n"
"ds_read_b64_tr_b16 v[64:65], v162 offset:1536\n"
"v_mfma_f32_16x16x32_bf16 a[172:175], v[76:79], a[4:7], a[172:175]\n"
"s_waitcnt lgkmcnt(9)\n"
"ds_read_b64_tr_b16 v[66:67], v162 offset:8704\n"
"s_add_u32 m0, m0, 0x400\n"
"v_mfma_f32_16x16x32_bf16 a[176:179], v[80:83], a[0:3], a[176:179]\n"
"ds_read_b64_tr_b16 v[68:69], v163 offset:1536\n"
"buffer_load_dwordx4 v158, s[40:43], s55 offen lds\n"
"v_mfma_f32_16x16x32_bf16 a[180:183], v[80:83], a[4:7], a[180:183]\n"
"ds_read_b64_tr_b16 v[70:71], v163 offset:8704\n"
"v_mfma_f32_16x16x32_bf16 a[184:187], v[84:87], a[0:3], a[184:187]\n"
"ds_read_b64_tr_b16 v[72:73], v162 offset:14336\n"
"v_mfma_f32_16x16x32_bf16 a[188:191], v[84:87], a[4:7], a[188:191]\n"
"s_waitcnt lgkmcnt(9)\n"
"ds_read_b64_tr_b16 v[74:75], v162 offset:21504\n"
"s_add_u32 s55, s55, 57344\n"
"s_add_u32 s56, s56, 28672\n"
"s_sub_u32 s96, s56, 143360\n"
"s_cmp_ge_u32 s56, 143360\n"
"s_cselect_b32 s56, s96, s56\n"
"v_mfma_f32_16x16x32_bf16 a[192:195], v[88:91], a[0:3], a[192:195]\n"
"ds_read_b64_tr_b16 v[76:77], v163 offset:14336\n"
"v_mfma_f32_16x16x32_bf16 a[196:199], v[88:91], a[4:7], a[196:199]\n"
"ds_read_b64_tr_b16 v[78:79], v163 offset:21504\n"
"v_mfma_f32_16x16x32_bf16 a[200:203], v[92:95], a[0:3], a[200:203]\n"
"ds_read_b64_tr_b16 v[80:81], v162 offset:14592\n"
"v_mfma_f32_16x16x32_bf16 a[204:207], v[92:95], a[4:7], a[204:207]\n"
"s_waitcnt lgkmcnt(9)\n"
"ds_read_b64_tr_b16 v[82:83], v162 offset:21760\n"
"v_mfma_f32_16x16x32_bf16 a[208:211], v[56:59], a[0:3], a[208:211]\n"
"ds_read_b64_tr_b16 v[84:85], v163 offset:14592\n"
"v_mfma_f32_16x16x32_bf16 a[212:215], v[56:59], a[4:7], a[212:215]\n"
"ds_read_b64_tr_b16 v[86:87], v163 offset:21760\n"
"v_mfma_f32_16x16x32_bf16 a[216:219], v[60:63], a[0:3], a[216:219]\n"
"ds_read_b64_tr_b16 v[88:89], v162 offset:14848\n"
"v_mfma_f32_16x16x32_bf16 a[220:223], v[60:63], a[4:7], a[220:223]\n"
"s_waitcnt lgkmcnt(9)\n"
"ds_read_b64_tr_b16 v[90:91], v162 offset:22016\n"
"v_mfma_f32_16x16x32_bf16 a[224:227], v[64:67], a[0:3], a[224:227]\n"
"ds_read_b64_tr_b16 v[92:93], v163 offset:14848\n"
"v_mfma_f32_16x16x32_bf16 a[228:231], v[64:67], a[4:7], a[228:231]\n"
"ds_read_b64_tr_b16 v[94:95], v163 offset:22016\n"
"v_mfma_f32_16x16x32_bf16 a[232:235], v[68:71], a[0:3], a[232:235]\n"
"ds_read_b64_tr_b16 v[56:57], v162 offset:15104\n"
"v_mfma_f32_16x16x32_bf16 a[236:239], v[68:71], a[4:7], a[236:239]\n"
"s_waitcnt vmcnt(20)\n"
"s_waitcnt lgkmcnt(9)\n"
"ds_read_b64_tr_b16 v[58:59], v162 offset:22272\n"
"v_mfma_f32_16x16x32_bf16 a[128:131], v[72:75], a[8:11], a[128:131]\n"
"ds_read_b64_tr_b16 v[60:61], v163 offset:15104\n"
"v_mfma_f32_16x16x32_bf16 a[132:135], v[72:75], a[12:15], a[132:135]\n"
"ds_read_b64_tr_b16 v[62:63], v163 offset:22272\n"
"v_mfma_f32_16x16x32_bf16 a[136:139], v[76:79], a[8:11], a[136:139]\n"
"ds_read_b64_tr_b16 v[64:65], v162 offset:15360\n"
"v_mfma_f32_16x16x32_bf16 a[140:143], v[76:79], a[12:15], a[140:143]\n"
"s_waitcnt lgkmcnt(9)\n"
"ds_read_b64_tr_b16 v[66:67], v162 offset:22528\n"
"v_mfma_f32_16x16x32_bf16 a[144:147], v[80:83], a[8:11], a[144:147]\n"
"ds_read_b64_tr_b16 v[68:69], v163 offset:15360\n"
"v_mfma_f32_16x16x32_bf16 a[148:151], v[80:83], a[12:15], a[148:151]\n"
"ds_read_b64_tr_b16 v[70:71], v163 offset:22528\n"
"v_mfma_f32_16x16x32_bf16 a[152:155], v[84:87], a[8:11], a[152:155]\n"
"ds_read_b64_tr_b16 v[72:73], v162 offset:15616\n"
"v_mfma_f32_16x16x32_bf16 a[156:159], v[84:87], a[12:15], a[156:159]\n"
"s_waitcnt lgkmcnt(9)\n"
"ds_read_b64_tr_b16 v[74:75], v162 offset:22784\n"
"v_mfma_f32_16x16x32_bf16 a[160:163], v[88:91], a[8:11], a[160:163]\n"
"ds_read_b64_tr_b16 v[76:77], v163 offset:15616\n"
"v_mfma_f32_16x16x32_bf16 a[164:167], v[88:91], a[12:15], a[164:167]\n"
"ds_read_b64_tr_b16 v[78:79], v163 offset:22784\n"
"v_mfma_f32_16x16x32_bf16 a[168:171], v[92:95], a[8:11], a[168:171]\n"
"ds_read_b64_tr_b16 v[80:81], v162 offset:15872\n"
"v_mfma_f32_16x16x32_bf16 a[172:175], v[92:95], a[12:15], a[172:175]\n"
"s_waitcnt lgkmcnt(9)\n"
"ds_read_b64_tr_b16 v[82:83], v162 offset:23040\n"
"v_mfma_f32_16x16x32_bf16 a[176:179], v[56:59], a[8:11], a[176:179]\n"
"ds_read_b64_tr_b16 v[84:85], v163 offset:15872\n"
"v_mfma_f32_16x16x32_bf16 a[180:183], v[56:59], a[12:15], a[180:183]\n"
"ds_read_b64_tr_b16 v[86:87], v163 offset:23040\n"
"s_waitcnt vmcnt(31)\n"
"s_barrier\n"
"v_add_u32 v162, s59, v160\n"
"v_add_u32 v163, s59, v161\n"
"s_add_u32 s59, s59, 28672\n"
"s_sub_u32 s96, s59, 143360\n"
"s_cmp_ge_u32 s59, 143360\n"
"s_cselect_b32 s59, s96, s59\n"
"v_mfma_f32_16x16x32_bf16 a[184:187], v[60:63], a[8:11], a[184:187]\n"
"ds_read_b64_tr_b16 v[88:89], v162 offset:0\n"
"v_mfma_f32_16x16x32_bf16 a[188:191], v[60:63], a[12:15], a[188:191]\n"
"s_waitcnt lgkmcnt(9)\n"
"ds_read_b64_tr_b16 v[90:91], v162 offset:7168\n"
"v_mfma_f32_16x16x32_bf16 a[192:195], v[64:67], a[8:11], a[192:195]\n"
"ds_read_b64_tr_b16 v[92:93], v163 offset:0\n"
"v_mfma_f32_16x16x32_bf16 a[196:199], v[64:67], a[12:15], a[196:199]\n"
"ds_read_b64_tr_b16 v[94:95], v163 offset:7168\n"
"v_mfma_f32_16x16x32_bf16 a[200:203], v[68:71], a[8:11], a[200:203]\n"
"ds_read_b64_tr_b16 v[56:57], v162 offset:256\n"
"v_mfma_f32_16x16x32_bf16 a[204:207], v[68:71], a[12:15], a[204:207]\n"
"s_waitcnt lgkmcnt(9)\n"
"ds_read_b64_tr_b16 v[58:59], v162 offset:7424\n"
"v_mfma_f32_16x16x32_bf16 a[208:211], v[72:75], a[8:11], a[208:211]\n"
"ds_read_b64_tr_b16 v[60:61], v163 offset:256\n"
"v_mfma_f32_16x16x32_bf16 a[212:215], v[72:75], a[12:15], a[212:215]\n"
"ds_read_b64_tr_b16 v[62:63], v163 offset:7424\n"
"v_mfma_f32_16x16x32_bf16 a[216:219], v[76:79], a[8:11], a[216:219]\n"
"ds_read_b64_tr_b16 v[64:65], v162 offset:512\n"
"v_mfma_f32_16x16x32_bf16 a[220:223], v[76:79], a[12:15], a[220:223]\n"
"s_waitcnt lgkmcnt(9)\n"
"ds_read_b64_tr_b16 v[66:67], v162 offset:7680\n"
"v_mfma_f32_16x16x32_bf16 a[224:227], v[80:83], a[8:11], a[224:227]\n"
"ds_read_b64_tr_b16 v[68:69], v163 offset:512\n"
"v_mfma_f32_16x16x32_bf16 a[228:231], v[80:83], a[12:15], a[228:231]\n"
"ds_read_b64_tr_b16 v[70:71], v163 offset:7680\n"
"v_mfma_f32_16x16x32_bf16 a[232:235], v[84:87], a[8:11], a[232:235]\n"
"ds_read_b64_tr_b16 v[72:73], v162 offset:768\n"
"v_mfma_f32_16x16x32_bf16 a[236:239], v[84:87], a[12:15], a[236:239]\n"
"s_waitcnt vmcnt(18)\n"
"s_waitcnt lgkmcnt(9)\n"
"ds_read_b64_tr_b16 v[74:75], v162 offset:7936\n"
"v_mfma_f32_16x16x32_bf16 a[128:131], v[88:91], a[16:19], a[128:131]\n"
"ds_read_b64_tr_b16 v[76:77], v163 offset:768\n"
"v_mfma_f32_16x16x32_bf16 a[132:135], v[88:91], a[20:23], a[132:135]\n"
"ds_read_b64_tr_b16 v[78:79], v163 offset:7936\n"
"v_mfma_f32_16x16x32_bf16 a[136:139], v[92:95], a[16:19], a[136:139]\n"
"ds_read_b64_tr_b16 v[80:81], v162 offset:1024\n"
"v_mfma_f32_16x16x32_bf16 a[140:143], v[92:95], a[20:23], a[140:143]\n"
"s_waitcnt lgkmcnt(9)\n"
"ds_read_b64_tr_b16 v[82:83], v162 offset:8192\n"
"v_mfma_f32_16x16x32_bf16 a[144:147], v[56:59], a[16:19], a[144:147]\n"
"ds_read_b64_tr_b16 v[84:85], v163 offset:1024\n"
"v_mfma_f32_16x16x32_bf16 a[148:151], v[56:59], a[20:23], a[148:151]\n"
"ds_read_b64_tr_b16 v[86:87], v163 offset:8192\n"
"v_mfma_f32_16x16x32_bf16 a[152:155], v[60:63], a[16:19], a[152:155]\n"
"ds_read_b64_tr_b16 v[88:89], v162 offset:1280\n"
"v_mfma_f32_16x16x32_bf16 a[156:159], v[60:63], a[20:23], a[156:159]\n"
"s_waitcnt lgkmcnt(9)\n"
"ds_read_b64_tr_b16 v[90:91], v162 offset:8448\n"
"v_mfma_f32_16x16x32_bf16 a[160:163], v[64:67], a[16:19], a[160:163]\n"
"ds_read_b64_tr_b16 v[92:93], v163 offset:1280\n"
"v_mfma_f32_16x16x32_bf16 a[164:167], v[64:67], a[20:23], a[164:167]\n"
"ds_read_b64_tr_b16 v[94:95], v163 offset:8448\n"
"v_mfma_f32_16x16x32_bf16 a[168:171], v[68:71], a[16:19], a[168:171]\n"
"ds_read_b64_tr_b16 v[56:57], v162 offset:1536\n"
"v_mfma_f32_16x16x32_bf16 a[172:175], v[68:71], a[20:23], a[172:175]\n"
"s_waitcnt lgkmcnt(9)\n"
"ds_read_b64_tr_b16 v[58:59], v162 offset:8704\n"
"v_mfma_f32_16x16x32_bf16 a[176:179], v[72:75], a[16:19], a[176:179]\n"
"ds_read_b64_tr_b16 v[60:61], v163 offset:1536\n"
"v_mfma_f32_16x16x32_bf16 a[180:183], v[72:75], a[20:23], a[180:183]\n"
"ds_read_b64_tr_b16 v[62:63], v163 offset:8704\n"
"v_mfma_f32_16x16x32_bf16 a[184:187], v[76:79], a[16:19], a[184:187]\n"
"ds_read_b64_tr_b16 v[64:65], v162 offset:14336\n"
"v_mfma_f32_16x16x32_bf16 a[188:191], v[76:79], a[20:23], a[188:191]\n"
"s_waitcnt lgkmcnt(9)\n"
"ds_read_b64_tr_b16 v[66:67], v162 offset:21504\n"
"v_mfma_f32_16x16x32_bf16 a[192:195], v[80:83], a[16:19], a[192:195]\n"
"ds_read_b64_tr_b16 v[68:69], v163 offset:14336\n"
"v_mfma_f32_16x16x32_bf16 a[196:199], v[80:83], a[20:23], a[196:199]\n"
"ds_read_b64_tr_b16 v[70:71], v163 offset:21504\n"
"v_mfma_f32_16x16x32_bf16 a[200:203], v[84:87], a[16:19], a[200:203]\n"
"ds_read_b64_tr_b16 v[72:73], v162 offset:14592\n"
"v_mfma_f32_16x16x32_bf16 a[204:207], v[84:87], a[20:23], a[204:207]\n"
"s_waitcnt lgkmcnt(9)\n"
"ds_read_b64_tr_b16 v[74:75], v162 offset:21760\n"
"v_mfma_f32_16x16x32_bf16 a[208:211], v[88:91], a[16:19], a[208:211]\n"
"ds_read_b64_tr_b16 v[76:77], v163 offset:14592\n"
"v_mfma_f32_16x16x32_bf16 a[212:215], v[88:91], a[20:23], a[212:215]\n"
"ds_read_b64_tr_b16 v[78:79], v163 offset:21760\n"
"v_mfma_f32_16x16x32_bf16 a[216:219], v[92:95], a[16:19], a[216:219]\n"
"ds_read_b64_tr_b16 v[80:81], v162 offset:14848\n"
"v_mfma_f32_16x16x32_bf16 a[220:223], v[92:95], a[20:23], a[220:223]\n"
"s_waitcnt lgkmcnt(9)\n"
"ds_read_b64_tr_b16 v[82:83], v162 offset:22016\n"
"v_mfma_f32_16x16x32_bf16 a[224:227], v[56:59], a[16:19], a[224:227]\n"
"ds_read_b64_tr_b16 v[84:85], v163 offset:14848\n"
"v_mfma_f32_16x16x32_bf16 a[228:231], v[56:59], a[20:23], a[228:231]\n"
"ds_read_b64_tr_b16 v[86:87], v163 offset:22016\n"
"v_mfma_f32_16x16x32_bf16 a[232:235], v[60:63], a[16:19], a[232:235]\n"
"ds_read_b64_tr_b16 v[88:89], v162 offset:15104\n"
"v_mfma_f32_16x16x32_bf16 a[236:239], v[60:63], a[20:23], a[236:239]\n"
"s_waitcnt vmcnt(15)\n"
"s_waitcnt lgkmcnt(9)\n"
"ds_read_b64_tr_b16 v[90:91], v162 offset:22272\n"
"v_mfma_f32_16x16x32_bf16 a[128:131], v[64:67], a[24:27], a[128:131]\n"
"ds_read_b64_tr_b16 v[92:93], v163 offset:15104\n"
"v_mfma_f32_16x16x32_bf16 a[132:135], v[64:67], a[28:31], a[132:135]\n"
"ds_read_b64_tr_b16 v[94:95], v163 offset:22272\n"
"v_mfma_f32_16x16x32_bf16 a[136:139], v[68:71], a[24:27], a[136:139]\n"
"ds_read_b64_tr_b16 v[56:57], v162 offset:15360\n"
"v_mfma_f32_16x16x32_bf16 a[140:143], v[68:71], a[28:31], a[140:143]\n"
"s_waitcnt lgkmcnt(9)\n"
"ds_read_b64_tr_b16 v[58:59], v162 offset:22528\n"
"v_mfma_f32_16x16x32_bf16 a[144:147], v[72:75], a[24:27], a[144:147]\n"
"ds_read_b64_tr_b16 v[60:61], v163 offset:15360\n"
"v_mfma_f32_16x16x32_bf16 a[148:151], v[72:75], a[28:31], a[148:151]\n"
"ds_read_b64_tr_b16 v[62:63], v163 offset:22528\n"
"v_mfma_f32_16x16x32_bf16 a[152:155], v[76:79], a[24:27], a[152:155]\n"
"ds_read_b64_tr_b16 v[64:65], v162 offset:15616\n"
"v_mfma_f32_16x16x32_bf16 a[156:159], v[76:79], a[28:31], a[156:159]\n"
"s_waitcnt lgkmcnt(9)\n"
"ds_read_b64_tr_b16 v[66:67], v162 offset:22784\n"
"v_mfma_f32_16x16x32_bf16 a[160:163], v[80:83], a[24:27], a[160:163]\n"
"ds_read_b64_tr_b16 v[68:69], v163 offset:15616\n"
"v_mfma_f32_16x16x32_bf16 a[164:167], v[80:83], a[28:31], a[164:167]\n"
"ds_read_b64_tr_b16 v[70:71], v163 offset:22784\n"
"v_mfma_f32_16x16x32_bf16 a[168:171], v[84:87], a[24:27], a[168:171]\n"
"ds_read_b64_tr_b16 v[72:73], v162 offset:15872\n"
"v_mfma_f32_16x16x32_bf16 a[172:175], v[84:87], a[28:31], a[172:175]\n"
"s_waitcnt lgkmcnt(9)\n"
"ds_read_b64_tr_b16 v[74:75], v162 offset:23040\n"
"v_mfma_f32_16x16x32_bf16 a[176:179], v[88:91], a[24:27], a[176:179]\n"
"ds_read_b64_tr_b16 v[76:77], v163 offset:15872\n"
"v_mfma_f32_16x16x32_bf16 a[180:183], v[88:91], a[28:31], a[180:183]\n"
"ds_read_b64_tr_b16 v[78:79], v163 offset:23040\n"
"s_waitcnt vmcnt(17)\n"
"s_barrier\n"
"v_add_u32 v162, s59, v160\n"
"v_add_u32 v163, s59, v161\n"
"s_add_u32 s59, s59, 28672\n"
"s_sub_u32 s96, s59, 143360\n"
"s_cmp_ge_u32 s59, 143360\n"
"s_cselect_b32 s59, s96, s59\n"
"v_mfma_f32_16x16x32_bf16 a[184:187], v[92:95], a[24:27], a[184:187]\n"
"ds_read_b64_tr_b16 v[80:81], v162 offset:0\n"
"v_mfma_f32_16x16x32_bf16 a[188:191], v[92:95], a[28:31], a[188:191]\n"
"s_waitcnt lgkmcnt(9)\n"
"ds_read_b64_tr_b16 v[82:83], v162 offset:7168\n"
"v_mfma_f32_16x16x32_bf16 a[192:195], v[56:59], a[24:27], a[192:195]\n"
"ds_read_b64_tr_b16 v[84:85], v163 offset:0\n"
"v_mfma_f32_16x16x32_bf16 a[196:199], v[56:59], a[28:31], a[196:199]\n"
"ds_read_b64_tr_b16 v[86:87], v163 offset:7168\n"
"v_mfma_f32_16x16x32_bf16 a[200:203], v[60:63], a[24:27], a[200:203]\n"
"ds_read_b64_tr_b16 v[88:89], v162 offset:256\n"
"v_mfma_f32_16x16x32_bf16 a[204:207], v[60:63], a[28:31], a[204:207]\n"
"s_waitcnt lgkmcnt(9)\n"
"ds_read_b64_tr_b16 v[90:91], v162 offset:7424\n"
"v_mfma_f32_16x16x32_bf16 a[208:211], v[64:67], a[24:27], a[208:211]\n"
"ds_read_b64_tr_b16 v[92:93], v163 offset:256\n"
"v_mfma_f32_16x16x32_bf16 a[212:215], v[64:67], a[28:31], a[212:215]\n"
"ds_read_b64_tr_b16 v[94:95], v163 offset:7424\n"
"v_mfma_f32_16x16x32_bf16 a[216:219], v[68:71], a[24:27], a[216:219]\n"
"ds_read_b64_tr_b16 v[56:57], v162 offset:512\n"
"v_mfma_f32_16x16x32_bf16 a[220:223], v[68:71], a[28:31], a[220:223]\n"
"s_waitcnt lgkmcnt(9)\n"
"ds_read_b64_tr_b16 v[58:59], v162 offset:7680\n"
"v_mfma_f32_16x16x32_bf16 a[224:227], v[72:75], a[24:27], a[224:227]\n"
"ds_read_b64_tr_b16 v[60:61], v163 offset:512\n"
"v_mfma_f32_16x16x32_bf16 a[228:231], v[72:75], a[28:31], a[228:231]\n"
"ds_read_b64_tr_b16 v[62:63], v163 offset:7680\n"
"v_mfma_f32_16x16x32_bf16 a[232:235], v[76:79], a[24:27], a[232:235]\n"
"ds_read_b64_tr_b16 v[64:65], v162 offset:768\n"
"v_mfma_f32_16x16x32_bf16 a[236:239], v[76:79], a[28:31], a[236:239]\n"
"s_waitcnt vmcnt(13)\n"
"s_waitcnt lgkmcnt(9)\n"
"ds_read_b64_tr_b16 v[66:67], v162 offset:7936\n"
"v_mfma_f32_16x16x32_bf16 a[128:131], v[80:83], a[32:35], a[128:131]\n"
"ds_read_b64_tr_b16 v[68:69], v163 offset:768\n"
"v_mfma_f32_16x16x32_bf16 a[132:135], v[80:83], a[36:39], a[132:135]\n"
"ds_read_b64_tr_b16 v[70:71], v163 offset:7936\n"
"v_mfma_f32_16x16x32_bf16 a[136:139], v[84:87], a[32:35], a[136:139]\n"
"ds_read_b64_tr_b16 v[72:73], v162 offset:1024\n"
"v_mfma_f32_16x16x32_bf16 a[140:143], v[84:87], a[36:39], a[140:143]\n"
"s_waitcnt lgkmcnt(9)\n"
"ds_read_b64_tr_b16 v[74:75], v162 offset:8192\n"
"v_mfma_f32_16x16x32_bf16 a[144:147], v[88:91], a[32:35], a[144:147]\n"
"ds_read_b64_tr_b16 v[76:77], v163 offset:1024\n"
"v_mfma_f32_16x16x32_bf16 a[148:151], v[88:91], a[36:39], a[148:151]\n"
"ds_read_b64_tr_b16 v[78:79], v163 offset:8192\n"
"v_mfma_f32_16x16x32_bf16 a[152:155], v[92:95], a[32:35], a[152:155]\n"
"ds_read_b64_tr_b16 v[80:81], v162 offset:1280\n"
"v_mfma_f32_16x16x32_bf16 a[156:159], v[92:95], a[36:39], a[156:159]\n"
"s_waitcnt lgkmcnt(9)\n"
"ds_read_b64_tr_b16 v[82:83], v162 offset:8448\n"
"v_mfma_f32_16x16x32_bf16 a[160:163], v[56:59], a[32:35], a[160:163]\n"
"ds_read_b64_tr_b16 v[84:85], v163 offset:1280\n"
"v_mfma_f32_16x16x32_bf16 a[164:167], v[56:59], a[36:39], a[164:167]\n"
"ds_read_b64_tr_b16 v[86:87], v163 offset:8448\n"
"v_mfma_f32_16x16x32_bf16 a[168:171], v[60:63], a[32:35], a[168:171]\n"
"ds_read_b64_tr_b16 v[88:89], v162 offset:1536\n"
"v_mfma_f32_16x16x32_bf16 a[172:175], v[60:63], a[36:39], a[172:175]\n"
"s_waitcnt lgkmcnt(9)\n"
"ds_read_b64_tr_b16 v[90:91], v162 offset:8704\n"
"v_mfma_f32_16x16x32_bf16 a[176:179], v[64:67], a[32:35], a[176:179]\n"
"ds_read_b64_tr_b16 v[92:93], v163 offset:1536\n"
"v_mfma_f32_16x16x32_bf16 a[180:183], v[64:67], a[36:39], a[180:183]\n"
"ds_read_b64_tr_b16 v[94:95], v163 offset:8704\n"
"v_mfma_f32_16x16x32_bf16 a[184:187], v[68:71], a[32:35], a[184:187]\n"
"ds_read_b64_tr_b16 v[56:57], v162 offset:14336\n"
"v_mfma_f32_16x16x32_bf16 a[188:191], v[68:71], a[36:39], a[188:191]\n"
"s_waitcnt lgkmcnt(9)\n"
"ds_read_b64_tr_b16 v[58:59], v162 offset:21504\n"
"v_mfma_f32_16x16x32_bf16 a[192:195], v[72:75], a[32:35], a[192:195]\n"
"ds_read_b64_tr_b16 v[60:61], v163 offset:14336\n"
"v_mfma_f32_16x16x32_bf16 a[196:199], v[72:75], a[36:39], a[196:199]\n"
"ds_read_b64_tr_b16 v[62:63], v163 offset:21504\n"
"v_mfma_f32_16x16x32_bf16 a[200:203], v[76:79], a[32:35], a[200:203]\n"
"ds_read_b64_tr_b16 v[64:65], v162 offset:14592\n"
"v_mfma_f32_16x16x32_bf16 a[204:207], v[76:79], a[36:39], a[204:207]\n"
"s_waitcnt lgkmcnt(9)\n"
"ds_read_b64_tr_b16 v[66:67], v162 offset:21760\n"
"v_mfma_f32_16x16x32_bf16 a[208:211], v[80:83], a[32:35], a[208:211]\n"
"ds_read_b64_tr_b16 v[68:69], v163 offset:14592\n"
"v_mfma_f32_16x16x32_bf16 a[212:215], v[80:83], a[36:39], a[212:215]\n"
"ds_read_b64_tr_b16 v[70:71], v163 offset:21760\n"
"v_mfma_f32_16x16x32_bf16 a[216:219], v[84:87], a[32:35], a[216:219]\n"
"ds_read_b64_tr_b16 v[72:73], v162 offset:14848\n"
"v_mfma_f32_16x16x32_bf16 a[220:223], v[84:87], a[36:39], a[220:223]\n"
"s_waitcnt lgkmcnt(9)\n"
"ds_read_b64_tr_b16 v[74:75], v162 offset:22016\n"
"v_mfma_f32_16x16x32_bf16 a[224:227], v[88:91], a[32:35], a[224:227]\n"
"ds_read_b64_tr_b16 v[76:77], v163 offset:14848\n"
"v_mfma_f32_16x16x32_bf16 a[228:231], v[88:91], a[36:39], a[228:231]\n"
"ds_read_b64_tr_b16 v[78:79], v163 offset:22016\n"
"v_mfma_f32_16x16x32_bf16 a[232:235], v[92:95], a[32:35], a[232:235]\n"
"ds_read_b64_tr_b16 v[80:81], v162 offset:15104\n"
"v_mfma_f32_16x16x32_bf16 a[236:239], v[92:95], a[36:39], a[236:239]\n"
"s_waitcnt vmcnt(11)\n"
"s_waitcnt lgkmcnt(9)\n"
"ds_read_b64_tr_b16 v[82:83], v162 offset:22272\n"
"v_mfma_f32_16x16x32_bf16 a[128:131], v[56:59], a[40:43], a[128:131]\n"
"ds_read_b64_tr_b16 v[84:85], v163 offset:15104\n"
"v_mfma_f32_16x16x32_bf16 a[132:135], v[56:59], a[44:47], a[132:135]\n"
"ds_read_b64_tr_b16 v[86:87], v163 offset:22272\n"
"v_mfma_f32_16x16x32_bf16 a[136:139], v[60:63], a[40:43], a[136:139]\n"
"ds_read_b64_tr_b16 v[88:89], v162 offset:15360\n"
"v_mfma_f32_16x16x32_bf16 a[140:143], v[60:63], a[44:47], a[140:143]\n"
"s_waitcnt lgkmcnt(9)\n"
"ds_read_b64_tr_b16 v[90:91], v162 offset:22528\n"
"v_mfma_f32_16x16x32_bf16 a[144:147], v[64:67], a[40:43], a[144:147]\n"
"ds_read_b64_tr_b16 v[92:93], v163 offset:15360\n"
"v_mfma_f32_16x16x32_bf16 a[148:151], v[64:67], a[44:47], a[148:151]\n"
"ds_read_b64_tr_b16 v[94:95], v163 offset:22528\n"
"v_mfma_f32_16x16x32_bf16 a[152:155], v[68:71], a[40:43], a[152:155]\n"
"ds_read_b64_tr_b16 v[56:57], v162 offset:15616\n"
"v_mfma_f32_16x16x32_bf16 a[156:159], v[68:71], a[44:47], a[156:159]\n"
"s_waitcnt lgkmcnt(9)\n"
"ds_read_b64_tr_b16 v[58:59], v162 offset:22784\n"
"v_mfma_f32_16x16x32_bf16 a[160:163], v[72:75], a[40:43], a[160:163]\n"
"ds_read_b64_tr_b16 v[60:61], v163 offset:15616\n"
"v_mfma_f32_16x16x32_bf16 a[164:167], v[72:75], a[44:47], a[164:167]\n"
"ds_read_b64_tr_b16 v[62:63], v163 offset:22784\n"
"v_mfma_f32_16x16x32_bf16 a[168:171], v[76:79], a[40:43], a[168:171]\n"
"ds_read_b64_tr_b16 v[64:65], v162 offset:15872\n"
"v_mfma_f32_16x16x32_bf16 a[172:175], v[76:79], a[44:47], a[172:175]\n"
"s_waitcnt lgkmcnt(9)\n"
"ds_read_b64_tr_b16 v[66:67], v162 offset:23040\n"
"v_mfma_f32_16x16x32_bf16 a[176:179], v[80:83], a[40:43], a[176:179]\n"
"ds_read_b64_tr_b16 v[68:69], v163 offset:15872\n"
"v_mfma_f32_16x16x32_bf16 a[180:183], v[80:83], a[44:47], a[180:183]\n"
"ds_read_b64_tr_b16 v[70:71], v163 offset:23040\n"
"s_waitcnt vmcnt(0)\n"
"s_barrier\n"
"v_add_u32 v162, s59, v160\n"
"v_add_u32 v163, s59, v161\n"
"s_add_u32 s59, s59, 28672\n"
"s_sub_u32 s96, s59, 143360\n"
"s_cmp_ge_u32 s59, 143360\n"
"s_cselect_b32 s59, s96, s59\n"
"v_mfma_f32_16x16x32_bf16 a[184:187], v[84:87], a[40:43], a[184:187]\n"
"ds_read_b64_tr_b16 v[72:73], v162 offset:0\n"
"v_mfma_f32_16x16x32_bf16 a[188:191], v[84:87], a[44:47], a[188:191]\n"
"s_waitcnt lgkmcnt(9)\n"
"ds_read_b64_tr_b16 v[74:75], v162 offset:7168\n"
"v_mfma_f32_16x16x32_bf16 a[192:195], v[88:91], a[40:43], a[192:195]\n"
"ds_read_b64_tr_b16 v[76:77], v163 offset:0\n"
"v_mfma_f32_16x16x32_bf16 a[196:199], v[88:91], a[44:47], a[196:199]\n"
"ds_read_b64_tr_b16 v[78:79], v163 offset:7168\n"
"v_mfma_f32_16x16x32_bf16 a[200:203], v[92:95], a[40:43], a[200:203]\n"
"ds_read_b64_tr_b16 v[80:81], v162 offset:256\n"
"v_mfma_f32_16x16x32_bf16 a[204:207], v[92:95], a[44:47], a[204:207]\n"
"s_waitcnt lgkmcnt(9)\n"
"ds_read_b64_tr_b16 v[82:83], v162 offset:7424\n"
"v_mfma_f32_16x16x32_bf16 a[208:211], v[56:59], a[40:43], a[208:211]\n"
"ds_read_b64_tr_b16 v[84:85], v163 offset:256\n"
"v_mfma_f32_16x16x32_bf16 a[212:215], v[56:59], a[44:47], a[212:215]\n"
"ds_read_b64_tr_b16 v[86:87], v163 offset:7424\n"
"v_mfma_f32_16x16x32_bf16 a[216:219], v[60:63], a[40:43], a[216:219]\n"
"ds_read_b64_tr_b16 v[88:89], v162 offset:512\n"
"v_mfma_f32_16x16x32_bf16 a[220:223], v[60:63], a[44:47], a[220:223]\n"
"s_waitcnt lgkmcnt(9)\n"
"ds_read_b64_tr_b16 v[90:91], v162 offset:7680\n"
"v_mfma_f32_16x16x32_bf16 a[224:227], v[64:67], a[40:43], a[224:227]\n"
"ds_read_b64_tr_b16 v[92:93], v163 offset:512\n"
"v_mfma_f32_16x16x32_bf16 a[228:231], v[64:67], a[44:47], a[228:231]\n"
"ds_read_b64_tr_b16 v[94:95], v163 offset:7680\n"
"v_mfma_f32_16x16x32_bf16 a[232:235], v[68:71], a[40:43], a[232:235]\n"
"ds_read_b64_tr_b16 v[56:57], v162 offset:768\n"
"v_mfma_f32_16x16x32_bf16 a[236:239], v[68:71], a[44:47], a[236:239]\n"
"s_waitcnt vmcnt(9)\n"
"s_waitcnt lgkmcnt(9)\n"
"ds_read_b64_tr_b16 v[58:59], v162 offset:7936\n"
"v_mfma_f32_16x16x32_bf16 a[128:131], v[72:75], a[48:51], a[128:131]\n"
"ds_read_b64_tr_b16 v[60:61], v163 offset:768\n"
"v_mfma_f32_16x16x32_bf16 a[132:135], v[72:75], a[52:55], a[132:135]\n"
"ds_read_b64_tr_b16 v[62:63], v163 offset:7936\n"
"v_mfma_f32_16x16x32_bf16 a[136:139], v[76:79], a[48:51], a[136:139]\n"
"ds_read_b64_tr_b16 v[64:65], v162 offset:1024\n"
"v_mfma_f32_16x16x32_bf16 a[140:143], v[76:79], a[52:55], a[140:143]\n"
"s_waitcnt lgkmcnt(9)\n"
"ds_read_b64_tr_b16 v[66:67], v162 offset:8192\n"
"v_mfma_f32_16x16x32_bf16 a[144:147], v[80:83], a[48:51], a[144:147]\n"
"ds_read_b64_tr_b16 v[68:69], v163 offset:1024\n"
"v_mfma_f32_16x16x32_bf16 a[148:151], v[80:83], a[52:55], a[148:151]\n"
"ds_read_b64_tr_b16 v[70:71], v163 offset:8192\n"
"v_mfma_f32_16x16x32_bf16 a[152:155], v[84:87], a[48:51], a[152:155]\n"
"ds_read_b64_tr_b16 v[72:73], v162 offset:1280\n"
"v_mfma_f32_16x16x32_bf16 a[156:159], v[84:87], a[52:55], a[156:159]\n"
"s_waitcnt lgkmcnt(9)\n"
"ds_read_b64_tr_b16 v[74:75], v162 offset:8448\n"
"v_mfma_f32_16x16x32_bf16 a[160:163], v[88:91], a[48:51], a[160:163]\n"
"ds_read_b64_tr_b16 v[76:77], v163 offset:1280\n"
"v_mfma_f32_16x16x32_bf16 a[164:167], v[88:91], a[52:55], a[164:167]\n"
"ds_read_b64_tr_b16 v[78:79], v163 offset:8448\n"
"v_mfma_f32_16x16x32_bf16 a[168:171], v[92:95], a[48:51], a[168:171]\n"
"ds_read_b64_tr_b16 v[80:81], v162 offset:1536\n"
"v_mfma_f32_16x16x32_bf16 a[172:175], v[92:95], a[52:55], a[172:175]\n"
"s_waitcnt lgkmcnt(9)\n"
"ds_read_b64_tr_b16 v[82:83], v162 offset:8704\n"
"v_mfma_f32_16x16x32_bf16 a[176:179], v[56:59], a[48:51], a[176:179]\n"
"ds_read_b64_tr_b16 v[84:85], v163 offset:1536\n"
"v_mfma_f32_16x16x32_bf16 a[180:183], v[56:59], a[52:55], a[180:183]\n"
"ds_read_b64_tr_b16 v[86:87], v163 offset:8704\n"
"v_mfma_f32_16x16x32_bf16 a[184:187], v[60:63], a[48:51], a[184:187]\n"
"ds_read_b64_tr_b16 v[88:89], v162 offset:14336\n"
"v_mfma_f32_16x16x32_bf16 a[188:191], v[60:63], a[52:55], a[188:191]\n"
"s_waitcnt lgkmcnt(9)\n"
"ds_read_b64_tr_b16 v[90:91], v162 offset:21504\n"
"v_mfma_f32_16x16x32_bf16 a[192:195], v[64:67], a[48:51], a[192:195]\n"
"ds_read_b64_tr_b16 v[92:93], v163 offset:14336\n"
"v_mfma_f32_16x16x32_bf16 a[196:199], v[64:67], a[52:55], a[196:199]\n"
"ds_read_b64_tr_b16 v[94:95], v163 offset:21504\n"
"v_mfma_f32_16x16x32_bf16 a[200:203], v[68:71], a[48:51], a[200:203]\n"
"ds_read_b64_tr_b16 v[56:57], v162 offset:14592\n"
"v_mfma_f32_16x16x32_bf16 a[204:207], v[68:71], a[52:55], a[204:207]\n"
"s_waitcnt lgkmcnt(9)\n"
"ds_read_b64_tr_b16 v[58:59], v162 offset:21760\n"
"v_mfma_f32_16x16x32_bf16 a[208:211], v[72:75], a[48:51], a[208:211]\n"
"ds_read_b64_tr_b16 v[60:61], v163 offset:14592\n"
"v_mfma_f32_16x16x32_bf16 a[212:215], v[72:75], a[52:55], a[212:215]\n"
"ds_read_b64_tr_b16 v[62:63], v163 offset:21760\n"
"v_mfma_f32_16x16x32_bf16 a[216:219], v[76:79], a[48:51], a[216:219]\n"
"ds_read_b64_tr_b16 v[64:65], v162 offset:14848\n"
"v_mfma_f32_16x16x32_bf16 a[220:223], v[76:79], a[52:55], a[220:223]\n"
"s_waitcnt lgkmcnt(9)\n"
"ds_read_b64_tr_b16 v[66:67], v162 offset:22016\n"
"v_mfma_f32_16x16x32_bf16 a[224:227], v[80:83], a[48:51], a[224:227]\n"
"ds_read_b64_tr_b16 v[68:69], v163 offset:14848\n"
"v_mfma_f32_16x16x32_bf16 a[228:231], v[80:83], a[52:55], a[228:231]\n"
"ds_read_b64_tr_b16 v[70:71], v163 offset:22016\n"
"v_mfma_f32_16x16x32_bf16 a[232:235], v[84:87], a[48:51], a[232:235]\n"
"ds_read_b64_tr_b16 v[72:73], v162 offset:15104\n"
"v_mfma_f32_16x16x32_bf16 a[236:239], v[84:87], a[52:55], a[236:239]\n"
"s_waitcnt vmcnt(7)\n"
"s_waitcnt lgkmcnt(9)\n"
"ds_read_b64_tr_b16 v[74:75], v162 offset:22272\n"
"v_mfma_f32_16x16x32_bf16 a[128:131], v[88:91], a[56:59], a[128:131]\n"
"ds_read_b64_tr_b16 v[76:77], v163 offset:15104\n"
"v_mfma_f32_16x16x32_bf16 a[132:135], v[88:91], a[60:63], a[132:135]\n"
"ds_read_b64_tr_b16 v[78:79], v163 offset:22272\n"
"v_mfma_f32_16x16x32_bf16 a[136:139], v[92:95], a[56:59], a[136:139]\n"
"ds_read_b64_tr_b16 v[80:81], v162 offset:15360\n"
"v_mfma_f32_16x16x32_bf16 a[140:143], v[92:95], a[60:63], a[140:143]\n"
"s_waitcnt lgkmcnt(9)\n"
"ds_read_b64_tr_b16 v[82:83], v162 offset:22528\n"
"v_mfma_f32_16x16x32_bf16 a[144:147], v[56:59], a[56:59], a[144:147]\n"
"ds_read_b64_tr_b16 v[84:85], v163 offset:15360\n"
"v_mfma_f32_16x16x32_bf16 a[148:151], v[56:59], a[60:63], a[148:151]\n"
"ds_read_b64_tr_b16 v[86:87], v163 offset:22528\n"
"v_mfma_f32_16x16x32_bf16 a[152:155], v[60:63], a[56:59], a[152:155]\n"
"ds_read_b64_tr_b16 v[88:89], v162 offset:15616\n"
"v_mfma_f32_16x16x32_bf16 a[156:159], v[60:63], a[60:63], a[156:159]\n"
"s_waitcnt lgkmcnt(9)\n"
"ds_read_b64_tr_b16 v[90:91], v162 offset:22784\n"
"v_mfma_f32_16x16x32_bf16 a[160:163], v[64:67], a[56:59], a[160:163]\n"
"ds_read_b64_tr_b16 v[92:93], v163 offset:15616\n"
"v_mfma_f32_16x16x32_bf16 a[164:167], v[64:67], a[60:63], a[164:167]\n"
"ds_read_b64_tr_b16 v[94:95], v163 offset:22784\n"
"v_mfma_f32_16x16x32_bf16 a[168:171], v[68:71], a[56:59], a[168:171]\n"
"ds_read_b64_tr_b16 v[56:57], v162 offset:15872\n"
"v_mfma_f32_16x16x32_bf16 a[172:175], v[68:71], a[60:63], a[172:175]\n"
"s_waitcnt lgkmcnt(9)\n"
"ds_read_b64_tr_b16 v[58:59], v162 offset:23040\n"
"v_mfma_f32_16x16x32_bf16 a[176:179], v[72:75], a[56:59], a[176:179]\n"
"ds_read_b64_tr_b16 v[60:61], v163 offset:15872\n"
"v_mfma_f32_16x16x32_bf16 a[180:183], v[72:75], a[60:63], a[180:183]\n"
"ds_read_b64_tr_b16 v[62:63], v163 offset:23040\n"
"v_mfma_f32_16x16x32_bf16 a[184:187], v[76:79], a[56:59], a[184:187]\n"
"v_mfma_f32_16x16x32_bf16 a[188:191], v[76:79], a[60:63], a[188:191]\n"
"s_waitcnt lgkmcnt(8)\n"
"v_mfma_f32_16x16x32_bf16 a[192:195], v[80:83], a[56:59], a[192:195]\n"
"v_mfma_f32_16x16x32_bf16 a[196:199], v[80:83], a[60:63], a[196:199]\n"
"v_mfma_f32_16x16x32_bf16 a[200:203], v[84:87], a[56:59], a[200:203]\n"
"v_mfma_f32_16x16x32_bf16 a[204:207], v[84:87], a[60:63], a[204:207]\n"
"s_waitcnt lgkmcnt(4)\n"
"v_mfma_f32_16x16x32_bf16 a[208:211], v[88:91], a[56:59], a[208:211]\n"
"v_mfma_f32_16x16x32_bf16 a[212:215], v[88:91], a[60:63], a[212:215]\n"
"v_mfma_f32_16x16x32_bf16 a[216:219], v[92:95], a[56:59], a[216:219]\n"
"v_mfma_f32_16x16x32_bf16 a[220:223], v[92:95], a[60:63], a[220:223]\n"
"s_waitcnt lgkmcnt(0)\n"
"v_mfma_f32_16x16x32_bf16 a[224:227], v[56:59], a[56:59], a[224:227]\n"
"v_mfma_f32_16x16x32_bf16 a[228:231], v[56:59], a[60:63], a[228:231]\n"
"v_mfma_f32_16x16x32_bf16 a[232:235], v[60:63], a[56:59], a[232:235]\n"
"v_mfma_f32_16x16x32_bf16 a[236:239], v[60:63], a[60:63], a[236:239]\n"
"s_branch L_end_%=\n"
"L_tr_b00_%=:\n"
"s_getpc_b64 s[82:83]\n"
"L_trh_b00_%=:\n"
"s_sub_u32 s82, s82, L_trh_b00_%=-L_rt_b00_%=\n"
"s_subb_u32 s83, s83, 0\n"
"s_branch L_rare1_%=\n"
"L_tr_b01_%=:\n"
"s_getpc_b64 s[82:83]\n"
"L_trh_b01_%=:\n"
"s_sub_u32 s82, s82, L_trh_b01_%=-L_rt_b01_%=\n"
"s_subb_u32 s83, s83, 0\n"
"s_branch L_rare1_%=\n"
"L_tr_b10_%=:\n"
"s_getpc_b64 s[82:83]\n"
"L_trh_b10_%=:\n"
"s_sub_u32 s82, s82, L_trh_b10_%=-L_rt_b10_%=\n"
"s_subb_u32 s83, s83, 0\n"
"s_branch L_rare0_%=\n"
"L_tr_b11_%=:\n"
"s_getpc_b64 s[82:83]\n"
"L_trh_b11_%=:\n"
"s_sub_u32 s82, s82, L_trh_b11_%=-L_rt_b11_%=\n"
"s_subb_u32 s83, s83, 0\n"
"s_branch L_rare0_%=\n"
"L_tr_l0_%=:\n"
"s_getpc_b64 s[82:83]\n"
"L_trh_l0_%=:\n"
"s_sub_u32 s82, s82, L_trh_l0_%=-L_rt_l0_%=\n"
"s_subb_u32 s83, s83, 0\n"
"s_branch L_rare0_%=\n"
"L_tr_lm0_%=:\n"
"s_getpc_b64 s[82:83]\n"
"L_trh_lm0_%=:\n"
"s_sub_u32 s82, s82, L_trh_lm0_%=-L_rt_lm0_%=\n"
"s_subb_u32 s83, s83, 0\n"
"s_branch L_rare0_%=\n"
"L_tr_ls0_%=:\n"
"s_getpc_b64 s[82:83]\n"
"L_trh_ls0_%=:\n"
"s_sub_u32 s82, s82, L_trh_ls0_%=-L_rt_ls0_%=\n"
"s_subb_u32 s83, s83, 0\n"
"s_branch L_rare0_%=\n"
"L_tr_l1_%=:\n"
"s_getpc_b64 s[82:83]\n"
"L_trh_l1_%=:\n"
"s_sub_u32 s82, s82, L_trh_l1_%=-L_rt_l1_%=\n"
"s_subb_u32 s83, s83, 0\n"
"s_branch L_rare1_%=\n"
"L_tr_lm1_%=:\n"
"s_getpc_b64 s[82:83]\n"
"L_trh_lm1_%=:\n"
"s_sub_u32 s82, s82, L_trh_lm1_%=-L_rt_lm1_%=\n"
"s_subb_u32 s83, s83, 0\n"
"s_branch L_rare1_%=\n"
"L_tr_ls1_%=:\n"
"s_getpc_b64 s[82:83]\n"
"L_trh_ls1_%=:\n"
"s_sub_u32 s82, s82, L_trh_ls1_%=-L_rt_ls1_%=\n"
"s_subb_u32 s83, s83, 0\n"
"s_branch L_rare1_%=\n"
"L_ev_f0_%=:\n"
"s_cmp_eq_u32 s63, s57\n"
"s_cselect_b32 s60, s64, s60\n"
"s_cmp_eq_u32 s63, s58\n"
"s_cselect_b32 s60, 0xf0000000, s60\n"
"s_cbranch_scc0 L_evla_f0_%=\n"
"s_lshl_b32 s96, s53, 5\n"
"s_add_u32 s96, s96, 126881792\n"
"v_lshlrev_b32 v237, 2, v0\n"
"v_add_u32 v237, s96, v237\n"
"s_lshl_b32 s97, s68, 4\n"
"s_add_u32 s97, s97, 126882816\n"
"s_cmp_eq_u32 s69, 0\n"
"s_cselect_b32 s97, s97, s96\n"
"v_mov_b32 v14, s97\n"
"v_cmp_gt_u32 vcc, 8, v0\n"
"s_nop 1\n"
"v_cndmask_b32 v237, v14, v237, vcc\n"
"buffer_load_dword v236, v237, s[36:39], 0 offen sc1\n"
"L_evla_f0_%=:\n"
"s_cmp_lg_u32 s63, s59\n"
"s_cbranch_scc1 L_evn_f0_%=\n"
"v_mov_b32 v15, 0\n"
"s_cmp_lg_u32 s49, 0\n"
"s_cbranch_scc1 L_evn_f0_%=\n"
"v_mov_b32 v14, s35\n"
"s_mov_b64 exec, 1\n"
"global_store_dword v15, v14, s[80:81] sc1\n"
"s_mov_b64 exec, -1\n"
"L_evn_f0_%=:\n"
"s_mov_b32 s96, 0x7fffffff\n"
"s_cmp_gt_u32 s57, s63\n"
"s_cselect_b32 s97, s57, 0x7fffffff\n"
"s_min_u32 s96, s96, s97\n"
"s_cmp_gt_u32 s58, s63\n"
"s_cselect_b32 s97, s58, 0x7fffffff\n"
"s_min_u32 s96, s96, s97\n"
"s_cmp_gt_u32 s59, s63\n"
"s_cselect_b32 s97, s59, 0x7fffffff\n"
"s_min_u32 s96, s96, s97\n"
"s_sub_u32 s62, s96, s63\n"
"s_sub_u32 s62, s62, 1\n"
"s_mov_b32 s63, s96\n"
"s_branch L_evr_f0_%=\n"
"L_ev_b00_%=:\n"
"s_cmp_eq_u32 s63, s57\n"
"s_cselect_b32 s60, s64, s60\n"
"s_cmp_eq_u32 s63, s58\n"
"s_cselect_b32 s60, 0xf0000000, s60\n"
"s_cbranch_scc0 L_evla_b00_%=\n"
"s_lshl_b32 s96, s53, 5\n"
"s_add_u32 s96, s96, 126881792\n"
"v_lshlrev_b32 v237, 2, v0\n"
"v_add_u32 v237, s96, v237\n"
"s_lshl_b32 s97, s68, 4\n"
"s_add_u32 s97, s97, 126882816\n"
"s_cmp_eq_u32 s69, 0\n"
"s_cselect_b32 s97, s97, s96\n"
"v_mov_b32 v14, s97\n"
"v_cmp_gt_u32 vcc, 8, v0\n"
"s_nop 1\n"
"v_cndmask_b32 v237, v14, v237, vcc\n"
"buffer_load_dword v236, v237, s[36:39], 0 offen sc1\n"
"L_evla_b00_%=:\n"
"s_cmp_lg_u32 s63, s59\n"
"s_cbranch_scc1 L_evn_b00_%=\n"
"v_mov_b32 v15, 0\n"
"s_cmp_lg_u32 s49, 0\n"
"s_cbranch_scc1 L_evn_b00_%=\n"
"v_mov_b32 v14, s35\n"
"s_mov_b64 exec, 1\n"
"global_store_dword v15, v14, s[80:81] sc1\n"
"s_mov_b64 exec, -1\n"
"L_evn_b00_%=:\n"
"s_mov_b32 s96, 0x7fffffff\n"
"s_cmp_gt_u32 s57, s63\n"
"s_cselect_b32 s97, s57, 0x7fffffff\n"
"s_min_u32 s96, s96, s97\n"
"s_cmp_gt_u32 s58, s63\n"
"s_cselect_b32 s97, s58, 0x7fffffff\n"
"s_min_u32 s96, s96, s97\n"
"s_cmp_gt_u32 s59, s63\n"
"s_cselect_b32 s97, s59, 0x7fffffff\n"
"s_min_u32 s96, s96, s97\n"
"s_sub_u32 s62, s96, s63\n"
"s_sub_u32 s62, s62, 1\n"
"s_mov_b32 s63, s96\n"
"s_branch L_evr_b00_%=\n"
"L_ev_b01_%=:\n"
"s_cmp_eq_u32 s63, s57\n"
"s_cselect_b32 s60, s64, s60\n"
"s_cmp_eq_u32 s63, s58\n"
"s_cselect_b32 s60, 0xf0000000, s60\n"
"s_cbranch_scc0 L_evla_b01_%=\n"
"s_lshl_b32 s96, s53, 5\n"
"s_add_u32 s96, s96, 126881792\n"
"v_lshlrev_b32 v237, 2, v0\n"
"v_add_u32 v237, s96, v237\n"
"s_lshl_b32 s97, s68, 4\n"
"s_add_u32 s97, s97, 126882816\n"
"s_cmp_eq_u32 s69, 0\n"
"s_cselect_b32 s97, s97, s96\n"
"v_mov_b32 v14, s97\n"
"v_cmp_gt_u32 vcc, 8, v0\n"
"s_nop 1\n"
"v_cndmask_b32 v237, v14, v237, vcc\n"
"buffer_load_dword v236, v237, s[36:39], 0 offen sc1\n"
"L_evla_b01_%=:\n"
"s_cmp_lg_u32 s63, s59\n"
"s_cbranch_scc1 L_evn_b01_%=\n"
"v_mov_b32 v15, 0\n"
"s_cmp_lg_u32 s49, 0\n"
"s_cbranch_scc1 L_evn_b01_%=\n"
"v_mov_b32 v14, s35\n"
"s_mov_b64 exec, 1\n"
"global_store_dword v15, v14, s[80:81] sc1\n"
"s_mov_b64 exec, -1\n"
"L_evn_b01_%=:\n"
"s_mov_b32 s96, 0x7fffffff\n"
"s_cmp_gt_u32 s57, s63\n"
"s_cselect_b32 s97, s57, 0x7fffffff\n"
"s_min_u32 s96, s96, s97\n"
"s_cmp_gt_u32 s58, s63\n"
"s_cselect_b32 s97, s58, 0x7fffffff\n"
"s_min_u32 s96, s96, s97\n"
"s_cmp_gt_u32 s59, s63\n"
"s_cselect_b32 s97, s59, 0x7fffffff\n"
"s_min_u32 s96, s96, s97\n"
"s_sub_u32 s62, s96, s63\n"
"s_sub_u32 s62, s62, 1\n"
"s_mov_b32 s63, s96\n"
"s_branch L_evr_b01_%=\n"
"L_ev_b10_%=:\n"
"s_cmp_eq_u32 s63, s57\n"
"s_cselect_b32 s61, s64, s61\n"
"s_cmp_eq_u32 s63, s58\n"
"s_cselect_b32 s61, 0xf0000000, s61\n"
"s_cbranch_scc0 L_evla_b10_%=\n"
"s_lshl_b32 s96, s53, 5\n"
"s_add_u32 s96, s96, 126881792\n"
"v_lshlrev_b32 v237, 2, v0\n"
"v_add_u32 v237, s96, v237\n"
"s_lshl_b32 s97, s68, 4\n"
"s_add_u32 s97, s97, 126882816\n"
"s_cmp_eq_u32 s69, 0\n"
"s_cselect_b32 s97, s97, s96\n"
"v_mov_b32 v14, s97\n"
"v_cmp_gt_u32 vcc, 8, v0\n"
"s_nop 1\n"
"v_cndmask_b32 v237, v14, v237, vcc\n"
"buffer_load_dword v236, v237, s[36:39], 0 offen sc1\n"
"L_evla_b10_%=:\n"
"s_cmp_lg_u32 s63, s59\n"
"s_cbranch_scc1 L_evn_b10_%=\n"
"v_mov_b32 v15, 0\n"
"s_cmp_lg_u32 s49, 0\n"
"s_cbranch_scc1 L_evn_b10_%=\n"
"v_mov_b32 v14, s35\n"
"s_mov_b64 exec, 1\n"
"global_store_dword v15, v14, s[80:81] sc1\n"
"s_mov_b64 exec, -1\n"
"L_evn_b10_%=:\n"
"s_mov_b32 s96, 0x7fffffff\n"
"s_cmp_gt_u32 s57, s63\n"
"s_cselect_b32 s97, s57, 0x7fffffff\n"
"s_min_u32 s96, s96, s97\n"
"s_cmp_gt_u32 s58, s63\n"
"s_cselect_b32 s97, s58, 0x7fffffff\n"
"s_min_u32 s96, s96, s97\n"
"s_cmp_gt_u32 s59, s63\n"
"s_cselect_b32 s97, s59, 0x7fffffff\n"
"s_min_u32 s96, s96, s97\n"
"s_sub_u32 s62, s96, s63\n"
"s_sub_u32 s62, s62, 1\n"
"s_mov_b32 s63, s96\n"
"s_branch L_evr_b10_%=\n"
"L_ev_b11_%=:\n"
"s_cmp_eq_u32 s63, s57\n"
"s_cselect_b32 s61, s64, s61\n"
"s_cmp_eq_u32 s63, s58\n"
"s_cselect_b32 s61, 0xf0000000, s61\n"
"s_cbranch_scc0 L_evla_b11_%=\n"
"s_lshl_b32 s96, s53, 5\n"
"s_add_u32 s96, s96, 126881792\n"
"v_lshlrev_b32 v237, 2, v0\n"
"v_add_u32 v237, s96, v237\n"
"s_lshl_b32 s97, s68, 4\n"
"s_add_u32 s97, s97, 126882816\n"
"s_cmp_eq_u32 s69, 0\n"
"s_cselect_b32 s97, s97, s96\n"
"v_mov_b32 v14, s97\n"
"v_cmp_gt_u32 vcc, 8, v0\n"
"s_nop 1\n"
"v_cndmask_b32 v237, v14, v237, vcc\n"
"buffer_load_dword v236, v237, s[36:39], 0 offen sc1\n"
"L_evla_b11_%=:\n"
"s_cmp_lg_u32 s63, s59\n"
"s_cbranch_scc1 L_evn_b11_%=\n"
"v_mov_b32 v15, 0\n"
"s_cmp_lg_u32 s49, 0\n"
"s_cbranch_scc1 L_evn_b11_%=\n"
"v_mov_b32 v14, s35\n"
"s_mov_b64 exec, 1\n"
"global_store_dword v15, v14, s[80:81] sc1\n"
"s_mov_b64 exec, -1\n"
"L_evn_b11_%=:\n"
"s_mov_b32 s96, 0x7fffffff\n"
"s_cmp_gt_u32 s57, s63\n"
"s_cselect_b32 s97, s57, 0x7fffffff\n"
"s_min_u32 s96, s96, s97\n"
"s_cmp_gt_u32 s58, s63\n"
"s_cselect_b32 s97, s58, 0x7fffffff\n"
"s_min_u32 s96, s96, s97\n"
"s_cmp_gt_u32 s59, s63\n"
"s_cselect_b32 s97, s59, 0x7fffffff\n"
"s_min_u32 s96, s96, s97\n"
"s_sub_u32 s62, s96, s63\n"
"s_sub_u32 s62, s62, 1\n"
"s_mov_b32 s63, s96\n"
"s_branch L_evr_b11_%=\n"
"L_ev_fs0_%=:\n"
"s_cmp_eq_u32 s63, s57\n"
"s_cselect_b32 s61, s64, s61\n"
"s_cmp_eq_u32 s63, s58\n"
"s_cselect_b32 s61, 0xf0000000, s61\n"
"s_cbranch_scc0 L_evla_fs0_%=\n"
"s_lshl_b32 s96, s53, 5\n"
"s_add_u32 s96, s96, 126881792\n"
"v_lshlrev_b32 v237, 2, v0\n"
"v_add_u32 v237, s96, v237\n"
"s_lshl_b32 s97, s68, 4\n"
"s_add_u32 s97, s97, 126882816\n"
"s_cmp_eq_u32 s69, 0\n"
"s_cselect_b32 s97, s97, s96\n"
"v_mov_b32 v14, s97\n"
"v_cmp_gt_u32 vcc, 8, v0\n"
"s_nop 1\n"
"v_cndmask_b32 v237, v14, v237, vcc\n"
"buffer_load_dword v236, v237, s[36:39], 0 offen sc1\n"
"L_evla_fs0_%=:\n"
"s_cmp_lg_u32 s63, s59\n"
"s_cbranch_scc1 L_evn_fs0_%=\n"
"v_mov_b32 v15, 0\n"
"s_cmp_lg_u32 s49, 0\n"
"s_cbranch_scc1 L_evn_fs0_%=\n"
"v_mov_b32 v14, s35\n"
"s_mov_b64 exec, 1\n"
"global_store_dword v15, v14, s[80:81] sc1\n"
"s_mov_b64 exec, -1\n"
"L_evn_fs0_%=:\n"
"s_mov_b32 s96, 0x7fffffff\n"
"s_cmp_gt_u32 s57, s63\n"
"s_cselect_b32 s97, s57, 0x7fffffff\n"
"s_min_u32 s96, s96, s97\n"
"s_cmp_gt_u32 s58, s63\n"
"s_cselect_b32 s97, s58, 0x7fffffff\n"
"s_min_u32 s96, s96, s97\n"
"s_cmp_gt_u32 s59, s63\n"
"s_cselect_b32 s97, s59, 0x7fffffff\n"
"s_min_u32 s96, s96, s97\n"
"s_sub_u32 s62, s96, s63\n"
"s_sub_u32 s62, s62, 1\n"
"s_mov_b32 s63, s96\n"
"s_branch L_evr_fs0_%=\n"
"L_ev_fs1_%=:\n"
"s_cmp_eq_u32 s63, s57\n"
"s_cselect_b32 s60, s64, s60\n"
"s_cmp_eq_u32 s63, s58\n"
"s_cselect_b32 s60, 0xf0000000, s60\n"
"s_cbranch_scc0 L_evla_fs1_%=\n"
"s_lshl_b32 s96, s53, 5\n"
"s_add_u32 s96, s96, 126881792\n"
"v_lshlrev_b32 v237, 2, v0\n"
"v_add_u32 v237, s96, v237\n"
"s_lshl_b32 s97, s68, 4\n"
"s_add_u32 s97, s97, 126882816\n"
"s_cmp_eq_u32 s69, 0\n"
"s_cselect_b32 s97, s97, s96\n"
"v_mov_b32 v14, s97\n"
"v_cmp_gt_u32 vcc, 8, v0\n"
"s_nop 1\n"
"v_cndmask_b32 v237, v14, v237, vcc\n"
"buffer_load_dword v236, v237, s[36:39], 0 offen sc1\n"
"L_evla_fs1_%=:\n"
"s_cmp_lg_u32 s63, s59\n"
"s_cbranch_scc1 L_evn_fs1_%=\n"
"v_mov_b32 v15, 0\n"
"s_cmp_lg_u32 s49, 0\n"
"s_cbranch_scc1 L_evn_fs1_%=\n"
"v_mov_b32 v14, s35\n"
"s_mov_b64 exec, 1\n"
"global_store_dword v15, v14, s[80:81] sc1\n"
"s_mov_b64 exec, -1\n"
"L_evn_fs1_%=:\n"
"s_mov_b32 s96, 0x7fffffff\n"
"s_cmp_gt_u32 s57, s63\n"
"s_cselect_b32 s97, s57, 0x7fffffff\n"
"s_min_u32 s96, s96, s97\n"
"s_cmp_gt_u32 s58, s63\n"
"s_cselect_b32 s97, s58, 0x7fffffff\n"
"s_min_u32 s96, s96, s97\n"
"s_cmp_gt_u32 s59, s63\n"
"s_cselect_b32 s97, s59, 0x7fffffff\n"
"s_min_u32 s96, s96, s97\n"
"s_sub_u32 s62, s96, s63\n"
"s_sub_u32 s62, s62, 1\n"
"s_mov_b32 s63, s96\n"
"s_branch L_evr_fs1_%=\n"
"L_rare0_%=:\n"
"s_nop 15\n"
"v_max3_f32 v24, v32, v33, v34\n"
"v_max3_f32 v24, v24, v35, v40\n"
"v_max3_f32 v24, v24, v41, v42\n"
"v_max3_f32 v24, v24, v43, v48\n"
"v_max3_f32 v24, v24, v49, v50\n"
"v_max3_f32 v24, v24, v51, v56\n"
"v_max3_f32 v24, v24, v57, v58\n"
"v_max_f32 v24, v24, v59\n"
"v_max3_f32 v25, v36, v37, v38\n"
"v_max3_f32 v25, v25, v39, v44\n"
"v_max3_f32 v25, v25, v45, v46\n"
"v_max3_f32 v25, v25, v47, v52\n"
"v_max3_f32 v25, v25, v53, v54\n"
"v_max3_f32 v25, v25, v55, v60\n"
"v_max3_f32 v25, v25, v61, v62\n"
"v_max_f32 v25, v25, v63\n"
"v_mov_b32 v11, v24\n"
"s_nop 1\n"
"v_permlane16_swap_b32 v24, v11\n"
"s_nop 1\n"
"v_max_f32 v24, v24, v11\n"
"v_mov_b32 v11, v24\n"
"s_nop 1\n"
"v_permlane32_swap_b32 v24, v11\n"
"s_nop 1\n"
"v_max_f32 v24, v24, v11\n"
"v_max_f32 v24, 0, v24\n"
"v_mov_b32 v11, v25\n"
"s_nop 1\n"
"v_permlane16_swap_b32 v25, v11\n"
"s_nop 1\n"
"v_max_f32 v25, v25, v11\n"
"v_mov_b32 v11, v25\n"
"s_nop 1\n"
"v_permlane32_swap_b32 v25, v11\n"
"s_nop 1\n"
"v_max_f32 v25, v25, v11\n"
"v_max_f32 v25, 0, v25\n"
"v_exp_f32 v26, -v24\n"
"v_exp_f32 v27, -v25\n"
"s_nop 0\n"
"v_mul_f32 v18, v18, v26\n"
"v_mul_f32 v19, v19, v27\n"
"v_accvgpr_read_b32 v136, a0\n"
"v_accvgpr_read_b32 v137, a1\n"
"v_accvgpr_read_b32 v138, a2\n"
"v_accvgpr_read_b32 v139, a3\n"
"v_mul_f32 v136, v136, v26\n"
"v_mul_f32 v137, v137, v26\n"
"v_mul_f32 v138, v138, v26\n"
"v_mul_f32 v139, v139, v26\n"
"v_accvgpr_write_b32 a0, v136\n"
"v_accvgpr_write_b32 a1, v137\n"
"v_accvgpr_write_b32 a2, v138\n"
"v_accvgpr_write_b32 a3, v139\n"
"v_accvgpr_read_b32 v140, a4\n"
"v_accvgpr_read_b32 v141, a5\n"
"v_accvgpr_read_b32 v142, a6\n"
"v_accvgpr_read_b32 v143, a7\n"
"v_mul_f32 v140, v140, v27\n"
"v_mul_f32 v141, v141, v27\n"
"v_mul_f32 v142, v142, v27\n"
"v_mul_f32 v143, v143, v27\n"
"v_accvgpr_write_b32 a4, v140\n"
"v_accvgpr_write_b32 a5, v141\n"
"v_accvgpr_write_b32 a6, v142\n"
"v_accvgpr_write_b32 a7, v143\n"
"v_accvgpr_read_b32 v144, a8\n"
"v_accvgpr_read_b32 v145, a9\n"
"v_accvgpr_read_b32 v146, a10\n"
"v_accvgpr_read_b32 v147, a11\n"
"v_mul_f32 v144, v144, v26\n"
"v_mul_f32 v145, v145, v26\n"
"v_mul_f32 v146, v146, v26\n"
"v_mul_f32 v147, v147, v26\n"
"v_accvgpr_write_b32 a8, v144\n"
"v_accvgpr_write_b32 a9, v145\n"
"v_accvgpr_write_b32 a10, v146\n"
"v_accvgpr_write_b32 a11, v147\n"
"v_accvgpr_read_b32 v148, a12\n"
"v_accvgpr_read_b32 v149, a13\n"
"v_accvgpr_read_b32 v150, a14\n"
"v_accvgpr_read_b32 v151, a15\n"
"v_mul_f32 v148, v148, v27\n"
"v_mul_f32 v149, v149, v27\n"
"v_mul_f32 v150, v150, v27\n"
"v_mul_f32 v151, v151, v27\n"
"v_accvgpr_write_b32 a12, v148\n"
"v_accvgpr_write_b32 a13, v149\n"
"v_accvgpr_write_b32 a14, v150\n"
"v_accvgpr_write_b32 a15, v151\n"
"v_accvgpr_read_b32 v152, a16\n"
"v_accvgpr_read_b32 v153, a17\n"
"v_accvgpr_read_b32 v154, a18\n"
"v_accvgpr_read_b32 v155, a19\n"
"v_mul_f32 v152, v152, v26\n"
"v_mul_f32 v153, v153, v26\n"
"v_mul_f32 v154, v154, v26\n"
"v_mul_f32 v155, v155, v26\n"
"v_accvgpr_write_b32 a16, v152\n"
"v_accvgpr_write_b32 a17, v153\n"
"v_accvgpr_write_b32 a18, v154\n"
"v_accvgpr_write_b32 a19, v155\n"
"v_accvgpr_read_b32 v156, a20\n"
"v_accvgpr_read_b32 v157, a21\n"
"v_accvgpr_read_b32 v158, a22\n"
"v_accvgpr_read_b32 v159, a23\n"
"v_mul_f32 v156, v156, v27\n"
"v_mul_f32 v157, v157, v27\n"
"v_mul_f32 v158, v158, v27\n"
"v_mul_f32 v159, v159, v27\n"
"v_accvgpr_write_b32 a20, v156\n"
"v_accvgpr_write_b32 a21, v157\n"
"v_accvgpr_write_b32 a22, v158\n"
"v_accvgpr_write_b32 a23, v159\n"
"v_accvgpr_read_b32 v160, a24\n"
"v_accvgpr_read_b32 v161, a25\n"
"v_accvgpr_read_b32 v162, a26\n"
"v_accvgpr_read_b32 v163, a27\n"
"v_mul_f32 v160, v160, v26\n"
"v_mul_f32 v161, v161, v26\n"
"v_mul_f32 v162, v162, v26\n"
"v_mul_f32 v163, v163, v26\n"
"v_accvgpr_write_b32 a24, v160\n"
"v_accvgpr_write_b32 a25, v161\n"
"v_accvgpr_write_b32 a26, v162\n"
"v_accvgpr_write_b32 a27, v163\n"
"v_accvgpr_read_b32 v164, a28\n"
"v_accvgpr_read_b32 v165, a29\n"
"v_accvgpr_read_b32 v166, a30\n"
"v_accvgpr_read_b32 v167, a31\n"
"v_mul_f32 v164, v164, v27\n"
"v_mul_f32 v165, v165, v27\n"
"v_mul_f32 v166, v166, v27\n"
"v_mul_f32 v167, v167, v27\n"
"v_accvgpr_write_b32 a28, v164\n"
"v_accvgpr_write_b32 a29, v165\n"
"v_accvgpr_write_b32 a30, v166\n"
"v_accvgpr_write_b32 a31, v167\n"
"v_accvgpr_read_b32 v136, a32\n"
"v_accvgpr_read_b32 v137, a33\n"
"v_accvgpr_read_b32 v138, a34\n"
"v_accvgpr_read_b32 v139, a35\n"
"v_mul_f32 v136, v136, v26\n"
"v_mul_f32 v137, v137, v26\n"
"v_mul_f32 v138, v138, v26\n"
"v_mul_f32 v139, v139, v26\n"
"v_accvgpr_write_b32 a32, v136\n"
"v_accvgpr_write_b32 a33, v137\n"
"v_accvgpr_write_b32 a34, v138\n"
"v_accvgpr_write_b32 a35, v139\n"
"v_accvgpr_read_b32 v140, a36\n"
"v_accvgpr_read_b32 v141, a37\n"
"v_accvgpr_read_b32 v142, a38\n"
"v_accvgpr_read_b32 v143, a39\n"
"v_mul_f32 v140, v140, v27\n"
"v_mul_f32 v141, v141, v27\n"
"v_mul_f32 v142, v142, v27\n"
"v_mul_f32 v143, v143, v27\n"
"v_accvgpr_write_b32 a36, v140\n"
"v_accvgpr_write_b32 a37, v141\n"
"v_accvgpr_write_b32 a38, v142\n"
"v_accvgpr_write_b32 a39, v143\n"
"v_accvgpr_read_b32 v144, a40\n"
"v_accvgpr_read_b32 v145, a41\n"
"v_accvgpr_read_b32 v146, a42\n"
"v_accvgpr_read_b32 v147, a43\n"
"v_mul_f32 v144, v144, v26\n"
"v_mul_f32 v145, v145, v26\n"
"v_mul_f32 v146, v146, v26\n"
"v_mul_f32 v147, v147, v26\n"
"v_accvgpr_write_b32 a40, v144\n"
"v_accvgpr_write_b32 a41, v145\n"
"v_accvgpr_write_b32 a42, v146\n"
"v_accvgpr_write_b32 a43, v147\n"
"v_accvgpr_read_b32 v148, a44\n"
"v_accvgpr_read_b32 v149, a45\n"
"v_accvgpr_read_b32 v150, a46\n"
"v_accvgpr_read_b32 v151, a47\n"
"v_mul_f32 v148, v148, v27\n"
"v_mul_f32 v149, v149, v27\n"
"v_mul_f32 v150, v150, v27\n"
"v_mul_f32 v151, v151, v27\n"
"v_accvgpr_write_b32 a44, v148\n"
"v_accvgpr_write_b32 a45, v149\n"
"v_accvgpr_write_b32 a46, v150\n"
"v_accvgpr_write_b32 a47, v151\n"
"v_accvgpr_read_b32 v152, a48\n"
"v_accvgpr_read_b32 v153, a49\n"
"v_accvgpr_read_b32 v154, a50\n"
"v_accvgpr_read_b32 v155, a51\n"
"v_mul_f32 v152, v152, v26\n"
"v_mul_f32 v153, v153, v26\n"
"v_mul_f32 v154, v154, v26\n"
"v_mul_f32 v155, v155, v26\n"
"v_accvgpr_write_b32 a48, v152\n"
"v_accvgpr_write_b32 a49, v153\n"
"v_accvgpr_write_b32 a50, v154\n"
"v_accvgpr_write_b32 a51, v155\n"
"v_accvgpr_read_b32 v156, a52\n"
"v_accvgpr_read_b32 v157, a53\n"
"v_accvgpr_read_b32 v158, a54\n"
"v_accvgpr_read_b32 v159, a55\n"
"v_mul_f32 v156, v156, v27\n"
"v_mul_f32 v157, v157, v27\n"
"v_mul_f32 v158, v158, v27\n"
"v_mul_f32 v159, v159, v27\n"
"v_accvgpr_write_b32 a52, v156\n"
"v_accvgpr_write_b32 a53, v157\n"
"v_accvgpr_write_b32 a54, v158\n"
"v_accvgpr_write_b32 a55, v159\n"
"v_accvgpr_read_b32 v160, a56\n"
"v_accvgpr_read_b32 v161, a57\n"
"v_accvgpr_read_b32 v162, a58\n"
"v_accvgpr_read_b32 v163, a59\n"
"v_mul_f32 v160, v160, v26\n"
"v_mul_f32 v161, v161, v26\n"
"v_mul_f32 v162, v162, v26\n"
"v_mul_f32 v163, v163, v26\n"
"v_accvgpr_write_b32 a56, v160\n"
"v_accvgpr_write_b32 a57, v161\n"
"v_accvgpr_write_b32 a58, v162\n"
"v_accvgpr_write_b32 a59, v163\n"
"v_accvgpr_read_b32 v164, a60\n"
"v_accvgpr_read_b32 v165, a61\n"
"v_accvgpr_read_b32 v166, a62\n"
"v_accvgpr_read_b32 v167, a63\n"
"v_mul_f32 v164, v164, v27\n"
"v_mul_f32 v165, v165, v27\n"
"v_mul_f32 v166, v166, v27\n"
"v_mul_f32 v167, v167, v27\n"
"v_accvgpr_write_b32 a60, v164\n"
"v_accvgpr_write_b32 a61, v165\n"
"v_accvgpr_write_b32 a62, v166\n"
"v_accvgpr_write_b32 a63, v167\n"
"v_accvgpr_read_b32 v136, a64\n"
"v_accvgpr_read_b32 v137, a65\n"
"v_accvgpr_read_b32 v138, a66\n"
"v_accvgpr_read_b32 v139, a67\n"
"v_mul_f32 v136, v136, v26\n"
"v_mul_f32 v137, v137, v26\n"
"v_mul_f32 v138, v138, v26\n"
"v_mul_f32 v139, v139, v26\n"
"v_accvgpr_write_b32 a64, v136\n"
"v_accvgpr_write_b32 a65, v137\n"
"v_accvgpr_write_b32 a66, v138\n"
"v_accvgpr_write_b32 a67, v139\n"
"v_accvgpr_read_b32 v140, a68\n"
"v_accvgpr_read_b32 v141, a69\n"
"v_accvgpr_read_b32 v142, a70\n"
"v_accvgpr_read_b32 v143, a71\n"
"v_mul_f32 v140, v140, v27\n"
"v_mul_f32 v141, v141, v27\n"
"v_mul_f32 v142, v142, v27\n"
"v_mul_f32 v143, v143, v27\n"
"v_accvgpr_write_b32 a68, v140\n"
"v_accvgpr_write_b32 a69, v141\n"
"v_accvgpr_write_b32 a70, v142\n"
"v_accvgpr_write_b32 a71, v143\n"
"v_accvgpr_read_b32 v144, a72\n"
"v_accvgpr_read_b32 v145, a73\n"
"v_accvgpr_read_b32 v146, a74\n"
"v_accvgpr_read_b32 v147, a75\n"
"v_mul_f32 v144, v144, v26\n"
"v_mul_f32 v145, v145, v26\n"
"v_mul_f32 v146, v146, v26\n"
"v_mul_f32 v147, v147, v26\n"
"v_accvgpr_write_b32 a72, v144\n"
"v_accvgpr_write_b32 a73, v145\n"
"v_accvgpr_write_b32 a74, v146\n"
"v_accvgpr_write_b32 a75, v147\n"
"v_accvgpr_read_b32 v148, a76\n"
"v_accvgpr_read_b32 v149, a77\n"
"v_accvgpr_read_b32 v150, a78\n"
"v_accvgpr_read_b32 v151, a79\n"
"v_mul_f32 v148, v148, v27\n"
"v_mul_f32 v149, v149, v27\n"
"v_mul_f32 v150, v150, v27\n"
"v_mul_f32 v151, v151, v27\n"
"v_accvgpr_write_b32 a76, v148\n"
"v_accvgpr_write_b32 a77, v149\n"
"v_accvgpr_write_b32 a78, v150\n"
"v_accvgpr_write_b32 a79, v151\n"
"v_accvgpr_read_b32 v152, a80\n"
"v_accvgpr_read_b32 v153, a81\n"
"v_accvgpr_read_b32 v154, a82\n"
"v_accvgpr_read_b32 v155, a83\n"
"v_mul_f32 v152, v152, v26\n"
"v_mul_f32 v153, v153, v26\n"
"v_mul_f32 v154, v154, v26\n"
"v_mul_f32 v155, v155, v26\n"
"v_accvgpr_write_b32 a80, v152\n"
"v_accvgpr_write_b32 a81, v153\n"
"v_accvgpr_write_b32 a82, v154\n"
"v_accvgpr_write_b32 a83, v155\n"
"v_accvgpr_read_b32 v156, a84\n"
"v_accvgpr_read_b32 v157, a85\n"
"v_accvgpr_read_b32 v158, a86\n"
"v_accvgpr_read_b32 v159, a87\n"
"v_mul_f32 v156, v156, v27\n"
"v_mul_f32 v157, v157, v27\n"
"v_mul_f32 v158, v158, v27\n"
"v_mul_f32 v159, v159, v27\n"
"v_accvgpr_write_b32 a84, v156\n"
"v_accvgpr_write_b32 a85, v157\n"
"v_accvgpr_write_b32 a86, v158\n"
"v_accvgpr_write_b32 a87, v159\n"
"v_accvgpr_read_b32 v160, a88\n"
"v_accvgpr_read_b32 v161, a89\n"
"v_accvgpr_read_b32 v162, a90\n"
"v_accvgpr_read_b32 v163, a91\n"
"v_mul_f32 v160, v160, v26\n"
"v_mul_f32 v161, v161, v26\n"
"v_mul_f32 v162, v162, v26\n"
"v_mul_f32 v163, v163, v26\n"
"v_accvgpr_write_b32 a88, v160\n"
"v_accvgpr_write_b32 a89, v161\n"
"v_accvgpr_write_b32 a90, v162\n"
"v_accvgpr_write_b32 a91, v163\n"
"v_accvgpr_read_b32 v164, a92\n"
"v_accvgpr_read_b32 v165, a93\n"
"v_accvgpr_read_b32 v166, a94\n"
"v_accvgpr_read_b32 v167, a95\n"
"v_mul_f32 v164, v164, v27\n"
"v_mul_f32 v165, v165, v27\n"
"v_mul_f32 v166, v166, v27\n"
"v_mul_f32 v167, v167, v27\n"
"v_accvgpr_write_b32 a92, v164\n"
"v_accvgpr_write_b32 a93, v165\n"
"v_accvgpr_write_b32 a94, v166\n"
"v_accvgpr_write_b32 a95, v167\n"
"v_accvgpr_read_b32 v136, a96\n"
"v_accvgpr_read_b32 v137, a97\n"
"v_accvgpr_read_b32 v138, a98\n"
"v_accvgpr_read_b32 v139, a99\n"
"v_mul_f32 v136, v136, v26\n"
"v_mul_f32 v137, v137, v26\n"
"v_mul_f32 v138, v138, v26\n"
"v_mul_f32 v139, v139, v26\n"
"v_accvgpr_write_b32 a96, v136\n"
"v_accvgpr_write_b32 a97, v137\n"
"v_accvgpr_write_b32 a98, v138\n"
"v_accvgpr_write_b32 a99, v139\n"
"v_accvgpr_read_b32 v140, a100\n"
"v_accvgpr_read_b32 v141, a101\n"
"v_accvgpr_read_b32 v142, a102\n"
"v_accvgpr_read_b32 v143, a103\n"
"v_mul_f32 v140, v140, v27\n"
"v_mul_f32 v141, v141, v27\n"
"v_mul_f32 v142, v142, v27\n"
"v_mul_f32 v143, v143, v27\n"
"v_accvgpr_write_b32 a100, v140\n"
"v_accvgpr_write_b32 a101, v141\n"
"v_accvgpr_write_b32 a102, v142\n"
"v_accvgpr_write_b32 a103, v143\n"
"v_accvgpr_read_b32 v144, a104\n"
"v_accvgpr_read_b32 v145, a105\n"
"v_accvgpr_read_b32 v146, a106\n"
"v_accvgpr_read_b32 v147, a107\n"
"v_mul_f32 v144, v144, v26\n"
"v_mul_f32 v145, v145, v26\n"
"v_mul_f32 v146, v146, v26\n"
"v_mul_f32 v147, v147, v26\n"
"v_accvgpr_write_b32 a104, v144\n"
"v_accvgpr_write_b32 a105, v145\n"
"v_accvgpr_write_b32 a106, v146\n"
"v_accvgpr_write_b32 a107, v147\n"
"v_accvgpr_read_b32 v148, a108\n"
"v_accvgpr_read_b32 v149, a109\n"
"v_accvgpr_read_b32 v150, a110\n"
"v_accvgpr_read_b32 v151, a111\n"
"v_mul_f32 v148, v148, v27\n"
"v_mul_f32 v149, v149, v27\n"
"v_mul_f32 v150, v150, v27\n"
"v_mul_f32 v151, v151, v27\n"
"v_accvgpr_write_b32 a108, v148\n"
"v_accvgpr_write_b32 a109, v149\n"
"v_accvgpr_write_b32 a110, v150\n"
"v_accvgpr_write_b32 a111, v151\n"
"v_accvgpr_read_b32 v152, a112\n"
"v_accvgpr_read_b32 v153, a113\n"
"v_accvgpr_read_b32 v154, a114\n"
"v_accvgpr_read_b32 v155, a115\n"
"v_mul_f32 v152, v152, v26\n"
"v_mul_f32 v153, v153, v26\n"
"v_mul_f32 v154, v154, v26\n"
"v_mul_f32 v155, v155, v26\n"
"v_accvgpr_write_b32 a112, v152\n"
"v_accvgpr_write_b32 a113, v153\n"
"v_accvgpr_write_b32 a114, v154\n"
"v_accvgpr_write_b32 a115, v155\n"
"v_accvgpr_read_b32 v156, a116\n"
"v_accvgpr_read_b32 v157, a117\n"
"v_accvgpr_read_b32 v158, a118\n"
"v_accvgpr_read_b32 v159, a119\n"
"v_mul_f32 v156, v156, v27\n"
"v_mul_f32 v157, v157, v27\n"
"v_mul_f32 v158, v158, v27\n"
"v_mul_f32 v159, v159, v27\n"
"v_accvgpr_write_b32 a116, v156\n"
"v_accvgpr_write_b32 a117, v157\n"
"v_accvgpr_write_b32 a118, v158\n"
"v_accvgpr_write_b32 a119, v159\n"
"v_accvgpr_read_b32 v160, a120\n"
"v_accvgpr_read_b32 v161, a121\n"
"v_accvgpr_read_b32 v162, a122\n"
"v_accvgpr_read_b32 v163, a123\n"
"v_mul_f32 v160, v160, v26\n"
"v_mul_f32 v161, v161, v26\n"
"v_mul_f32 v162, v162, v26\n"
"v_mul_f32 v163, v163, v26\n"
"v_accvgpr_write_b32 a120, v160\n"
"v_accvgpr_write_b32 a121, v161\n"
"v_accvgpr_write_b32 a122, v162\n"
"v_accvgpr_write_b32 a123, v163\n"
"v_accvgpr_read_b32 v164, a124\n"
"v_accvgpr_read_b32 v165, a125\n"
"v_accvgpr_read_b32 v166, a126\n"
"v_accvgpr_read_b32 v167, a127\n"
"v_mul_f32 v164, v164, v27\n"
"v_mul_f32 v165, v165, v27\n"
"v_mul_f32 v166, v166, v27\n"
"v_mul_f32 v167, v167, v27\n"
"v_accvgpr_write_b32 a124, v164\n"
"v_accvgpr_write_b32 a125, v165\n"
"v_accvgpr_write_b32 a126, v166\n"
"v_accvgpr_write_b32 a127, v167\n"
"v_add_f32 v16, v16, v24\n"
"v_sub_f32 v128, 0, v16\n"
"v_sub_f32 v129, 0, v16\n"
"v_sub_f32 v130, 0, v16\n"
"v_sub_f32 v131, 0, v16\n"
"v_add_f32 v17, v17, v25\n"
"v_sub_f32 v132, 0, v17\n"
"v_sub_f32 v133, 0, v17\n"
"v_sub_f32 v134, 0, v17\n"
"v_sub_f32 v135, 0, v17\n"
"v_sub_f32 v32, v32, v24\n"
"v_sub_f32 v33, v33, v24\n"
"v_sub_f32 v34, v34, v24\n"
"v_sub_f32 v35, v35, v24\n"
"v_sub_f32 v36, v36, v25\n"
"v_sub_f32 v37, v37, v25\n"
"v_sub_f32 v38, v38, v25\n"
"v_sub_f32 v39, v39, v25\n"
"v_sub_f32 v40, v40, v24\n"
"v_sub_f32 v41, v41, v24\n"
"v_sub_f32 v42, v42, v24\n"
"v_sub_f32 v43, v43, v24\n"
"v_sub_f32 v44, v44, v25\n"
"v_sub_f32 v45, v45, v25\n"
"v_sub_f32 v46, v46, v25\n"
"v_sub_f32 v47, v47, v25\n"
"v_sub_f32 v48, v48, v24\n"
"v_sub_f32 v49, v49, v24\n"
"v_sub_f32 v50, v50, v24\n"
"v_sub_f32 v51, v51, v24\n"
"v_sub_f32 v52, v52, v25\n"
"v_sub_f32 v53, v53, v25\n"
"v_sub_f32 v54, v54, v25\n"
"v_sub_f32 v55, v55, v25\n"
"v_sub_f32 v56, v56, v24\n"
"v_sub_f32 v57, v57, v24\n"
"v_sub_f32 v58, v58, v24\n"
"v_sub_f32 v59, v59, v24\n"
"v_sub_f32 v60, v60, v25\n"
"v_sub_f32 v61, v61, v25\n"
"v_sub_f32 v62, v62, v25\n"
"v_sub_f32 v63, v63, v25\n"
"v_sub_f32 v64, v64, v24\n"
"v_sub_f32 v65, v65, v24\n"
"v_sub_f32 v66, v66, v24\n"
"v_sub_f32 v67, v67, v24\n"
"v_sub_f32 v68, v68, v25\n"
"v_sub_f32 v69, v69, v25\n"
"v_sub_f32 v70, v70, v25\n"
"v_sub_f32 v71, v71, v25\n"
"v_sub_f32 v72, v72, v24\n"
"v_sub_f32 v73, v73, v24\n"
"v_sub_f32 v74, v74, v24\n"
"v_sub_f32 v75, v75, v24\n"
"v_sub_f32 v76, v76, v25\n"
"v_sub_f32 v77, v77, v25\n"
"v_sub_f32 v78, v78, v25\n"
"v_sub_f32 v79, v79, v25\n"
"v_exp_f32 v136, v32\n"
"v_exp_f32 v137, v33\n"
"v_exp_f32 v138, v34\n"
"v_exp_f32 v139, v35\n"
"v_exp_f32 v140, v36\n"
"v_exp_f32 v141, v37\n"
"v_exp_f32 v142, v38\n"
"v_exp_f32 v143, v39\n"
"v_exp_f32 v144, v40\n"
"v_exp_f32 v145, v41\n"
"v_exp_f32 v146, v42\n"
"v_exp_f32 v147, v43\n"
"v_exp_f32 v148, v44\n"
"v_exp_f32 v149, v45\n"
"v_exp_f32 v150, v46\n"
"v_exp_f32 v151, v47\n"
"v_exp_f32 v152, v48\n"
"v_exp_f32 v153, v49\n"
"v_exp_f32 v154, v50\n"
"v_exp_f32 v155, v51\n"
"v_exp_f32 v156, v52\n"
"v_exp_f32 v157, v53\n"
"v_exp_f32 v158, v54\n"
"v_exp_f32 v159, v55\n"
"v_exp_f32 v160, v56\n"
"v_exp_f32 v161, v57\n"
"v_exp_f32 v162, v58\n"
"v_exp_f32 v163, v59\n"
"v_exp_f32 v164, v60\n"
"v_exp_f32 v165, v61\n"
"v_exp_f32 v166, v62\n"
"v_exp_f32 v167, v63\n"
"s_nop 0\n"
"v_add_f32 v18, v18, v136\n"
"v_add_f32 v18, v18, v137\n"
"v_add_f32 v18, v18, v138\n"
"v_add_f32 v18, v18, v139\n"
"v_cvt_pk_bf16_f32 v96, v136, v137\n"
"v_cvt_pk_bf16_f32 v97, v138, v139\n"
"v_add_f32 v19, v19, v140\n"
"v_add_f32 v19, v19, v141\n"
"v_add_f32 v19, v19, v142\n"
"v_add_f32 v19, v19, v143\n"
"v_cvt_pk_bf16_f32 v100, v140, v141\n"
"v_cvt_pk_bf16_f32 v101, v142, v143\n"
"v_add_f32 v18, v18, v144\n"
"v_add_f32 v18, v18, v145\n"
"v_add_f32 v18, v18, v146\n"
"v_add_f32 v18, v18, v147\n"
"v_cvt_pk_bf16_f32 v98, v144, v145\n"
"v_cvt_pk_bf16_f32 v99, v146, v147\n"
"v_add_f32 v19, v19, v148\n"
"v_add_f32 v19, v19, v149\n"
"v_add_f32 v19, v19, v150\n"
"v_add_f32 v19, v19, v151\n"
"v_cvt_pk_bf16_f32 v102, v148, v149\n"
"v_cvt_pk_bf16_f32 v103, v150, v151\n"
"v_add_f32 v18, v18, v152\n"
"v_add_f32 v18, v18, v153\n"
"v_add_f32 v18, v18, v154\n"
"v_add_f32 v18, v18, v155\n"
"v_cvt_pk_bf16_f32 v104, v152, v153\n"
"v_cvt_pk_bf16_f32 v105, v154, v155\n"
"v_add_f32 v19, v19, v156\n"
"v_add_f32 v19, v19, v157\n"
"v_add_f32 v19, v19, v158\n"
"v_add_f32 v19, v19, v159\n"
"v_cvt_pk_bf16_f32 v108, v156, v157\n"
"v_cvt_pk_bf16_f32 v109, v158, v159\n"
"v_add_f32 v18, v18, v160\n"
"v_add_f32 v18, v18, v161\n"
"v_add_f32 v18, v18, v162\n"
"v_add_f32 v18, v18, v163\n"
"v_cvt_pk_bf16_f32 v106, v160, v161\n"
"v_cvt_pk_bf16_f32 v107, v162, v163\n"
"v_add_f32 v19, v19, v164\n"
"v_add_f32 v19, v19, v165\n"
"v_add_f32 v19, v19, v166\n"
"v_add_f32 v19, v19, v167\n"
"v_cvt_pk_bf16_f32 v110, v164, v165\n"
"v_cvt_pk_bf16_f32 v111, v166, v167\n"
"s_nop 1\n"
"s_setpc_b64 s[82:83]\n"
"L_rare1_%=:\n"
"s_nop 15\n"
"v_max3_f32 v24, v64, v65, v66\n"
"v_max3_f32 v24, v24, v67, v72\n"
"v_max3_f32 v24, v24, v73, v74\n"
"v_max3_f32 v24, v24, v75, v80\n"
"v_max3_f32 v24, v24, v81, v82\n"
"v_max3_f32 v24, v24, v83, v88\n"
"v_max3_f32 v24, v24, v89, v90\n"
"v_max_f32 v24, v24, v91\n"
"v_max3_f32 v25, v68, v69, v70\n"
"v_max3_f32 v25, v25, v71, v76\n"
"v_max3_f32 v25, v25, v77, v78\n"
"v_max3_f32 v25, v25, v79, v84\n"
"v_max3_f32 v25, v25, v85, v86\n"
"v_max3_f32 v25, v25, v87, v92\n"
"v_max3_f32 v25, v25, v93, v94\n"
"v_max_f32 v25, v25, v95\n"
"v_mov_b32 v11, v24\n"
"s_nop 1\n"
"v_permlane16_swap_b32 v24, v11\n"
"s_nop 1\n"
"v_max_f32 v24, v24, v11\n"
"v_mov_b32 v11, v24\n"
"s_nop 1\n"
"v_permlane32_swap_b32 v24, v11\n"
"s_nop 1\n"
"v_max_f32 v24, v24, v11\n"
"v_max_f32 v24, 0, v24\n"
"v_mov_b32 v11, v25\n"
"s_nop 1\n"
"v_permlane16_swap_b32 v25, v11\n"
"s_nop 1\n"
"v_max_f32 v25, v25, v11\n"
"v_mov_b32 v11, v25\n"
"s_nop 1\n"
"v_permlane32_swap_b32 v25, v11\n"
"s_nop 1\n"
"v_max_f32 v25, v25, v11\n"
"v_max_f32 v25, 0, v25\n"
"v_exp_f32 v26, -v24\n"
"v_exp_f32 v27, -v25\n"
"s_nop 0\n"
"v_mul_f32 v18, v18, v26\n"
"v_mul_f32 v19, v19, v27\n"
"v_accvgpr_read_b32 v136, a0\n"
"v_accvgpr_read_b32 v137, a1\n"
"v_accvgpr_read_b32 v138, a2\n"
"v_accvgpr_read_b32 v139, a3\n"
"v_mul_f32 v136, v136, v26\n"
"v_mul_f32 v137, v137, v26\n"
"v_mul_f32 v138, v138, v26\n"
"v_mul_f32 v139, v139, v26\n"
"v_accvgpr_write_b32 a0, v136\n"
"v_accvgpr_write_b32 a1, v137\n"
"v_accvgpr_write_b32 a2, v138\n"
"v_accvgpr_write_b32 a3, v139\n"
"v_accvgpr_read_b32 v140, a4\n"
"v_accvgpr_read_b32 v141, a5\n"
"v_accvgpr_read_b32 v142, a6\n"
"v_accvgpr_read_b32 v143, a7\n"
"v_mul_f32 v140, v140, v27\n"
"v_mul_f32 v141, v141, v27\n"
"v_mul_f32 v142, v142, v27\n"
"v_mul_f32 v143, v143, v27\n"
"v_accvgpr_write_b32 a4, v140\n"
"v_accvgpr_write_b32 a5, v141\n"
"v_accvgpr_write_b32 a6, v142\n"
"v_accvgpr_write_b32 a7, v143\n"
"v_accvgpr_read_b32 v144, a8\n"
"v_accvgpr_read_b32 v145, a9\n"
"v_accvgpr_read_b32 v146, a10\n"
"v_accvgpr_read_b32 v147, a11\n"
"v_mul_f32 v144, v144, v26\n"
"v_mul_f32 v145, v145, v26\n"
"v_mul_f32 v146, v146, v26\n"
"v_mul_f32 v147, v147, v26\n"
"v_accvgpr_write_b32 a8, v144\n"
"v_accvgpr_write_b32 a9, v145\n"
"v_accvgpr_write_b32 a10, v146\n"
"v_accvgpr_write_b32 a11, v147\n"
"v_accvgpr_read_b32 v148, a12\n"
"v_accvgpr_read_b32 v149, a13\n"
"v_accvgpr_read_b32 v150, a14\n"
"v_accvgpr_read_b32 v151, a15\n"
"v_mul_f32 v148, v148, v27\n"
"v_mul_f32 v149, v149, v27\n"
"v_mul_f32 v150, v150, v27\n"
"v_mul_f32 v151, v151, v27\n"
"v_accvgpr_write_b32 a12, v148\n"
"v_accvgpr_write_b32 a13, v149\n"
"v_accvgpr_write_b32 a14, v150\n"
"v_accvgpr_write_b32 a15, v151\n"
"v_accvgpr_read_b32 v152, a16\n"
"v_accvgpr_read_b32 v153, a17\n"
"v_accvgpr_read_b32 v154, a18\n"
"v_accvgpr_read_b32 v155, a19\n"
"v_mul_f32 v152, v152, v26\n"
"v_mul_f32 v153, v153, v26\n"
"v_mul_f32 v154, v154, v26\n"
"v_mul_f32 v155, v155, v26\n"
"v_accvgpr_write_b32 a16, v152\n"
"v_accvgpr_write_b32 a17, v153\n"
"v_accvgpr_write_b32 a18, v154\n"
"v_accvgpr_write_b32 a19, v155\n"
"v_accvgpr_read_b32 v156, a20\n"
"v_accvgpr_read_b32 v157, a21\n"
"v_accvgpr_read_b32 v158, a22\n"
"v_accvgpr_read_b32 v159, a23\n"
"v_mul_f32 v156, v156, v27\n"
"v_mul_f32 v157, v157, v27\n"
"v_mul_f32 v158, v158, v27\n"
"v_mul_f32 v159, v159, v27\n"
"v_accvgpr_write_b32 a20, v156\n"
"v_accvgpr_write_b32 a21, v157\n"
"v_accvgpr_write_b32 a22, v158\n"
"v_accvgpr_write_b32 a23, v159\n"
"v_accvgpr_read_b32 v160, a24\n"
"v_accvgpr_read_b32 v161, a25\n"
"v_accvgpr_read_b32 v162, a26\n"
"v_accvgpr_read_b32 v163, a27\n"
"v_mul_f32 v160, v160, v26\n"
"v_mul_f32 v161, v161, v26\n"
"v_mul_f32 v162, v162, v26\n"
"v_mul_f32 v163, v163, v26\n"
"v_accvgpr_write_b32 a24, v160\n"
"v_accvgpr_write_b32 a25, v161\n"
"v_accvgpr_write_b32 a26, v162\n"
"v_accvgpr_write_b32 a27, v163\n"
"v_accvgpr_read_b32 v164, a28\n"
"v_accvgpr_read_b32 v165, a29\n"
"v_accvgpr_read_b32 v166, a30\n"
"v_accvgpr_read_b32 v167, a31\n"
"v_mul_f32 v164, v164, v27\n"
"v_mul_f32 v165, v165, v27\n"
"v_mul_f32 v166, v166, v27\n"
"v_mul_f32 v167, v167, v27\n"
"v_accvgpr_write_b32 a28, v164\n"
"v_accvgpr_write_b32 a29, v165\n"
"v_accvgpr_write_b32 a30, v166\n"
"v_accvgpr_write_b32 a31, v167\n"
"v_accvgpr_read_b32 v136, a32\n"
"v_accvgpr_read_b32 v137, a33\n"
"v_accvgpr_read_b32 v138, a34\n"
"v_accvgpr_read_b32 v139, a35\n"
"v_mul_f32 v136, v136, v26\n"
"v_mul_f32 v137, v137, v26\n"
"v_mul_f32 v138, v138, v26\n"
"v_mul_f32 v139, v139, v26\n"
"v_accvgpr_write_b32 a32, v136\n"
"v_accvgpr_write_b32 a33, v137\n"
"v_accvgpr_write_b32 a34, v138\n"
"v_accvgpr_write_b32 a35, v139\n"
"v_accvgpr_read_b32 v140, a36\n"
"v_accvgpr_read_b32 v141, a37\n"
"v_accvgpr_read_b32 v142, a38\n"
"v_accvgpr_read_b32 v143, a39\n"
"v_mul_f32 v140, v140, v27\n"
"v_mul_f32 v141, v141, v27\n"
"v_mul_f32 v142, v142, v27\n"
"v_mul_f32 v143, v143, v27\n"
"v_accvgpr_write_b32 a36, v140\n"
"v_accvgpr_write_b32 a37, v141\n"
"v_accvgpr_write_b32 a38, v142\n"
"v_accvgpr_write_b32 a39, v143\n"
"v_accvgpr_read_b32 v144, a40\n"
"v_accvgpr_read_b32 v145, a41\n"
"v_accvgpr_read_b32 v146, a42\n"
"v_accvgpr_read_b32 v147, a43\n"
"v_mul_f32 v144, v144, v26\n"
"v_mul_f32 v145, v145, v26\n"
"v_mul_f32 v146, v146, v26\n"
"v_mul_f32 v147, v147, v26\n"
"v_accvgpr_write_b32 a40, v144\n"
"v_accvgpr_write_b32 a41, v145\n"
"v_accvgpr_write_b32 a42, v146\n"
"v_accvgpr_write_b32 a43, v147\n"
"v_accvgpr_read_b32 v148, a44\n"
"v_accvgpr_read_b32 v149, a45\n"
"v_accvgpr_read_b32 v150, a46\n"
"v_accvgpr_read_b32 v151, a47\n"
"v_mul_f32 v148, v148, v27\n"
"v_mul_f32 v149, v149, v27\n"
"v_mul_f32 v150, v150, v27\n"
"v_mul_f32 v151, v151, v27\n"
"v_accvgpr_write_b32 a44, v148\n"
"v_accvgpr_write_b32 a45, v149\n"
"v_accvgpr_write_b32 a46, v150\n"
"v_accvgpr_write_b32 a47, v151\n"
"v_accvgpr_read_b32 v152, a48\n"
"v_accvgpr_read_b32 v153, a49\n"
"v_accvgpr_read_b32 v154, a50\n"
"v_accvgpr_read_b32 v155, a51\n"
"v_mul_f32 v152, v152, v26\n"
"v_mul_f32 v153, v153, v26\n"
"v_mul_f32 v154, v154, v26\n"
"v_mul_f32 v155, v155, v26\n"
"v_accvgpr_write_b32 a48, v152\n"
"v_accvgpr_write_b32 a49, v153\n"
"v_accvgpr_write_b32 a50, v154\n"
"v_accvgpr_write_b32 a51, v155\n"
"v_accvgpr_read_b32 v156, a52\n"
"v_accvgpr_read_b32 v157, a53\n"
"v_accvgpr_read_b32 v158, a54\n"
"v_accvgpr_read_b32 v159, a55\n"
"v_mul_f32 v156, v156, v27\n"
"v_mul_f32 v157, v157, v27\n"
"v_mul_f32 v158, v158, v27\n"
"v_mul_f32 v159, v159, v27\n"
"v_accvgpr_write_b32 a52, v156\n"
"v_accvgpr_write_b32 a53, v157\n"
"v_accvgpr_write_b32 a54, v158\n"
"v_accvgpr_write_b32 a55, v159\n"
"v_accvgpr_read_b32 v160, a56\n"
"v_accvgpr_read_b32 v161, a57\n"
"v_accvgpr_read_b32 v162, a58\n"
"v_accvgpr_read_b32 v163, a59\n"
"v_mul_f32 v160, v160, v26\n"
"v_mul_f32 v161, v161, v26\n"
"v_mul_f32 v162, v162, v26\n"
"v_mul_f32 v163, v163, v26\n"
"v_accvgpr_write_b32 a56, v160\n"
"v_accvgpr_write_b32 a57, v161\n"
"v_accvgpr_write_b32 a58, v162\n"
"v_accvgpr_write_b32 a59, v163\n"
"v_accvgpr_read_b32 v164, a60\n"
"v_accvgpr_read_b32 v165, a61\n"
"v_accvgpr_read_b32 v166, a62\n"
"v_accvgpr_read_b32 v167, a63\n"
"v_mul_f32 v164, v164, v27\n"
"v_mul_f32 v165, v165, v27\n"
"v_mul_f32 v166, v166, v27\n"
"v_mul_f32 v167, v167, v27\n"
"v_accvgpr_write_b32 a60, v164\n"
"v_accvgpr_write_b32 a61, v165\n"
"v_accvgpr_write_b32 a62, v166\n"
"v_accvgpr_write_b32 a63, v167\n"
"v_accvgpr_read_b32 v136, a64\n"
"v_accvgpr_read_b32 v137, a65\n"
"v_accvgpr_read_b32 v138, a66\n"
"v_accvgpr_read_b32 v139, a67\n"
"v_mul_f32 v136, v136, v26\n"
"v_mul_f32 v137, v137, v26\n"
"v_mul_f32 v138, v138, v26\n"
"v_mul_f32 v139, v139, v26\n"
"v_accvgpr_write_b32 a64, v136\n"
"v_accvgpr_write_b32 a65, v137\n"
"v_accvgpr_write_b32 a66, v138\n"
"v_accvgpr_write_b32 a67, v139\n"
"v_accvgpr_read_b32 v140, a68\n"
"v_accvgpr_read_b32 v141, a69\n"
"v_accvgpr_read_b32 v142, a70\n"
"v_accvgpr_read_b32 v143, a71\n"
"v_mul_f32 v140, v140, v27\n"
"v_mul_f32 v141, v141, v27\n"
"v_mul_f32 v142, v142, v27\n"
"v_mul_f32 v143, v143, v27\n"
"v_accvgpr_write_b32 a68, v140\n"
"v_accvgpr_write_b32 a69, v141\n"
"v_accvgpr_write_b32 a70, v142\n"
"v_accvgpr_write_b32 a71, v143\n"
"v_accvgpr_read_b32 v144, a72\n"
"v_accvgpr_read_b32 v145, a73\n"
"v_accvgpr_read_b32 v146, a74\n"
"v_accvgpr_read_b32 v147, a75\n"
"v_mul_f32 v144, v144, v26\n"
"v_mul_f32 v145, v145, v26\n"
"v_mul_f32 v146, v146, v26\n"
"v_mul_f32 v147, v147, v26\n"
"v_accvgpr_write_b32 a72, v144\n"
"v_accvgpr_write_b32 a73, v145\n"
"v_accvgpr_write_b32 a74, v146\n"
"v_accvgpr_write_b32 a75, v147\n"
"v_accvgpr_read_b32 v148, a76\n"
"v_accvgpr_read_b32 v149, a77\n"
"v_accvgpr_read_b32 v150, a78\n"
"v_accvgpr_read_b32 v151, a79\n"
"v_mul_f32 v148, v148, v27\n"
"v_mul_f32 v149, v149, v27\n"
"v_mul_f32 v150, v150, v27\n"
"v_mul_f32 v151, v151, v27\n"
"v_accvgpr_write_b32 a76, v148\n"
"v_accvgpr_write_b32 a77, v149\n"
"v_accvgpr_write_b32 a78, v150\n"
"v_accvgpr_write_b32 a79, v151\n"
"v_accvgpr_read_b32 v152, a80\n"
"v_accvgpr_read_b32 v153, a81\n"
"v_accvgpr_read_b32 v154, a82\n"
"v_accvgpr_read_b32 v155, a83\n"
"v_mul_f32 v152, v152, v26\n"
"v_mul_f32 v153, v153, v26\n"
"v_mul_f32 v154, v154, v26\n"
"v_mul_f32 v155, v155, v26\n"
"v_accvgpr_write_b32 a80, v152\n"
"v_accvgpr_write_b32 a81, v153\n"
"v_accvgpr_write_b32 a82, v154\n"
"v_accvgpr_write_b32 a83, v155\n"
"v_accvgpr_read_b32 v156, a84\n"
"v_accvgpr_read_b32 v157, a85\n"
"v_accvgpr_read_b32 v158, a86\n"
"v_accvgpr_read_b32 v159, a87\n"
"v_mul_f32 v156, v156, v27\n"
"v_mul_f32 v157, v157, v27\n"
"v_mul_f32 v158, v158, v27\n"
"v_mul_f32 v159, v159, v27\n"
"v_accvgpr_write_b32 a84, v156\n"
"v_accvgpr_write_b32 a85, v157\n"
"v_accvgpr_write_b32 a86, v158\n"
"v_accvgpr_write_b32 a87, v159\n"
"v_accvgpr_read_b32 v160, a88\n"
"v_accvgpr_read_b32 v161, a89\n"
"v_accvgpr_read_b32 v162, a90\n"
"v_accvgpr_read_b32 v163, a91\n"
"v_mul_f32 v160, v160, v26\n"
"v_mul_f32 v161, v161, v26\n"
"v_mul_f32 v162, v162, v26\n"
"v_mul_f32 v163, v163, v26\n"
"v_accvgpr_write_b32 a88, v160\n"
"v_accvgpr_write_b32 a89, v161\n"
"v_accvgpr_write_b32 a90, v162\n"
"v_accvgpr_write_b32 a91, v163\n"
"v_accvgpr_read_b32 v164, a92\n"
"v_accvgpr_read_b32 v165, a93\n"
"v_accvgpr_read_b32 v166, a94\n"
"v_accvgpr_read_b32 v167, a95\n"
"v_mul_f32 v164, v164, v27\n"
"v_mul_f32 v165, v165, v27\n"
"v_mul_f32 v166, v166, v27\n"
"v_mul_f32 v167, v167, v27\n"
"v_accvgpr_write_b32 a92, v164\n"
"v_accvgpr_write_b32 a93, v165\n"
"v_accvgpr_write_b32 a94, v166\n"
"v_accvgpr_write_b32 a95, v167\n"
"v_accvgpr_read_b32 v136, a96\n"
"v_accvgpr_read_b32 v137, a97\n"
"v_accvgpr_read_b32 v138, a98\n"
"v_accvgpr_read_b32 v139, a99\n"
"v_mul_f32 v136, v136, v26\n"
"v_mul_f32 v137, v137, v26\n"
"v_mul_f32 v138, v138, v26\n"
"v_mul_f32 v139, v139, v26\n"
"v_accvgpr_write_b32 a96, v136\n"
"v_accvgpr_write_b32 a97, v137\n"
"v_accvgpr_write_b32 a98, v138\n"
"v_accvgpr_write_b32 a99, v139\n"
"v_accvgpr_read_b32 v140, a100\n"
"v_accvgpr_read_b32 v141, a101\n"
"v_accvgpr_read_b32 v142, a102\n"
"v_accvgpr_read_b32 v143, a103\n"
"v_mul_f32 v140, v140, v27\n"
"v_mul_f32 v141, v141, v27\n"
"v_mul_f32 v142, v142, v27\n"
"v_mul_f32 v143, v143, v27\n"
"v_accvgpr_write_b32 a100, v140\n"
"v_accvgpr_write_b32 a101, v141\n"
"v_accvgpr_write_b32 a102, v142\n"
"v_accvgpr_write_b32 a103, v143\n"
"v_accvgpr_read_b32 v144, a104\n"
"v_accvgpr_read_b32 v145, a105\n"
"v_accvgpr_read_b32 v146, a106\n"
"v_accvgpr_read_b32 v147, a107\n"
"v_mul_f32 v144, v144, v26\n"
"v_mul_f32 v145, v145, v26\n"
"v_mul_f32 v146, v146, v26\n"
"v_mul_f32 v147, v147, v26\n"
"v_accvgpr_write_b32 a104, v144\n"
"v_accvgpr_write_b32 a105, v145\n"
"v_accvgpr_write_b32 a106, v146\n"
"v_accvgpr_write_b32 a107, v147\n"
"v_accvgpr_read_b32 v148, a108\n"
"v_accvgpr_read_b32 v149, a109\n"
"v_accvgpr_read_b32 v150, a110\n"
"v_accvgpr_read_b32 v151, a111\n"
"v_mul_f32 v148, v148, v27\n"
"v_mul_f32 v149, v149, v27\n"
"v_mul_f32 v150, v150, v27\n"
"v_mul_f32 v151, v151, v27\n"
"v_accvgpr_write_b32 a108, v148\n"
"v_accvgpr_write_b32 a109, v149\n"
"v_accvgpr_write_b32 a110, v150\n"
"v_accvgpr_write_b32 a111, v151\n"
"v_accvgpr_read_b32 v152, a112\n"
"v_accvgpr_read_b32 v153, a113\n"
"v_accvgpr_read_b32 v154, a114\n"
"v_accvgpr_read_b32 v155, a115\n"
"v_mul_f32 v152, v152, v26\n"
"v_mul_f32 v153, v153, v26\n"
"v_mul_f32 v154, v154, v26\n"
"v_mul_f32 v155, v155, v26\n"
"v_accvgpr_write_b32 a112, v152\n"
"v_accvgpr_write_b32 a113, v153\n"
"v_accvgpr_write_b32 a114, v154\n"
"v_accvgpr_write_b32 a115, v155\n"
"v_accvgpr_read_b32 v156, a116\n"
"v_accvgpr_read_b32 v157, a117\n"
"v_accvgpr_read_b32 v158, a118\n"
"v_accvgpr_read_b32 v159, a119\n"
"v_mul_f32 v156, v156, v27\n"
"v_mul_f32 v157, v157, v27\n"
"v_mul_f32 v158, v158, v27\n"
"v_mul_f32 v159, v159, v27\n"
"v_accvgpr_write_b32 a116, v156\n"
"v_accvgpr_write_b32 a117, v157\n"
"v_accvgpr_write_b32 a118, v158\n"
"v_accvgpr_write_b32 a119, v159\n"
"v_accvgpr_read_b32 v160, a120\n"
"v_accvgpr_read_b32 v161, a121\n"
"v_accvgpr_read_b32 v162, a122\n"
"v_accvgpr_read_b32 v163, a123\n"
"v_mul_f32 v160, v160, v26\n"
"v_mul_f32 v161, v161, v26\n"
"v_mul_f32 v162, v162, v26\n"
"v_mul_f32 v163, v163, v26\n"
"v_accvgpr_write_b32 a120, v160\n"
"v_accvgpr_write_b32 a121, v161\n"
"v_accvgpr_write_b32 a122, v162\n"
"v_accvgpr_write_b32 a123, v163\n"
"v_accvgpr_read_b32 v164, a124\n"
"v_accvgpr_read_b32 v165, a125\n"
"v_accvgpr_read_b32 v166, a126\n"
"v_accvgpr_read_b32 v167, a127\n"
"v_mul_f32 v164, v164, v27\n"
"v_mul_f32 v165, v165, v27\n"
"v_mul_f32 v166, v166, v27\n"
"v_mul_f32 v167, v167, v27\n"
"v_accvgpr_write_b32 a124, v164\n"
"v_accvgpr_write_b32 a125, v165\n"
"v_accvgpr_write_b32 a126, v166\n"
"v_accvgpr_write_b32 a127, v167\n"
"v_add_f32 v16, v16, v24\n"
"v_sub_f32 v128, 0, v16\n"
"v_sub_f32 v129, 0, v16\n"
"v_sub_f32 v130, 0, v16\n"
"v_sub_f32 v131, 0, v16\n"
"v_add_f32 v17, v17, v25\n"
"v_sub_f32 v132, 0, v17\n"
"v_sub_f32 v133, 0, v17\n"
"v_sub_f32 v134, 0, v17\n"
"v_sub_f32 v135, 0, v17\n"
"v_sub_f32 v64, v64, v24\n"
"v_sub_f32 v65, v65, v24\n"
"v_sub_f32 v66, v66, v24\n"
"v_sub_f32 v67, v67, v24\n"
"v_sub_f32 v68, v68, v25\n"
"v_sub_f32 v69, v69, v25\n"
"v_sub_f32 v70, v70, v25\n"
"v_sub_f32 v71, v71, v25\n"
"v_sub_f32 v72, v72, v24\n"
"v_sub_f32 v73, v73, v24\n"
"v_sub_f32 v74, v74, v24\n"
"v_sub_f32 v75, v75, v24\n"
"v_sub_f32 v76, v76, v25\n"
"v_sub_f32 v77, v77, v25\n"
"v_sub_f32 v78, v78, v25\n"
"v_sub_f32 v79, v79, v25\n"
"v_sub_f32 v80, v80, v24\n"
"v_sub_f32 v81, v81, v24\n"
"v_sub_f32 v82, v82, v24\n"
"v_sub_f32 v83, v83, v24\n"
"v_sub_f32 v84, v84, v25\n"
"v_sub_f32 v85, v85, v25\n"
"v_sub_f32 v86, v86, v25\n"
"v_sub_f32 v87, v87, v25\n"
"v_sub_f32 v88, v88, v24\n"
"v_sub_f32 v89, v89, v24\n"
"v_sub_f32 v90, v90, v24\n"
"v_sub_f32 v91, v91, v24\n"
"v_sub_f32 v92, v92, v25\n"
"v_sub_f32 v93, v93, v25\n"
"v_sub_f32 v94, v94, v25\n"
"v_sub_f32 v95, v95, v25\n"
"v_sub_f32 v32, v32, v24\n"
"v_sub_f32 v33, v33, v24\n"
"v_sub_f32 v34, v34, v24\n"
"v_sub_f32 v35, v35, v24\n"
"v_sub_f32 v36, v36, v25\n"
"v_sub_f32 v37, v37, v25\n"
"v_sub_f32 v38, v38, v25\n"
"v_sub_f32 v39, v39, v25\n"
"v_sub_f32 v40, v40, v24\n"
"v_sub_f32 v41, v41, v24\n"
"v_sub_f32 v42, v42, v24\n"
"v_sub_f32 v43, v43, v24\n"
"v_sub_f32 v44, v44, v25\n"
"v_sub_f32 v45, v45, v25\n"
"v_sub_f32 v46, v46, v25\n"
"v_sub_f32 v47, v47, v25\n"
"v_exp_f32 v136, v64\n"
"v_exp_f32 v137, v65\n"
"v_exp_f32 v138, v66\n"
"v_exp_f32 v139, v67\n"
"v_exp_f32 v140, v68\n"
"v_exp_f32 v141, v69\n"
"v_exp_f32 v142, v70\n"
"v_exp_f32 v143, v71\n"
"v_exp_f32 v144, v72\n"
"v_exp_f32 v145, v73\n"
"v_exp_f32 v146, v74\n"
"v_exp_f32 v147, v75\n"
"v_exp_f32 v148, v76\n"
"v_exp_f32 v149, v77\n"
"v_exp_f32 v150, v78\n"
"v_exp_f32 v151, v79\n"
"v_exp_f32 v152, v80\n"
"v_exp_f32 v153, v81\n"
"v_exp_f32 v154, v82\n"
"v_exp_f32 v155, v83\n"
"v_exp_f32 v156, v84\n"
"v_exp_f32 v157, v85\n"
"v_exp_f32 v158, v86\n"
"v_exp_f32 v159, v87\n"
"v_exp_f32 v160, v88\n"
"v_exp_f32 v161, v89\n"
"v_exp_f32 v162, v90\n"
"v_exp_f32 v163, v91\n"
"v_exp_f32 v164, v92\n"
"v_exp_f32 v165, v93\n"
"v_exp_f32 v166, v94\n"
"v_exp_f32 v167, v95\n"
"s_nop 0\n"
"v_add_f32 v18, v18, v136\n"
"v_add_f32 v18, v18, v137\n"
"v_add_f32 v18, v18, v138\n"
"v_add_f32 v18, v18, v139\n"
"v_cvt_pk_bf16_f32 v112, v136, v137\n"
"v_cvt_pk_bf16_f32 v113, v138, v139\n"
"v_add_f32 v19, v19, v140\n"
"v_add_f32 v19, v19, v141\n"
"v_add_f32 v19, v19, v142\n"
"v_add_f32 v19, v19, v143\n"
"v_cvt_pk_bf16_f32 v116, v140, v141\n"
"v_cvt_pk_bf16_f32 v117, v142, v143\n"
"v_add_f32 v18, v18, v144\n"
"v_add_f32 v18, v18, v145\n"
"v_add_f32 v18, v18, v146\n"
"v_add_f32 v18, v18, v147\n"
"v_cvt_pk_bf16_f32 v114, v144, v145\n"
"v_cvt_pk_bf16_f32 v115, v146, v147\n"
"v_add_f32 v19, v19, v148\n"
"v_add_f32 v19, v19, v149\n"
"v_add_f32 v19, v19, v150\n"
"v_add_f32 v19, v19, v151\n"
"v_cvt_pk_bf16_f32 v118, v148, v149\n"
"v_cvt_pk_bf16_f32 v119, v150, v151\n"
"v_add_f32 v18, v18, v152\n"
"v_add_f32 v18, v18, v153\n"
"v_add_f32 v18, v18, v154\n"
"v_add_f32 v18, v18, v155\n"
"v_cvt_pk_bf16_f32 v120, v152, v153\n"
"v_cvt_pk_bf16_f32 v121, v154, v155\n"
"v_add_f32 v19, v19, v156\n"
"v_add_f32 v19, v19, v157\n"
"v_add_f32 v19, v19, v158\n"
"v_add_f32 v19, v19, v159\n"
"v_cvt_pk_bf16_f32 v124, v156, v157\n"
"v_cvt_pk_bf16_f32 v125, v158, v159\n"
"v_add_f32 v18, v18, v160\n"
"v_add_f32 v18, v18, v161\n"
"v_add_f32 v18, v18, v162\n"
"v_add_f32 v18, v18, v163\n"
"v_cvt_pk_bf16_f32 v122, v160, v161\n"
"v_cvt_pk_bf16_f32 v123, v162, v163\n"
"v_add_f32 v19, v19, v164\n"
"v_add_f32 v19, v19, v165\n"
"v_add_f32 v19, v19, v166\n"
"v_add_f32 v19, v19, v167\n"
"v_cvt_pk_bf16_f32 v126, v164, v165\n"
"v_cvt_pk_bf16_f32 v127, v166, v167\n"
"s_nop 1\n"
"s_setpc_b64 s[82:83]\n"
"L_end_%=:\n"
"L_k3done_%=:\n"
"s_nop 15\n"
"s_lshl_b32 s97, s51, 1\n"
"s_add_u32 s61, s97, s52\n"
"s_lshl_b32 s96, s54, 5\n"
"s_add_u32 s96, s96, s61\n"
"s_mul_i32 s96, s96, 56\n"
"s_mul_i32 s97, s50, 14\n"
"s_add_u32 s96, s96, s97\n"
"s_lshl_b32 s60, s96, 10\n"
"s_add_u32 s60, s60, 100663296\n"
"v_accvgpr_read_b32 v56, a128\n"
"v_accvgpr_read_b32 v57, a129\n"
"v_accvgpr_read_b32 v58, a130\n"
"v_accvgpr_read_b32 v59, a131\n"
"v_accvgpr_read_b32 v60, a136\n"
"v_accvgpr_read_b32 v61, a137\n"
"v_accvgpr_read_b32 v62, a138\n"
"v_accvgpr_read_b32 v63, a139\n"
"v_cvt_pk_bf16_f32 v64, v56, v57\n"
"v_cvt_pk_bf16_f32 v65, v58, v59\n"
"v_cvt_pk_bf16_f32 v66, v60, v61\n"
"v_cvt_pk_bf16_f32 v67, v62, v63\n"
"buffer_store_dwordx4 v[64:67], v1, s[36:39], s60 offen offset:0 sc1\n"
"v_accvgpr_read_b32 v72, a132\n"
"v_accvgpr_read_b32 v73, a133\n"
"v_accvgpr_read_b32 v74, a134\n"
"v_accvgpr_read_b32 v75, a135\n"
"v_accvgpr_read_b32 v76, a140\n"
"v_accvgpr_read_b32 v77, a141\n"
"v_accvgpr_read_b32 v78, a142\n"
"v_accvgpr_read_b32 v79, a143\n"
"v_cvt_pk_bf16_f32 v80, v72, v73\n"
"v_cvt_pk_bf16_f32 v81, v74, v75\n"
"v_cvt_pk_bf16_f32 v82, v76, v77\n"
"v_cvt_pk_bf16_f32 v83, v78, v79\n"
"buffer_store_dwordx4 v[80:83], v1, s[36:39], s60 offen offset:1024 sc1\n"
"v_accvgpr_read_b32 v88, a144\n"
"v_accvgpr_read_b32 v89, a145\n"
"v_accvgpr_read_b32 v90, a146\n"
"v_accvgpr_read_b32 v91, a147\n"
"v_accvgpr_read_b32 v92, a152\n"
"v_accvgpr_read_b32 v93, a153\n"
"v_accvgpr_read_b32 v94, a154\n"
"v_accvgpr_read_b32 v95, a155\n"
"v_cvt_pk_bf16_f32 v96, v88, v89\n"
"v_cvt_pk_bf16_f32 v97, v90, v91\n"
"v_cvt_pk_bf16_f32 v98, v92, v93\n"
"v_cvt_pk_bf16_f32 v99, v94, v95\n"
"buffer_store_dwordx4 v[96:99], v1, s[36:39], s60 offen offset:2048 sc1\n"
"v_accvgpr_read_b32 v104, a148\n"
"v_accvgpr_read_b32 v105, a149\n"
"v_accvgpr_read_b32 v106, a150\n"
"v_accvgpr_read_b32 v107, a151\n"
"v_accvgpr_read_b32 v108, a156\n"
"v_accvgpr_read_b32 v109, a157\n"
"v_accvgpr_read_b32 v110, a158\n"
"v_accvgpr_read_b32 v111, a159\n"
"v_cvt_pk_bf16_f32 v112, v104, v105\n"
"v_cvt_pk_bf16_f32 v113, v106, v107\n"
"v_cvt_pk_bf16_f32 v114, v108, v109\n"
"v_cvt_pk_bf16_f32 v115, v110, v111\n"
"buffer_store_dwordx4 v[112:115], v1, s[36:39], s60 offen offset:3072 sc1\n"
"s_add_u32 s60, s60, 4096\n"
"v_accvgpr_read_b32 v56, a160\n"
"v_accvgpr_read_b32 v57, a161\n"
"v_accvgpr_read_b32 v58, a162\n"
"v_accvgpr_read_b32 v59, a163\n"
"v_accvgpr_read_b32 v60, a168\n"
"v_accvgpr_read_b32 v61, a169\n"
"v_accvgpr_read_b32 v62, a170\n"
"v_accvgpr_read_b32 v63, a171\n"
"v_cvt_pk_bf16_f32 v64, v56, v57\n"
"v_cvt_pk_bf16_f32 v65, v58, v59\n"
"v_cvt_pk_bf16_f32 v66, v60, v61\n"
"v_cvt_pk_bf16_f32 v67, v62, v63\n"
"buffer_store_dwordx4 v[64:67], v1, s[36:39], s60 offen offset:0 sc1\n"
"v_accvgpr_read_b32 v72, a164\n"
"v_accvgpr_read_b32 v73, a165\n"
"v_accvgpr_read_b32 v74, a166\n"
"v_accvgpr_read_b32 v75, a167\n"
"v_accvgpr_read_b32 v76, a172\n"
"v_accvgpr_read_b32 v77, a173\n"
"v_accvgpr_read_b32 v78, a174\n"
"v_accvgpr_read_b32 v79, a175\n"
"v_cvt_pk_bf16_f32 v80, v72, v73\n"
"v_cvt_pk_bf16_f32 v81, v74, v75\n"
"v_cvt_pk_bf16_f32 v82, v76, v77\n"
"v_cvt_pk_bf16_f32 v83, v78, v79\n"
"buffer_store_dwordx4 v[80:83], v1, s[36:39], s60 offen offset:1024 sc1\n"
"v_accvgpr_read_b32 v88, a176\n"
"v_accvgpr_read_b32 v89, a177\n"
"v_accvgpr_read_b32 v90, a178\n"
"v_accvgpr_read_b32 v91, a179\n"
"v_accvgpr_read_b32 v92, a184\n"
"v_accvgpr_read_b32 v93, a185\n"
"v_accvgpr_read_b32 v94, a186\n"
"v_accvgpr_read_b32 v95, a187\n"
"v_cvt_pk_bf16_f32 v96, v88, v89\n"
"v_cvt_pk_bf16_f32 v97, v90, v91\n"
"v_cvt_pk_bf16_f32 v98, v92, v93\n"
"v_cvt_pk_bf16_f32 v99, v94, v95\n"
"buffer_store_dwordx4 v[96:99], v1, s[36:39], s60 offen offset:2048 sc1\n"
"v_accvgpr_read_b32 v104, a180\n"
"v_accvgpr_read_b32 v105, a181\n"
"v_accvgpr_read_b32 v106, a182\n"
"v_accvgpr_read_b32 v107, a183\n"
"v_accvgpr_read_b32 v108, a188\n"
"v_accvgpr_read_b32 v109, a189\n"
"v_accvgpr_read_b32 v110, a190\n"
"v_accvgpr_read_b32 v111, a191\n"
"v_cvt_pk_bf16_f32 v112, v104, v105\n"
"v_cvt_pk_bf16_f32 v113, v106, v107\n"
"v_cvt_pk_bf16_f32 v114, v108, v109\n"
"v_cvt_pk_bf16_f32 v115, v110, v111\n"
"buffer_store_dwordx4 v[112:115], v1, s[36:39], s60 offen offset:3072 sc1\n"
"s_add_u32 s60, s60, 4096\n"
"v_accvgpr_read_b32 v56, a192\n"
"v_accvgpr_read_b32 v57, a193\n"
"v_accvgpr_read_b32 v58, a194\n"
"v_accvgpr_read_b32 v59, a195\n"
"v_accvgpr_read_b32 v60, a200\n"
"v_accvgpr_read_b32 v61, a201\n"
"v_accvgpr_read_b32 v62, a202\n"
"v_accvgpr_read_b32 v63, a203\n"
"v_cvt_pk_bf16_f32 v64, v56, v57\n"
"v_cvt_pk_bf16_f32 v65, v58, v59\n"
"v_cvt_pk_bf16_f32 v66, v60, v61\n"
"v_cvt_pk_bf16_f32 v67, v62, v63\n"
"buffer_store_dwordx4 v[64:67], v1, s[36:39], s60 offen offset:0 sc1\n"
"v_accvgpr_read_b32 v72, a196\n"
"v_accvgpr_read_b32 v73, a197\n"
"v_accvgpr_read_b32 v74, a198\n"
"v_accvgpr_read_b32 v75, a199\n"
"v_accvgpr_read_b32 v76, a204\n"
"v_accvgpr_read_b32 v77, a205\n"
"v_accvgpr_read_b32 v78, a206\n"
"v_accvgpr_read_b32 v79, a207\n"
"v_cvt_pk_bf16_f32 v80, v72, v73\n"
"v_cvt_pk_bf16_f32 v81, v74, v75\n"
"v_cvt_pk_bf16_f32 v82, v76, v77\n"
"v_cvt_pk_bf16_f32 v83, v78, v79\n"
"buffer_store_dwordx4 v[80:83], v1, s[36:39], s60 offen offset:1024 sc1\n"
"v_accvgpr_read_b32 v88, a208\n"
"v_accvgpr_read_b32 v89, a209\n"
"v_accvgpr_read_b32 v90, a210\n"
"v_accvgpr_read_b32 v91, a211\n"
"v_accvgpr_read_b32 v92, a216\n"
"v_accvgpr_read_b32 v93, a217\n"
"v_accvgpr_read_b32 v94, a218\n"
"v_accvgpr_read_b32 v95, a219\n"
"v_cvt_pk_bf16_f32 v96, v88, v89\n"
"v_cvt_pk_bf16_f32 v97, v90, v91\n"
"v_cvt_pk_bf16_f32 v98, v92, v93\n"
"v_cvt_pk_bf16_f32 v99, v94, v95\n"
"buffer_store_dwordx4 v[96:99], v1, s[36:39], s60 offen offset:2048 sc1\n"
"v_accvgpr_read_b32 v104, a212\n"
"v_accvgpr_read_b32 v105, a213\n"
"v_accvgpr_read_b32 v106, a214\n"
"v_accvgpr_read_b32 v107, a215\n"
"v_accvgpr_read_b32 v108, a220\n"
"v_accvgpr_read_b32 v109, a221\n"
"v_accvgpr_read_b32 v110, a222\n"
"v_accvgpr_read_b32 v111, a223\n"
"v_cvt_pk_bf16_f32 v112, v104, v105\n"
"v_cvt_pk_bf16_f32 v113, v106, v107\n"
"v_cvt_pk_bf16_f32 v114, v108, v109\n"
"v_cvt_pk_bf16_f32 v115, v110, v111\n"
"buffer_store_dwordx4 v[112:115], v1, s[36:39], s60 offen offset:3072 sc1\n"
"s_add_u32 s60, s60, 4096\n"
"v_accvgpr_read_b32 v56, a224\n"
"v_accvgpr_read_b32 v57, a225\n"
"v_accvgpr_read_b32 v58, a226\n"
"v_accvgpr_read_b32 v59, a227\n"
"v_accvgpr_read_b32 v60, a232\n"
"v_accvgpr_read_b32 v61, a233\n"
"v_accvgpr_read_b32 v62, a234\n"
"v_accvgpr_read_b32 v63, a235\n"
"v_cvt_pk_bf16_f32 v64, v56, v57\n"
"v_cvt_pk_bf16_f32 v65, v58, v59\n"
"v_cvt_pk_bf16_f32 v66, v60, v61\n"
"v_cvt_pk_bf16_f32 v67, v62, v63\n"
"buffer_store_dwordx4 v[64:67], v1, s[36:39], s60 offen offset:0 sc1\n"
"v_accvgpr_read_b32 v72, a228\n"
"v_accvgpr_read_b32 v73, a229\n"
"v_accvgpr_read_b32 v74, a230\n"
"v_accvgpr_read_b32 v75, a231\n"
"v_accvgpr_read_b32 v76, a236\n"
"v_accvgpr_read_b32 v77, a237\n"
"v_accvgpr_read_b32 v78, a238\n"
"v_accvgpr_read_b32 v79, a239\n"
"v_cvt_pk_bf16_f32 v80, v72, v73\n"
"v_cvt_pk_bf16_f32 v81, v74, v75\n"
"v_cvt_pk_bf16_f32 v82, v76, v77\n"
"v_cvt_pk_bf16_f32 v83, v78, v79\n"
"buffer_store_dwordx4 v[80:83], v1, s[36:39], s60 offen offset:1024 sc1\n"
"s_waitcnt vmcnt(0)\n"
"s_barrier\n"
"s_lshl_b32 s97, s61, 3\n"
"s_add_u32 s97, s97, s54\n"
"s_mov_b64 s[62:63], %0\n"
"s_lshl_b32 s96, s97, 2\n"
"s_add_u32 s96, s96, 126889984\n"
"s_add_u32 s62, s62, s96\n"
"s_addc_u32 s63, s63, 0\n"
"s_cmp_lg_u32 s50, 0\n"
"s_cbranch_scc1 L_sg_c2_%=\n"
"v_mov_b32 v11, 0\n"
"v_mov_b32 v10, s35\n"
"s_mov_b64 exec, 1\n"
"global_store_dword v11, v10, s[62:63] sc1\n"
"s_mov_b64 exec, -1\n"
"L_sg_c2_%=:\n"
"s_mov_b64 s[62:63], %0\n"
"s_lshl_b32 s96, s61, 5\n"
"s_add_u32 s96, s96, 126889984\n"
"s_add_u32 s62, s62, s96\n"
"s_addc_u32 s63, s63, 0\n"
"s_cmp_lg_u32 s50, 0\n"
"s_cbranch_scc1 L_pd_c2_%=\n"
"v_lshlrev_b32 v11, 2, v0\n"
"v_min_u32 v11, 28, v11\n"
"s_mov_b32 s99, 0\n"
"L_pl_c2_%=:\n"
"global_load_dword v10, v11, s[62:63] sc1\n"
"s_waitcnt vmcnt(0)\n"
"v_cmp_ne_u32 vcc, s35, v10\n"
"s_nop 4\n"
"s_cmp_eq_u64 vcc, 0\n"
"s_cbranch_scc1 L_pd_c2_%=\n"
"s_sleep 1\n"
"s_add_u32 s99, s99, 1\n"
"s_cmp_lt_u32 s99, 0x200000\n"
"s_cbranch_scc1 L_pl_c2_%=\n"
"L_pd_c2_%=:\n"
"s_barrier\n"
"s_mul_i32 s96, s54, 7\n"
"s_lshl_b32 s97, s50, 1\n"
"s_add_u32 s97, s97, 0\n"
"s_add_u32 s96, s96, s97\n"
"s_mul_i32 s98, s61, 56\n"
"s_add_u32 s98, s98, s96\n"
"s_lshl_b32 s60, s98, 10\n"
"s_add_u32 s60, s60, 100663296\n"
"buffer_load_dwordx4 v[56:59], v1, s[36:39], s60 offen sc1\n"
"s_add_u32 s60, s60, 1835008\n"
"buffer_load_dwordx4 v[60:63], v1, s[36:39], s60 offen sc1\n"
"s_add_u32 s60, s60, 1835008\n"
"buffer_load_dwordx4 v[64:67], v1, s[36:39], s60 offen sc1\n"
"s_add_u32 s60, s60, 1835008\n"
"buffer_load_dwordx4 v[68:71], v1, s[36:39], s60 offen sc1\n"
"s_add_u32 s60, s60, 1835008\n"
"buffer_load_dwordx4 v[72:75], v1, s[36:39], s60 offen sc1\n"
"s_add_u32 s60, s60, 1835008\n"
"buffer_load_dwordx4 v[76:79], v1, s[36:39], s60 offen sc1\n"
"s_add_u32 s60, s60, 1835008\n"
"buffer_load_dwordx4 v[80:83], v1, s[36:39], s60 offen sc1\n"
"s_add_u32 s60, s60, 1835008\n"
"buffer_load_dwordx4 v[84:87], v1, s[36:39], s60 offen sc1\n"
"s_and_b32 s96, s54, 1\n"
"s_mul_i32 s96, s96, 7\n"
"s_add_u32 s96, s96, s97\n"
"s_lshr_b32 s97, s54, 1\n"
"s_lshl_b32 s97, s97, 5\n"
"s_lshl_b32 s98, s51, 7\n"
"s_add_u32 s97, s97, s98\n"
"s_and_b32 s98, s96, 1\n"
"s_lshl_b32 s98, s98, 4\n"
"s_add_u32 s97, s97, s98\n"
"s_mul_i32 s97, s97, 896\n"
"s_lshr_b32 s96, s96, 1\n"
"s_lshl_b32 s96, s96, 6\n"
"s_add_u32 s97, s97, s96\n"
"s_mul_i32 s96, s52, 448\n"
"s_add_u32 s64, s97, s96\n"
"s_cmp_eq_u32 s50, 3\n"
"s_cbranch_scc1 L_k3end_%=\n"
"s_mul_i32 s96, s54, 7\n"
"s_lshl_b32 s97, s50, 1\n"
"s_add_u32 s97, s97, 1\n"
"s_add_u32 s96, s96, s97\n"
"s_mul_i32 s98, s61, 56\n"
"s_add_u32 s98, s98, s96\n"
"s_lshl_b32 s60, s98, 10\n"
"s_add_u32 s60, s60, 100663296\n"
"buffer_load_dwordx4 v[88:91], v1, s[36:39], s60 offen sc1\n"
"s_add_u32 s60, s60, 1835008\n"
"buffer_load_dwordx4 v[92:95], v1, s[36:39], s60 offen sc1\n"
"s_add_u32 s60, s60, 1835008\n"
"buffer_load_dwordx4 v[96:99], v1, s[36:39], s60 offen sc1\n"
"s_add_u32 s60, s60, 1835008\n"
"buffer_load_dwordx4 v[100:103], v1, s[36:39], s60 offen sc1\n"
"s_add_u32 s60, s60, 1835008\n"
"buffer_load_dwordx4 v[104:107], v1, s[36:39], s60 offen sc1\n"
"s_add_u32 s60, s60, 1835008\n"
"buffer_load_dwordx4 v[108:111], v1, s[36:39], s60 offen sc1\n"
"s_add_u32 s60, s60, 1835008\n"
"buffer_load_dwordx4 v[112:115], v1, s[36:39], s60 offen sc1\n"
"s_add_u32 s60, s60, 1835008\n"
"buffer_load_dwordx4 v[116:119], v1, s[36:39], s60 offen sc1\n"
"s_and_b32 s96, s54, 1\n"
"s_mul_i32 s96, s96, 7\n"
"s_add_u32 s96, s96, s97\n"
"s_lshr_b32 s97, s54, 1\n"
"s_lshl_b32 s97, s97, 5\n"
"s_lshl_b32 s98, s51, 7\n"
"s_add_u32 s97, s97, s98\n"
"s_and_b32 s98, s96, 1\n"
"s_lshl_b32 s98, s98, 4\n"
"s_add_u32 s97, s97, s98\n"
"s_mul_i32 s97, s97, 896\n"
"s_lshr_b32 s96, s96, 1\n"
"s_lshl_b32 s96, s96, 6\n"
"s_add_u32 s97, s97, s96\n"
"s_mul_i32 s96, s52, 448\n"
"s_add_u32 s65, s97, s96\n"
"v_and_b32 v2, 15, v0\n"
"v_mul_u32_u24 v2, 896, v2\n"
"v_lshrrev_b32 v3, 4, v0\n"
"v_lshl_add_u32 v2, v3, 3, v2\n"
"s_waitcnt vmcnt(8)\n"
"v_lshlrev_b32 v32, 16, v56\n"
"v_and_b32 v33, 0xffff0000, v56\n"
"v_lshlrev_b32 v34, 16, v57\n"
"v_and_b32 v35, 0xffff0000, v57\n"
"v_lshlrev_b32 v36, 16, v58\n"
"v_and_b32 v37, 0xffff0000, v58\n"
"v_lshlrev_b32 v38, 16, v59\n"
"v_and_b32 v39, 0xffff0000, v59\n"
"v_lshlrev_b32 v40, 16, v60\n"
"v_and_b32 v41, 0xffff0000, v60\n"
"v_add_f32 v32, v32, v40\n"
"v_add_f32 v33, v33, v41\n"
"v_lshlrev_b32 v40, 16, v61\n"
"v_and_b32 v41, 0xffff0000, v61\n"
"v_add_f32 v34, v34, v40\n"
"v_add_f32 v35, v35, v41\n"
"v_lshlrev_b32 v40, 16, v62\n"
"v_and_b32 v41, 0xffff0000, v62\n"
"v_add_f32 v36, v36, v40\n"
"v_add_f32 v37, v37, v41\n"
"v_lshlrev_b32 v40, 16, v63\n"
"v_and_b32 v41, 0xffff0000, v63\n"
"v_add_f32 v38, v38, v40\n"
"v_add_f32 v39, v39, v41\n"
"v_lshlrev_b32 v40, 16, v64\n"
"v_and_b32 v41, 0xffff0000, v64\n"
"v_add_f32 v32, v32, v40\n"
"v_add_f32 v33, v33, v41\n"
"v_lshlrev_b32 v40, 16, v65\n"
"v_and_b32 v41, 0xffff0000, v65\n"
"v_add_f32 v34, v34, v40\n"
"v_add_f32 v35, v35, v41\n"
"v_lshlrev_b32 v40, 16, v66\n"
"v_and_b32 v41, 0xffff0000, v66\n"
"v_add_f32 v36, v36, v40\n"
"v_add_f32 v37, v37, v41\n"
"v_lshlrev_b32 v40, 16, v67\n"
"v_and_b32 v41, 0xffff0000, v67\n"
"v_add_f32 v38, v38, v40\n"
"v_add_f32 v39, v39, v41\n"
"v_lshlrev_b32 v40, 16, v68\n"
"v_and_b32 v41, 0xffff0000, v68\n"
"v_add_f32 v32, v32, v40\n"
"v_add_f32 v33, v33, v41\n"
"v_lshlrev_b32 v40, 16, v69\n"
"v_and_b32 v41, 0xffff0000, v69\n"
"v_add_f32 v34, v34, v40\n"
"v_add_f32 v35, v35, v41\n"
"v_lshlrev_b32 v40, 16, v70\n"
"v_and_b32 v41, 0xffff0000, v70\n"
"v_add_f32 v36, v36, v40\n"
"v_add_f32 v37, v37, v41\n"
"v_lshlrev_b32 v40, 16, v71\n"
"v_and_b32 v41, 0xffff0000, v71\n"
"v_add_f32 v38, v38, v40\n"
"v_add_f32 v39, v39, v41\n"
"v_lshlrev_b32 v40, 16, v72\n"
"v_and_b32 v41, 0xffff0000, v72\n"
"v_add_f32 v32, v32, v40\n"
"v_add_f32 v33, v33, v41\n"
"v_lshlrev_b32 v40, 16, v73\n"
"v_and_b32 v41, 0xffff0000, v73\n"
"v_add_f32 v34, v34, v40\n"
"v_add_f32 v35, v35, v41\n"
"v_lshlrev_b32 v40, 16, v74\n"
"v_and_b32 v41, 0xffff0000, v74\n"
"v_add_f32 v36, v36, v40\n"
"v_add_f32 v37, v37, v41\n"
"v_lshlrev_b32 v40, 16, v75\n"
"v_and_b32 v41, 0xffff0000, v75\n"
"v_add_f32 v38, v38, v40\n"
"v_add_f32 v39, v39, v41\n"
"v_lshlrev_b32 v40, 16, v76\n"
"v_and_b32 v41, 0xffff0000, v76\n"
"v_add_f32 v32, v32, v40\n"
"v_add_f32 v33, v33, v41\n"
"v_lshlrev_b32 v40, 16, v77\n"
"v_and_b32 v41, 0xffff0000, v77\n"
"v_add_f32 v34, v34, v40\n"
"v_add_f32 v35, v35, v41\n"
"v_lshlrev_b32 v40, 16, v78\n"
"v_and_b32 v41, 0xffff0000, v78\n"
"v_add_f32 v36, v36, v40\n"
"v_add_f32 v37, v37, v41\n"
"v_lshlrev_b32 v40, 16, v79\n"
"v_and_b32 v41, 0xffff0000, v79\n"
"v_add_f32 v38, v38, v40\n"
"v_add_f32 v39, v39, v41\n"
"v_lshlrev_b32 v40, 16, v80\n"
"v_and_b32 v41, 0xffff0000, v80\n"
"v_add_f32 v32, v32, v40\n"
"v_add_f32 v33, v33, v41\n"
"v_lshlrev_b32 v40, 16, v81\n"
"v_and_b32 v41, 0xffff0000, v81\n"
"v_add_f32 v34, v34, v40\n"
"v_add_f32 v35, v35, v41\n"
"v_lshlrev_b32 v40, 16, v82\n"
"v_and_b32 v41, 0xffff0000, v82\n"
"v_add_f32 v36, v36, v40\n"
"v_add_f32 v37, v37, v41\n"
"v_lshlrev_b32 v40, 16, v83\n"
"v_and_b32 v41, 0xffff0000, v83\n"
"v_add_f32 v38, v38, v40\n"
"v_add_f32 v39, v39, v41\n"
"v_lshlrev_b32 v40, 16, v84\n"
"v_and_b32 v41, 0xffff0000, v84\n"
"v_add_f32 v32, v32, v40\n"
"v_add_f32 v33, v33, v41\n"
"v_lshlrev_b32 v40, 16, v85\n"
"v_and_b32 v41, 0xffff0000, v85\n"
"v_add_f32 v34, v34, v40\n"
"v_add_f32 v35, v35, v41\n"
"v_lshlrev_b32 v40, 16, v86\n"
"v_and_b32 v41, 0xffff0000, v86\n"
"v_add_f32 v36, v36, v40\n"
"v_add_f32 v37, v37, v41\n"
"v_lshlrev_b32 v40, 16, v87\n"
"v_and_b32 v41, 0xffff0000, v87\n"
"v_add_f32 v38, v38, v40\n"
"v_add_f32 v39, v39, v41\n"
"v_cvt_pk_bf16_f32 v42, v32, v33\n"
"v_cvt_pk_bf16_f32 v43, v34, v35\n"
"v_cvt_pk_bf16_f32 v44, v36, v37\n"
"v_cvt_pk_bf16_f32 v45, v38, v39\n"
"buffer_store_dwordx2 v[42:43], v2, s[72:75], s64 offen\n"
"buffer_store_dwordx2 v[44:45], v2, s[72:75], s64 offen offset:32\n"
"s_waitcnt vmcnt(2)\n"
"v_lshlrev_b32 v32, 16, v88\n"
"v_and_b32 v33, 0xffff0000, v88\n"
"v_lshlrev_b32 v34, 16, v89\n"
"v_and_b32 v35, 0xffff0000, v89\n"
"v_lshlrev_b32 v36, 16, v90\n"
"v_and_b32 v37, 0xffff0000, v90\n"
"v_lshlrev_b32 v38, 16, v91\n"
"v_and_b32 v39, 0xffff0000, v91\n"
"v_lshlrev_b32 v40, 16, v92\n"
"v_and_b32 v41, 0xffff0000, v92\n"
"v_add_f32 v32, v32, v40\n"
"v_add_f32 v33, v33, v41\n"
"v_lshlrev_b32 v40, 16, v93\n"
"v_and_b32 v41, 0xffff0000, v93\n"
"v_add_f32 v34, v34, v40\n"
"v_add_f32 v35, v35, v41\n"
"v_lshlrev_b32 v40, 16, v94\n"
"v_and_b32 v41, 0xffff0000, v94\n"
"v_add_f32 v36, v36, v40\n"
"v_add_f32 v37, v37, v41\n"
"v_lshlrev_b32 v40, 16, v95\n"
"v_and_b32 v41, 0xffff0000, v95\n"
"v_add_f32 v38, v38, v40\n"
"v_add_f32 v39, v39, v41\n"
"v_lshlrev_b32 v40, 16, v96\n"
"v_and_b32 v41, 0xffff0000, v96\n"
"v_add_f32 v32, v32, v40\n"
"v_add_f32 v33, v33, v41\n"
"v_lshlrev_b32 v40, 16, v97\n"
"v_and_b32 v41, 0xffff0000, v97\n"
"v_add_f32 v34, v34, v40\n"
"v_add_f32 v35, v35, v41\n"
"v_lshlrev_b32 v40, 16, v98\n"
"v_and_b32 v41, 0xffff0000, v98\n"
"v_add_f32 v36, v36, v40\n"
"v_add_f32 v37, v37, v41\n"
"v_lshlrev_b32 v40, 16, v99\n"
"v_and_b32 v41, 0xffff0000, v99\n"
"v_add_f32 v38, v38, v40\n"
"v_add_f32 v39, v39, v41\n"
"v_lshlrev_b32 v40, 16, v100\n"
"v_and_b32 v41, 0xffff0000, v100\n"
"v_add_f32 v32, v32, v40\n"
"v_add_f32 v33, v33, v41\n"
"v_lshlrev_b32 v40, 16, v101\n"
"v_and_b32 v41, 0xffff0000, v101\n"
"v_add_f32 v34, v34, v40\n"
"v_add_f32 v35, v35, v41\n"
"v_lshlrev_b32 v40, 16, v102\n"
"v_and_b32 v41, 0xffff0000, v102\n"
"v_add_f32 v36, v36, v40\n"
"v_add_f32 v37, v37, v41\n"
"v_lshlrev_b32 v40, 16, v103\n"
"v_and_b32 v41, 0xffff0000, v103\n"
"v_add_f32 v38, v38, v40\n"
"v_add_f32 v39, v39, v41\n"
"v_lshlrev_b32 v40, 16, v104\n"
"v_and_b32 v41, 0xffff0000, v104\n"
"v_add_f32 v32, v32, v40\n"
"v_add_f32 v33, v33, v41\n"
"v_lshlrev_b32 v40, 16, v105\n"
"v_and_b32 v41, 0xffff0000, v105\n"
"v_add_f32 v34, v34, v40\n"
"v_add_f32 v35, v35, v41\n"
"v_lshlrev_b32 v40, 16, v106\n"
"v_and_b32 v41, 0xffff0000, v106\n"
"v_add_f32 v36, v36, v40\n"
"v_add_f32 v37, v37, v41\n"
"v_lshlrev_b32 v40, 16, v107\n"
"v_and_b32 v41, 0xffff0000, v107\n"
"v_add_f32 v38, v38, v40\n"
"v_add_f32 v39, v39, v41\n"
"v_lshlrev_b32 v40, 16, v108\n"
"v_and_b32 v41, 0xffff0000, v108\n"
"v_add_f32 v32, v32, v40\n"
"v_add_f32 v33, v33, v41\n"
"v_lshlrev_b32 v40, 16, v109\n"
"v_and_b32 v41, 0xffff0000, v109\n"
"v_add_f32 v34, v34, v40\n"
"v_add_f32 v35, v35, v41\n"
"v_lshlrev_b32 v40, 16, v110\n"
"v_and_b32 v41, 0xffff0000, v110\n"
"v_add_f32 v36, v36, v40\n"
"v_add_f32 v37, v37, v41\n"
"v_lshlrev_b32 v40, 16, v111\n"
"v_and_b32 v41, 0xffff0000, v111\n"
"v_add_f32 v38, v38, v40\n"
"v_add_f32 v39, v39, v41\n"
"v_lshlrev_b32 v40, 16, v112\n"
"v_and_b32 v41, 0xffff0000, v112\n"
"v_add_f32 v32, v32, v40\n"
"v_add_f32 v33, v33, v41\n"
"v_lshlrev_b32 v40, 16, v113\n"
"v_and_b32 v41, 0xffff0000, v113\n"
"v_add_f32 v34, v34, v40\n"
"v_add_f32 v35, v35, v41\n"
"v_lshlrev_b32 v40, 16, v114\n"
"v_and_b32 v41, 0xffff0000, v114\n"
"v_add_f32 v36, v36, v40\n"
"v_add_f32 v37, v37, v41\n"
"v_lshlrev_b32 v40, 16, v115\n"
"v_and_b32 v41, 0xffff0000, v115\n"
"v_add_f32 v38, v38, v40\n"
"v_add_f32 v39, v39, v41\n"
"v_lshlrev_b32 v40, 16, v116\n"
"v_and_b32 v41, 0xffff0000, v116\n"
"v_add_f32 v32, v32, v40\n"
"v_add_f32 v33, v33, v41\n"
"v_lshlrev_b32 v40, 16, v117\n"
"v_and_b32 v41, 0xffff0000, v117\n"
"v_add_f32 v34, v34, v40\n"
"v_add_f32 v35, v35, v41\n"
"v_lshlrev_b32 v40, 16, v118\n"
"v_and_b32 v41, 0xffff0000, v118\n"
"v_add_f32 v36, v36, v40\n"
"v_add_f32 v37, v37, v41\n"
"v_lshlrev_b32 v40, 16, v119\n"
"v_and_b32 v41, 0xffff0000, v119\n"
"v_add_f32 v38, v38, v40\n"
"v_add_f32 v39, v39, v41\n"
"v_cvt_pk_bf16_f32 v42, v32, v33\n"
"v_cvt_pk_bf16_f32 v43, v34, v35\n"
"v_cvt_pk_bf16_f32 v44, v36, v37\n"
"v_cvt_pk_bf16_f32 v45, v38, v39\n"
"buffer_store_dwordx2 v[42:43], v2, s[72:75], s65 offen\n"
"buffer_store_dwordx2 v[44:45], v2, s[72:75], s65 offen offset:32\n"
"s_branch L_k3fin_%=\n"
"L_k3end_%=:\n"
"v_and_b32 v2, 15, v0\n"
"v_mul_u32_u24 v2, 896, v2\n"
"v_lshrrev_b32 v3, 4, v0\n"
"v_lshl_add_u32 v2, v3, 3, v2\n"
"s_waitcnt vmcnt(0)\n"
"v_lshlrev_b32 v32, 16, v56\n"
"v_and_b32 v33, 0xffff0000, v56\n"
"v_lshlrev_b32 v34, 16, v57\n"
"v_and_b32 v35, 0xffff0000, v57\n"
"v_lshlrev_b32 v36, 16, v58\n"
"v_and_b32 v37, 0xffff0000, v58\n"
"v_lshlrev_b32 v38, 16, v59\n"
"v_and_b32 v39, 0xffff0000, v59\n"
"v_lshlrev_b32 v40, 16, v60\n"
"v_and_b32 v41, 0xffff0000, v60\n"
"v_add_f32 v32, v32, v40\n"
"v_add_f32 v33, v33, v41\n"
"v_lshlrev_b32 v40, 16, v61\n"
"v_and_b32 v41, 0xffff0000, v61\n"
"v_add_f32 v34, v34, v40\n"
"v_add_f32 v35, v35, v41\n"
"v_lshlrev_b32 v40, 16, v62\n"
"v_and_b32 v41, 0xffff0000, v62\n"
"v_add_f32 v36, v36, v40\n"
"v_add_f32 v37, v37, v41\n"
"v_lshlrev_b32 v40, 16, v63\n"
"v_and_b32 v41, 0xffff0000, v63\n"
"v_add_f32 v38, v38, v40\n"
"v_add_f32 v39, v39, v41\n"
"v_lshlrev_b32 v40, 16, v64\n"
"v_and_b32 v41, 0xffff0000, v64\n"
"v_add_f32 v32, v32, v40\n"
"v_add_f32 v33, v33, v41\n"
"v_lshlrev_b32 v40, 16, v65\n"
"v_and_b32 v41, 0xffff0000, v65\n"
"v_add_f32 v34, v34, v40\n"
"v_add_f32 v35, v35, v41\n"
"v_lshlrev_b32 v40, 16, v66\n"
"v_and_b32 v41, 0xffff0000, v66\n"
"v_add_f32 v36, v36, v40\n"
"v_add_f32 v37, v37, v41\n"
"v_lshlrev_b32 v40, 16, v67\n"
"v_and_b32 v41, 0xffff0000, v67\n"
"v_add_f32 v38, v38, v40\n"
"v_add_f32 v39, v39, v41\n"
"v_lshlrev_b32 v40, 16, v68\n"
"v_and_b32 v41, 0xffff0000, v68\n"
"v_add_f32 v32, v32, v40\n"
"v_add_f32 v33, v33, v41\n"
"v_lshlrev_b32 v40, 16, v69\n"
"v_and_b32 v41, 0xffff0000, v69\n"
"v_add_f32 v34, v34, v40\n"
"v_add_f32 v35, v35, v41\n"
"v_lshlrev_b32 v40, 16, v70\n"
"v_and_b32 v41, 0xffff0000, v70\n"
"v_add_f32 v36, v36, v40\n"
"v_add_f32 v37, v37, v41\n"
"v_lshlrev_b32 v40, 16, v71\n"
"v_and_b32 v41, 0xffff0000, v71\n"
"v_add_f32 v38, v38, v40\n"
"v_add_f32 v39, v39, v41\n"
"v_lshlrev_b32 v40, 16, v72\n"
"v_and_b32 v41, 0xffff0000, v72\n"
"v_add_f32 v32, v32, v40\n"
"v_add_f32 v33, v33, v41\n"
"v_lshlrev_b32 v40, 16, v73\n"
"v_and_b32 v41, 0xffff0000, v73\n"
"v_add_f32 v34, v34, v40\n"
"v_add_f32 v35, v35, v41\n"
"v_lshlrev_b32 v40, 16, v74\n"
"v_and_b32 v41, 0xffff0000, v74\n"
"v_add_f32 v36, v36, v40\n"
"v_add_f32 v37, v37, v41\n"
"v_lshlrev_b32 v40, 16, v75\n"
"v_and_b32 v41, 0xffff0000, v75\n"
"v_add_f32 v38, v38, v40\n"
"v_add_f32 v39, v39, v41\n"
"v_lshlrev_b32 v40, 16, v76\n"
"v_and_b32 v41, 0xffff0000, v76\n"
"v_add_f32 v32, v32, v40\n"
"v_add_f32 v33, v33, v41\n"
"v_lshlrev_b32 v40, 16, v77\n"
"v_and_b32 v41, 0xffff0000, v77\n"
"v_add_f32 v34, v34, v40\n"
"v_add_f32 v35, v35, v41\n"
"v_lshlrev_b32 v40, 16, v78\n"
"v_and_b32 v41, 0xffff0000, v78\n"
"v_add_f32 v36, v36, v40\n"
"v_add_f32 v37, v37, v41\n"
"v_lshlrev_b32 v40, 16, v79\n"
"v_and_b32 v41, 0xffff0000, v79\n"
"v_add_f32 v38, v38, v40\n"
"v_add_f32 v39, v39, v41\n"
"v_lshlrev_b32 v40, 16, v80\n"
"v_and_b32 v41, 0xffff0000, v80\n"
"v_add_f32 v32, v32, v40\n"
"v_add_f32 v33, v33, v41\n"
"v_lshlrev_b32 v40, 16, v81\n"
"v_and_b32 v41, 0xffff0000, v81\n"
"v_add_f32 v34, v34, v40\n"
"v_add_f32 v35, v35, v41\n"
"v_lshlrev_b32 v40, 16, v82\n"
"v_and_b32 v41, 0xffff0000, v82\n"
"v_add_f32 v36, v36, v40\n"
"v_add_f32 v37, v37, v41\n"
"v_lshlrev_b32 v40, 16, v83\n"
"v_and_b32 v41, 0xffff0000, v83\n"
"v_add_f32 v38, v38, v40\n"
"v_add_f32 v39, v39, v41\n"
"v_lshlrev_b32 v40, 16, v84\n"
"v_and_b32 v41, 0xffff0000, v84\n"
"v_add_f32 v32, v32, v40\n"
"v_add_f32 v33, v33, v41\n"
"v_lshlrev_b32 v40, 16, v85\n"
"v_and_b32 v41, 0xffff0000, v85\n"
"v_add_f32 v34, v34, v40\n"
"v_add_f32 v35, v35, v41\n"
"v_lshlrev_b32 v40, 16, v86\n"
"v_and_b32 v41, 0xffff0000, v86\n"
"v_add_f32 v36, v36, v40\n"
"v_add_f32 v37, v37, v41\n"
"v_lshlrev_b32 v40, 16, v87\n"
"v_and_b32 v41, 0xffff0000, v87\n"
"v_add_f32 v38, v38, v40\n"
"v_add_f32 v39, v39, v41\n"
"v_cvt_pk_bf16_f32 v42, v32, v33\n"
"v_cvt_pk_bf16_f32 v43, v34, v35\n"
"v_cvt_pk_bf16_f32 v44, v36, v37\n"
"v_cvt_pk_bf16_f32 v45, v38, v39\n"
"buffer_store_dwordx2 v[42:43], v2, s[72:75], s64 offen\n"
"buffer_store_dwordx2 v[44:45], v2, s[72:75], s64 offen offset:32\n"
"L_k3fin_%=:\n"
"s_waitcnt vmcnt(0)\n"
"L_cpb1_%=:\n"
"GM_CODE_END:\n"
    :: "s"(ka[6]), "s"((int)blockIdx.x), "s"(wid), "s"(ka[8]), "s"(ka[9]), "s"(ka[10]), "s"(ka[3]), "s"(ka[4]), "s"(tag)
    : "v0", "v1", "v2", "v3", "v4", "v5", "v6", "v7", "v8", "v9", "v10", "v11", "v12", "v13", "v14", "v15", "v16", "v17", "v18", "v19", "v20", "v21", "v22", "v23", "v24", "v25", "v26", "v27", "v28", "v29", "v30", "v31", "v32", "v33", "v34", "v35", "v36", "v37", "v38", "v39", "v40", "v41", "v42", "v43", "v44", "v45", "v46", "v47", "v48", "v49", "v50", "v51", "v52", "v53", "v54", "v55", "v56", "v57", "v58", "v59", "v60", "v61", "v62", "v63", "v64", "v65", "v66", "v67", "v68", "v69", "v70", "v71", "v72", "v73", "v74", "v75", "v76", "v77", "v78", "v79", "v80", "v81", "v82", "v83", "v84", "v85", "v86", "v87", "v88", "v89", "v90", "v91", "v92", "v93", "v94", "v95", "v96", "v97", "v98", "v99", "v100", "v101", "v102", "v103", "v104", "v105", "v106", "v107", "v108", "v109", "v110", "v111", "v112", "v113", "v114", "v115", "v116", "v117", "v118", "v119", "v120", "v121", "v122", "v123", "v124", "v125", "v126", "v127", "v128", "v129", "v130", "v131", "v132", "v133", "v134", "v135", "v136", "v137", "v138", "v139", "v140", "v141", "v142", "v143", "v144", "v145", "v146", "v147", "v148", "v149", "v150", "v151", "v152", "v153", "v154", "v155", "v156", "v157", "v158", "v159", "v160", "v161", "v162", "v163", "v164", "v165", "v166", "v167", "v168", "v169", "v170", "v171", "v172", "v173", "v174", "v175", "v176", "v177", "v178", "v179", "v180", "v181", "v182", "v183", "v184", "v185", "v186", "v187", "v188", "v189", "v190", "v191", "v192", "v193", "v194", "v195", "v196", "v197", "v198", "v199", "v200", "v201", "v202", "v203", "v204", "v205", "v206", "v207", "v208", "v209", "v210", "v211", "v212", "v213", "v214", "v215", "v216", "v217", "v218", "v219", "v220", "v221", "v222", "v223", "v224", "v225", "v226", "v227", "v228", "v229", "v230", "v231", "v232", "v233", "v234", "v235", "v236", "v237", "v238", "v239", "v240", "v241", "v242", "v243", "v244", "v245", "v246", "v247", "v248", "v249", "v250", "v251", "v252", "v253", "v254", "v255", "a0", "a1", "a2", "a3", "a4", "a5", "a6", "a7", "a8", "a9", "a10", "a11", "a12", "a13", "a14", "a15", "a16", "a17", "a18", "a19", "a20", "a21", "a22", "a23", "a24", "a25", "a26", "a27", "a28", "a29", "a30", "a31", "a32", "a33", "a34", "a35", "a36", "a37", "a38", "a39", "a40", "a41", "a42", "a43", "a44", "a45", "a46", "a47", "a48", "a49", "a50", "a51", "a52", "a53", "a54", "a55", "a56", "a57", "a58", "a59", "a60", "a61", "a62", "a63", "a64", "a65", "a66", "a67", "a68", "a69", "a70", "a71", "a72", "a73", "a74", "a75", "a76", "a77", "a78", "a79", "a80", "a81", "a82", "a83", "a84", "a85", "a86", "a87", "a88", "a89", "a90", "a91", "a92", "a93", "a94", "a95", "a96", "a97", "a98", "a99", "a100", "a101", "a102", "a103", "a104", "a105", "a106", "a107", "a108", "a109", "a110", "a111", "a112", "a113", "a114", "a115", "a116", "a117", "a118", "a119", "a120", "a121", "a122", "a123", "a124", "a125", "a126", "a127", "a128", "a129", "a130", "a131", "a132", "a133", "a134", "a135", "a136", "a137", "a138", "a139", "a140", "a141", "a142", "a143", "a144", "a145", "a146", "a147", "a148", "a149", "a150", "a151", "a152", "a153", "a154", "a155", "a156", "a157", "a158", "a159", "a160", "a161", "a162", "a163", "a164", "a165", "a166", "a167", "a168", "a169", "a170", "a171", "a172", "a173", "a174", "a175", "a176", "a177", "a178", "a179", "a180", "a181", "a182", "a183", "a184", "a185", "a186", "a187", "a188", "a189", "a190", "a191", "a192", "a193", "a194", "a195", "a196", "a197", "a198", "a199", "a200", "a201", "a202", "a203", "a204", "a205", "a206", "a207", "a208", "a209", "a210", "a211", "a212", "a213", "a214", "a215", "a216", "a217", "a218", "a219", "a220", "a221", "a222", "a223", "a224", "a225", "a226", "a227", "a228", "a229", "a230", "a231", "a232", "a233", "a234", "a235", "a236", "a237", "a238", "a239", "a240", "a241", "a242", "a243", "a244", "a245", "a246", "a247", "a248", "a249", "a250", "a251", "a252", "a253", "a254", "a255", "s35", "s36", "s37", "s38", "s39", "s40", "s41", "s42", "s43", "s44", "s45", "s46", "s47", "s48", "s49", "s50", "s51", "s52", "s53", "s54", "s55", "s56", "s57", "s58", "s59", "s60", "s61", "s62", "s63", "s64", "s65", "s66", "s67", "s68", "s69", "s70", "s71", "s72", "s73", "s74", "s75", "s76", "s77", "s78", "s79", "s80", "s81", "s82", "s83", "s84", "s85", "s86", "s87", "s88", "s89", "s90", "s91", "s92", "s93", "s94", "s95", "s96", "s97", "s98", "s99", "vcc", "scc", "memory");
  asm volatile("" : "+s"(ka));
  if (blockIdx.x == 0) {
    int lane; asm volatile("v_mbcnt_lo_u32_b32 %0, -1, 0\n\tv_mbcnt_hi_u32_b32 %0, -1, %0" : "=v"(lane));
    const int tid = wid * 64 + lane;
    for (int spin = 0; spin < (1 << 20) && ld_dev32(ARG(6) + F_ARR + 4 * tid) != (u32)tag; ++spin) __builtin_amdgcn_s_sleep(2);
    __syncthreads();
    if (tid == 0) st_dev32(ARG(6) + F_EPOCH, (u32)tag);
  }
}

extern "C" void kernel_launch(void* const* d_in, const int* in_sizes, int n_in, void* d_out, int out_size, void* d_ws, size_t ws_size, hipStream_t stream) {
  (void)hipFuncSetAttribute((const void*)gm_fused, hipFuncAttributeMaxDynamicSharedMemorySize, 163840);
  gm_fused<<<256, 256, 163840, stream>>>(d_in[1], d_in[3], d_in[4], d_in[0], d_in[2], d_in[7], d_ws, d_in[9], d_in[5], d_in[8], d_out);
}
```
